```python
import torch

def _softmax(x, axis=-1):
    return torch.softmax(x, dim=axis)

def _gelu(x):
    return torch.nn.functional.gelu(x, approximate='tanh')

def _silu(x):
    return torch.nn.functional.silu(x)

def _sigmoid(x):
    return torch.sigmoid(x)

def _layernorm(x, axis=-1, eps=1e-05):
    m = x.mean(dim=axis, keepdim=True)
    v = x.var(dim=axis, keepdim=True, unbiased=False)
    return (x - m) / torch.sqrt(v + eps)

def _scatter_add(tgt, idx, src, axis=0):
    return tgt.clone().index_add_(axis, idx, src)

def _segment_sum(src, seg, n):
    out = torch.zeros((n,) + src.shape[1:], dtype=src.dtype)
    return out.index_add_(0, seg, src)

def _pad(x, pw):
    flat = tuple((v for pair in reversed(pw) for v in pair))
    return torch.nn.functional.pad(x, flat)
kernel_name = 'adaLN_cond__spectral_gfnet__dense_activation_silu__prompt'

def setup_inputs():

    _w = lambda a: (lambda t: t.to(torch.bfloat16) if t.is_floating_point() else t)(torch.as_tensor(a))

    def _A_setup():
        B, S, D = (1, 2048, 512)
        Df = D // 2 + 1
        return {'x': _w((lambda *s: torch.randn(*s, dtype=torch.bfloat16) * 0.05)(B, S, D)), 'w_r': _w((lambda *s: torch.randn(*s, dtype=torch.bfloat16) * 1.6)(S, Df) * 0.02), 'w_i': _w((lambda *s: torch.randn(*s, dtype=torch.bfloat16) * 1.6)(S, Df) * 0.02)}
    _A_in = _A_setup()

    def _B_setup():
        B, S, D, F = (1, 2048, 512, 1792)
        return {'x': _w((lambda *s: torch.randn(*s, dtype=torch.bfloat16) * 0.05)(B, S, D)), 'w1': _w((lambda *s: torch.randn(*s, dtype=torch.bfloat16) * 0.8)(D, F) * D ** (-0.5)), 'w2': _w((lambda *s: torch.randn(*s, dtype=torch.bfloat16) * 0.2)(F, D) * F ** (-0.5))}
    _B_in = _B_setup()
    return {'A_x': _A_in['x'], 'A_w_r': _A_in['w_r'], 'A_w_i': _A_in['w_i'], 'B_x': _B_in['x'], 'B_w1': _B_in['w1'], 'B_w2': _B_in['w2'], 'c': _w((lambda *s: torch.randn(*s, dtype=torch.bfloat16) * 0.2)(1, 512) * 8.0), 'w_mod': _w((lambda *s: torch.randn(*s, dtype=torch.bfloat16) * 0.2)(512, 6 * 512) * 512 ** (-0.5) * 8.0)}

def reference(A_x, A_w_r, A_w_i, B_x, B_w1, B_w2, c, w_mod):

    def _A_ref(x, w_r, w_i):
        B, S, D = x.shape
        X = (lambda x, axes=(-2, -1): torch.fft.rfft2(x.float(), dim=axes))(x, axes=(-2, -1))
        W = w_r + 1j * w_i
        X = X * W
        y = (lambda x, s, axes=(-2, -1): torch.fft.irfft2(x, s=s, dim=axes).to(torch.bfloat16))(X, (S, D), axes=(-2, -1))
        return y

    def _B_ref(x, w1, w2):
        h = torch.einsum('bsd,df->bsf', x, w1)
        h = _silu(h)
        out = torch.einsum('bsf,fd->bsd', h, w2)
        return out
    x = A_x
    mod = (_silu(c) @ w_mod).reshape(c.shape[0], 6, 1, -1)
    sa, ca, ga, sb, cb, gb = (mod[:, 0].clone(), mod[:, 1].clone(), mod[:, 2].clone(), mod[:, 3].clone(), mod[:, 4].clone(), mod[:, 5].clone())
    h = _layernorm(x, axis=-1) * (1 + ca) + sa
    x = x + ga * _A_ref(h, A_w_r, A_w_i)
    h = _layernorm(x, axis=-1) * (1 + cb) + sb
    x = x + gb * _B_ref(h, B_w1, B_w2)
    return x
```

```cpp
#include <hip/hip_runtime.h>
#include <hip/hip_bf16.h>
#include <stdint.h>

typedef float f2 __attribute__((ext_vector_type(2)));
typedef float f4 __attribute__((ext_vector_type(4)));
typedef unsigned u2v __attribute__((ext_vector_type(2)));
typedef unsigned u4v __attribute__((ext_vector_type(4)));
typedef short bf16x8 __attribute__((ext_vector_type(8)));
typedef unsigned short u16;

constexpr int S_ = 2048, D_ = 512, F_ = 1792, DF_ = 257;
constexpr int NSPLIT = 4;

constexpr size_t OFF_MODP = 0;
constexpr size_t OFF_MODF = OFF_MODP + (size_t)NSPLIT * 3072 * 4;
constexpr size_t OFF_WT = 1 << 20;
constexpr size_t OFF_XT = 4 << 20;
constexpr size_t OFF_ZT = 8 << 20;
constexpr size_t OFF_H2F = 12 << 20;
constexpr size_t OFF_X1 = 14 << 20;
constexpr size_t OFF_W1F = 16 << 20;
constexpr size_t OFF_W2F = 18 << 20;
constexpr size_t OFF_HF = 20 << 20;

__device__ __forceinline__ float bf_lo(unsigned u) { return __builtin_bit_cast(float, u << 16); }
__device__ __forceinline__ float bf_hi(unsigned u) { return __builtin_bit_cast(float, u & 0xffff0000u); }
__device__ __forceinline__ float bfr(float x) {
  return (float)(__bf16)x;
}
__device__ __forceinline__ unsigned pack_bf(float lo, float hi) {
  typedef __bf16 b2 __attribute__((ext_vector_type(2)));
  b2 v;
  v.x = (__bf16)lo;
  v.y = (__bf16)hi;
  return __builtin_bit_cast(unsigned, v);
}
__device__ __forceinline__ f2 cmul(f2 a, f2 b) { return f2{a.x * b.x - a.y * b.y, a.x * b.y + a.y * b.x}; }
__device__ __forceinline__ f2 cmulc(f2 a, f2 b) { return f2{a.x * b.x + a.y * b.y, a.y * b.x - a.x * b.y}; }
__device__ __forceinline__ f2 twid(float rev) { return f2{__builtin_amdgcn_cosf(rev), -__builtin_amdgcn_sinf(rev)}; }

template <int DIR>
__device__ __forceinline__ f2 rot90(f2 a) {
  return DIR < 0 ? f2{a.y, -a.x} : f2{-a.y, a.x};
}
template <int DIR>
__device__ __forceinline__ void dft4(f2& c0, f2& c1, f2& c2, f2& c3) {
  f2 d0 = c0 + c2, d2 = c0 - c2, d1 = c1 + c3, d3 = rot90<DIR>(c1 - c3);
  c0 = d0 + d1;
  c2 = d0 - d1;
  c1 = d2 + d3;
  c3 = d2 - d3;
}
template <int DIR>
__device__ __forceinline__ void dft8(f2 (&v)[8]) {
  constexpr float R = 0.70710678118654752440f;
  f2 b0 = v[0] + v[4], b4 = v[0] - v[4];
  f2 b1 = v[1] + v[5], b5 = v[1] - v[5];
  f2 b2_ = v[2] + v[6], b6 = v[2] - v[6];
  f2 b3 = v[3] + v[7], b7 = v[3] - v[7];
  f2 t5 = rot90<DIR>(b5);
  b5 = (b5 + t5) * R;
  b6 = rot90<DIR>(b6);
  f2 t7 = rot90<DIR>(b7);
  b7 = (t7 - b7) * R;
  dft4<DIR>(b0, b1, b2_, b3);
  dft4<DIR>(b4, b5, b6, b7);
  v[0] = b0; v[2] = b1; v[4] = b2_; v[6] = b3;
  v[1] = b4; v[3] = b5; v[5] = b6; v[7] = b7;
}

__device__ __forceinline__ void wave_sync() {
  __builtin_amdgcn_fence(__ATOMIC_RELEASE, "wavefront");
  __builtin_amdgcn_wave_barrier();
  __builtin_amdgcn_fence(__ATOMIC_ACQUIRE, "wavefront");
}

__device__ __forceinline__ void fft256_fwd(f2 (&v)[8], int l, f2* buf, const f2 (&tw2)[8], const f2 (&tw3)[8]) {
  dft8<-1>(v);
#pragma unroll
  for (int r = 1; r < 8; ++r) v[r] = cmul(v[r], tw2[r]);
#pragma unroll
  for (int r = 0; r < 8; ++r) buf[36 * r + l] = v[r];
  wave_sync();
  const int r2 = l >> 2, c = l & 3;
#pragma unroll
  for (int j = 0; j < 8; ++j) v[j] = buf[36 * r2 + c + 4 * j];
  wave_sync();
  dft8<-1>(v);
#pragma unroll
  for (int r = 1; r < 8; ++r) v[r] = cmul(v[r], tw3[r]);
#pragma unroll
  for (int r = 0; r < 8; ++r) buf[36 * r2 + 9 * c + r] = v[r];
  wave_sync();
#pragma unroll
  for (int e = 0; e < 2; ++e)
#pragma unroll
    for (int cc = 0; cc < 4; ++cc) v[4 * e + cc] = buf[36 * r2 + 9 * cc + c + 4 * e];
  wave_sync();
  dft4<-1>(v[0], v[1], v[2], v[3]);
  dft4<-1>(v[4], v[5], v[6], v[7]);
}
__device__ __forceinline__ void fft256_inv(f2 (&v)[8], int l, f2* buf, const f2 (&tw2)[8], const f2 (&tw3)[8]) {
  const int r2 = l >> 2, c = l & 3;
  dft4<1>(v[0], v[1], v[2], v[3]);
  dft4<1>(v[4], v[5], v[6], v[7]);
#pragma unroll
  for (int e = 0; e < 2; ++e)
#pragma unroll
    for (int cc = 0; cc < 4; ++cc) buf[36 * r2 + 9 * cc + c + 4 * e] = v[4 * e + cc];
  wave_sync();
#pragma unroll
  for (int r = 0; r < 8; ++r) v[r] = buf[36 * r2 + 9 * c + r];
  wave_sync();
#pragma unroll
  for (int r = 1; r < 8; ++r) v[r] = cmulc(v[r], tw3[r]);
  dft8<1>(v);
#pragma unroll
  for (int j = 0; j < 8; ++j) buf[36 * r2 + c + 4 * j] = v[j];
  wave_sync();
#pragma unroll
  for (int r = 0; r < 8; ++r) v[r] = buf[36 * r + l];
  wave_sync();
#pragma unroll
  for (int r = 1; r < 8; ++r) v[r] = cmulc(v[r], tw2[r]);
  dft8<1>(v);
}
__device__ __forceinline__ void make_tw23(int l, f2 (&tw2)[8], f2 (&tw3)[8]) {
  const int c = l & 3;
  tw2[0] = f2{1.f, 0.f};
  tw3[0] = f2{1.f, 0.f};
#pragma unroll
  for (int r = 1; r < 8; ++r) {
    tw2[r] = twid((float)(l * r) * (1.0f / 256.0f));
    tw3[r] = twid((float)(c * r) * (1.0f / 32.0f));
  }
}

__device__ __forceinline__ float half_wave_sum(float x) {
  x += __shfl_xor(x, 1);
  x += __shfl_xor(x, 2);
  x += __shfl_xor(x, 4);
  x += __shfl_xor(x, 8);
  x += __shfl_xor(x, 16);
  return x;
}

constexpr int K0_GEMV = 48 * NSPLIT, K0_WT = 256, K0_W1 = 224, K0_W2 = 224;
__global__ __launch_bounds__(256) void k0_prep(const u16* __restrict__ c, const u16* __restrict__ wmod,
                                               const u16* __restrict__ wr, const u16* __restrict__ wi,
                                               const u16* __restrict__ w1, const u16* __restrict__ w2,
                                               char* __restrict__ ws) {
  __shared__ __attribute__((aligned(16))) char smem[8704 + 64];
  const int tid = threadIdx.x;
  int b = blockIdx.x;
  if (b < K0_GEMV) {
    const int cgp = b % 48, ks = b / 48;
    const int cg = tid & 7, r = tid >> 3;
    constexpr int RPS = 512 / NSPLIT;
    float acc[8];
#pragma unroll
    for (int i = 0; i < 8; ++i) acc[i] = 0.f;
    u4v wv[RPS / 32];
    float sc[RPS / 32];
#pragma unroll
    for (int it = 0; it < RPS / 32; ++it) {
      const int row = ks * RPS + it * 32 + r;
      wv[it] = *(const u4v*)(wmod + (size_t)row * 3072 + cgp * 64 + cg * 8);
      float cv = bf_lo(c[row]);
      sc[it] = bfr(cv / (1.0f + __expf(-cv)));
    }
#pragma unroll
    for (int it = 0; it < RPS / 32; ++it) {
#pragma unroll
      for (int i = 0; i < 4; ++i) {
        acc[2 * i] += sc[it] * bf_lo(wv[it][i]);
        acc[2 * i + 1] += sc[it] * bf_hi(wv[it][i]);
      }
    }
#pragma unroll
    for (int i = 0; i < 8; ++i) {
      acc[i] += __shfl_xor(acc[i], 8);
      acc[i] += __shfl_xor(acc[i], 16);
      acc[i] += __shfl_xor(acc[i], 32);
    }
    float* red = (float*)smem;
    if ((tid & 63) < 8) {
#pragma unroll
      for (int i = 0; i < 8; ++i) red[(tid >> 6) * 64 + cg * 8 + i] = acc[i];
    }
    __syncthreads();
    if (tid < 64) {
      float s = red[tid] + red[64 + tid] + red[128 + tid] + red[192 + tid];
      ((float*)(ws + OFF_MODP))[ks * 3072 + cgp * 64 + tid] = s;
    }
    return;
  }
  b -= K0_GEMV;
  if (b < K0_WT) {
    u16* sr = (u16*)smem;
    u16* si = (u16*)(smem + 4352);
    const size_t base = (size_t)b * 8 * DF_;
    ((u4v*)sr)[tid] = *(const u4v*)(wr + base + tid * 8);
    ((u4v*)si)[tid] = *(const u4v*)(wi + base + tid * 8);
    if (tid == 0) {
      ((u4v*)sr)[256] = *(const u4v*)(wr + base + 2048);
      ((u4v*)si)[256] = *(const u4v*)(wi + base + 2048);
    }
    __syncthreads();
    unsigned* WT = (unsigned*)(ws + OFF_WT);
    for (int k = tid; k < DF_; k += 256) {
      u4v o0, o1;
#pragma unroll
      for (int r = 0; r < 4; ++r) {
        o0[r] = (unsigned)sr[r * DF_ + k] | ((unsigned)si[r * DF_ + k] << 16);
        o1[r] = (unsigned)sr[(r + 4) * DF_ + k] | ((unsigned)si[(r + 4) * DF_ + k] << 16);
      }
      u4v* dst = (u4v*)(WT + (size_t)k * S_ + b * 8);
      dst[0] = o0;
      dst[1] = o1;
    }
    return;
  }
  b -= K0_WT;
  if (b < K0_W1) {
    const int fg = b % 14, ks = b / 14;
    u16* t = (u16*)smem;
#pragma unroll
    for (int it = 0; it < 2; ++it) {
      const int idx = it * 256 + tid;
      const int rr = idx >> 4, ch = idx & 15;
      const int g = rr >> 3, j = rr & 7;
      const int ci = ks * 4 + g, half = ci >> 5, l = ci & 31;
      const int d = 2 * l + 64 * (4 * half + (j >> 1)) + (j & 1);
      *(u4v*)(t + rr * 136 + ch * 8) = *(const u4v*)(w1 + (size_t)d * F_ + fg * 128 + ch * 8);
    }
    __syncthreads();
    const int lane = tid & 63, w = tid >> 6, g = lane >> 4, fr = lane & 15;
#pragma unroll
    for (int i = 0; i < 2; ++i) {
      const int ftl = w * 2 + i;
      u4v o;
#pragma unroll
      for (int jj = 0; jj < 4; ++jj)
        o[jj] = (unsigned)t[(g * 8 + 2 * jj) * 136 + ftl * 16 + fr] |
                ((unsigned)t[(g * 8 + 2 * jj + 1) * 136 + ftl * 16 + fr] << 16);
      const int ft = fg * 8 + ftl;
      *(u4v*)(ws + OFF_W1F + ((size_t)(ft * 16 + ks) * 64 + lane) * 16) = o;
    }
    return;
  }
  b -= K0_W1;
  {
    const int ng = b & 3, ks2 = b >> 2;
    const int e = ks2 / 7, i7 = ks2 % 7;
    int T0, T1;
    if (i7 < 3) { T0 = 14 * e + 2 * i7; T1 = T0 + 1; }
    else if (i7 < 6) { T0 = 14 * e + 7 + 2 * (i7 - 3); T1 = T0 + 1; }
    else { T0 = 14 * e + 6; T1 = 14 * e + 13; }
    u16* t = (u16*)smem;
#pragma unroll
    for (int it = 0; it < 2; ++it) {
      const int idx = it * 256 + tid;
      const int rr = idx >> 4, ch = idx & 15;
      const int g = rr >> 3, j = rr & 7;
      const int f = (j < 4) ? (16 * T0 + 4 * g + j) : (16 * T1 + 4 * g + (j - 4));
      *(u4v*)(t + rr * 136 + ch * 8) = *(const u4v*)(w2 + (size_t)f * D_ + ng * 128 + ch * 8);
    }
    __syncthreads();
    const int lane = tid & 63, w = tid >> 6, g = lane >> 4, r = lane & 15;
#pragma unroll
    for (int i = 0; i < 2; ++i) {
      const int ntl = w * 2 + i;
      const int nl = 32 * w + 8 * (r >> 2) + 4 * i + (r & 3);
      u4v o;
#pragma unroll
      for (int jj = 0; jj < 4; ++jj)
        o[jj] = (unsigned)t[(g * 8 + 2 * jj) * 136 + nl] | ((unsigned)t[(g * 8 + 2 * jj + 1) * 136 + nl] << 16);
      const int nt = ng * 8 + ntl;
      *(u4v*)(ws + OFF_W2F + ((size_t)(nt * 56 + ks2) * 64 + lane) * 16) = o;
    }
  }
}

constexpr int ZRS = 288;
__global__ __launch_bounds__(256) void k1_rowfft(const u16* __restrict__ x, char* __restrict__ ws) {
  __shared__ __attribute__((aligned(16))) f2 zs[8 * ZRS];
  __shared__ __attribute__((aligned(16))) float mods[1024];
  const int tid = threadIdx.x, row = tid >> 5, l = tid & 31;
  const int row0 = blockIdx.x * 8;
  unsigned xr[8];
  const unsigned* xrow = (const unsigned*)(x + (size_t)(row0 + row) * D_);
#pragma unroll
  for (int j = 0; j < 8; ++j) xr[j] = xrow[l + 32 * j];
  {
    const float* mp = (const float*)(ws + OFF_MODP);
    f4 s = *(const f4*)(mp + tid * 4);
#pragma unroll
    for (int k = 1; k < NSPLIT; ++k) s += *(const f4*)(mp + k * 3072 + tid * 4);
    f4 o;
#pragma unroll
    for (int i = 0; i < 4; ++i) {
      float v = bfr(s[i]);
      o[i] = (tid >= 128) ? bfr(1.0f + v) : v;
    }
    *(f4*)(mods + tid * 4) = o;
    if (blockIdx.x < 3) {
      const int idx = blockIdx.x * 1024 + tid * 4;
      f4 t = *(const f4*)(mp + idx);
#pragma unroll
      for (int k = 1; k < NSPLIT; ++k) t += *(const f4*)(mp + k * 3072 + idx);
      f4 q;
#pragma unroll
      for (int i = 0; i < 4; ++i) q[i] = bfr(t[i]);
      *(f4*)((float*)(ws + OFF_MODF) + idx) = q;
    }
  }
  f2 tw2[8], tw3[8];
  make_tw23(l, tw2, tw3);
  f2 v[8];
  float s = 0.f;
#pragma unroll
  for (int j = 0; j < 8; ++j) {
    v[j] = f2{bf_lo(xr[j]), bf_hi(xr[j])};
    s += v[j].x + v[j].y;
  }
  s = half_wave_sum(s);
  const float meanf = s * (1.0f / D_);
  float q = 0.f;
#pragma unroll
  for (int j = 0; j < 8; ++j) {
    f2 d = v[j] - meanf;
    q += d.x * d.x + d.y * d.y;
  }
  q = half_wave_sum(q);
  const float mean = bfr(meanf);
  const float den = bfr(sqrtf(bfr(bfr(q * (1.0f / D_)) + 1e-5f)));
  const float rden = 1.0f / den;
  __syncthreads();
#pragma unroll
  for (int j = 0; j < 8; ++j) {
    const int d = 2 * (l + 32 * j);
    f2 sa = *(const f2*)(mods + d);
    f2 ca1 = *(const f2*)(mods + 512 + d);
    float a = bfr(bfr(v[j].x - mean) * rden), bb = bfr(bfr(v[j].y - mean) * rden);
    v[j].x = bfr(bfr(a * ca1.x) + sa.x);
    v[j].y = bfr(bfr(bb * ca1.y) + sa.y);
  }
  f2* buf = zs + row * ZRS;
  fft256_fwd(v, l, buf, tw2, tw3);
  {
    const int r2 = l >> 2, qq = l & 3;
#pragma unroll
    for (int e = 0; e < 2; ++e)
#pragma unroll
      for (int r4 = 0; r4 < 4; ++r4) buf[r2 + 8 * (qq + 4 * e) + 64 * r4] = v[4 * e + r4];
  }
  __syncthreads();
  {
    const int rp = tid & 3, kk = tid >> 2;
    f2* XT = (f2*)(ws + OFF_XT);
#pragma unroll
    for (int i = 0; i < 2; ++i) {
      const int k = kk + 64 * i;
      const int k2 = (k == 0) ? 128 : 256 - k;
      const f2 w = twid((float)k * (1.0f / 512.0f));
      f4 oa, ob;
#pragma unroll
      for (int h = 0; h < 2; ++h) {
        const f2* zr = zs + (2 * rp + h) * ZRS;
        f2 za = zr[k], zb = zr[k2];
        f2 ra, rb;
        if (k == 0) {
          ra = f2{2.f * (za.x + za.y), 2.f * (za.x - za.y)};
          rb = f2{2.f * zb.x, -2.f * zb.y};
        } else {
          f2 E = f2{za.x + zb.x, za.y - zb.y};
          f2 Dm = f2{za.x - zb.x, za.y + zb.y};
          f2 O = f2{Dm.y, -Dm.x};
          f2 wo = cmul(w, O);
          ra = E + wo;
          f2 t = E - wo;
          rb = f2{t.x, -t.y};
        }
        oa[2 * h] = ra.x; oa[2 * h + 1] = ra.y;
        ob[2 * h] = rb.x; ob[2 * h + 1] = rb.y;
      }
      *(f4*)(XT + (size_t)k * S_ + row0 + 2 * rp) = oa;
      *(f4*)(XT + (size_t)k2 * S_ + row0 + 2 * rp) = ob;
    }
  }
}

__global__ __launch_bounds__(256) void k2_colfft(char* __restrict__ ws) {
  __shared__ __attribute__((aligned(16))) f2 buf[8 * 288];
  const int tid = threadIdx.x, l = tid & 31, r1t = tid >> 5;
  const int k = blockIdx.x;
  const f2* XT = (const f2*)(ws + OFF_XT) + (size_t)k * S_;
  f2* ZT = (f2*)(ws + OFF_ZT) + (size_t)k * S_;
  const unsigned* WT = (const unsigned*)(ws + OFF_WT);
  f2 v[8];
#pragma unroll
  for (int j = 0; j < 8; ++j) v[j] = XT[tid + 256 * j];
  const int ubase = r1t + 8 * (l >> 2) + 64 * (l & 3);
  unsigned wv[8];
#pragma unroll
  for (int e = 0; e < 2; ++e)
#pragma unroll
    for (int r4 = 0; r4 < 4; ++r4) wv[4 * e + r4] = WT[(size_t)k * S_ + ubase + 256 * e + 512 * r4];
  f2 tw1[8], tw2[8], tw3[8];
  make_tw23(l, tw2, tw3);
#pragma unroll
  for (int r = 1; r < 8; ++r) tw1[r] = twid((float)(tid * r) * (1.0f / 2048.0f));
  dft8<-1>(v);
#pragma unroll
  for (int r = 1; r < 8; ++r) v[r] = cmul(v[r], tw1[r]);
#pragma unroll
  for (int r = 0; r < 8; ++r) buf[288 * r + tid] = v[r];
  __syncthreads();
  f2* hb = buf + 288 * r1t;
#pragma unroll
  for (int j = 0; j < 8; ++j) v[j] = hb[l + 32 * j];
  wave_sync();
  fft256_fwd(v, l, hb, tw2, tw3);
  constexpr float SC = 1.0f / (2.0f * 2048.0f * 512.0f);
  if (k != 0) {
#pragma unroll
    for (int i = 0; i < 8; ++i) {
      f2 w = f2{bf_lo(wv[i]) * SC, bf_hi(wv[i]) * SC};
      v[i] = cmul(v[i], w);
    }
  } else {
    __syncthreads();
#pragma unroll
    for (int e = 0; e < 2; ++e)
#pragma unroll
      for (int r4 = 0; r4 < 4; ++r4) buf[ubase + 256 * e + 512 * r4] = v[4 * e + r4];
    __syncthreads();
#pragma unroll
    for (int e = 0; e < 2; ++e)
#pragma unroll
      for (int r4 = 0; r4 < 4; ++r4) {
        const int u = ubase + 256 * e + 512 * r4, un = (S_ - u) & (S_ - 1);
        f2 cm = buf[un];
        unsigned a0 = wv[4 * e + r4], a0n = WT[un], b0 = WT[(size_t)256 * S_ + u], b0n = WT[(size_t)256 * S_ + un];
        f2 h0 = f2{bf_lo(a0) + bf_lo(a0n), bf_hi(a0) - bf_hi(a0n)};
        f2 h1 = f2{bf_lo(b0) + bf_lo(b0n), bf_hi(b0) - bf_hi(b0n)};
        f2 wp = (h0 + h1) * (0.25f * SC), wm = (h0 - h1) * (0.25f * SC);
        f2 cc = f2{cm.x, -cm.y};
        v[4 * e + r4] = cmul(v[4 * e + r4], wp) + cmul(cc, wm);
      }
    __syncthreads();
  }
  fft256_inv(v, l, hb, tw2, tw3);
#pragma unroll
  for (int j = 0; j < 8; ++j) hb[l + 32 * j] = v[j];
  __syncthreads();
#pragma unroll
  for (int r = 0; r < 8; ++r) v[r] = buf[288 * r + tid];
#pragma unroll
  for (int r = 1; r < 8; ++r) v[r] = cmulc(v[r], tw1[r]);
  dft8<1>(v);
#pragma unroll
  for (int j = 0; j < 8; ++j) ZT[tid + 256 * j] = v[j];
}

__global__ __launch_bounds__(256) void k3_rowifft(const u16* __restrict__ x, char* __restrict__ ws) {
  __shared__ __attribute__((aligned(16))) f2 zs[8 * ZRS];
  const int tid = threadIdx.x, row = tid >> 5, l = tid & 31;
  const int row0 = blockIdx.x * 8;
  const f2* ZT = (const f2*)(ws + OFF_ZT);
  const float* modf_ = (const float*)(ws + OFF_MODF);
  {
    const int rp = tid & 3, kk = tid >> 2;
    f4 ya[2], yb[2];
#pragma unroll
    for (int i = 0; i < 2; ++i) {
      const int k = kk + 64 * i;
      const int k2 = (k == 0) ? 128 : 256 - k;
      ya[i] = *(const f4*)(ZT + (size_t)k * S_ + row0 + 2 * rp);
      yb[i] = *(const f4*)(ZT + (size_t)k2 * S_ + row0 + 2 * rp);
    }
#pragma unroll
    for (int i = 0; i < 2; ++i) {
      const int k = kk + 64 * i;
      const int k2 = (k == 0) ? 128 : 256 - k;
      const f2 w = twid((float)k * (1.0f / 512.0f));
#pragma unroll
      for (int h = 0; h < 2; ++h) {
        f2 a = f2{ya[i][2 * h], ya[i][2 * h + 1]}, b = f2{yb[i][2 * h], yb[i][2 * h + 1]};
        f2* zr = zs + (2 * rp + h) * ZRS;
        if (k == 0) {
          zr[0] = f2{a.x + a.y, a.x - a.y};
          zr[128] = f2{2.f * b.x, -2.f * b.y};
        } else {
          f2 Ze = f2{a.x + b.x, a.y - b.y};
          f2 Dm = f2{a.x - b.x, a.y + b.y};
          f2 Zo = cmulc(Dm, w);
          zr[k] = f2{Ze.x - Zo.y, Ze.y + Zo.x};
          zr[k2] = f2{Ze.x + Zo.y, -Ze.y + Zo.x};
        }
      }
    }
  }
  unsigned xr[8];
  const unsigned* xrow = (const unsigned*)(x + (size_t)(row0 + row) * D_);
#pragma unroll
  for (int j = 0; j < 8; ++j) xr[j] = xrow[l + 32 * j];
  f2 ga[8], sb[8], cb[8];
#pragma unroll
  for (int j = 0; j < 8; ++j) {
    const int d = 2 * (l + 32 * j);
    ga[j] = *(const f2*)(modf_ + 1024 + d);
    sb[j] = *(const f2*)(modf_ + 1536 + d);
    cb[j] = *(const f2*)(modf_ + 2048 + d);
  }
  f2 tw2[8], tw3[8];
  make_tw23(l, tw2, tw3);
  __syncthreads();
  f2* buf = zs + row * ZRS;
  f2 v[8];
  {
    const int r2 = l >> 2, qq = l & 3;
#pragma unroll
    for (int e = 0; e < 2; ++e)
#pragma unroll
      for (int r4 = 0; r4 < 4; ++r4) v[4 * e + r4] = buf[r2 + 8 * (qq + 4 * e) + 64 * r4];
  }
  wave_sync();
  fft256_inv(v, l, buf, tw2, tw3);
  float s = 0.f;
#pragma unroll
  for (int j = 0; j < 8; ++j) {
    float a = bfr(bf_lo(xr[j]) + bfr(ga[j].x * bfr(v[j].x)));
    float b = bfr(bf_hi(xr[j]) + bfr(ga[j].y * bfr(v[j].y)));
    v[j] = f2{a, b};
    s += a + b;
  }
  unsigned* x1row = (unsigned*)(ws + OFF_X1) + (size_t)(row0 + row) * (D_ / 2);
#pragma unroll
  for (int j = 0; j < 8; ++j) x1row[l + 32 * j] = pack_bf(v[j].x, v[j].y);
  s = half_wave_sum(s);
  const float meanf = s * (1.0f / D_);
  float q = 0.f;
#pragma unroll
  for (int j = 0; j < 8; ++j) {
    f2 d = v[j] - meanf;
    q += d.x * d.x + d.y * d.y;
  }
  q = half_wave_sum(q);
  const float mean = bfr(meanf);
  const float den = bfr(sqrtf(bfr(bfr(q * (1.0f / D_)) + 1e-5f)));
  const float rden = 1.0f / den;
  unsigned hp[8];
#pragma unroll
  for (int j = 0; j < 8; ++j) {
    float a = bfr(bfr(v[j].x - mean) * rden), b = bfr(bfr(v[j].y - mean) * rden);
    a = bfr(a * bfr(1.0f + cb[j].x)) + sb[j].x;
    b = bfr(b * bfr(1.0f + cb[j].y)) + sb[j].y;
    hp[j] = pack_bf(a, b);
  }
  const int grow = row0 + row, mt = grow >> 4, r16 = grow & 15;
#pragma unroll
  for (int half = 0; half < 2; ++half) {
    const int ci = half * 32 + l, ks = ci >> 2, g = ci & 3;
    u4v o = u4v{hp[4 * half], hp[4 * half + 1], hp[4 * half + 2], hp[4 * half + 3]};
    *(u4v*)(ws + OFF_H2F + ((size_t)(mt * 16 + ks) * 64 + g * 16 + r16) * 16) = o;
  }
}

__device__ __forceinline__ f4 mfma16(u4v a, u4v b, f4 c) {
  return __builtin_amdgcn_mfma_f32_16x16x32_bf16(__builtin_bit_cast(bf16x8, a), __builtin_bit_cast(bf16x8, b), c, 0, 0,
                                                  0);
}
__device__ __forceinline__ float silu_bf(float a) {
  float h = bfr(a);
  return h * __builtin_amdgcn_rcpf(1.0f + __expf(-h));
}

__global__ __launch_bounds__(256, 1) void k4_gemm1(char* __restrict__ ws) {
  extern __shared__ __attribute__((aligned(16))) char dsm[];
  const int tid = threadIdx.x, lane = tid & 63, w = tid >> 6;
  const int bid = blockIdx.x;
  const int p = (bid & 7) * 2 + ((bid >> 3) >> 4), c = (bid >> 3) & 15;
  const char* w1t = ws + OFF_W1F + (size_t)c * 7 * 16 * 1024;
#pragma unroll
  for (int i = 0; i < 28; ++i) {
    const int piece = i * 4 + w;
    __builtin_amdgcn_global_load_lds((const unsigned*)(w1t + piece * 1024 + lane * 16),
                                     (__attribute__((address_space(3))) unsigned*)(dsm + piece * 1024), 16, 0, 0);
  }
  u4v B[2][16];
#pragma unroll
  for (int mt = 0; mt < 2; ++mt)
#pragma unroll
    for (int ks = 0; ks < 16; ++ks)
      B[mt][ks] = *(const u4v*)(ws + OFF_H2F + ((size_t)((8 * p + 2 * w + mt) * 16 + ks) * 64 + lane) * 16);
  f4 acc[7][2];
#pragma unroll
  for (int ft = 0; ft < 7; ++ft) acc[ft][0] = acc[ft][1] = f4{0.f, 0.f, 0.f, 0.f};
  asm volatile("s_waitcnt vmcnt(0)" ::: "memory");
  __syncthreads();
#pragma unroll
  for (int ks = 0; ks < 16; ++ks) {
#pragma unroll
    for (int ft = 0; ft < 7; ++ft) {
      u4v A = *(const u4v*)(dsm + (ft * 16 + ks) * 1024 + lane * 16);
      acc[ft][0] = mfma16(A, B[0][ks], acc[ft][0]);
      acc[ft][1] = mfma16(A, B[1][ks], acc[ft][1]);
    }
  }
  const int e = c >> 1, odd = c & 1;
#pragma unroll
  for (int mt = 0; mt < 2; ++mt) {
    char* hb = ws + OFF_HF + ((size_t)((8 * p + 2 * w + mt) * 56 + 7 * e) * 64 + lane) * 16;
#pragma unroll
    for (int i = 0; i < 3; ++i) {
      f4 a = acc[2 * i][mt], b = acc[2 * i + 1][mt];
      u4v o = u4v{pack_bf(silu_bf(a[0]), silu_bf(a[1])), pack_bf(silu_bf(a[2]), silu_bf(a[3])),
                  pack_bf(silu_bf(b[0]), silu_bf(b[1])), pack_bf(silu_bf(b[2]), silu_bf(b[3]))};
      *(u4v*)(hb + (size_t)(odd * 3 + i) * 1024) = o;
    }
    f4 a = acc[6][mt];
    u2v o = u2v{pack_bf(silu_bf(a[0]), silu_bf(a[1])), pack_bf(silu_bf(a[2]), silu_bf(a[3]))};
    *(u2v*)(hb + 6 * 1024 + odd * 8) = o;
  }
}

__global__ __launch_bounds__(256, 1) void k5_gemm2(char* __restrict__ ws, u16* __restrict__ out) {
  __shared__ __attribute__((aligned(16))) f4 red[4][16][64];
  const int tid = threadIdx.x, lane = tid & 63, w = tid >> 6;
  const int bid = blockIdx.x;
  const int p = (bid & 7) * 2 + ((bid >> 3) >> 4), c = (bid >> 3) & 15;
  const int mt0 = 8 * p + 4 * (c >> 3), nt0 = 4 * (c & 7);
  const char* Ab = ws + OFF_W2F + ((size_t)(nt0 * 56 + 14 * w) * 64 + lane) * 16;
  const char* Bb = ws + OFF_HF + ((size_t)(mt0 * 56 + 14 * w) * 64 + lane) * 16;
  f4 acc[4][4];
#pragma unroll
  for (int i = 0; i < 4; ++i)
#pragma unroll
    for (int j = 0; j < 4; ++j) acc[i][j] = f4{0.f, 0.f, 0.f, 0.f};
  u4v A[14][4], B[14][4];
#pragma unroll
  for (int ks = 0; ks < 7; ++ks)
#pragma unroll
    for (int i = 0; i < 4; ++i) {
      A[ks][i] = *(const u4v*)(Ab + (size_t)(i * 56 + ks) * 1024);
      B[ks][i] = *(const u4v*)(Bb + (size_t)(i * 56 + ks) * 1024);
    }
#pragma unroll
  for (int ks = 0; ks < 14; ++ks) {
    if (ks + 7 < 14) {
#pragma unroll
      for (int i = 0; i < 4; ++i) {
        A[ks + 7][i] = *(const u4v*)(Ab + (size_t)(i * 56 + ks + 7) * 1024);
        B[ks + 7][i] = *(const u4v*)(Bb + (size_t)(i * 56 + ks + 7) * 1024);
      }
    }
#pragma unroll
    for (int nt = 0; nt < 4; ++nt)
#pragma unroll
      for (int mt = 0; mt < 4; ++mt) acc[nt][mt] = mfma16(A[ks][nt], B[ks][mt], acc[nt][mt]);
  }
#pragma unroll
  for (int nt = 0; nt < 4; ++nt)
#pragma unroll
    for (int mt = 0; mt < 4; ++mt) red[w][nt * 4 + mt][lane] = acc[nt][mt];
  __syncthreads();
  const int g = lane >> 4, mr = lane & 15;
  const int m = (mt0 + w) * 16 + mr;
  const float* gb = (const float*)(ws + OFF_MODF) + 2560;
#pragma unroll
  for (int P = 0; P < 2; ++P) {
    f4 s0 = red[0][(2 * P) * 4 + w][lane] + red[1][(2 * P) * 4 + w][lane] + red[2][(2 * P) * 4 + w][lane] +
            red[3][(2 * P) * 4 + w][lane];
    f4 s1 = red[0][(2 * P + 1) * 4 + w][lane] + red[1][(2 * P + 1) * 4 + w][lane] +
            red[2][(2 * P + 1) * 4 + w][lane] + red[3][(2 * P + 1) * 4 + w][lane];
    const int n = nt0 * 16 + 32 * P + 8 * g;
    f4 g0 = *(const f4*)(gb + n), g1 = *(const f4*)(gb + n + 4);
    u4v xv = *(const u4v*)((const u16*)(ws + OFF_X1) + (size_t)m * D_ + n);
    u4v o;
    o[0] = pack_bf(bf_lo(xv[0]) + bfr(g0[0] * bfr(s0[0])), bf_hi(xv[0]) + bfr(g0[1] * bfr(s0[1])));
    o[1] = pack_bf(bf_lo(xv[1]) + bfr(g0[2] * bfr(s0[2])), bf_hi(xv[1]) + bfr(g0[3] * bfr(s0[3])));
    o[2] = pack_bf(bf_lo(xv[2]) + bfr(g1[0] * bfr(s1[0])), bf_hi(xv[2]) + bfr(g1[1] * bfr(s1[1])));
    o[3] = pack_bf(bf_lo(xv[3]) + bfr(g1[2] * bfr(s1[2])), bf_hi(xv[3]) + bfr(g1[3] * bfr(s1[3])));
    *(u4v*)(out + (size_t)m * D_ + n) = o;
  }
}

extern "C" void kernel_launch(void* const* d_in, const int* in_sizes, int n_in, void* d_out, int out_size, void* d_ws,
                              size_t ws_size, hipStream_t stream) {
  const u16* x = (const u16*)d_in[0];
  const u16* wr = (const u16*)d_in[1];
  const u16* wi = (const u16*)d_in[2];
  const u16* w1 = (const u16*)d_in[4];
  const u16* w2 = (const u16*)d_in[5];
  const u16* c = (const u16*)d_in[6];
  const u16* wmod = (const u16*)d_in[7];
  char* ws = (char*)d_ws;
  u16* out = (u16*)d_out;
  (void)hipFuncSetAttribute((const void*)k4_gemm1, hipFuncAttributeMaxDynamicSharedMemorySize, 112 * 1024);
  k0_prep<<<K0_GEMV + K0_WT + K0_W1 + K0_W2, 256, 0, stream>>>(c, wmod, wr, wi, w1, w2, ws);
  k1_rowfft<<<256, 256, 0, stream>>>(x, ws);
  k2_colfft<<<256, 256, 0, stream>>>(ws);
  k3_rowifft<<<256, 256, 0, stream>>>(x, ws);
  k4_gemm1<<<256, 256, 112 * 1024, stream>>>(ws);
  k5_gemm2<<<256, 256, 0, stream>>>(ws, out);
}
```

```cpp
#include <hip/hip_runtime.h>
#include <hip/hip_bf16.h>
#include <stdint.h>

typedef float f2 __attribute__((ext_vector_type(2)));
typedef float f4 __attribute__((ext_vector_type(4)));
typedef unsigned u2v __attribute__((ext_vector_type(2)));
typedef unsigned u4v __attribute__((ext_vector_type(4)));
typedef short bf16x8 __attribute__((ext_vector_type(8)));
typedef unsigned short u16;

constexpr int S_ = 2048, D_ = 512, F_ = 1792, DF_ = 257;
constexpr int NSPLIT = 4;

constexpr size_t OFF_CNT = 0;
constexpr size_t OFF_FLG = 16384;
constexpr size_t OFF_MODP = 32768;
constexpr size_t OFF_MODF = OFF_MODP + (size_t)NSPLIT * 3072 * 4;
constexpr size_t OFF_MODC = OFF_MODF + 3072 * 4;
constexpr size_t OFF_WT = 1 << 20;
constexpr size_t OFF_XT = 4 << 20;
constexpr size_t OFF_ZT = 8 << 20;
constexpr size_t OFF_H2F = 12 << 20;
constexpr size_t OFF_X1 = 14 << 20;
constexpr size_t OFF_W2F = 18 << 20;
constexpr size_t OFF_HF = 20 << 20;
constexpr int WS_SPAN = 28 << 20;
constexpr float FSC = 1.0f / (2.0f * 2048.0f * 512.0f);

#define STAMP(kid, i)

__device__ __forceinline__ float bf_lo(unsigned u) { return __builtin_bit_cast(float, u << 16); }
__device__ __forceinline__ float bf_hi(unsigned u) { return __builtin_bit_cast(float, u & 0xffff0000u); }
__device__ __forceinline__ float bfr(float x) {
  return (float)(__bf16)x;
}
__device__ __forceinline__ unsigned pack_bf(float lo, float hi) {
  typedef __bf16 b2 __attribute__((ext_vector_type(2)));
  return __builtin_bit_cast(unsigned, __builtin_convertvector(f2{lo, hi}, b2));
}
__device__ __forceinline__ f2 cmul(f2 a, f2 b) {
  f2 t = a * f2{b.x, b.x};
  return __builtin_elementwise_fma(f2{a.y, a.x}, f2{-b.y, b.y}, t);
}
__device__ __forceinline__ f2 cmulc(f2 a, f2 b) {
  f2 t = a * f2{b.x, b.x};
  return __builtin_elementwise_fma(f2{a.y, a.x}, f2{b.y, -b.y}, t);
}
__device__ __forceinline__ f2 bfr2(f2 a) {
  unsigned u = pack_bf(a.x, a.y);
  asm("" : "+v"(u));
  return f2{bf_lo(u), bf_hi(u)};
}
__device__ __forceinline__ float msel(unsigned m, float a, float b) {
  unsigned ua = __builtin_bit_cast(unsigned, a), ub = __builtin_bit_cast(unsigned, b);
  return __builtin_bit_cast(float, (m & ua) | (~m & ub));
}
#define PIN2(x) asm volatile("" : "+v"(x))
constexpr int AUX_SC1 = 16;
#define WS_RSRC(ws) __builtin_amdgcn_make_buffer_rsrc((ws), 0, WS_SPAN, 0x00020000)
#define ST128(v, off) __builtin_amdgcn_raw_buffer_store_b128((v), rs, (int)(off), 0, AUX_SC1)
#define ST64(v, off) __builtin_amdgcn_raw_buffer_store_b64((v), rs, (int)(off), 0, AUX_SC1)
#define LD128(off) __builtin_amdgcn_raw_buffer_load_b128(rs, (int)(off), 0, AUX_SC1)
__device__ __forceinline__ f2 twid(float rev) { return f2{__builtin_amdgcn_cosf(rev), -__builtin_amdgcn_sinf(rev)}; }

template <int DIR>
__device__ __forceinline__ f2 rot90(f2 a) {
  return DIR < 0 ? f2{a.y, -a.x} : f2{-a.y, a.x};
}
template <int DIR>
__device__ __forceinline__ void dft4(f2& c0, f2& c1, f2& c2, f2& c3) {
  f2 d0 = c0 + c2, d2 = c0 - c2, d1 = c1 + c3, e = c1 - c3;
  c0 = d0 + d1;
  c2 = d0 - d1;
  const f2 es = f2{e.y, e.x};
  c1 = __builtin_elementwise_fma(es, DIR < 0 ? f2{1.f, -1.f} : f2{-1.f, 1.f}, d2);
  c3 = __builtin_elementwise_fma(es, DIR < 0 ? f2{-1.f, 1.f} : f2{1.f, -1.f}, d2);
}

__device__ __forceinline__ void sw32(float& a, float& b) {
  auto r = __builtin_amdgcn_permlane32_swap(__builtin_bit_cast(unsigned, a), __builtin_bit_cast(unsigned, b), false, false);
  a = __builtin_bit_cast(float, (unsigned)r[0]);
  b = __builtin_bit_cast(float, (unsigned)r[1]);
}
__device__ __forceinline__ void sw16(float& a, float& b) {
  auto r = __builtin_amdgcn_permlane16_swap(__builtin_bit_cast(unsigned, a), __builtin_bit_cast(unsigned, b), false, false);
  a = __builtin_bit_cast(float, (unsigned)r[0]);
  b = __builtin_bit_cast(float, (unsigned)r[1]);
}
template <int CTRL, int BANK>
__device__ __forceinline__ float udpp(float old, float src) {
  return __builtin_bit_cast(float, __builtin_amdgcn_update_dpp(__builtin_bit_cast(int, old), __builtin_bit_cast(int, src),
                                                               CTRL, 0xf, BANK, false));
}
template <int CTRL>
__device__ __forceinline__ float mdpp(float src) {
  return __builtin_bit_cast(float, __builtin_amdgcn_mov_dpp(__builtin_bit_cast(int, src), CTRL, 0xf, 0xf, false));
}
__device__ __forceinline__ void xp_hi1(float& v0, float& v1, float& v2, float& v3) {
  sw32(v0, v2); sw32(v1, v3); sw16(v0, v1); sw16(v2, v3);
}
__device__ __forceinline__ void xp_mid1(float& v0, float& v1, float& v2, float& v3) {
  float a0 = udpp<0x128, 0xC>(v0, v2), a2 = udpp<0x128, 0x3>(v2, v0);
  float a1 = udpp<0x128, 0xC>(v1, v3), a3 = udpp<0x128, 0x3>(v3, v1);
  v0 = udpp<0x124, 0xA>(a0, a1); v1 = udpp<0x12C, 0x5>(a1, a0);
  v2 = udpp<0x124, 0xA>(a2, a3); v3 = udpp<0x12C, 0x5>(a3, a2);
}
__device__ __forceinline__ void xp_lo1(float& v0, float& v1, float& v2, float& v3, unsigned b1, unsigned b0) {
  float s0 = mdpp<0x4E>(msel(b1, v0, v2)), s1 = mdpp<0x4E>(msel(b1, v1, v3));
  float a0 = msel(b1, s0, v0), a2 = msel(b1, v2, s0), a1 = msel(b1, s1, v1), a3 = msel(b1, v3, s1);
  float t0 = mdpp<0xB1>(msel(b0, a0, a1)), t2 = mdpp<0xB1>(msel(b0, a2, a3));
  v0 = msel(b0, t0, a0); v1 = msel(b0, a1, t0); v2 = msel(b0, t2, a2); v3 = msel(b0, a3, t2);
}
#define XP_CPLX(NAME, FN, ...)                                        \
  {                                                                   \
    float x0 = v[0].x, x1 = v[1].x, x2 = v[2].x, x3 = v[3].x;         \
    float y0 = v[0].y, y1 = v[1].y, y2 = v[2].y, y3 = v[3].y;         \
    FN(x0, x1, x2, x3, ##__VA_ARGS__);                                \
    FN(y0, y1, y2, y3, ##__VA_ARGS__);                                \
    v[0] = f2{x0, y0}; v[1] = f2{x1, y1}; v[2] = f2{x2, y2}; v[3] = f2{x3, y3}; \
  }
__device__ __forceinline__ void xp_hi(f2 (&v)[4]) XP_CPLX(hi, xp_hi1)
__device__ __forceinline__ void xp_mid(f2 (&v)[4]) XP_CPLX(mid, xp_mid1)
__device__ __forceinline__ void xp_lo(f2 (&v)[4], unsigned b1, unsigned b0) XP_CPLX(lo, xp_lo1, b1, b0)

struct Tw256 {
  f2 a[3], b[3], c[3];
  unsigned m1, m0;
};
__device__ __forceinline__ void make_tw256(int lane, Tw256& t) {
#pragma unroll
  for (int r = 1; r < 4; ++r) {
    t.a[r - 1] = twid((float)(lane * r) * (1.0f / 256.0f));
    t.b[r - 1] = twid((float)((lane & 15) * r) * (1.0f / 64.0f));
    t.c[r - 1] = twid((float)((lane & 3) * r) * (1.0f / 16.0f));
  }
  t.m1 = 0u - (unsigned)((lane >> 1) & 1);
  t.m0 = 0u - (unsigned)(lane & 1);
#pragma unroll
  for (int r = 0; r < 3; ++r) {
    PIN2(t.a[r]);
    PIN2(t.b[r]);
    PIN2(t.c[r]);
  }
  PIN2(t.m1);
  PIN2(t.m0);
}
struct NoHook {
  __device__ __forceinline__ void operator()(int) const {}
};
template <class H>
__device__ __forceinline__ void fft256_fwd(f2 (&v)[4], int lane, const Tw256& t, const H& hook) {
  const unsigned b1 = t.m1, b0 = t.m0;
  dft4<-1>(v[0], v[1], v[2], v[3]);
#pragma unroll
  for (int r = 1; r < 4; ++r) v[r] = cmul(v[r], t.a[r - 1]);
  xp_hi(v);
  hook(0);
  dft4<-1>(v[0], v[1], v[2], v[3]);
#pragma unroll
  for (int r = 1; r < 4; ++r) v[r] = cmul(v[r], t.b[r - 1]);
  xp_mid(v);
  hook(1);
  dft4<-1>(v[0], v[1], v[2], v[3]);
#pragma unroll
  for (int r = 1; r < 4; ++r) v[r] = cmul(v[r], t.c[r - 1]);
  xp_lo(v, b1, b0);
  hook(2);
  dft4<-1>(v[0], v[1], v[2], v[3]);
}
template <class H>
__device__ __forceinline__ void fft256_inv(f2 (&v)[4], int lane, const Tw256& t, const H& hook) {
  const unsigned b1 = t.m1, b0 = t.m0;
  dft4<1>(v[0], v[1], v[2], v[3]);
  xp_lo(v, b1, b0);
  hook(0);
#pragma unroll
  for (int r = 1; r < 4; ++r) v[r] = cmulc(v[r], t.c[r - 1]);
  dft4<1>(v[0], v[1], v[2], v[3]);
  xp_mid(v);
  hook(1);
#pragma unroll
  for (int r = 1; r < 4; ++r) v[r] = cmulc(v[r], t.b[r - 1]);
  dft4<1>(v[0], v[1], v[2], v[3]);
  xp_hi(v);
  hook(2);
#pragma unroll
  for (int r = 1; r < 4; ++r) v[r] = cmulc(v[r], t.a[r - 1]);
  dft4<1>(v[0], v[1], v[2], v[3]);
}

__device__ __forceinline__ float wave_sum(float x) {
  x += mdpp<0xB1>(x);
  x += mdpp<0x4E>(x);
  x += mdpp<0x141>(x);
  x += mdpp<0x140>(x);
  float r0 = __builtin_bit_cast(float, __builtin_amdgcn_readlane(__builtin_bit_cast(int, x), 0));
  float r1 = __builtin_bit_cast(float, __builtin_amdgcn_readlane(__builtin_bit_cast(int, x), 16));
  float r2 = __builtin_bit_cast(float, __builtin_amdgcn_readlane(__builtin_bit_cast(int, x), 32));
  float r3 = __builtin_bit_cast(float, __builtin_amdgcn_readlane(__builtin_bit_cast(int, x), 48));
  return (r0 + r1) + (r2 + r3);
}

__device__ __forceinline__ int zpos(int k) { return 17 * (k & 3) + 4 * ((k >> 2) & 3) + ((k >> 4) & 3) + 68 * (k >> 6); }
constexpr int ZRS = 276;

constexpr int K0_GEMV = 48 * NSPLIT;
__global__ __launch_bounds__(512) void k0_mod(const u16* __restrict__ c, const u16* __restrict__ wmod,
                                              char* __restrict__ ws) {
  __shared__ __attribute__((aligned(16))) char smem[2048];
  const int tid = threadIdx.x, row = tid >> 6, lane = tid & 63;
  const int b = blockIdx.x;
  STAMP(0, 0);
  if (b == 0) {
#pragma unroll
    for (int i = 0; i < 4; ++i) ((u4v*)(ws + OFF_CNT))[i * 512 + tid] = u4v{0u, 0u, 0u, 0u};
  }
  float* red = (float*)smem;
  const int cgp = b % 48, ks = b / 48;
  const int cg = tid & 7, r = tid >> 3;
  constexpr int RPS = 512 / NSPLIT;
  float acc[8];
#pragma unroll
  for (int i = 0; i < 8; ++i) acc[i] = 0.f;
  u4v wv[RPS / 64];
  float sc[RPS / 64];
#pragma unroll
  for (int it = 0; it < RPS / 64; ++it) {
    const int rw = ks * RPS + it * 64 + r;
    wv[it] = *(const u4v*)(wmod + (size_t)rw * 3072 + cgp * 64 + cg * 8);
    float cv = bf_lo(c[rw]);
    sc[it] = bfr(cv / (1.0f + __expf(-cv)));
  }
#pragma unroll
  for (int it = 0; it < RPS / 64; ++it) {
#pragma unroll
    for (int i = 0; i < 4; ++i) {
      acc[2 * i] += sc[it] * bf_lo(wv[it][i]);
      acc[2 * i + 1] += sc[it] * bf_hi(wv[it][i]);
    }
  }
#pragma unroll
  for (int i = 0; i < 8; ++i) {
    acc[i] += mdpp<0x128>(acc[i]);
    acc[i] += __shfl_xor(acc[i], 16);
    acc[i] += __shfl_xor(acc[i], 32);
  }
  if (lane < 8) {
#pragma unroll
    for (int i = 0; i < 8; ++i) red[row * 64 + cg * 8 + i] = acc[i];
  }
  __syncthreads();
  if (tid < 64) {
    float s = 0.f;
#pragma unroll
    for (int i = 0; i < 8; ++i) s += red[i * 64 + tid];
    ((float*)(ws + OFF_MODP))[ks * 3072 + cgp * 64 + tid] = s;
  }
  STAMP(0, 1);
}

constexpr int K1_WT = 128, K1_W2 = 112;
__global__ __launch_bounds__(512) void k1_rowfft(const u16* __restrict__ x, const u16* __restrict__ wr,
                                                 const u16* __restrict__ wi, const u16* __restrict__ w2,
                                                 char* __restrict__ ws) {
  __shared__ __attribute__((aligned(16))) char smem[8 * ZRS * 8 + 4096];
  const int tid = threadIdx.x, row = tid >> 6, lane = tid & 63;
  const auto rs = WS_RSRC(ws);
  STAMP(1, 0);
  if (blockIdx.x >= 256) {
    int b = blockIdx.x - 256;
    if (b < K1_WT) {
      char* sr = smem;
      char* si = smem + 16 * 544;
      constexpr int LASTCH = (S_ * DF_ * 2) / 16 - 1;
      for (int it = tid; it < 16 * 34; it += 512) {
        const int i = it / 34, ch = it % 34;
        int gch = ((b + 128 * i) * (DF_ * 2)) / 16 + ch;
        gch = gch > LASTCH ? LASTCH : gch;
        *(u4v*)(sr + i * 544 + ch * 16) = *(const u4v*)((const char*)wr + (size_t)gch * 16);
        *(u4v*)(si + i * 544 + ch * 16) = *(const u4v*)((const char*)wi + (size_t)gch * 16);
      }
      __syncthreads();
      const int tbase = 64 * (b & 7) + 16 * ((b >> 3) & 3) + 4 * ((b >> 5) & 3);
      for (int it = tid; it < DF_ * 4; it += 512) {
        const int k = it >> 2, r3 = it & 3;
        u4v o;
#pragma unroll
        for (int r4 = 0; r4 < 4; ++r4) {
          const int i = r3 + 4 * r4;
          const int off = i * 544 + (((b + 128 * i) * (DF_ * 2)) & 15) + 2 * k;
          o[r4] = (unsigned)*(const u16*)(sr + off) | ((unsigned)*(const u16*)(si + off) << 16);
        }
        ST128(o, OFF_WT + (k * 512 + tbase + r3) * 16);
      }
    } else {
      b -= K1_WT;
      u16* t = (u16*)smem;
      const int g = lane >> 4, r = lane & 15;
      const int ng = b & 3, kp = b >> 2;
#pragma unroll
      for (int it = 0; it < 2; ++it) {
        const int idx = it * 512 + tid;
        const int rr = idx >> 4, ch = idx & 15;
        const int ks2 = kp * 2 + (rr >> 5), gg = (rr >> 3) & 3, j = rr & 7;
        const int e = ks2 / 7, i7 = ks2 % 7;
        int T0, T1;
        if (i7 < 3) { T0 = 14 * e + 2 * i7; T1 = T0 + 1; }
        else if (i7 < 6) { T0 = 14 * e + 7 + 2 * (i7 - 3); T1 = T0 + 1; }
        else { T0 = 14 * e + 6; T1 = 14 * e + 13; }
        const int f = (j < 4) ? (16 * T0 + 4 * gg + j) : (16 * T1 + 4 * gg + (j - 4));
        *(u4v*)(t + rr * 136 + ch * 8) = *(const u4v*)(w2 + (size_t)f * D_ + ng * 128 + ch * 8);
      }
      __syncthreads();
      const int nl = 32 * (row >> 1) + 8 * (r >> 2) + 4 * (row & 1) + (r & 3);
#pragma unroll
      for (int ksl = 0; ksl < 2; ++ksl) {
        u4v o;
#pragma unroll
        for (int jj = 0; jj < 4; ++jj)
          o[jj] = (unsigned)t[(ksl * 32 + g * 8 + 2 * jj) * 136 + nl] |
                  ((unsigned)t[(ksl * 32 + g * 8 + 2 * jj + 1) * 136 + nl] << 16);
        const int nt = ng * 8 + row;
        ST128(o, OFF_W2F + ((nt * 56 + kp * 2 + ksl) * 64 + lane) * 16);
      }
    }
    STAMP(1, 5);
    return;
  }
  f2* zs = (f2*)smem;
  float* mods = (float*)(smem + 8 * ZRS * 8);
  const int row0 = blockIdx.x * 8;
  unsigned xr[4];
  const unsigned* xrow = (const unsigned*)(x + (size_t)(row0 + row) * D_);
#pragma unroll
  for (int j = 0; j < 4; ++j) xr[j] = xrow[lane + 64 * j];
  {
    const float* mp = (const float*)(ws + OFF_MODP);
    f2 s = *(const f2*)(mp + tid * 2);
#pragma unroll
    for (int k = 1; k < NSPLIT; ++k) s += *(const f2*)(mp + k * 3072 + tid * 2);
    f2 o;
#pragma unroll
    for (int i = 0; i < 2; ++i) {
      float v = bfr(s[i]);
      o[i] = (tid >= 256) ? bfr(1.0f + v) : v;
    }
    *(f2*)(mods + tid * 2) = o;
    if (blockIdx.x < 3) {
      const int idx = blockIdx.x * 1024 + tid * 2;
      f2 t = *(const f2*)(mp + idx);
#pragma unroll
      for (int k = 1; k < NSPLIT; ++k) t += *(const f2*)(mp + k * 3072 + idx);
      t = bfr2(t);
      *(f2*)((float*)(ws + OFF_MODF) + idx) = t;
      const int vec = (idx >> 9) - 2, d = idx & 511;
      if (vec >= 0 && vec < 3) {
        if (vec == 2) t += 1.0f;
        ((unsigned*)(ws + OFF_MODC))[(vec * 64 + ((d >> 1) & 63)) * 4 + (d >> 7)] = pack_bf(t.x, t.y);
      }
    }
  }
  Tw256 tw;
  make_tw256(lane, tw);
  const int rp = tid & 3, kk = tid >> 2;
  f2 wpost = twid((float)kk * (1.0f / 512.0f));
  PIN2(wpost);
  f2 v[4];
  float s = 0.f;
#pragma unroll
  for (int j = 0; j < 4; ++j) {
    v[j] = f2{bf_lo(xr[j]), bf_hi(xr[j])};
    s += v[j].x + v[j].y;
  }
  s = wave_sum(s);
  const float meanf = s * (1.0f / D_);
  float q = 0.f;
#pragma unroll
  for (int j = 0; j < 4; ++j) {
    f2 d = v[j] - meanf;
    q += d.x * d.x + d.y * d.y;
  }
  q = wave_sum(q);
  const float mean = bfr(meanf);
  const float den = bfr(sqrtf(bfr(bfr(q * (1.0f / D_)) + 1e-5f)));
  const float rden = 1.0f / den;
  STAMP(1, 1);
  __syncthreads();
  STAMP(1, 2);
#pragma unroll
  for (int j = 0; j < 4; ++j) {
    const int d = 2 * (lane + 64 * j);
    f2 sa = *(const f2*)(mods + d);
    f2 ca1 = *(const f2*)(mods + 512 + d);
    v[j] = bfr2(__builtin_elementwise_fma(v[j] - mean, ca1 * rden, sa));
  }
  fft256_fwd(v, lane, tw, NoHook{});
  {
    f2* buf = zs + row * ZRS + lane + (lane >> 4);
#pragma unroll
    for (int r4 = 0; r4 < 4; ++r4) buf[68 * r4] = v[r4];
  }
  STAMP(1, 3);
  __syncthreads();
  STAMP(1, 4);
  {
    const int k = kk;
    const int k2 = (k == 0) ? 128 : 256 - k;
    const int pa = zpos(k), pb = zpos(k2);
    f4 oa, ob;
#pragma unroll
    for (int h = 0; h < 2; ++h) {
      const f2* zr = zs + (2 * rp + h) * ZRS;
      f2 za = zr[pa], zb = zr[pb];
      f2 E = f2{za.x + zb.x, za.y - zb.y};
      f2 Dm = f2{za.x - zb.x, za.y + zb.y};
      f2 O = f2{Dm.y, -Dm.x};
      f2 wo = cmul(wpost, O);
      f2 ra = E + wo;
      f2 t = E - wo;
      f2 rb = f2{t.x, -t.y};
      if (k == 0) {
        ra = f2{2.f * (za.x + za.y), 2.f * (za.x - za.y)};
        rb = f2{2.f * zb.x, -2.f * zb.y};
      }
      oa[2 * h] = ra.x; oa[2 * h + 1] = ra.y;
      ob[2 * h] = rb.x; ob[2 * h + 1] = rb.y;
    }
    ST128(__builtin_bit_cast(u4v, oa), OFF_XT + (k * S_ + row0 + 2 * rp) * 8);
    ST128(__builtin_bit_cast(u4v, ob), OFF_XT + (k2 * S_ + row0 + 2 * rp) * 8);
  }
  STAMP(1, 5);
}

__device__ __forceinline__ f4 mfma16(u4v a, u4v b, f4 c) {
  return __builtin_amdgcn_mfma_f32_16x16x32_bf16(__builtin_bit_cast(bf16x8, a), __builtin_bit_cast(bf16x8, b), c, 0, 0,
                                                  0);
}
__device__ __forceinline__ f2 silu2(float a, float b) {
  f2 h = f2{a, b};
  f2 t = h * -1.44269504088896340736f;
  f2 d = f2{__builtin_amdgcn_exp2f(t.x), __builtin_amdgcn_exp2f(t.y)} + 1.0f;
  return h * f2{__builtin_amdgcn_rcpf(d.x), __builtin_amdgcn_rcpf(d.y)};
}
__device__ __forceinline__ unsigned silu_pk(float a, float b) {
  f2 r = silu2(a, b);
  return pack_bf(r.x, r.y);
}
typedef __attribute__((address_space(1))) unsigned gu32;
#define DRAIN() asm volatile("s_waitcnt vmcnt(0)" ::: "memory")
typedef __attribute__((address_space(1))) unsigned long long gu64;
__device__ __forceinline__ void wait_pair(gu64* flags) {
  for (unsigned spin = 0; spin < (1u << 20); ++spin) {
    if (__hip_atomic_load(flags, __ATOMIC_RELAXED, __HIP_MEMORY_SCOPE_AGENT) == 0x0000000100000001ull) break;
    __builtin_amdgcn_s_sleep(1);
  }
  asm volatile("" ::: "memory");
}

__device__ __forceinline__ void wait_cols(__amdgpu_buffer_rsrc_t rs, int off) {
  for (unsigned spin = 0; spin < (1u << 20); ++spin) {
    const u4v f = LD128(off);
    if (__builtin_amdgcn_ballot_w64((f[0] & f[1] & f[2] & f[3]) != 1u) == 0ull) break;
    __builtin_amdgcn_s_sleep(2);
    asm volatile("" ::: "memory");
  }
}

constexpr int L_W1 = 0;
constexpr int L_T = 112 * 1024;
constexpr int L_ZS = 112 * 1024;
constexpr int L_ST = L_ZS + 8 * ZRS * 8;
constexpr int L_TOTAL = 160 * 1024;
__device__ __forceinline__ constexpr int g1_n(int g) { return g < 13 ? 7 : (g < 16 ? 6 : 3); }
__device__ __forceinline__ constexpr int g1_ft(int g, int j) { return g < 13 ? j : (g < 16 ? 2 * (g - 13) + (j & 1) : 6); }
__device__ __forceinline__ constexpr int g1_ks(int g, int j) { return g < 13 ? g : (g < 16 ? 13 + (j >> 1) : 13 + j); }
#define SCHED() __builtin_amdgcn_sched_barrier(0)
typedef short v4i16 __attribute__((ext_vector_type(4)));
__device__ __forceinline__ u4v w1frag(const char* lw, int ft, int ks) {
  typedef __attribute__((address_space(3))) v4i16* lp;
  const u2v a = __builtin_bit_cast(u2v, __builtin_amdgcn_ds_read_tr16_b64_v4i16((lp)(lw + (2 * ks) * 3584 + ft * 32)));
  const u2v b = __builtin_bit_cast(u2v, __builtin_amdgcn_ds_read_tr16_b64_v4i16((lp)(lw + (2 * ks + 1) * 3584 + ft * 32)));
  return u4v{a[0], a[1], b[0], b[1]};
}
__global__ __launch_bounds__(512, 2) void k23_fused(const u16* __restrict__ x, const u16* __restrict__ w1,
                                                    char* __restrict__ ws, u16* __restrict__ out) {
  extern __shared__ __attribute__((aligned(16))) char dsm[];
  const int tid = threadIdx.x, lane = tid & 63;
  const int w = __builtin_amdgcn_readfirstlane(tid >> 6);
  const int bid = blockIdx.x;
  const int p = (bid & 7) * 2 + (bid >> 7), c = (bid >> 3) & 15;
  const int row0 = 128 * p + 8 * c;
  STAMP(2, 0);
  const auto rs = WS_RSRC(ws);
  gu32* myA = (gu32*)(ws + OFF_CNT) + (p * 8 + (c >> 1)) * 16 + (c & 1);
  gu32* myB = myA + 128 * 16;
  gu64* depA = (gu64*)(ws + OFF_CNT) + (p * 8 + w) * 8;
  gu64* depB = depA + 128 * 8;
  const float* modf_ = (const float*)(ws + OFF_MODF);
  const int mt0 = 8 * p + 4 * (c >> 3), nt0 = 4 * (c & 7);
  const int l56 = lane < 56 ? lane : lane - 56;
  const int r4_ = l56 / 14, ch_ = l56 - 14 * r4_;
  const char* w1g = (const char*)w1 + (size_t)(4 * w + r4_) * (F_ * 2) + c * 224 + ch_ * 16;
  char* w1l = dsm + L_W1 + ((w >> 1) * 32 + (2 * (w & 1) + (r4_ >> 1)) * 4 + (r4_ & 1)) * 224 + ch_ * 16;
  const char* Ab = ws + OFF_W2F + ((size_t)(nt0 * 56 + 7 * w) * 64 + lane) * 16;
  u4v S1[16];
  u4v A2[28];
  auto pf_load = [&](int i) __attribute__((always_inline)) {
    if (i < 16) S1[i] = *(const u4v*)(w1g + (size_t)i * (32 * F_ * 2));
    else if (i < 44) A2[i - 16] = *(const u4v*)(Ab + (size_t)(((i - 16) & 3) * 56 + ((i - 16) >> 2)) * 1024);
  };
  auto pf_put = [&](int i) __attribute__((always_inline)) {
    *(u4v*)(w1l + ((i & 3) * 128 + (i >> 3) * 16 + ((i >> 2) & 1) * 2) * 224) = S1[i];
  };
  auto pf = [&](int st) __attribute__((always_inline)) {
    SCHED();
    pf_load(2 * st + 2);
    pf_load(2 * st + 3);
    SCHED();
  };
  Tw256 tw;
  {
    f2* T = (f2*)(dsm + L_T);
    f2* T2 = (f2*)(dsm + L_T + 16384);
    f2* T3 = (f2*)(dsm + L_T + 32768);
    const int k = bid;
    const f2* XT = (const f2*)(ws + OFF_XT) + (size_t)k * S_;
    const unsigned* WT = (const unsigned*)(ws + OFF_WT);
    f2 v[4];
#pragma unroll
    for (int j = 0; j < 4; ++j) v[j] = XT[tid + 512 * j];
    const u4v wv = *(const u4v*)(WT + ((size_t)k * 512 + tid) * 4);
    u4v bv;
    unsigned wn[4], bn[4];
    int mirr[4];
    SCHED();
    pf_load(0); pf_load(1); pf_load(2); pf_load(3);
    SCHED();
    make_tw256(lane, tw);
    f2 tw1[4];
#pragma unroll
    for (int j = 0; j < 4; ++j) {
      tw1[j] = twid((float)((lane + 64 * j) * w) * (1.0f / 2048.0f));
      PIN2(tw1[j]);
    }
    const int hb = w >> 2, a = tid & 255;
    constexpr float R = 0.70710678118654752440f;
    dft4<-1>(v[0], v[1], v[2], v[3]);
    if (hb) {
      v[1] = f2{v[1].x + v[1].y, v[1].y - v[1].x} * R;
      v[2] = f2{v[2].y, -v[2].x};
      v[3] = f2{v[3].y - v[3].x, -v[3].x - v[3].y} * R;
    }
#pragma unroll
    for (int q = 0; q < 4; ++q) T[(hb * 4 + q) * 256 + a] = v[q];
    STAMP(2, 1);
    pf(1);
    if (k == 0) {
      bv = *(const u4v*)(WT + ((size_t)256 * 512 + tid) * 4);
      const int m0 = (lane >> 4) + 4 * ((lane >> 2) & 3) + 16 * (lane & 3);
#pragma unroll
      for (int r4 = 0; r4 < 4; ++r4) {
        const int un = (S_ - (w + 8 * (m0 + 64 * r4))) & (S_ - 1);
        const int mm = un >> 3;
        mirr[r4] = ((un & 7) * 64 + 16 * (mm & 3) + 4 * ((mm >> 2) & 3) + ((mm >> 4) & 3)) * 4 + (mm >> 6);
      }
      if (w != 0) {
        const u4v a4 = *(const u4v*)(WT + (64 * (8 - w) + 63 - lane) * 4);
        const u4v b4 = *(const u4v*)(WT + (size_t)256 * 2048 + (64 * (8 - w) + 63 - lane) * 4);
#pragma unroll
        for (int r4 = 0; r4 < 4; ++r4) { wn[r4] = a4[3 - r4]; bn[r4] = b4[3 - r4]; }
      } else {
#pragma unroll
        for (int r4 = 0; r4 < 4; ++r4) {
          wn[r4] = WT[mirr[r4]];
          bn[r4] = WT[(size_t)256 * 2048 + mirr[r4]];
        }
      }
    }
    __syncthreads();
    {
      const int q = w & 3;
      const float sg = (w < 4) ? 1.f : -1.f;
#pragma unroll
      for (int j = 0; j < 4; ++j) {
        f2 e = T[q * 256 + lane + 64 * j], o = T[(4 + q) * 256 + lane + 64 * j];
        v[j] = cmul(e + o * sg, tw1[j]);
      }
    }
    fft256_fwd(v, lane, tw, [&](int i) __attribute__((always_inline)) { pf(2 + i); });
    STAMP(2, 2);
    if (k != 0) {
#pragma unroll
      for (int i = 0; i < 4; ++i) {
        f2 wf = f2{bf_lo(wv[i]), bf_hi(wv[i])} * FSC;
        v[i] = cmul(v[i], wf);
      }
    } else {
#pragma unroll
      for (int r4 = 0; r4 < 4; ++r4) T3[r4 * 512 + tid] = v[r4];
      __syncthreads();
#pragma unroll
      for (int r4 = 0; r4 < 4; ++r4) {
        f2 cm = T3[(mirr[r4] & 3) * 512 + (mirr[r4] >> 2)];
        f2 h0 = f2{bf_lo(wv[r4]) + bf_lo(wn[r4]), bf_hi(wv[r4]) - bf_hi(wn[r4])};
        f2 h1 = f2{bf_lo(bv[r4]) + bf_lo(bn[r4]), bf_hi(bv[r4]) - bf_hi(bn[r4])};
        f2 wp = (h0 + h1) * (0.25f * FSC), wm = (h0 - h1) * (0.25f * FSC);
        f2 cc = f2{cm.x, -cm.y};
        v[r4] = cmul(v[r4], wp) + cmul(cc, wm);
      }
    }
    fft256_inv(v, lane, tw, [&](int i) __attribute__((always_inline)) { pf(5 + i); });
#pragma unroll
    for (int j = 0; j < 4; ++j) T2[w * 256 + lane + 64 * j] = cmulc(v[j], tw1[j]);
    STAMP(2, 3);
    __syncthreads();
    {
      const float sg = hb ? -1.f : 1.f;
#pragma unroll
      for (int q = 0; q < 4; ++q) v[q] = T2[q * 256 + a] + T2[(q + 4) * 256 + a] * sg;
      if (hb) {
        v[1] = f2{v[1].x - v[1].y, v[1].y + v[1].x} * R;
        v[2] = f2{-v[2].y, v[2].x};
        v[3] = f2{-v[3].x - v[3].y, v[3].x - v[3].y} * R;
      }
    }
    dft4<1>(v[0], v[1], v[2], v[3]);
#pragma unroll
    for (int j = 0; j < 4; ++j) ST64(__builtin_bit_cast(u2v, v[j]), OFF_ZT + (k * S_ + tid + 512 * j) * 8);
    STAMP(2, 4);
#pragma unroll
    for (int i = 0; i < 16; ++i) pf_put(i);
    DRAIN();
    __syncthreads();
    if (tid < 16)
      __hip_atomic_store((gu32*)(ws + OFF_FLG) + tid * 256 + k, 1u, __ATOMIC_RELAXED, __HIP_MEMORY_SCOPE_AGENT);
  }
  STAMP(2, 5);
  {
    f2* zs = (f2*)(dsm + L_ZS);
    const int rp = tid & 3, k = tid >> 2;
    const int k2 = (k == 0) ? 128 : 256 - k;
    u4v xr;
    const unsigned* xrow = (const unsigned*)(x + (size_t)(row0 + w) * D_);
#pragma unroll
    for (int j = 0; j < 4; ++j) xr[j] = xrow[lane + 64 * j];
    const u4v gav = *(const u4v*)(ws + OFF_MODC + lane * 16);
    const u4v sbv = *(const u4v*)(ws + OFF_MODC + 1024 + lane * 16);
    const u4v cbv = *(const u4v*)(ws + OFF_MODC + 2048 + lane * 16);
    if (w != 0) pf(8);
    f2 wq = twid((float)k * (1.0f / 512.0f));
    PIN2(wq);
    const int pa = zpos(k), pb = zpos(k2);
    if (w == 0) wait_cols(rs, (int)OFF_FLG + (bid & 15) * 1024 + lane * 16);
    asm volatile("s_barrier" ::: "memory");
    STAMP(3, 0);
    const f4 ya = __builtin_bit_cast(f4, LD128(OFF_ZT + (k * S_ + row0 + 2 * rp) * 8));
    const f4 yb = __builtin_bit_cast(f4, LD128(OFF_ZT + (k2 * S_ + row0 + 2 * rp) * 8));
    if (w == 0) pf(8);
    pf(9);
#pragma unroll
    for (int h = 0; h < 2; ++h) {
      f2 a = f2{ya[2 * h], ya[2 * h + 1]}, b = f2{yb[2 * h], yb[2 * h + 1]};
      f2* zr = zs + (2 * rp + h) * ZRS;
      f2 Ze = f2{a.x + b.x, a.y - b.y};
      f2 Dm = f2{a.x - b.x, a.y + b.y};
      f2 Zo = cmulc(Dm, wq);
      f2 za = f2{Ze.x - Zo.y, Ze.y + Zo.x};
      f2 zb = f2{Ze.x + Zo.y, -Ze.y + Zo.x};
      if (k == 0) {
        za = f2{a.x + a.y, a.x - a.y};
        zb = f2{2.f * b.x, -2.f * b.y};
      }
      zr[pa] = za;
      zr[pb] = zb;
    }
    STAMP(3, 1);
    pf(10);
    __syncthreads();
    f2 v[4];
    {
      const f2* buf = zs + w * ZRS + lane + (lane >> 4);
#pragma unroll
      for (int r4 = 0; r4 < 4; ++r4) v[r4] = buf[68 * r4];
    }
    fft256_inv(v, lane, tw, [&](int i) __attribute__((always_inline)) { pf(11 + i); });
    STAMP(3, 2);
    float s = 0.f;
    u4v x1p;
#pragma unroll
    for (int j = 0; j < 4; ++j) {
      f2 t = f2{bf_lo(xr[j]), bf_hi(xr[j])} + bfr2(f2{bf_lo(gav[j]), bf_hi(gav[j])} * bfr2(v[j]));
      x1p[j] = pack_bf(t.x, t.y);
      v[j] = f2{bf_lo(x1p[j]), bf_hi(x1p[j])};
      s += v[j].x + v[j].y;
    }
    ST128(x1p, OFF_X1 + ((row0 + w) * 64 + lane) * 16);
    s = wave_sum(s);
    const float meanf = s * (1.0f / D_);
    float q = 0.f;
#pragma unroll
    for (int j = 0; j < 4; ++j) {
      f2 d = v[j] - meanf;
      q += d.x * d.x + d.y * d.y;
    }
    q = wave_sum(q);
    const float mean = bfr(meanf);
    const float den = bfr(sqrtf(bfr(bfr(q * (1.0f / D_)) + 1e-5f)));
    const float rden = 1.0f / den;
    u4v hp;
#pragma unroll
    for (int j = 0; j < 4; ++j) {
      f2 t = __builtin_elementwise_fma(v[j] - mean, f2{bf_lo(cbv[j]), bf_hi(cbv[j])} * rden,
                                       f2{bf_lo(sbv[j]), bf_hi(sbv[j])});
      hp[j] = pack_bf(t.x, t.y);
    }
    *(u4v*)(dsm + L_ST + (lane * 9 + w) * 16) = hp;
    __syncthreads();
    {
      const int ch = tid >> 3, r = tid & 7;
      u4v o = *(const u4v*)(dsm + L_ST + (ch * 9 + r) * 16);
      const int mt = row0 >> 4, r16 = (row0 & 8) + r;
      ST128(o, OFF_H2F + ((mt * 16 + (ch >> 2)) * 64 + (ch & 3) * 16 + r16) * 16);
    }
  }
  STAMP(3, 3);
  DRAIN();
  __syncthreads();
  if (tid == 0) __hip_atomic_store(myA, 1u, __ATOMIC_RELAXED, __HIP_MEMORY_SCOPE_AGENT);
  {
    const int ch = tid >> 3, r = tid & 7;
    u4v t = LD128(OFF_H2F + (((row0 >> 4) * 16 + (ch >> 2)) * 64 + (ch & 3) * 16 + (row0 & 8) + r) * 16);
    asm volatile("" ::"v"(t));
  }
  STAMP(3, 4);
  pf(14);
  pf(15);
  wait_pair(depA);
  STAMP(3, 5);
  {
    u4v B[16];
#pragma unroll
    for (int ks = 0; ks < 16; ++ks) B[ks] = LD128(OFF_H2F + (((8 * p + w) * 16 + ks) * 64 + lane) * 16);
    f4 acc[7];
#pragma unroll
    for (int ft = 0; ft < 7; ++ft) acc[ft] = f4{0.f, 0.f, 0.f, 0.f};
    const char* lw = dsm + L_W1 + (lane >> 2) * 224 + (lane & 3) * 8;
    const int e = c >> 1, odd = c & 1;
    const int hb = (int)OFF_HF + (((8 * p + w) * 56 + 7 * e) * 64 + lane) * 16;
    u4v Af[2][7];
#pragma unroll
    for (int j = 0; j < 7; ++j) Af[0][j] = w1frag(lw, g1_ft(0, j), g1_ks(0, j));
#pragma unroll
    for (int g = 0; g < 17; ++g) {
#pragma unroll
      for (int j = 0; j < 7; ++j) {
        if (j < g1_n(g)) acc[g1_ft(g, j)] = mfma16(Af[g & 1][j], B[g1_ks(g, j)], acc[g1_ft(g, j)]);
        if (g + 1 < 17 && j < g1_n(g + 1)) Af[(g + 1) & 1][j] = w1frag(lw, g1_ft(g + 1, j), g1_ks(g + 1, j));
        SCHED();
      }
      if (g >= 2 && g < 12) pf_load(32 + g);
      if (g >= 14) {
        const int i = g - 14;
        f4 a = acc[2 * i], b = acc[2 * i + 1];
        u4v o = u4v{silu_pk(a[0], a[1]), silu_pk(a[2], a[3]), silu_pk(b[0], b[1]), silu_pk(b[2], b[3])};
        ST128(o, hb + (odd * 3 + i) * 1024);
      }
      SCHED();
      if (g == 12) STAMP(3, 6);
    }
    f4 a = acc[6];
    u2v o = u2v{silu_pk(a[0], a[1]), silu_pk(a[2], a[3])};
    ST64(o, hb + 6 * 1024 + odd * 8);
  }
  STAMP(3, 7);
  DRAIN();
  __syncthreads();
  if (tid == 0) __hip_atomic_store(myB, 1u, __ATOMIC_RELAXED, __HIP_MEMORY_SCOPE_AGENT);
  {
    const int hb = (int)OFF_HF + (((8 * p + w) * 56 + 7 * (c >> 1) + 3 * (c & 1)) * 64 + lane) * 16;
    u4v t0 = LD128(hb), t1 = LD128(hb + 1024), t2 = LD128(hb + 2048);
    asm volatile("" ::"v"(t0), "v"(t1), "v"(t2));
  }
  wait_pair(depB);
  STAMP(3, 8);
  {
    const int Bb = (int)OFF_HF + ((mt0 * 56 + 7 * w) * 64 + lane) * 16;
    u4v B[7][4];
#pragma unroll
    for (int ks = 0; ks < 4; ++ks)
#pragma unroll
      for (int mt = 0; mt < 4; ++mt) B[ks][mt] = LD128(Bb + (mt * 56 + ks) * 1024);
    f4 acc[4][4];
#pragma unroll
    for (int i = 0; i < 4; ++i)
#pragma unroll
      for (int j = 0; j < 4; ++j) acc[i][j] = f4{0.f, 0.f, 0.f, 0.f};
#pragma unroll
    for (int ks = 0; ks < 7; ++ks) {
#pragma unroll
      for (int nt = 0; nt < 4; ++nt)
#pragma unroll
        for (int mt = 0; mt < 4; ++mt) acc[nt][mt] = mfma16(A2[ks * 4 + nt], B[ks][mt], acc[nt][mt]);
      if (ks + 4 < 7) {
#pragma unroll
        for (int mt = 0; mt < 4; ++mt) B[ks + 4][mt] = LD128(Bb + (mt * 56 + ks + 4) * 1024);
      }
      SCHED();
    }
    STAMP(3, 9);
    const int g = lane >> 4, mr = lane & 15;
    const int fm = w >> 1, P = w & 1;
    const int m = (mt0 + fm) * 16 + mr;
    const int n = nt0 * 16 + 32 * P + 8 * g;
    unsigned xv[4];
#pragma unroll
    for (int i = 0; i < 4; ++i)
      xv[i] = __builtin_amdgcn_raw_buffer_load_b32(
          rs, (int)OFF_X1 + (m * 64 + (((n >> 1) + i) & 63)) * 16 + (n >> 7) * 4, 0, AUX_SC1);
    const f4 g0 = *(const f4*)(modf_ + 2560 + n), g1 = *(const f4*)(modf_ + 2560 + n + 4);
    f4* red = (f4*)dsm;
#pragma unroll
    for (int nt = 0; nt < 4; ++nt)
#pragma unroll
      for (int mt = 0; mt < 4; ++mt) red[(w * 16 + nt * 4 + mt) * 64 + lane] = acc[nt][mt];
    __syncthreads();
    STAMP(3, 10);
    f4 s0 = f4{0.f, 0.f, 0.f, 0.f}, s1 = s0;
#pragma unroll
    for (int ww = 0; ww < 8; ++ww) {
      s0 += red[(ww * 16 + (2 * P) * 4 + fm) * 64 + lane];
      s1 += red[(ww * 16 + (2 * P + 1) * 4 + fm) * 64 + lane];
    }
    u4v o;
    {
      f2 t0 = bfr2(f2{g0[0], g0[1]} * bfr2(f2{s0[0], s0[1]})), t1 = bfr2(f2{g0[2], g0[3]} * bfr2(f2{s0[2], s0[3]}));
      f2 t2 = bfr2(f2{g1[0], g1[1]} * bfr2(f2{s1[0], s1[1]})), t3 = bfr2(f2{g1[2], g1[3]} * bfr2(f2{s1[2], s1[3]}));
      o[0] = pack_bf(bf_lo(xv[0]) + t0.x, bf_hi(xv[0]) + t0.y);
      o[1] = pack_bf(bf_lo(xv[1]) + t1.x, bf_hi(xv[1]) + t1.y);
      o[2] = pack_bf(bf_lo(xv[2]) + t2.x, bf_hi(xv[2]) + t2.y);
      o[3] = pack_bf(bf_lo(xv[3]) + t3.x, bf_hi(xv[3]) + t3.y);
    }
    *(u4v*)(out + (size_t)m * D_ + n) = o;
  }
  STAMP(3, 11);
}

extern "C" void kernel_launch(void* const* d_in, const int* in_sizes, int n_in, void* d_out, int out_size, void* d_ws,
                              size_t ws_size, hipStream_t stream) {
  const u16* x = (const u16*)d_in[0];
  const u16* wr = (const u16*)d_in[1];
  const u16* wi = (const u16*)d_in[2];
  const u16* w1 = (const u16*)d_in[4];
  const u16* w2 = (const u16*)d_in[5];
  const u16* c = (const u16*)d_in[6];
  const u16* wmod = (const u16*)d_in[7];
  char* ws = (char*)d_ws;
  u16* out = (u16*)d_out;
  (void)hipFuncSetAttribute((const void*)k23_fused, hipFuncAttributeMaxDynamicSharedMemorySize, L_TOTAL);
  k0_mod<<<K0_GEMV, 512, 0, stream>>>(c, wmod, ws);
  k1_rowfft<<<256 + K1_WT + K1_W2, 512, 0, stream>>>(x, wr, wi, w2, ws);
  k23_fused<<<256, 512, L_TOTAL, stream>>>(x, w1, ws, out);
}
```

```cpp
#include <hip/hip_runtime.h>
#include <hip/hip_bf16.h>
#include <stdint.h>

typedef float f2 __attribute__((ext_vector_type(2)));
typedef float f4 __attribute__((ext_vector_type(4)));
typedef unsigned u2v __attribute__((ext_vector_type(2)));
typedef unsigned u4v __attribute__((ext_vector_type(4)));
typedef short bf16x8 __attribute__((ext_vector_type(8)));
typedef unsigned short u16;

constexpr int S_ = 2048, D_ = 512, F_ = 1792, DF_ = 257;
constexpr int NSPLIT = 4;

constexpr size_t OFF_CNT = 0;
constexpr size_t OFF_FLG = 16384;
constexpr size_t OFF_MODP = 32768;
constexpr size_t OFF_MODF = OFF_MODP + (size_t)NSPLIT * 3072 * 4;
constexpr size_t OFF_MODC = OFF_MODF + 3072 * 4;
constexpr size_t OFF_WT = 1 << 20;
constexpr size_t OFF_XT = 4 << 20;
constexpr size_t OFF_ZT = 8 << 20;
constexpr size_t OFF_H2F = 12 << 20;
constexpr size_t OFF_X1 = 14 << 20;
constexpr size_t OFF_W2F = 18 << 20;
constexpr size_t OFF_HF = 20 << 20;
constexpr int WS_SPAN = 28 << 20;
constexpr float FSC = 1.0f / (2.0f * 2048.0f * 512.0f);

#define STAMP(kid, i)

__device__ __forceinline__ float bf_lo(unsigned u) { return __builtin_bit_cast(float, u << 16); }
__device__ __forceinline__ float bf_hi(unsigned u) { return __builtin_bit_cast(float, u & 0xffff0000u); }
__device__ __forceinline__ float bfr(float x) {
  return (float)(__bf16)x;
}
__device__ __forceinline__ unsigned pack_bf(float lo, float hi) {
  typedef __bf16 b2 __attribute__((ext_vector_type(2)));
  return __builtin_bit_cast(unsigned, __builtin_convertvector(f2{lo, hi}, b2));
}
__device__ __forceinline__ f2 cmul(f2 a, f2 b) {
  f2 t = a * f2{b.x, b.x};
  return __builtin_elementwise_fma(f2{a.y, a.x}, f2{-b.y, b.y}, t);
}
__device__ __forceinline__ f2 cmulc(f2 a, f2 b) {
  f2 t = a * f2{b.x, b.x};
  return __builtin_elementwise_fma(f2{a.y, a.x}, f2{b.y, -b.y}, t);
}
__device__ __forceinline__ f2 bfr2(f2 a) {
  unsigned u = pack_bf(a.x, a.y);
  asm("" : "+v"(u));
  return f2{bf_lo(u), bf_hi(u)};
}
__device__ __forceinline__ float msel(unsigned m, float a, float b) {
  unsigned ua = __builtin_bit_cast(unsigned, a), ub = __builtin_bit_cast(unsigned, b);
  return __builtin_bit_cast(float, (m & ua) | (~m & ub));
}
#define PIN2(x) asm volatile("" : "+v"(x))
constexpr int AUX_SC1 = 16;
#define WS_RSRC(ws) __builtin_amdgcn_make_buffer_rsrc((ws), 0, WS_SPAN, 0x00020000)
#define ST128(v, off) __builtin_amdgcn_raw_buffer_store_b128((v), rs, (int)(off), 0, AUX_SC1)
#define ST64(v, off) __builtin_amdgcn_raw_buffer_store_b64((v), rs, (int)(off), 0, AUX_SC1)
#define LD128(off) __builtin_amdgcn_raw_buffer_load_b128(rs, (int)(off), 0, AUX_SC1)
__device__ __forceinline__ f2 twid(float rev) { return f2{__builtin_amdgcn_cosf(rev), -__builtin_amdgcn_sinf(rev)}; }

template <int DIR>
__device__ __forceinline__ f2 rot90(f2 a) {
  return DIR < 0 ? f2{a.y, -a.x} : f2{-a.y, a.x};
}
template <int DIR>
__device__ __forceinline__ void dft4(f2& c0, f2& c1, f2& c2, f2& c3) {
  f2 d0 = c0 + c2, d2 = c0 - c2, d1 = c1 + c3, e = c1 - c3;
  c0 = d0 + d1;
  c2 = d0 - d1;
  const f2 es = f2{e.y, e.x};
  c1 = __builtin_elementwise_fma(es, DIR < 0 ? f2{1.f, -1.f} : f2{-1.f, 1.f}, d2);
  c3 = __builtin_elementwise_fma(es, DIR < 0 ? f2{-1.f, 1.f} : f2{1.f, -1.f}, d2);
}

__device__ __forceinline__ void sw32(float& a, float& b) {
  auto r = __builtin_amdgcn_permlane32_swap(__builtin_bit_cast(unsigned, a), __builtin_bit_cast(unsigned, b), false, false);
  a = __builtin_bit_cast(float, (unsigned)r[0]);
  b = __builtin_bit_cast(float, (unsigned)r[1]);
}
__device__ __forceinline__ void sw16(float& a, float& b) {
  auto r = __builtin_amdgcn_permlane16_swap(__builtin_bit_cast(unsigned, a), __builtin_bit_cast(unsigned, b), false, false);
  a = __builtin_bit_cast(float, (unsigned)r[0]);
  b = __builtin_bit_cast(float, (unsigned)r[1]);
}
template <int CTRL, int BANK>
__device__ __forceinline__ float udpp(float old, float src) {
  return __builtin_bit_cast(float, __builtin_amdgcn_update_dpp(__builtin_bit_cast(int, old), __builtin_bit_cast(int, src),
                                                               CTRL, 0xf, BANK, false));
}
template <int CTRL>
__device__ __forceinline__ float mdpp(float src) {
  return __builtin_bit_cast(float, __builtin_amdgcn_mov_dpp(__builtin_bit_cast(int, src), CTRL, 0xf, 0xf, false));
}
__device__ __forceinline__ void xp_hi1(float& v0, float& v1, float& v2, float& v3) {
  sw32(v0, v2); sw32(v1, v3); sw16(v0, v1); sw16(v2, v3);
}
__device__ __forceinline__ void xp_mid1(float& v0, float& v1, float& v2, float& v3) {
  float a0 = udpp<0x128, 0xC>(v0, v2), a2 = udpp<0x128, 0x3>(v2, v0);
  float a1 = udpp<0x128, 0xC>(v1, v3), a3 = udpp<0x128, 0x3>(v3, v1);
  v0 = udpp<0x124, 0xA>(a0, a1); v1 = udpp<0x12C, 0x5>(a1, a0);
  v2 = udpp<0x124, 0xA>(a2, a3); v3 = udpp<0x12C, 0x5>(a3, a2);
}
__device__ __forceinline__ void xp_lo1(float& v0, float& v1, float& v2, float& v3, unsigned b1, unsigned b0) {
  float s0 = mdpp<0x4E>(msel(b1, v0, v2)), s1 = mdpp<0x4E>(msel(b1, v1, v3));
  float a0 = msel(b1, s0, v0), a2 = msel(b1, v2, s0), a1 = msel(b1, s1, v1), a3 = msel(b1, v3, s1);
  float t0 = mdpp<0xB1>(msel(b0, a0, a1)), t2 = mdpp<0xB1>(msel(b0, a2, a3));
  v0 = msel(b0, t0, a0); v1 = msel(b0, a1, t0); v2 = msel(b0, t2, a2); v3 = msel(b0, a3, t2);
}
#define XP_CPLX(NAME, FN, ...)                                        \
  {                                                                   \
    float x0 = v[0].x, x1 = v[1].x, x2 = v[2].x, x3 = v[3].x;         \
    float y0 = v[0].y, y1 = v[1].y, y2 = v[2].y, y3 = v[3].y;         \
    FN(x0, x1, x2, x3, ##__VA_ARGS__);                                \
    FN(y0, y1, y2, y3, ##__VA_ARGS__);                                \
    v[0] = f2{x0, y0}; v[1] = f2{x1, y1}; v[2] = f2{x2, y2}; v[3] = f2{x3, y3}; \
  }
__device__ __forceinline__ void xp_hi(f2 (&v)[4]) XP_CPLX(hi, xp_hi1)
__device__ __forceinline__ void xp_mid(f2 (&v)[4]) XP_CPLX(mid, xp_mid1)
__device__ __forceinline__ void xp_lo(f2 (&v)[4], unsigned b1, unsigned b0) XP_CPLX(lo, xp_lo1, b1, b0)

struct Tw256 {
  f2 a[3], b[3], c[3];
  unsigned m1, m0;
};
__device__ __forceinline__ void make_tw256(int lane, Tw256& t) {
#pragma unroll
  for (int r = 1; r < 4; ++r) {
    t.a[r - 1] = twid((float)(lane * r) * (1.0f / 256.0f));
    t.b[r - 1] = twid((float)((lane & 15) * r) * (1.0f / 64.0f));
    t.c[r - 1] = twid((float)((lane & 3) * r) * (1.0f / 16.0f));
  }
  t.m1 = 0u - (unsigned)((lane >> 1) & 1);
  t.m0 = 0u - (unsigned)(lane & 1);
#pragma unroll
  for (int r = 0; r < 3; ++r) {
    PIN2(t.a[r]);
    PIN2(t.b[r]);
    PIN2(t.c[r]);
  }
  PIN2(t.m1);
  PIN2(t.m0);
}
struct NoHook {
  __device__ __forceinline__ void operator()(int) const {}
};
template <class H>
__device__ __forceinline__ void fft256_fwd(f2 (&v)[4], int lane, const Tw256& t, const H& hook) {
  const unsigned b1 = t.m1, b0 = t.m0;
  dft4<-1>(v[0], v[1], v[2], v[3]);
#pragma unroll
  for (int r = 1; r < 4; ++r) v[r] = cmul(v[r], t.a[r - 1]);
  xp_hi(v);
  hook(0);
  dft4<-1>(v[0], v[1], v[2], v[3]);
#pragma unroll
  for (int r = 1; r < 4; ++r) v[r] = cmul(v[r], t.b[r - 1]);
  xp_mid(v);
  hook(1);
  dft4<-1>(v[0], v[1], v[2], v[3]);
#pragma unroll
  for (int r = 1; r < 4; ++r) v[r] = cmul(v[r], t.c[r - 1]);
  xp_lo(v, b1, b0);
  hook(2);
  dft4<-1>(v[0], v[1], v[2], v[3]);
}
template <class H>
__device__ __forceinline__ void fft256_inv(f2 (&v)[4], int lane, const Tw256& t, const H& hook) {
  const unsigned b1 = t.m1, b0 = t.m0;
  dft4<1>(v[0], v[1], v[2], v[3]);
  xp_lo(v, b1, b0);
  hook(0);
#pragma unroll
  for (int r = 1; r < 4; ++r) v[r] = cmulc(v[r], t.c[r - 1]);
  dft4<1>(v[0], v[1], v[2], v[3]);
  xp_mid(v);
  hook(1);
#pragma unroll
  for (int r = 1; r < 4; ++r) v[r] = cmulc(v[r], t.b[r - 1]);
  dft4<1>(v[0], v[1], v[2], v[3]);
  xp_hi(v);
  hook(2);
#pragma unroll
  for (int r = 1; r < 4; ++r) v[r] = cmulc(v[r], t.a[r - 1]);
  dft4<1>(v[0], v[1], v[2], v[3]);
}

__device__ __forceinline__ float wave_sum(float x) {
  x += mdpp<0xB1>(x);
  x += mdpp<0x4E>(x);
  x += mdpp<0x141>(x);
  x += mdpp<0x140>(x);
  float r0 = __builtin_bit_cast(float, __builtin_amdgcn_readlane(__builtin_bit_cast(int, x), 0));
  float r1 = __builtin_bit_cast(float, __builtin_amdgcn_readlane(__builtin_bit_cast(int, x), 16));
  float r2 = __builtin_bit_cast(float, __builtin_amdgcn_readlane(__builtin_bit_cast(int, x), 32));
  float r3 = __builtin_bit_cast(float, __builtin_amdgcn_readlane(__builtin_bit_cast(int, x), 48));
  return (r0 + r1) + (r2 + r3);
}

__device__ __forceinline__ int zpos(int k) { return 17 * (k & 3) + 4 * ((k >> 2) & 3) + ((k >> 4) & 3) + 68 * (k >> 6); }
constexpr int ZRS = 276;

constexpr int K0_GEMV = 48 * NSPLIT, K0_WARM = 240;
__global__ __launch_bounds__(512) void k0_mod(const u16* __restrict__ c, const u16* __restrict__ wmod,
                                              const u16* __restrict__ x, const u16* __restrict__ wr,
                                              const u16* __restrict__ wi, const u16* __restrict__ w1,
                                              const u16* __restrict__ w2, char* __restrict__ ws) {
  __shared__ __attribute__((aligned(16))) char smem[2048];
  const int tid = threadIdx.x, row = tid >> 6, lane = tid & 63;
  const int b = blockIdx.x;
  STAMP(0, 0);
  if (b >= K0_GEMV) {
    int q = b - K0_GEMV;
    const char* src;
    if (q < 64) src = (const char*)x;
    else if ((q -= 64) < 32) src = (const char*)wr;
    else if ((q -= 32) < 32) src = (const char*)wi;
    else if ((q -= 32) < 56) src = (const char*)w2;
    else if ((q -= 56) < 56) src = (const char*)w1;
    else {
      unsigned long long pc;
      asm volatile("s_getpc_b64 %0" : "=s"(pc));
      const char* code = (const char*)(pc & ~63ull) + tid * 16;
      u4v t0 = *(const u4v*)(code), t1 = *(const u4v*)(code + 8192), t2 = *(const u4v*)(code + 14336);
      asm volatile("" ::"v"(t0), "v"(t1), "v"(t2));
      return;
    }
    u4v t[4];
#pragma unroll
    for (int i = 0; i < 4; ++i) t[i] = *(const u4v*)(src + (size_t)q * 32768 + i * 8192 + tid * 16);
#pragma unroll
    for (int i = 0; i < 4; ++i) asm volatile("" ::"v"(t[i]));
    STAMP(0, 1);
    return;
  }
  if (b == 0) {
#pragma unroll
    for (int i = 0; i < 4; ++i) ((u4v*)(ws + OFF_CNT))[i * 512 + tid] = u4v{0u, 0u, 0u, 0u};
  }
  float* red = (float*)smem;
  const int cgp = b % 48, ks = b / 48;
  const int cg = tid & 7, r = tid >> 3;
  constexpr int RPS = 512 / NSPLIT;
  float acc[8];
#pragma unroll
  for (int i = 0; i < 8; ++i) acc[i] = 0.f;
  u4v wv[RPS / 64];
  float sc[RPS / 64];
#pragma unroll
  for (int it = 0; it < RPS / 64; ++it) {
    const int rw = ks * RPS + it * 64 + r;
    wv[it] = *(const u4v*)(wmod + (size_t)rw * 3072 + cgp * 64 + cg * 8);
    float cv = bf_lo(c[rw]);
    sc[it] = bfr(cv / (1.0f + __expf(-cv)));
  }
#pragma unroll
  for (int it = 0; it < RPS / 64; ++it) {
#pragma unroll
    for (int i = 0; i < 4; ++i) {
      acc[2 * i] += sc[it] * bf_lo(wv[it][i]);
      acc[2 * i + 1] += sc[it] * bf_hi(wv[it][i]);
    }
  }
#pragma unroll
  for (int i = 0; i < 8; ++i) {
    acc[i] += mdpp<0x128>(acc[i]);
    acc[i] += __shfl_xor(acc[i], 16);
    acc[i] += __shfl_xor(acc[i], 32);
  }
  if (lane < 8) {
#pragma unroll
    for (int i = 0; i < 8; ++i) red[row * 64 + cg * 8 + i] = acc[i];
  }
  __syncthreads();
  if (tid < 64) {
    float s = 0.f;
#pragma unroll
    for (int i = 0; i < 8; ++i) s += red[i * 64 + tid];
    ((float*)(ws + OFF_MODP))[ks * 3072 + cgp * 64 + tid] = s;
  }
  STAMP(0, 1);
}

constexpr int K1_WT = 128, K1_W2 = 112;
__global__ __launch_bounds__(512) void k1_rowfft(const u16* __restrict__ x, const u16* __restrict__ wr,
                                                 const u16* __restrict__ wi, const u16* __restrict__ w2,
                                                 char* __restrict__ ws) {
  __shared__ __attribute__((aligned(16))) char smem[8 * ZRS * 8 + 4096];
  const int tid = threadIdx.x, row = tid >> 6, lane = tid & 63;
  const auto rs = WS_RSRC(ws);
  STAMP(1, 0);
  if (blockIdx.x >= 256) {
    int b = blockIdx.x - 256;
    if (b < K1_WT) {
      char* sr = smem;
      char* si = smem + 16 * 544;
      constexpr int LASTCH = (S_ * DF_ * 2) / 16 - 1;
      for (int it = tid; it < 16 * 34; it += 512) {
        const int i = it / 34, ch = it % 34;
        int gch = ((b + 128 * i) * (DF_ * 2)) / 16 + ch;
        gch = gch > LASTCH ? LASTCH : gch;
        *(u4v*)(sr + i * 544 + ch * 16) = *(const u4v*)((const char*)wr + (size_t)gch * 16);
        *(u4v*)(si + i * 544 + ch * 16) = *(const u4v*)((const char*)wi + (size_t)gch * 16);
      }
      __syncthreads();
      const int tbase = 64 * (b & 7) + 16 * ((b >> 3) & 3) + 4 * ((b >> 5) & 3);
      for (int it = tid; it < DF_ * 4; it += 512) {
        const int k = it >> 2, r3 = it & 3;
        u4v o;
#pragma unroll
        for (int r4 = 0; r4 < 4; ++r4) {
          const int i = r3 + 4 * r4;
          const int off = i * 544 + (((b + 128 * i) * (DF_ * 2)) & 15) + 2 * k;
          o[r4] = (unsigned)*(const u16*)(sr + off) | ((unsigned)*(const u16*)(si + off) << 16);
        }
        ST128(o, OFF_WT + (k * 512 + tbase + r3) * 16);
      }
    } else {
      b -= K1_WT;
      u16* t = (u16*)smem;
      const int g = lane >> 4, r = lane & 15;
      const int ng = b & 3, kp = b >> 2;
#pragma unroll
      for (int it = 0; it < 2; ++it) {
        const int idx = it * 512 + tid;
        const int rr = idx >> 4, ch = idx & 15;
        const int ks2 = kp * 2 + (rr >> 5), gg = (rr >> 3) & 3, j = rr & 7;
        const int e = ks2 / 7, i7 = ks2 % 7;
        int T0, T1;
        if (i7 < 3) { T0 = 14 * e + 2 * i7; T1 = T0 + 1; }
        else if (i7 < 6) { T0 = 14 * e + 7 + 2 * (i7 - 3); T1 = T0 + 1; }
        else { T0 = 14 * e + 6; T1 = 14 * e + 13; }
        const int f = (j < 4) ? (16 * T0 + 4 * gg + j) : (16 * T1 + 4 * gg + (j - 4));
        *(u4v*)(t + rr * 136 + ch * 8) = *(const u4v*)(w2 + (size_t)f * D_ + ng * 128 + ch * 8);
      }
      __syncthreads();
      const int nl = 32 * (row >> 1) + 8 * (r >> 2) + 4 * (row & 1) + (r & 3);
#pragma unroll
      for (int ksl = 0; ksl < 2; ++ksl) {
        u4v o;
#pragma unroll
        for (int jj = 0; jj < 4; ++jj)
          o[jj] = (unsigned)t[(ksl * 32 + g * 8 + 2 * jj) * 136 + nl] |
                  ((unsigned)t[(ksl * 32 + g * 8 + 2 * jj + 1) * 136 + nl] << 16);
        const int nt = ng * 8 + row;
        ST128(o, OFF_W2F + ((nt * 56 + kp * 2 + ksl) * 64 + lane) * 16);
      }
    }
    STAMP(1, 5);
    return;
  }
  f2* zs = (f2*)smem;
  float* mods = (float*)(smem + 8 * ZRS * 8);
  const int row0 = blockIdx.x * 8;
  unsigned xr[4];
  const unsigned* xrow = (const unsigned*)(x + (size_t)(row0 + row) * D_);
#pragma unroll
  for (int j = 0; j < 4; ++j) xr[j] = xrow[lane + 64 * j];
  {
    const float* mp = (const float*)(ws + OFF_MODP);
    f2 s = *(const f2*)(mp + tid * 2);
#pragma unroll
    for (int k = 1; k < NSPLIT; ++k) s += *(const f2*)(mp + k * 3072 + tid * 2);
    f2 o;
#pragma unroll
    for (int i = 0; i < 2; ++i) {
      float v = bfr(s[i]);
      o[i] = (tid >= 256) ? bfr(1.0f + v) : v;
    }
    *(f2*)(mods + tid * 2) = o;
    if (blockIdx.x < 3) {
      const int idx = blockIdx.x * 1024 + tid * 2;
      f2 t = *(const f2*)(mp + idx);
#pragma unroll
      for (int k = 1; k < NSPLIT; ++k) t += *(const f2*)(mp + k * 3072 + idx);
      t = bfr2(t);
      *(f2*)((float*)(ws + OFF_MODF) + idx) = t;
      const int vec = (idx >> 9) - 2, d = idx & 511;
      if (vec >= 0 && vec < 3) {
        if (vec == 2) t += 1.0f;
        ((unsigned*)(ws + OFF_MODC))[(vec * 64 + ((d >> 1) & 63)) * 4 + (d >> 7)] = pack_bf(t.x, t.y);
      }
    }
  }
  Tw256 tw;
  make_tw256(lane, tw);
  const int rp = tid & 3, kk = tid >> 2;
  f2 wpost = twid((float)kk * (1.0f / 512.0f));
  PIN2(wpost);
  f2 v[4];
  float s = 0.f;
#pragma unroll
  for (int j = 0; j < 4; ++j) {
    v[j] = f2{bf_lo(xr[j]), bf_hi(xr[j])};
    s += v[j].x + v[j].y;
  }
  s = wave_sum(s);
  const float meanf = s * (1.0f / D_);
  float q = 0.f;
#pragma unroll
  for (int j = 0; j < 4; ++j) {
    f2 d = v[j] - meanf;
    q += d.x * d.x + d.y * d.y;
  }
  q = wave_sum(q);
  const float mean = bfr(meanf);
  const float den = bfr(sqrtf(bfr(bfr(q * (1.0f / D_)) + 1e-5f)));
  const float rden = 1.0f / den;
  STAMP(1, 1);
  __syncthreads();
  STAMP(1, 2);
#pragma unroll
  for (int j = 0; j < 4; ++j) {
    const int d = 2 * (lane + 64 * j);
    f2 sa = *(const f2*)(mods + d);
    f2 ca1 = *(const f2*)(mods + 512 + d);
    v[j] = bfr2(__builtin_elementwise_fma(v[j] - mean, ca1 * rden, sa));
  }
  fft256_fwd(v, lane, tw, NoHook{});
  {
    f2* buf = zs + row * ZRS + lane + (lane >> 4);
#pragma unroll
    for (int r4 = 0; r4 < 4; ++r4) buf[68 * r4] = v[r4];
  }
  STAMP(1, 3);
  __syncthreads();
  STAMP(1, 4);
  {
    const int k = kk;
    const int k2 = (k == 0) ? 128 : 256 - k;
    const int pa = zpos(k), pb = zpos(k2);
    f4 oa, ob;
#pragma unroll
    for (int h = 0; h < 2; ++h) {
      const f2* zr = zs + (2 * rp + h) * ZRS;
      f2 za = zr[pa], zb = zr[pb];
      f2 E = f2{za.x + zb.x, za.y - zb.y};
      f2 Dm = f2{za.x - zb.x, za.y + zb.y};
      f2 O = f2{Dm.y, -Dm.x};
      f2 wo = cmul(wpost, O);
      f2 ra = E + wo;
      f2 t = E - wo;
      f2 rb = f2{t.x, -t.y};
      if (k == 0) {
        ra = f2{2.f * (za.x + za.y), 2.f * (za.x - za.y)};
        rb = f2{2.f * zb.x, -2.f * zb.y};
      }
      oa[2 * h] = ra.x; oa[2 * h + 1] = ra.y;
      ob[2 * h] = rb.x; ob[2 * h + 1] = rb.y;
    }
    ST128(__builtin_bit_cast(u4v, oa), OFF_XT + (k * S_ + row0 + 2 * rp) * 8);
    ST128(__builtin_bit_cast(u4v, ob), OFF_XT + (k2 * S_ + row0 + 2 * rp) * 8);
  }
  STAMP(1, 5);
}

__device__ __forceinline__ f4 mfma16(u4v a, u4v b, f4 c) {
  return __builtin_amdgcn_mfma_f32_16x16x32_bf16(__builtin_bit_cast(bf16x8, a), __builtin_bit_cast(bf16x8, b), c, 0, 0,
                                                  0);
}
__device__ __forceinline__ f2 silu2(float a, float b) {
  f2 h = f2{a, b};
  f2 t = h * -1.44269504088896340736f;
  f2 d = f2{__builtin_amdgcn_exp2f(t.x), __builtin_amdgcn_exp2f(t.y)} + 1.0f;
  return h * f2{__builtin_amdgcn_rcpf(d.x), __builtin_amdgcn_rcpf(d.y)};
}
__device__ __forceinline__ unsigned silu_pk(float a, float b) {
  f2 r = silu2(a, b);
  return pack_bf(r.x, r.y);
}
typedef __attribute__((address_space(1))) unsigned gu32;
#define DRAIN() asm volatile("s_waitcnt vmcnt(0)" ::: "memory")
typedef __attribute__((address_space(1))) unsigned long long gu64;
__device__ __forceinline__ void wait_pair(gu64* flags) {
  for (unsigned spin = 0; spin < (1u << 20); ++spin) {
    if (__hip_atomic_load(flags, __ATOMIC_RELAXED, __HIP_MEMORY_SCOPE_AGENT) == 0x0000000100000001ull) break;
    __builtin_amdgcn_s_sleep(1);
  }
  asm volatile("" ::: "memory");
}

__device__ __forceinline__ void wait_cols(__amdgpu_buffer_rsrc_t rs, int off) {
  for (unsigned spin = 0; spin < (1u << 20); ++spin) {
    const u4v f = LD128(off);
    if (__builtin_amdgcn_ballot_w64((f[0] & f[1] & f[2] & f[3]) != 1u) == 0ull) break;
    __builtin_amdgcn_s_sleep(2);
    asm volatile("" ::: "memory");
  }
}

constexpr int L_W1 = 0;
constexpr int L_T = 112 * 1024;
constexpr int L_ZS = 112 * 1024;
constexpr int L_ST = L_ZS + 8 * ZRS * 8;
constexpr int L_TOTAL = 160 * 1024;
__device__ __forceinline__ constexpr int g1_n(int g) { return g < 13 ? 7 : (g < 16 ? 6 : 3); }
__device__ __forceinline__ constexpr int g1_ft(int g, int j) { return g < 13 ? j : (g < 16 ? 2 * (g - 13) + (j & 1) : 6); }
__device__ __forceinline__ constexpr int g1_ks(int g, int j) { return g < 13 ? g : (g < 16 ? 13 + (j >> 1) : 13 + j); }
#define SCHED() __builtin_amdgcn_sched_barrier(0)
typedef short v4i16 __attribute__((ext_vector_type(4)));
__device__ __forceinline__ u4v w1frag(const char* lw, int ft, int ks) {
  typedef __attribute__((address_space(3))) v4i16* lp;
  const u2v a = __builtin_bit_cast(u2v, __builtin_amdgcn_ds_read_tr16_b64_v4i16((lp)(lw + (2 * ks) * 3584 + ft * 32)));
  const u2v b = __builtin_bit_cast(u2v, __builtin_amdgcn_ds_read_tr16_b64_v4i16((lp)(lw + (2 * ks + 1) * 3584 + ft * 32)));
  return u4v{a[0], a[1], b[0], b[1]};
}
__global__ __launch_bounds__(512, 2) void k23_fused(const u16* __restrict__ x, const u16* __restrict__ w1,
                                                    char* __restrict__ ws, u16* __restrict__ out) {
  extern __shared__ __attribute__((aligned(16))) char dsm[];
  const int tid = threadIdx.x, lane = tid & 63;
  const int w = __builtin_amdgcn_readfirstlane(tid >> 6);
  const int bid = blockIdx.x;
  const int p = (bid & 7) * 2 + (bid >> 7), c = (bid >> 3) & 15;
  const int row0 = 128 * p + 8 * c;
  STAMP(2, 0);
  const auto rs = WS_RSRC(ws);
  gu32* myA = (gu32*)(ws + OFF_CNT) + (p * 8 + (c >> 1)) * 16 + (c & 1);
  gu32* myB = myA + 128 * 16;
  gu64* depA = (gu64*)(ws + OFF_CNT) + (p * 8 + w) * 8;
  gu64* depB = depA + 128 * 8;
  const float* modf_ = (const float*)(ws + OFF_MODF);
  const int mt0 = 8 * p + 4 * (c >> 3), nt0 = 4 * (c & 7);
  const int l56 = lane < 56 ? lane : lane - 56;
  const int r4_ = l56 / 14, ch_ = l56 - 14 * r4_;
  const char* w1g = (const char*)w1 + (size_t)(4 * w + r4_) * (F_ * 2) + c * 224 + ch_ * 16;
  char* w1l = dsm + L_W1 + ((w >> 1) * 32 + (2 * (w & 1) + (r4_ >> 1)) * 4 + (r4_ & 1)) * 224 + ch_ * 16;
  const char* Ab = ws + OFF_W2F + ((size_t)(nt0 * 56 + 7 * w) * 64 + lane) * 16;
  u4v S1[16];
  u4v A2[28];
  auto pf_load = [&](int i) __attribute__((always_inline)) {
    if (i < 16) S1[i] = *(const u4v*)(w1g + (size_t)i * (32 * F_ * 2));
    else if (i < 44) A2[i - 16] = *(const u4v*)(Ab + (size_t)(((i - 16) & 3) * 56 + ((i - 16) >> 2)) * 1024);
  };
  auto pf_put = [&](int i) __attribute__((always_inline)) {
    *(u4v*)(w1l + ((i & 3) * 128 + (i >> 3) * 16 + ((i >> 2) & 1) * 2) * 224) = S1[i];
  };
  auto pf = [&](int i0, int n) __attribute__((always_inline)) {
    SCHED();
#pragma unroll
    for (int i = 0; i < n; ++i) pf_load(i0 + i);
    SCHED();
  };
  Tw256 tw;
  {
    f2* T = (f2*)(dsm + L_T);
    f2* T2 = (f2*)(dsm + L_T + 16384);
    f2* T3 = (f2*)(dsm + L_T + 32768);
    const int k = bid;
    const f2* XT = (const f2*)(ws + OFF_XT) + (size_t)k * S_;
    const unsigned* WT = (const unsigned*)(ws + OFF_WT);
    f2 v[4];
#pragma unroll
    for (int j = 0; j < 4; ++j) v[j] = XT[tid + 512 * j];
    const u4v wv = *(const u4v*)(WT + ((size_t)k * 512 + tid) * 4);
    u4v bv;
    unsigned wn[4], bn[4];
    int mirr[4];
    SCHED();
    make_tw256(lane, tw);
    f2 tw1[4];
#pragma unroll
    for (int j = 0; j < 4; ++j) {
      tw1[j] = twid((float)((lane + 64 * j) * w) * (1.0f / 2048.0f));
      PIN2(tw1[j]);
    }
    pf(0, 6);
    const int hb = w >> 2, a = tid & 255;
    constexpr float R = 0.70710678118654752440f;
    dft4<-1>(v[0], v[1], v[2], v[3]);
    if (hb) {
      v[1] = f2{v[1].x + v[1].y, v[1].y - v[1].x} * R;
      v[2] = f2{v[2].y, -v[2].x};
      v[3] = f2{v[3].y - v[3].x, -v[3].x - v[3].y} * R;
    }
#pragma unroll
    for (int q = 0; q < 4; ++q) T[(hb * 4 + q) * 256 + a] = v[q];
    STAMP(2, 1);
    pf(6, 3);
    if (k == 0) {
      bv = *(const u4v*)(WT + ((size_t)256 * 512 + tid) * 4);
      const int m0 = (lane >> 4) + 4 * ((lane >> 2) & 3) + 16 * (lane & 3);
#pragma unroll
      for (int r4 = 0; r4 < 4; ++r4) {
        const int un = (S_ - (w + 8 * (m0 + 64 * r4))) & (S_ - 1);
        const int mm = un >> 3;
        mirr[r4] = ((un & 7) * 64 + 16 * (mm & 3) + 4 * ((mm >> 2) & 3) + ((mm >> 4) & 3)) * 4 + (mm >> 6);
      }
      if (w != 0) {
        const u4v a4 = *(const u4v*)(WT + (64 * (8 - w) + 63 - lane) * 4);
        const u4v b4 = *(const u4v*)(WT + (size_t)256 * 2048 + (64 * (8 - w) + 63 - lane) * 4);
#pragma unroll
        for (int r4 = 0; r4 < 4; ++r4) { wn[r4] = a4[3 - r4]; bn[r4] = b4[3 - r4]; }
      } else {
#pragma unroll
        for (int r4 = 0; r4 < 4; ++r4) {
          wn[r4] = WT[mirr[r4]];
          bn[r4] = WT[(size_t)256 * 2048 + mirr[r4]];
        }
      }
    }
    __syncthreads();
    {
      const int q = w & 3;
      const float sg = (w < 4) ? 1.f : -1.f;
#pragma unroll
      for (int j = 0; j < 4; ++j) {
        f2 e = T[q * 256 + lane + 64 * j], o = T[(4 + q) * 256 + lane + 64 * j];
        v[j] = cmul(e + o * sg, tw1[j]);
      }
    }
    fft256_fwd(v, lane, tw, [&](int i) __attribute__((always_inline)) { pf(9 + 2 * i, 2); });
    pf(15, 1);
    STAMP(2, 2);
    if (k != 0) {
#pragma unroll
      for (int i = 0; i < 4; ++i) {
        f2 wf = f2{bf_lo(wv[i]), bf_hi(wv[i])} * FSC;
        v[i] = cmul(v[i], wf);
      }
    } else {
#pragma unroll
      for (int r4 = 0; r4 < 4; ++r4) T3[r4 * 512 + tid] = v[r4];
      __syncthreads();
#pragma unroll
      for (int r4 = 0; r4 < 4; ++r4) {
        f2 cm = T3[(mirr[r4] & 3) * 512 + (mirr[r4] >> 2)];
        f2 h0 = f2{bf_lo(wv[r4]) + bf_lo(wn[r4]), bf_hi(wv[r4]) - bf_hi(wn[r4])};
        f2 h1 = f2{bf_lo(bv[r4]) + bf_lo(bn[r4]), bf_hi(bv[r4]) - bf_hi(bn[r4])};
        f2 wp = (h0 + h1) * (0.25f * FSC), wm = (h0 - h1) * (0.25f * FSC);
        f2 cc = f2{cm.x, -cm.y};
        v[r4] = cmul(v[r4], wp) + cmul(cc, wm);
      }
    }
    fft256_inv(v, lane, tw, [&](int i) __attribute__((always_inline)) {
      SCHED();
#pragma unroll
      for (int q = 0; q < 4; ++q) pf_put(4 * i + q);
      SCHED();
    });
#pragma unroll
    for (int j = 0; j < 4; ++j) T2[w * 256 + lane + 64 * j] = cmulc(v[j], tw1[j]);
#pragma unroll
    for (int q = 12; q < 16; ++q) pf_put(q);
    STAMP(2, 3);
    __syncthreads();
    {
      const float sg = hb ? -1.f : 1.f;
#pragma unroll
      for (int q = 0; q < 4; ++q) v[q] = T2[q * 256 + a] + T2[(q + 4) * 256 + a] * sg;
      if (hb) {
        v[1] = f2{v[1].x - v[1].y, v[1].y + v[1].x} * R;
        v[2] = f2{-v[2].y, v[2].x};
        v[3] = f2{-v[3].x - v[3].y, v[3].x - v[3].y} * R;
      }
    }
    dft4<1>(v[0], v[1], v[2], v[3]);
#pragma unroll
    for (int j = 0; j < 4; ++j) ST64(__builtin_bit_cast(u2v, v[j]), OFF_ZT + (k * S_ + tid + 512 * j) * 8);
    STAMP(2, 4);
    DRAIN();
    __syncthreads();
    if (tid < 16)
      __hip_atomic_store((gu32*)(ws + OFF_FLG) + tid * 256 + k, 1u, __ATOMIC_RELAXED, __HIP_MEMORY_SCOPE_AGENT);
  }
  STAMP(2, 5);
  pf(16, 4);
  {
    const char* lw = dsm + L_W1 + (lane >> 2) * 224 + (lane & 3) * 8 + w * (2 * 7168);
    u4v fr[14];
#pragma unroll
    for (int i = 0; i < 14; ++i) fr[i] = w1frag(lw, i % 7, i / 7);
    asm volatile("" ::: "memory");
#pragma unroll
    for (int i = 0; i < 14; ++i) *(u4v*)(dsm + L_W1 + w * (2 * 7168) + i * 1024 + lane * 16) = fr[i];
  }
  STAMP(2, 6);
  {
    f2* zs = (f2*)(dsm + L_ZS);
    const int rp = tid & 3, k = tid >> 2;
    const int k2 = (k == 0) ? 128 : 256 - k;
    u4v xr;
    const unsigned* xrow = (const unsigned*)(x + (size_t)(row0 + w) * D_);
#pragma unroll
    for (int j = 0; j < 4; ++j) xr[j] = xrow[lane + 64 * j];
    const u4v gav = *(const u4v*)(ws + OFF_MODC + lane * 16);
    const u4v sbv = *(const u4v*)(ws + OFF_MODC + 1024 + lane * 16);
    const u4v cbv = *(const u4v*)(ws + OFF_MODC + 2048 + lane * 16);
    if (w != 0) pf(20, 4);
    f2 wq = twid((float)k * (1.0f / 512.0f));
    PIN2(wq);
    const int pa = zpos(k), pb = zpos(k2);
    if (w == 0) wait_cols(rs, (int)OFF_FLG + (bid & 15) * 1024 + lane * 16);
    asm volatile("s_barrier" ::: "memory");
    STAMP(3, 0);
    const f4 ya = __builtin_bit_cast(f4, LD128(OFF_ZT + (k * S_ + row0 + 2 * rp) * 8));
    const f4 yb = __builtin_bit_cast(f4, LD128(OFF_ZT + (k2 * S_ + row0 + 2 * rp) * 8));
    if (w == 0) pf(20, 4);
#pragma unroll
    for (int h = 0; h < 2; ++h) {
      f2 a = f2{ya[2 * h], ya[2 * h + 1]}, b = f2{yb[2 * h], yb[2 * h + 1]};
      f2* zr = zs + (2 * rp + h) * ZRS;
      f2 Ze = f2{a.x + b.x, a.y - b.y};
      f2 Dm = f2{a.x - b.x, a.y + b.y};
      f2 Zo = cmulc(Dm, wq);
      f2 za = f2{Ze.x - Zo.y, Ze.y + Zo.x};
      f2 zb = f2{Ze.x + Zo.y, -Ze.y + Zo.x};
      if (k == 0) {
        za = f2{a.x + a.y, a.x - a.y};
        zb = f2{2.f * b.x, -2.f * b.y};
      }
      zr[pa] = za;
      zr[pb] = zb;
    }
    STAMP(3, 1);
    pf(24, 2);
    __syncthreads();
    f2 v[4];
    {
      const f2* buf = zs + w * ZRS + lane + (lane >> 4);
#pragma unroll
      for (int r4 = 0; r4 < 4; ++r4) v[r4] = buf[68 * r4];
    }
    fft256_inv(v, lane, tw, [&](int i) __attribute__((always_inline)) { if (i < 2) pf(26 + 2 * i, 2); });
    STAMP(3, 2);
    float s = 0.f;
    u4v x1p;
#pragma unroll
    for (int j = 0; j < 4; ++j) {
      f2 t = f2{bf_lo(xr[j]), bf_hi(xr[j])} + bfr2(f2{bf_lo(gav[j]), bf_hi(gav[j])} * bfr2(v[j]));
      x1p[j] = pack_bf(t.x, t.y);
      v[j] = f2{bf_lo(x1p[j]), bf_hi(x1p[j])};
      s += v[j].x + v[j].y;
    }
    ST128(x1p, OFF_X1 + ((row0 + w) * 64 + lane) * 16);
    s = wave_sum(s);
    const float meanf = s * (1.0f / D_);
    float q = 0.f;
#pragma unroll
    for (int j = 0; j < 4; ++j) {
      f2 d = v[j] - meanf;
      q += d.x * d.x + d.y * d.y;
    }
    q = wave_sum(q);
    const float mean = bfr(meanf);
    const float den = bfr(sqrtf(bfr(bfr(q * (1.0f / D_)) + 1e-5f)));
    const float rden = 1.0f / den;
    u4v hp;
#pragma unroll
    for (int j = 0; j < 4; ++j) {
      f2 t = __builtin_elementwise_fma(v[j] - mean, f2{bf_lo(cbv[j]), bf_hi(cbv[j])} * rden,
                                       f2{bf_lo(sbv[j]), bf_hi(sbv[j])});
      hp[j] = pack_bf(t.x, t.y);
    }
    *(u4v*)(dsm + L_ST + (lane * 9 + w) * 16) = hp;
    __syncthreads();
    {
      const int ch = tid >> 3, r = tid & 7;
      u4v o = *(const u4v*)(dsm + L_ST + (ch * 9 + r) * 16);
      const int mt = row0 >> 4, r16 = (row0 & 8) + r;
      ST128(o, OFF_H2F + ((mt * 16 + (ch >> 2)) * 64 + (ch & 3) * 16 + r16) * 16);
    }
  }
  STAMP(3, 3);
  DRAIN();
  __syncthreads();
  if (tid == 0) __hip_atomic_store(myA, 1u, __ATOMIC_RELAXED, __HIP_MEMORY_SCOPE_AGENT);
  {
    const int ch = tid >> 3, r = tid & 7;
    u4v t = LD128(OFF_H2F + (((row0 >> 4) * 16 + (ch >> 2)) * 64 + (ch & 3) * 16 + (row0 & 8) + r) * 16);
    asm volatile("" ::"v"(t));
  }
  STAMP(3, 4);
  pf(30, 4);
  wait_pair(depA);
  STAMP(3, 5);
  {
    u4v B[16];
#pragma unroll
    for (int ks = 0; ks < 16; ++ks) B[ks] = LD128(OFF_H2F + (((8 * p + w) * 16 + ks) * 64 + lane) * 16);
    f4 acc[7];
#pragma unroll
    for (int ft = 0; ft < 7; ++ft) acc[ft] = f4{0.f, 0.f, 0.f, 0.f};
    const char* lw = dsm + L_W1 + lane * 16;
    const int e = c >> 1, odd = c & 1;
    const int hb = (int)OFF_HF + (((8 * p + w) * 56 + 7 * e) * 64 + lane) * 16;
    u4v Af[2][7];
#pragma unroll
    for (int j = 0; j < 7; ++j) Af[0][j] = *(const u4v*)(lw + (g1_ks(0, j) * 7 + g1_ft(0, j)) * 1024);
#pragma unroll
    for (int g = 0; g < 17; ++g) {
#pragma unroll
      for (int j = 0; j < 7; ++j) {
        if (j < g1_n(g)) acc[g1_ft(g, j)] = mfma16(Af[g & 1][j], B[g1_ks(g, j)], acc[g1_ft(g, j)]);
        if (g + 1 < 17 && j < g1_n(g + 1))
          Af[(g + 1) & 1][j] = *(const u4v*)(lw + (g1_ks(g + 1, j) * 7 + g1_ft(g + 1, j)) * 1024);
        SCHED();
      }
      if (g >= 2 && g < 12) pf_load(32 + g);
      if (g >= 14) {
        const int i = g - 14;
        f4 a = acc[2 * i], b = acc[2 * i + 1];
        u4v o = u4v{silu_pk(a[0], a[1]), silu_pk(a[2], a[3]), silu_pk(b[0], b[1]), silu_pk(b[2], b[3])};
        ST128(o, hb + (odd * 3 + i) * 1024);
      }
      SCHED();
      if (g == 12) STAMP(3, 6);
    }
    f4 a = acc[6];
    u2v o = u2v{silu_pk(a[0], a[1]), silu_pk(a[2], a[3])};
    ST64(o, hb + 6 * 1024 + odd * 8);
  }
  STAMP(3, 7);
  DRAIN();
  __syncthreads();
  if (tid == 0) __hip_atomic_store(myB, 1u, __ATOMIC_RELAXED, __HIP_MEMORY_SCOPE_AGENT);
  {
    const int hb = (int)OFF_HF + (((8 * p + w) * 56 + 7 * (c >> 1) + 3 * (c & 1)) * 64 + lane) * 16;
    u4v t0 = LD128(hb), t1 = LD128(hb + 1024), t2 = LD128(hb + 2048);
    asm volatile("" ::"v"(t0), "v"(t1), "v"(t2));
  }
  wait_pair(depB);
  STAMP(3, 8);
  {
    const int Bb = (int)OFF_HF + ((mt0 * 56 + 7 * w) * 64 + lane) * 16;
    u4v B[7][4];
#pragma unroll
    for (int ks = 0; ks < 4; ++ks)
#pragma unroll
      for (int mt = 0; mt < 4; ++mt) B[ks][mt] = LD128(Bb + (mt * 56 + ks) * 1024);
    f4 acc[4][4];
#pragma unroll
    for (int i = 0; i < 4; ++i)
#pragma unroll
      for (int j = 0; j < 4; ++j) acc[i][j] = f4{0.f, 0.f, 0.f, 0.f};
#pragma unroll
    for (int ks = 0; ks < 7; ++ks) {
#pragma unroll
      for (int nt = 0; nt < 4; ++nt)
#pragma unroll
        for (int mt = 0; mt < 4; ++mt) acc[nt][mt] = mfma16(A2[ks * 4 + nt], B[ks][mt], acc[nt][mt]);
      if (ks + 4 < 7) {
#pragma unroll
        for (int mt = 0; mt < 4; ++mt) B[ks + 4][mt] = LD128(Bb + (mt * 56 + ks + 4) * 1024);
      }
      SCHED();
    }
    STAMP(3, 9);
    const int g = lane >> 4, mr = lane & 15;
    const int fm = w >> 1, P = w & 1;
    const int m = (mt0 + fm) * 16 + mr;
    const int n = nt0 * 16 + 32 * P + 8 * g;
    unsigned xv[4];
#pragma unroll
    for (int i = 0; i < 4; ++i)
      xv[i] = __builtin_amdgcn_raw_buffer_load_b32(
          rs, (int)OFF_X1 + (m * 64 + (((n >> 1) + i) & 63)) * 16 + (n >> 7) * 4, 0, AUX_SC1);
    const f4 g0 = *(const f4*)(modf_ + 2560 + n), g1 = *(const f4*)(modf_ + 2560 + n + 4);
    f4* red = (f4*)dsm;
#pragma unroll
    for (int nt = 0; nt < 4; ++nt)
#pragma unroll
      for (int mt = 0; mt < 4; ++mt) red[(w * 16 + nt * 4 + mt) * 64 + lane] = acc[nt][mt];
    __syncthreads();
    STAMP(3, 10);
    f4 s0 = f4{0.f, 0.f, 0.f, 0.f}, s1 = s0;
#pragma unroll
    for (int ww = 0; ww < 8; ++ww) {
      s0 += red[(ww * 16 + (2 * P) * 4 + fm) * 64 + lane];
      s1 += red[(ww * 16 + (2 * P + 1) * 4 + fm) * 64 + lane];
    }
    u4v o;
    {
      f2 t0 = bfr2(f2{g0[0], g0[1]} * bfr2(f2{s0[0], s0[1]})), t1 = bfr2(f2{g0[2], g0[3]} * bfr2(f2{s0[2], s0[3]}));
      f2 t2 = bfr2(f2{g1[0], g1[1]} * bfr2(f2{s1[0], s1[1]})), t3 = bfr2(f2{g1[2], g1[3]} * bfr2(f2{s1[2], s1[3]}));
      o[0] = pack_bf(bf_lo(xv[0]) + t0.x, bf_hi(xv[0]) + t0.y);
      o[1] = pack_bf(bf_lo(xv[1]) + t1.x, bf_hi(xv[1]) + t1.y);
      o[2] = pack_bf(bf_lo(xv[2]) + t2.x, bf_hi(xv[2]) + t2.y);
      o[3] = pack_bf(bf_lo(xv[3]) + t3.x, bf_hi(xv[3]) + t3.y);
    }
    *(u4v*)(out + (size_t)m * D_ + n) = o;
  }
  STAMP(3, 11);
}

extern "C" void kernel_launch(void* const* d_in, const int* in_sizes, int n_in, void* d_out, int out_size, void* d_ws,
                              size_t ws_size, hipStream_t stream) {
  const u16* x = (const u16*)d_in[0];
  const u16* wr = (const u16*)d_in[1];
  const u16* wi = (const u16*)d_in[2];
  const u16* w1 = (const u16*)d_in[4];
  const u16* w2 = (const u16*)d_in[5];
  const u16* c = (const u16*)d_in[6];
  const u16* wmod = (const u16*)d_in[7];
  char* ws = (char*)d_ws;
  u16* out = (u16*)d_out;
  (void)hipFuncSetAttribute((const void*)k23_fused, hipFuncAttributeMaxDynamicSharedMemorySize, L_TOTAL);
  k0_mod<<<K0_GEMV + K0_WARM, 512, 0, stream>>>(c, wmod, x, wr, wi, w1, w2, ws);
  k1_rowfft<<<256 + K1_WT + K1_W2, 512, 0, stream>>>(x, wr, wi, w2, ws);
  k23_fused<<<256, 512, L_TOTAL, stream>>>(x, w1, ws, out);
}
```

```cpp
#include <hip/hip_runtime.h>
#include <hip/hip_bf16.h>
#include <stdint.h>

typedef float f2 __attribute__((ext_vector_type(2)));
typedef float f4 __attribute__((ext_vector_type(4)));
typedef unsigned u2v __attribute__((ext_vector_type(2)));
typedef unsigned u4v __attribute__((ext_vector_type(4)));
typedef short bf16x8 __attribute__((ext_vector_type(8)));
typedef unsigned short u16;

constexpr int S_ = 2048, D_ = 512, F_ = 1792, DF_ = 257;
constexpr int NSPLIT = 4;

constexpr size_t OFF_CNT = 0;
#define FSPACE 16
#define FREP 16
constexpr int FSP = FSPACE, NREP = FREP;
constexpr size_t FTAB = (size_t)NREP * 256 * FSP;
constexpr size_t OFF_FLG = 16384;
constexpr size_t OFF_FLR = OFF_FLG + FTAB;
constexpr size_t OFF_FLM = OFF_FLR + FTAB;
constexpr size_t FLAG_BYTES = OFF_FLM + FTAB;
constexpr size_t OFF_MODP = 900 << 10;
constexpr size_t OFF_MODF = OFF_MODP + (size_t)NSPLIT * 3072 * 4;
constexpr size_t OFF_MODC = OFF_MODF + 3072 * 4;
constexpr size_t OFF_WT = 1 << 20;
constexpr size_t OFF_XT = 4 << 20;
constexpr size_t OFF_ZT = 28 << 20;
constexpr size_t OFF_H2F = 12 << 20;
constexpr size_t OFF_X1 = 14 << 20;
constexpr size_t OFF_W2F = 18 << 20;
constexpr size_t OFF_HF = 20 << 20;
constexpr int WS_SPAN = 36 << 20;
#define CPITCH (2048 + 32)
constexpr int CP = CPITCH;
constexpr int WT_PITCH = 520;
constexpr float FSC = 1.0f / (2.0f * 2048.0f * 512.0f);

#define STAMP(kid, i)

__device__ __forceinline__ float bf_lo(unsigned u) { return __builtin_bit_cast(float, u << 16); }
__device__ __forceinline__ float bf_hi(unsigned u) { return __builtin_bit_cast(float, u & 0xffff0000u); }
__device__ __forceinline__ float bfr(float x) {
  return (float)(__bf16)x;
}
__device__ __forceinline__ unsigned pack_bf(float lo, float hi) {
  typedef __bf16 b2 __attribute__((ext_vector_type(2)));
  return __builtin_bit_cast(unsigned, __builtin_convertvector(f2{lo, hi}, b2));
}
__device__ __forceinline__ f2 cmul(f2 a, f2 b) {
  f2 t = a * f2{b.x, b.x};
  return __builtin_elementwise_fma(f2{a.y, a.x}, f2{-b.y, b.y}, t);
}
__device__ __forceinline__ f2 cmulc(f2 a, f2 b) {
  f2 t = a * f2{b.x, b.x};
  return __builtin_elementwise_fma(f2{a.y, a.x}, f2{b.y, -b.y}, t);
}
__device__ __forceinline__ f2 bfr2(f2 a) {
  unsigned u = pack_bf(a.x, a.y);
  asm("" : "+v"(u));
  return f2{bf_lo(u), bf_hi(u)};
}
__device__ __forceinline__ float msel(unsigned m, float a, float b) {
  unsigned ua = __builtin_bit_cast(unsigned, a), ub = __builtin_bit_cast(unsigned, b);
  return __builtin_bit_cast(float, (m & ua) | (~m & ub));
}
#define PIN2(x) asm volatile("" : "+v"(x))
constexpr int AUX_SC1 = 16;
#define WS_RSRC(ws) __builtin_amdgcn_make_buffer_rsrc((ws), 0, WS_SPAN, 0x00020000)
#define ST128(v, off) __builtin_amdgcn_raw_buffer_store_b128((v), rs, (int)(off), 0, AUX_SC1)
#define ST64(v, off) __builtin_amdgcn_raw_buffer_store_b64((v), rs, (int)(off), 0, AUX_SC1)
#define ST32(v, off) __builtin_amdgcn_raw_buffer_store_b32((v), rs, (int)(off), 0, AUX_SC1)
#define LD64(off) __builtin_amdgcn_raw_buffer_load_b64(rs, (int)(off), 0, AUX_SC1)
#define LD32(off) __builtin_amdgcn_raw_buffer_load_b32(rs, (int)(off), 0, AUX_SC1)
#define LD128(off) __builtin_amdgcn_raw_buffer_load_b128(rs, (int)(off), 0, AUX_SC1)
__device__ __forceinline__ f2 twid(float rev) { return f2{__builtin_amdgcn_cosf(rev), -__builtin_amdgcn_sinf(rev)}; }

template <int DIR>
__device__ __forceinline__ f2 rot90(f2 a) {
  return DIR < 0 ? f2{a.y, -a.x} : f2{-a.y, a.x};
}
template <int DIR>
__device__ __forceinline__ void dft4(f2& c0, f2& c1, f2& c2, f2& c3) {
  f2 d0 = c0 + c2, d2 = c0 - c2, d1 = c1 + c3, e = c1 - c3;
  c0 = d0 + d1;
  c2 = d0 - d1;
  const f2 es = f2{e.y, e.x};
  c1 = __builtin_elementwise_fma(es, DIR < 0 ? f2{1.f, -1.f} : f2{-1.f, 1.f}, d2);
  c3 = __builtin_elementwise_fma(es, DIR < 0 ? f2{-1.f, 1.f} : f2{1.f, -1.f}, d2);
}

__device__ __forceinline__ void sw32(float& a, float& b) {
  auto r = __builtin_amdgcn_permlane32_swap(__builtin_bit_cast(unsigned, a), __builtin_bit_cast(unsigned, b), false, false);
  a = __builtin_bit_cast(float, (unsigned)r[0]);
  b = __builtin_bit_cast(float, (unsigned)r[1]);
}
__device__ __forceinline__ void sw16(float& a, float& b) {
  auto r = __builtin_amdgcn_permlane16_swap(__builtin_bit_cast(unsigned, a), __builtin_bit_cast(unsigned, b), false, false);
  a = __builtin_bit_cast(float, (unsigned)r[0]);
  b = __builtin_bit_cast(float, (unsigned)r[1]);
}
template <int CTRL, int BANK>
__device__ __forceinline__ float udpp(float old, float src) {
  return __builtin_bit_cast(float, __builtin_amdgcn_update_dpp(__builtin_bit_cast(int, old), __builtin_bit_cast(int, src),
                                                               CTRL, 0xf, BANK, false));
}
template <int CTRL>
__device__ __forceinline__ float mdpp(float src) {
  return __builtin_bit_cast(float, __builtin_amdgcn_mov_dpp(__builtin_bit_cast(int, src), CTRL, 0xf, 0xf, false));
}
__device__ __forceinline__ void xp_hi1(float& v0, float& v1, float& v2, float& v3) {
  sw32(v0, v2); sw32(v1, v3); sw16(v0, v1); sw16(v2, v3);
}
__device__ __forceinline__ void xp_mid1(float& v0, float& v1, float& v2, float& v3) {
  float a0 = udpp<0x128, 0xC>(v0, v2), a2 = udpp<0x128, 0x3>(v2, v0);
  float a1 = udpp<0x128, 0xC>(v1, v3), a3 = udpp<0x128, 0x3>(v3, v1);
  v0 = udpp<0x124, 0xA>(a0, a1); v1 = udpp<0x12C, 0x5>(a1, a0);
  v2 = udpp<0x124, 0xA>(a2, a3); v3 = udpp<0x12C, 0x5>(a3, a2);
}
__device__ __forceinline__ void xp_lo1(float& v0, float& v1, float& v2, float& v3, unsigned b1, unsigned b0) {
  float s0 = mdpp<0x4E>(msel(b1, v0, v2)), s1 = mdpp<0x4E>(msel(b1, v1, v3));
  float a0 = msel(b1, s0, v0), a2 = msel(b1, v2, s0), a1 = msel(b1, s1, v1), a3 = msel(b1, v3, s1);
  float t0 = mdpp<0xB1>(msel(b0, a0, a1)), t2 = mdpp<0xB1>(msel(b0, a2, a3));
  v0 = msel(b0, t0, a0); v1 = msel(b0, a1, t0); v2 = msel(b0, t2, a2); v3 = msel(b0, a3, t2);
}
#define XP_CPLX(NAME, FN, ...)                                        \
  {                                                                   \
    float x0 = v[0].x, x1 = v[1].x, x2 = v[2].x, x3 = v[3].x;         \
    float y0 = v[0].y, y1 = v[1].y, y2 = v[2].y, y3 = v[3].y;         \
    FN(x0, x1, x2, x3, ##__VA_ARGS__);                                \
    FN(y0, y1, y2, y3, ##__VA_ARGS__);                                \
    v[0] = f2{x0, y0}; v[1] = f2{x1, y1}; v[2] = f2{x2, y2}; v[3] = f2{x3, y3}; \
  }
__device__ __forceinline__ void xp_hi(f2 (&v)[4]) XP_CPLX(hi, xp_hi1)
__device__ __forceinline__ void xp_mid(f2 (&v)[4]) XP_CPLX(mid, xp_mid1)
__device__ __forceinline__ void xp_lo(f2 (&v)[4], unsigned b1, unsigned b0) XP_CPLX(lo, xp_lo1, b1, b0)

struct Tw256 {
  f2 a[3], b[3], c[3];
  unsigned m1, m0;
};
__device__ __forceinline__ void make_tw256(int lane, Tw256& t) {
#pragma unroll
  for (int r = 1; r < 4; ++r) {
    t.a[r - 1] = twid((float)(lane * r) * (1.0f / 256.0f));
    t.b[r - 1] = twid((float)((lane & 15) * r) * (1.0f / 64.0f));
    t.c[r - 1] = twid((float)((lane & 3) * r) * (1.0f / 16.0f));
  }
  t.m1 = 0u - (unsigned)((lane >> 1) & 1);
  t.m0 = 0u - (unsigned)(lane & 1);
#pragma unroll
  for (int r = 0; r < 3; ++r) {
    PIN2(t.a[r]);
    PIN2(t.b[r]);
    PIN2(t.c[r]);
  }
  PIN2(t.m1);
  PIN2(t.m0);
}
struct NoHook {
  __device__ __forceinline__ void operator()(int) const {}
};
template <class H>
__device__ __forceinline__ void fft256_fwd(f2 (&v)[4], int lane, const Tw256& t, const H& hook) {
  const unsigned b1 = t.m1, b0 = t.m0;
  dft4<-1>(v[0], v[1], v[2], v[3]);
#pragma unroll
  for (int r = 1; r < 4; ++r) v[r] = cmul(v[r], t.a[r - 1]);
  xp_hi(v);
  hook(0);
  dft4<-1>(v[0], v[1], v[2], v[3]);
#pragma unroll
  for (int r = 1; r < 4; ++r) v[r] = cmul(v[r], t.b[r - 1]);
  xp_mid(v);
  hook(1);
  dft4<-1>(v[0], v[1], v[2], v[3]);
#pragma unroll
  for (int r = 1; r < 4; ++r) v[r] = cmul(v[r], t.c[r - 1]);
  xp_lo(v, b1, b0);
  hook(2);
  dft4<-1>(v[0], v[1], v[2], v[3]);
}
template <class H>
__device__ __forceinline__ void fft256_inv(f2 (&v)[4], int lane, const Tw256& t, const H& hook) {
  const unsigned b1 = t.m1, b0 = t.m0;
  dft4<1>(v[0], v[1], v[2], v[3]);
  xp_lo(v, b1, b0);
  hook(0);
#pragma unroll
  for (int r = 1; r < 4; ++r) v[r] = cmulc(v[r], t.c[r - 1]);
  dft4<1>(v[0], v[1], v[2], v[3]);
  xp_mid(v);
  hook(1);
#pragma unroll
  for (int r = 1; r < 4; ++r) v[r] = cmulc(v[r], t.b[r - 1]);
  dft4<1>(v[0], v[1], v[2], v[3]);
  xp_hi(v);
  hook(2);
#pragma unroll
  for (int r = 1; r < 4; ++r) v[r] = cmulc(v[r], t.a[r - 1]);
  dft4<1>(v[0], v[1], v[2], v[3]);
}

__device__ __forceinline__ float wave_sum(float x) {
  x += mdpp<0xB1>(x);
  x += mdpp<0x4E>(x);
  x += mdpp<0x141>(x);
  x += mdpp<0x140>(x);
  float r0 = __builtin_bit_cast(float, __builtin_amdgcn_readlane(__builtin_bit_cast(int, x), 0));
  float r1 = __builtin_bit_cast(float, __builtin_amdgcn_readlane(__builtin_bit_cast(int, x), 16));
  float r2 = __builtin_bit_cast(float, __builtin_amdgcn_readlane(__builtin_bit_cast(int, x), 32));
  float r3 = __builtin_bit_cast(float, __builtin_amdgcn_readlane(__builtin_bit_cast(int, x), 48));
  return (r0 + r1) + (r2 + r3);
}

__device__ __forceinline__ int zpos(int k) { return 17 * (k & 3) + 4 * ((k >> 2) & 3) + ((k >> 4) & 3) + 68 * (k >> 6); }
constexpr int ZRS = 276;

__global__ __launch_bounds__(512) void kz(char* __restrict__ ws) {
  ((u4v*)ws)[blockIdx.x * 512 + threadIdx.x] = u4v{0u, 0u, 0u, 0u};
}

__device__ __forceinline__ f4 mfma16(u4v a, u4v b, f4 c) {
  return __builtin_amdgcn_mfma_f32_16x16x32_bf16(__builtin_bit_cast(bf16x8, a), __builtin_bit_cast(bf16x8, b), c, 0, 0,
                                                  0);
}
__device__ __forceinline__ f2 silu2(float a, float b) {
  f2 h = f2{a, b};
  f2 t = h * -1.44269504088896340736f;
  f2 d = f2{__builtin_amdgcn_exp2f(t.x), __builtin_amdgcn_exp2f(t.y)} + 1.0f;
  return h * f2{__builtin_amdgcn_rcpf(d.x), __builtin_amdgcn_rcpf(d.y)};
}
__device__ __forceinline__ unsigned silu_pk(float a, float b) {
  f2 r = silu2(a, b);
  return pack_bf(r.x, r.y);
}
typedef __attribute__((address_space(1))) unsigned gu32;
#define DRAIN() asm volatile("s_waitcnt vmcnt(0)" ::: "memory")
typedef __attribute__((address_space(1))) unsigned long long gu64;
constexpr unsigned SPIN_CAP = 1u << 18;
__device__ __forceinline__ void wait_pair(gu64* flags) {
#pragma nounroll
  for (unsigned spin = 0; spin < SPIN_CAP; ++spin) {
    if (__hip_atomic_load(flags, __ATOMIC_RELAXED, __HIP_MEMORY_SCOPE_AGENT) == 0x0000000100000001ull) break;
    __builtin_amdgcn_s_sleep(1);
  }
  __builtin_amdgcn_fence(__ATOMIC_ACQUIRE, "wavefront");
  asm volatile("" ::: "memory");
}

#define PSLEEP 2
__device__ __forceinline__ void wait_cols(gu32* flags) {
#pragma nounroll
  for (unsigned spin = 0; spin < SPIN_CAP; ++spin) {
    unsigned f = 1u;
#pragma unroll
    for (int j = 0; j < 4; ++j)
      f &= __hip_atomic_load(flags + j * 64 * (FSP / 4), __ATOMIC_RELAXED, __HIP_MEMORY_SCOPE_AGENT);
    if (__builtin_amdgcn_ballot_w64(f != 1u) == 0ull) break;
    __builtin_amdgcn_s_sleep(PSLEEP);
  }
}

__device__ __forceinline__ int wt_off(int k, int t) {
  const int prod = 2 * ((t >> 6) + 8 * ((t >> 4) & 3) + 32 * ((t >> 2) & 3)) + ((t >> 1) & 1);
  return (int)OFF_WT + (prod * WT_PITCH + 2 * k + (t & 1)) * 16;
}

constexpr int L_W1 = 0;
constexpr int L_T = 112 * 1024;
constexpr int L_ZS = 112 * 1024;
constexpr int L_ST = L_ZS + 8 * ZRS * 8;
constexpr int L_TOTAL = 160 * 1024;
__device__ __forceinline__ constexpr int g1_n(int g) { return g < 13 ? 7 : (g < 16 ? 6 : 3); }
__device__ __forceinline__ constexpr int g1_ft(int g, int j) { return g < 13 ? j : (g < 16 ? 2 * (g - 13) + (j & 1) : 6); }
__device__ __forceinline__ constexpr int g1_ks(int g, int j) { return g < 13 ? g : (g < 16 ? 13 + (j >> 1) : 13 + j); }
#define SCHED() __builtin_amdgcn_sched_barrier(0)
typedef short v4i16 __attribute__((ext_vector_type(4)));
__device__ __forceinline__ u4v w1frag(const char* lw, int ft, int ks) {
  typedef __attribute__((address_space(3))) v4i16* lp;
  const u2v a = __builtin_bit_cast(u2v, __builtin_amdgcn_ds_read_tr16_b64_v4i16((lp)(lw + (2 * ks) * 3584 + ft * 32)));
  const u2v b = __builtin_bit_cast(u2v, __builtin_amdgcn_ds_read_tr16_b64_v4i16((lp)(lw + (2 * ks + 1) * 3584 + ft * 32)));
  return u4v{a[0], a[1], b[0], b[1]};
}
constexpr int L_MODS = L_ZS + 8 * ZRS * 8;
constexpr int L_WS = L_MODS + 4096;
constexpr int L_W2S = L_WS + 2 * 8 * 544;
constexpr int L_RED = L_W2S + 32 * 136 * 2;
static_assert(L_RED + 2048 <= L_TOTAL, "LDS map");
__global__ __launch_bounds__(512, 2) void kmega(const u16* __restrict__ x, const u16* __restrict__ wr,
                                                const u16* __restrict__ wi, const u16* __restrict__ w1,
                                                const u16* __restrict__ w2, const u16* __restrict__ cvec,
                                                const u16* __restrict__ wmod, char* ws, u16* __restrict__ out) {
  extern __shared__ __attribute__((aligned(16))) char dsm[];
  const int tid = threadIdx.x, lane = tid & 63;
  const int w = __builtin_amdgcn_readfirstlane(tid >> 6);
  const int bid = blockIdx.x;
  const int p = (bid & 7) * 2 + (bid >> 7), c = (bid >> 3) & 15;
  const int row0 = 128 * p + 8 * c;
  STAMP(1, 0);
  const auto rs = WS_RSRC(ws);
  gu32* myA = (gu32*)(ws + OFF_CNT) + (p * 8 + (c >> 1)) * 16 + (c & 1);
  gu32* myB = myA + 128 * 16;
  gu64* depA = (gu64*)(ws + OFF_CNT) + (p * 8 + w) * 8;
  gu64* depB = depA + 128 * 8;
  const int mt0 = 8 * p + 4 * (c >> 3), nt0 = 4 * (c & 7);
  const int l56 = lane < 56 ? lane : lane - 56;
  const int r4_ = l56 / 14, ch_ = l56 - 14 * r4_;
  const char* w1g = (const char*)w1 + (size_t)(4 * w + r4_) * (F_ * 2) + c * 224 + ch_ * 16;
  char* w1l = dsm + L_W1 + ((w >> 1) * 32 + (2 * (w & 1) + (r4_ >> 1)) * 4 + (r4_ & 1)) * 224 + ch_ * 16;
  const int Ab = (int)OFF_W2F + ((nt0 * 56 + 7 * w) * 64 + lane) * 16;
  u4v S1[16];
  u4v A2[28];
  auto pf_load = [&](int i) __attribute__((always_inline)) {
    if (i < 16) S1[i] = *(const u4v*)(w1g + (size_t)i * (32 * F_ * 2));
    else if (i < 44) A2[i - 16] = LD128(Ab + (((i - 16) & 3) * 56 + ((i - 16) >> 2)) * 1024);
  };
  auto pf_put = [&](int i) __attribute__((always_inline)) {
    *(u4v*)(w1l + ((i & 3) * 128 + (i >> 3) * 16 + ((i >> 2) & 1) * 2) * 224) = S1[i];
  };
  auto pf = [&](int i0, int n) __attribute__((always_inline)) {
    SCHED();
#pragma unroll
    for (int i = 0; i < n; ++i) pf_load(i0 + i);
    SCHED();
  };
  Tw256 tw;
  {
    f2* zs = (f2*)(dsm + L_ZS);
    float* mods = (float*)(dsm + L_MODS);
    const int r0 = bid * 8;
    const int task = bid < 48 * NSPLIT ? bid : bid - 48 * NSPLIT;
    const int cgp = task % 48, ksp = task / 48;
    const int cg = tid & 7, gr = tid >> 3;
    const int rw = ksp * 128 + gr;
    const u4v wm0 = *(const u4v*)(wmod + (size_t)rw * 3072 + cgp * 64 + cg * 8);
    const u4v wm1 = *(const u4v*)(wmod + (size_t)(rw + 64) * 3072 + cgp * 64 + cg * 8);
    const unsigned c0 = cvec[rw], c1 = cvec[rw + 64];
    unsigned xr[4];
    const unsigned* xrow = (const unsigned*)(x + (size_t)(r0 + w) * D_);
#pragma unroll
    for (int j = 0; j < 4; ++j) xr[j] = xrow[lane + 64 * j];
    const int wb = bid >> 1, wh = bid & 1;
    u4v wra, wia;
    {
      const int t = tid < 272 ? tid : 0;
      const int i = t / 34, ch = t - 34 * i;
      const int u = wb + 128 * (2 * wh + (i & 1) + 4 * (i >> 1));
      constexpr int LASTCH = (S_ * DF_ * 2) / 16 - 1;
      int gch = (u * (DF_ * 2)) / 16 + ch;
      gch = gch > LASTCH ? LASTCH : gch;
      wra = *(const u4v*)((const char*)wr + (size_t)gch * 16);
      wia = *(const u4v*)((const char*)wi + (size_t)gch * 16);
    }
    const int tk = bid < 224 ? bid : bid - 224;
    u4v w2a;
    {
      const int ks2 = tk >> 2, rr = tid >> 4, gg = rr >> 3, j = rr & 7;
      const int e = ks2 / 7, i7 = ks2 - 7 * e;
      const int T0 = 14 * e + (i7 < 3 ? 2 * i7 : (i7 < 6 ? 2 * i7 + 1 : 6));
      const int T1 = i7 < 6 ? T0 + 1 : 14 * e + 13;
      const int f = (j < 4) ? (16 * T0 + 4 * gg + j) : (16 * T1 + 4 * gg + (j - 4));
      w2a = *(const u4v*)(w2 + (size_t)f * D_ + (tk & 3) * 128 + (tid & 15) * 8);
    }
    SCHED();
    make_tw256(lane, tw);
    const int rp = tid & 3, kk = tid >> 2;
    f2 wpost = twid((float)kk * (1.0f / 512.0f));
    PIN2(wpost);
    {
      float* red = (float*)(dsm + L_RED);
      const float cv0 = bf_lo(c0), cv1 = bf_lo(c1);
      const float s0 = bfr(cv0 / (1.0f + __expf(-cv0))), s1 = bfr(cv1 / (1.0f + __expf(-cv1)));
      float acc[8];
#pragma unroll
      for (int i = 0; i < 4; ++i) {
        acc[2 * i] = s0 * bf_lo(wm0[i]) + s1 * bf_lo(wm1[i]);
        acc[2 * i + 1] = s0 * bf_hi(wm0[i]) + s1 * bf_hi(wm1[i]);
      }
#pragma unroll
      for (int i = 0; i < 8; ++i) {
        acc[i] += mdpp<0x128>(acc[i]);
        acc[i] += __shfl_xor(acc[i], 16);
        acc[i] += __shfl_xor(acc[i], 32);
      }
      if (lane < 8) {
#pragma unroll
        for (int i = 0; i < 8; ++i) red[w * 64 + cg * 8 + i] = acc[i];
      }
      __syncthreads();
      if (w == 0 && bid < 48 * NSPLIT) {
        float sum = 0.f;
#pragma unroll
        for (int i = 0; i < 8; ++i) sum += red[i * 64 + lane];
        ST32(__builtin_bit_cast(unsigned, sum), OFF_MODP + (ksp * 3072 + cgp * 64 + lane) * 4);
        DRAIN();
      }
    }
    if (w == 0 && lane < NREP)
      __hip_atomic_store((gu32*)(ws + OFF_FLM + (lane * 256 + bid) * FSP), 1u, __ATOMIC_RELAXED, __HIP_MEMORY_SCOPE_AGENT);
    STAMP(1, 1);
    f2 v[4];
    float s = 0.f;
#pragma unroll
    for (int j = 0; j < 4; ++j) {
      v[j] = f2{bf_lo(xr[j]), bf_hi(xr[j])};
      s += v[j].x + v[j].y;
    }
    s = wave_sum(s);
    const float meanf = s * (1.0f / D_);
    float q = 0.f;
#pragma unroll
    for (int j = 0; j < 4; ++j) {
      f2 d = v[j] - meanf;
      q += d.x * d.x + d.y * d.y;
    }
    q = wave_sum(q);
    const float mean = bfr(meanf);
    const float den = bfr(sqrtf(bfr(bfr(q * (1.0f / D_)) + 1e-5f)));
    const float rden = 1.0f / den;
    if (tid < 272) {
      *(u4v*)(dsm + L_WS + tid * 16) = wra;
      *(u4v*)(dsm + L_WS + 8 * 544 + tid * 16) = wia;
    }
    *(u4v*)(dsm + L_W2S + ((tid >> 4) * 136 + (tid & 15) * 8) * 2) = w2a;
    __syncthreads();
    {
      for (int it = tid; it < DF_ * 2; it += 512) {
        const int k = it >> 1, rl = it & 1;
        u4v o;
#pragma unroll
        for (int r4 = 0; r4 < 4; ++r4) {
          const int u = wb + 128 * (2 * wh + rl + 4 * r4);
          const int off = (rl + 2 * r4) * 544 + ((u * (DF_ * 2)) & 15) + 2 * k;
          o[r4] = (unsigned)*(const u16*)(dsm + L_WS + off) | ((unsigned)*(const u16*)(dsm + L_WS + 8 * 544 + off) << 16);
        }
        ST128(o, OFF_WT + (bid * WT_PITCH + it) * 16);
      }
    }
    {
      const u16* t = (const u16*)(dsm + L_W2S);
      const int g = lane >> 4, r = lane & 15;
      const int nl = 32 * (w >> 1) + 8 * (r >> 2) + 4 * (w & 1) + (r & 3);
      u4v o;
#pragma unroll
      for (int jj = 0; jj < 4; ++jj)
        o[jj] = (unsigned)t[(g * 8 + 2 * jj) * 136 + nl] | ((unsigned)t[(g * 8 + 2 * jj + 1) * 136 + nl] << 16);
      if (bid < 224) ST128(o, OFF_W2F + ((((tk & 3) * 8 + w) * 56 + (tk >> 2)) * 64 + lane) * 16);
    }
    STAMP(1, 2);
    if (w == 0) wait_cols((gu32*)(ws + OFF_FLM + ((bid % NREP) * 256 + lane) * FSP));
    asm volatile("s_barrier" ::: "memory");
    __builtin_amdgcn_fence(__ATOMIC_ACQUIRE, "wavefront");
    STAMP(1, 3);
    {
      f2 sm = __builtin_bit_cast(f2, LD64(OFF_MODP + tid * 8));
#pragma unroll
      for (int k = 1; k < NSPLIT; ++k) sm += __builtin_bit_cast(f2, LD64(OFF_MODP + (k * 3072 + tid * 2) * 4));
      f2 fin;
      const int idx = bid * 12 + (tid < 6 ? tid : 0) * 2;
      fin = __builtin_bit_cast(f2, LD64(OFF_MODP + idx * 4));
#pragma unroll
      for (int k = 1; k < NSPLIT; ++k) fin += __builtin_bit_cast(f2, LD64(OFF_MODP + (k * 3072 + idx) * 4));
      sm = bfr2(sm);
      if (tid >= 256) sm = bfr2(sm + 1.0f);
      *(f2*)(mods + tid * 2) = sm;
      if (tid < 6) {
        fin = bfr2(fin);
        ST64(__builtin_bit_cast(u2v, fin), OFF_MODF + idx * 4);
        const int vec = (idx >> 9) - 2, d = idx & 511;
        if (vec >= 0 && vec < 3) {
          if (vec == 2) fin += 1.0f;
          ST32(pack_bf(fin.x, fin.y), OFF_MODC + ((vec * 64 + ((d >> 1) & 63)) * 4 + (d >> 7)) * 4);
        }
      }
    }
    __syncthreads();
    STAMP(1, 4);
    pf(0, 4);
#pragma unroll
    for (int j = 0; j < 4; ++j) {
      const int d = 2 * (lane + 64 * j);
      f2 sa = *(const f2*)(mods + d);
      f2 ca1 = *(const f2*)(mods + 512 + d);
      v[j] = bfr2(__builtin_elementwise_fma(v[j] - mean, ca1 * rden, sa));
    }
    fft256_fwd(v, lane, tw, NoHook{});
    {
      f2* buf = zs + w * ZRS + lane + (lane >> 4);
#pragma unroll
      for (int r4 = 0; r4 < 4; ++r4) buf[68 * r4] = v[r4];
    }
    STAMP(1, 5);
    __syncthreads();
    {
      const int k = kk;
      const int k2 = (k == 0) ? 128 : 256 - k;
      const int pa = zpos(k), pb = zpos(k2);
      f4 oa, ob;
#pragma unroll
      for (int h = 0; h < 2; ++h) {
        const f2* zr = zs + (2 * rp + h) * ZRS;
        f2 za = zr[pa], zb = zr[pb];
        f2 E = f2{za.x + zb.x, za.y - zb.y};
        f2 Dm = f2{za.x - zb.x, za.y + zb.y};
        f2 O = f2{Dm.y, -Dm.x};
        f2 wo = cmul(wpost, O);
        f2 ra = E + wo;
        f2 t = E - wo;
        f2 rb = f2{t.x, -t.y};
        if (k == 0) {
          ra = f2{2.f * (za.x + za.y), 2.f * (za.x - za.y)};
          rb = f2{2.f * zb.x, -2.f * zb.y};
        }
        oa[2 * h] = ra.x; oa[2 * h + 1] = ra.y;
        ob[2 * h] = rb.x; ob[2 * h + 1] = rb.y;
      }
      ST128(__builtin_bit_cast(u4v, oa), OFF_XT + (k * CP + r0 + 2 * rp) * 8);
      ST128(__builtin_bit_cast(u4v, ob), OFF_XT + (k2 * CP + r0 + 2 * rp) * 8);
    }
    STAMP(1, 6);
    DRAIN();
    __syncthreads();
    if (tid < NREP)
      __hip_atomic_store((gu32*)(ws + OFF_FLR + (tid * 256 + bid) * FSP), 1u, __ATOMIC_RELAXED, __HIP_MEMORY_SCOPE_AGENT);
    STAMP(1, 7);
  }
  {
    f2* T = (f2*)(dsm + L_T);
    f2* T2 = (f2*)(dsm + L_T + 16384);
    f2* T3 = (f2*)(dsm + L_T + 32768);
    const int k = bid;
    f2 tw1[4];
#pragma unroll
    for (int j = 0; j < 4; ++j) {
      tw1[j] = twid((float)((lane + 64 * j) * w) * (1.0f / 2048.0f));
      PIN2(tw1[j]);
    }
    if (w == 0) wait_cols((gu32*)(ws + OFF_FLR + ((bid % NREP) * 256 + lane) * FSP));
    asm volatile("s_barrier" ::: "memory");
    __builtin_amdgcn_fence(__ATOMIC_ACQUIRE, "wavefront");
    STAMP(2, 0);
    f2 v[4];
#pragma unroll
    for (int j = 0; j < 4; ++j) v[j] = __builtin_bit_cast(f2, LD64(OFF_XT + (k * CP + tid + 512 * j) * 8));
    u4v bv;
    unsigned wn[4], bn[4];
    int mirr[4];
#pragma unroll
    for (int j = 0; j < 4; ++j) PIN2(v[j]);
    pf(4, 4);
    const int hb = w >> 2, a = tid & 255;
    constexpr float R = 0.70710678118654752440f;
    dft4<-1>(v[0], v[1], v[2], v[3]);
    if (hb) {
      v[1] = f2{v[1].x + v[1].y, v[1].y - v[1].x} * R;
      v[2] = f2{v[2].y, -v[2].x};
      v[3] = f2{v[3].y - v[3].x, -v[3].x - v[3].y} * R;
    }
#pragma unroll
    for (int q = 0; q < 4; ++q) T[(hb * 4 + q) * 256 + a] = v[q];
    STAMP(2, 1);
    const u4v wv = LD128(wt_off(k, tid));
    if (k == 0) {
      bv = LD128(wt_off(256, tid));
      const int m0 = (lane >> 4) + 4 * ((lane >> 2) & 3) + 16 * (lane & 3);
#pragma unroll
      for (int r4 = 0; r4 < 4; ++r4) {
        const int un = (S_ - (w + 8 * (m0 + 64 * r4))) & (S_ - 1);
        const int mm = un >> 3;
        mirr[r4] = ((un & 7) * 64 + 16 * (mm & 3) + 4 * ((mm >> 2) & 3) + ((mm >> 4) & 3)) * 4 + (mm >> 6);
      }
      if (w != 0) {
        const u4v a4 = LD128(wt_off(0, 64 * (8 - w) + 63 - lane));
        const u4v b4 = LD128(wt_off(256, 64 * (8 - w) + 63 - lane));
#pragma unroll
        for (int r4 = 0; r4 < 4; ++r4) { wn[r4] = a4[3 - r4]; bn[r4] = b4[3 - r4]; }
      } else {
#pragma unroll
        for (int r4 = 0; r4 < 4; ++r4) {
          wn[r4] = LD32(wt_off(0, mirr[r4] >> 2) + (mirr[r4] & 3) * 4);
          bn[r4] = LD32(wt_off(256, mirr[r4] >> 2) + (mirr[r4] & 3) * 4);
        }
      }
    }
    __syncthreads();
    {
      const int q = w & 3;
      const float sg = (w < 4) ? 1.f : -1.f;
#pragma unroll
      for (int j = 0; j < 4; ++j) {
        f2 e = T[q * 256 + lane + 64 * j], o = T[(4 + q) * 256 + lane + 64 * j];
        v[j] = cmul(e + o * sg, tw1[j]);
      }
    }
    fft256_fwd(v, lane, tw, [&](int i) __attribute__((always_inline)) { if (i != 1) pf(8 + 2 * i, 4); });
    STAMP(2, 2);
    if (k != 0) {
#pragma unroll
      for (int i = 0; i < 4; ++i) {
        f2 wf = f2{bf_lo(wv[i]), bf_hi(wv[i])} * FSC;
        v[i] = cmul(v[i], wf);
      }
    } else {
#pragma unroll
      for (int r4 = 0; r4 < 4; ++r4) T3[r4 * 512 + tid] = v[r4];
      __syncthreads();
#pragma unroll
      for (int r4 = 0; r4 < 4; ++r4) {
        f2 cm = T3[(mirr[r4] & 3) * 512 + (mirr[r4] >> 2)];
        f2 h0 = f2{bf_lo(wv[r4]) + bf_lo(wn[r4]), bf_hi(wv[r4]) - bf_hi(wn[r4])};
        f2 h1 = f2{bf_lo(bv[r4]) + bf_lo(bn[r4]), bf_hi(bv[r4]) - bf_hi(bn[r4])};
        f2 wp = (h0 + h1) * (0.25f * FSC), wm = (h0 - h1) * (0.25f * FSC);
        f2 cc = f2{cm.x, -cm.y};
        v[r4] = cmul(v[r4], wp) + cmul(cc, wm);
      }
    }
    fft256_inv(v, lane, tw, [&](int i) __attribute__((always_inline)) {
      SCHED();
#pragma unroll
      for (int q = 0; q < 4; ++q) pf_put(4 * i + q);
      SCHED();
    });
#pragma unroll
    for (int j = 0; j < 4; ++j) T2[w * 256 + lane + 64 * j] = cmulc(v[j], tw1[j]);
#pragma unroll
    for (int q = 12; q < 16; ++q) pf_put(q);
    STAMP(2, 3);
    __syncthreads();
    {
      const float sg = hb ? -1.f : 1.f;
#pragma unroll
      for (int q = 0; q < 4; ++q) v[q] = T2[q * 256 + a] + T2[(q + 4) * 256 + a] * sg;
      if (hb) {
        v[1] = f2{v[1].x - v[1].y, v[1].y + v[1].x} * R;
        v[2] = f2{-v[2].y, v[2].x};
        v[3] = f2{-v[3].x - v[3].y, v[3].x - v[3].y} * R;
      }
    }
    dft4<1>(v[0], v[1], v[2], v[3]);
#pragma unroll
    for (int j = 0; j < 4; ++j) ST64(__builtin_bit_cast(u2v, v[j]), OFF_ZT + (k * CP + tid + 512 * j) * 8);
    STAMP(2, 4);
    DRAIN();
    __syncthreads();
    if (tid < NREP)
      __hip_atomic_store((gu32*)(ws + OFF_FLG + (tid * 256 + k) * FSP), 1u, __ATOMIC_RELAXED, __HIP_MEMORY_SCOPE_AGENT);
  }
  STAMP(2, 5);
  {
    const char* lw = dsm + L_W1 + (lane >> 2) * 224 + (lane & 3) * 8 + w * (2 * 7168);
    u4v fr[14];
#pragma unroll
    for (int i = 0; i < 14; ++i) fr[i] = w1frag(lw, i % 7, i / 7);
    asm volatile("" ::: "memory");
#pragma unroll
    for (int i = 0; i < 14; ++i) *(u4v*)(dsm + L_W1 + w * (2 * 7168) + i * 1024 + lane * 16) = fr[i];
  }
  STAMP(2, 6);
  {
    f2* zs = (f2*)(dsm + L_ZS);
    const int rp = tid & 3, k = tid >> 2;
    const int k2 = (k == 0) ? 128 : 256 - k;
    u4v xr;
    const unsigned* xrow = (const unsigned*)(x + (size_t)(row0 + w) * D_);
#pragma unroll
    for (int j = 0; j < 4; ++j) xr[j] = xrow[lane + 64 * j];
    const u4v gav = LD128(OFF_MODC + lane * 16);
    const u4v sbv = LD128(OFF_MODC + 1024 + lane * 16);
    const u4v cbv = LD128(OFF_MODC + 2048 + lane * 16);
    f2 wq = twid((float)k * (1.0f / 512.0f));
    PIN2(wq);
    const int pa = zpos(k), pb = zpos(k2);
    if (w == 0) wait_cols((gu32*)(ws + OFF_FLG + ((bid % NREP) * 256 + lane) * FSP));
    asm volatile("s_barrier" ::: "memory");
    __builtin_amdgcn_fence(__ATOMIC_ACQUIRE, "wavefront");
    STAMP(3, 0);
    const f4 ya = __builtin_bit_cast(f4, LD128(OFF_ZT + (k * CP + row0 + 2 * rp) * 8));
    const f4 yb = __builtin_bit_cast(f4, LD128(OFF_ZT + (k2 * CP + row0 + 2 * rp) * 8));
#pragma unroll
    for (int h = 0; h < 2; ++h) {
      f2 a = f2{ya[2 * h], ya[2 * h + 1]}, b = f2{yb[2 * h], yb[2 * h + 1]};
      f2* zr = zs + (2 * rp + h) * ZRS;
      f2 Ze = f2{a.x + b.x, a.y - b.y};
      f2 Dm = f2{a.x - b.x, a.y + b.y};
      f2 Zo = cmulc(Dm, wq);
      f2 za = f2{Ze.x - Zo.y, Ze.y + Zo.x};
      f2 zb = f2{Ze.x + Zo.y, -Ze.y + Zo.x};
      if (k == 0) {
        za = f2{a.x + a.y, a.x - a.y};
        zb = f2{2.f * b.x, -2.f * b.y};
      }
      zr[pa] = za;
      zr[pb] = zb;
    }
    STAMP(3, 1);
    pf(16, 4);
    __syncthreads();
    f2 v[4];
    {
      const f2* buf = zs + w * ZRS + lane + (lane >> 4);
#pragma unroll
      for (int r4 = 0; r4 < 4; ++r4) v[r4] = buf[68 * r4];
    }
    fft256_inv(v, lane, tw, [&](int i) __attribute__((always_inline)) { if (i == 1) pf(20, 3); });
    pf(23, 3);
    STAMP(3, 2);
    float s = 0.f;
    u4v x1p;
#pragma unroll
    for (int j = 0; j < 4; ++j) {
      f2 t = f2{bf_lo(xr[j]), bf_hi(xr[j])} + bfr2(f2{bf_lo(gav[j]), bf_hi(gav[j])} * bfr2(v[j]));
      x1p[j] = pack_bf(t.x, t.y);
      v[j] = f2{bf_lo(x1p[j]), bf_hi(x1p[j])};
      s += v[j].x + v[j].y;
    }
    ST128(x1p, OFF_X1 + ((row0 + w) * 64 + lane) * 16);
    s = wave_sum(s);
    const float meanf = s * (1.0f / D_);
    float q = 0.f;
#pragma unroll
    for (int j = 0; j < 4; ++j) {
      f2 d = v[j] - meanf;
      q += d.x * d.x + d.y * d.y;
    }
    q = wave_sum(q);
    const float mean = bfr(meanf);
    const float den = bfr(sqrtf(bfr(bfr(q * (1.0f / D_)) + 1e-5f)));
    const float rden = 1.0f / den;
    u4v hp;
#pragma unroll
    for (int j = 0; j < 4; ++j) {
      f2 t = __builtin_elementwise_fma(v[j] - mean, f2{bf_lo(cbv[j]), bf_hi(cbv[j])} * rden,
                                       f2{bf_lo(sbv[j]), bf_hi(sbv[j])});
      hp[j] = pack_bf(t.x, t.y);
    }
    *(u4v*)(dsm + L_ST + (lane * 9 + w) * 16) = hp;
    __syncthreads();
    {
      const int ch = tid >> 3, r = tid & 7;
      u4v o = *(const u4v*)(dsm + L_ST + (ch * 9 + r) * 16);
      const int mt = row0 >> 4, r16 = (row0 & 8) + r;
      ST128(o, OFF_H2F + ((mt * 16 + (ch >> 2)) * 64 + (ch & 3) * 16 + r16) * 16);
    }
  }
  STAMP(3, 3);
  DRAIN();
  __syncthreads();
  if (tid == 0) __hip_atomic_store(myA, 1u, __ATOMIC_RELAXED, __HIP_MEMORY_SCOPE_AGENT);
  {
    const int ch = tid >> 3, r = tid & 7;
    u4v t = LD128(OFF_H2F + (((row0 >> 4) * 16 + (ch >> 2)) * 64 + (ch & 3) * 16 + (row0 & 8) + r) * 16);
    asm volatile("" ::"v"(t));
  }
  STAMP(3, 4);
  wait_pair(depA);
  STAMP(3, 5);
  {
    u4v B[16];
#pragma unroll
    for (int ks = 0; ks < 16; ++ks) B[ks] = LD128(OFF_H2F + (((8 * p + w) * 16 + ks) * 64 + lane) * 16);
    f4 acc[7];
#pragma unroll
    for (int ft = 0; ft < 7; ++ft) acc[ft] = f4{0.f, 0.f, 0.f, 0.f};
    const char* lw = dsm + L_W1 + lane * 16;
    const int e = c >> 1, odd = c & 1;
    const int hb = (int)OFF_HF + (((8 * p + w) * 56 + 7 * e) * 64 + lane) * 16;
    u4v Af[2][7];
#pragma unroll
    for (int j = 0; j < 7; ++j) Af[0][j] = *(const u4v*)(lw + (g1_ks(0, j) * 7 + g1_ft(0, j)) * 1024);
#pragma unroll
    for (int g = 0; g < 17; ++g) {
#pragma unroll
      for (int j = 0; j < 7; ++j) {
        if (j < g1_n(g)) acc[g1_ft(g, j)] = mfma16(Af[g & 1][j], B[g1_ks(g, j)], acc[g1_ft(g, j)]);
        if (g + 1 < 17 && j < g1_n(g + 1))
          Af[(g + 1) & 1][j] = *(const u4v*)(lw + (g1_ks(g + 1, j) * 7 + g1_ft(g + 1, j)) * 1024);
        SCHED();
      }
      if (g >= 3 && g < 12) { pf_load(20 + 2 * g); pf_load(21 + 2 * g); }
      if (g >= 14) {
        const int i = g - 14;
        f4 a = acc[2 * i], b = acc[2 * i + 1];
        u4v o = u4v{silu_pk(a[0], a[1]), silu_pk(a[2], a[3]), silu_pk(b[0], b[1]), silu_pk(b[2], b[3])};
        ST128(o, hb + (odd * 3 + i) * 1024);
      }
      SCHED();
      if (g == 12) STAMP(3, 6);
    }
    f4 a = acc[6];
    u2v o = u2v{silu_pk(a[0], a[1]), silu_pk(a[2], a[3])};
    ST64(o, hb + 6 * 1024 + odd * 8);
  }
  STAMP(3, 7);
  DRAIN();
  __syncthreads();
  if (tid == 0) __hip_atomic_store(myB, 1u, __ATOMIC_RELAXED, __HIP_MEMORY_SCOPE_AGENT);
  {
    const int hb = (int)OFF_HF + (((8 * p + w) * 56 + 7 * (c >> 1) + 3 * (c & 1)) * 64 + lane) * 16;
    u4v t0 = LD128(hb), t1 = LD128(hb + 1024), t2 = LD128(hb + 2048);
    asm volatile("" ::"v"(t0), "v"(t1), "v"(t2));
  }
  wait_pair(depB);
  STAMP(3, 8);
  {
    const int Bb = (int)OFF_HF + ((mt0 * 56 + 7 * w) * 64 + lane) * 16;
    u4v B[7][4];
#pragma unroll
    for (int ks = 0; ks < 4; ++ks)
#pragma unroll
      for (int mt = 0; mt < 4; ++mt) B[ks][mt] = LD128(Bb + (mt * 56 + ks) * 1024);
    f4 acc[4][4];
#pragma unroll
    for (int i = 0; i < 4; ++i)
#pragma unroll
      for (int j = 0; j < 4; ++j) acc[i][j] = f4{0.f, 0.f, 0.f, 0.f};
#pragma unroll
    for (int ks = 0; ks < 7; ++ks) {
#pragma unroll
      for (int nt = 0; nt < 4; ++nt)
#pragma unroll
        for (int mt = 0; mt < 4; ++mt) acc[nt][mt] = mfma16(A2[ks * 4 + nt], B[ks][mt], acc[nt][mt]);
      if (ks + 4 < 7) {
#pragma unroll
        for (int mt = 0; mt < 4; ++mt) B[ks + 4][mt] = LD128(Bb + (mt * 56 + ks + 4) * 1024);
      }
      SCHED();
    }
    STAMP(3, 9);
    const int g = lane >> 4, mr = lane & 15;
    const int fm = w >> 1, P = w & 1;
    const int m = (mt0 + fm) * 16 + mr;
    const int n = nt0 * 16 + 32 * P + 8 * g;
    unsigned xv[4];
#pragma unroll
    for (int i = 0; i < 4; ++i)
      xv[i] = __builtin_amdgcn_raw_buffer_load_b32(
          rs, (int)OFF_X1 + (m * 64 + (((n >> 1) + i) & 63)) * 16 + (n >> 7) * 4, 0, AUX_SC1);
    const f4 g0 = __builtin_bit_cast(f4, LD128(OFF_MODF + (2560 + n) * 4));
    const f4 g1 = __builtin_bit_cast(f4, LD128(OFF_MODF + (2560 + n + 4) * 4));
    f4* red = (f4*)dsm;
#pragma unroll
    for (int nt = 0; nt < 4; ++nt)
#pragma unroll
      for (int mt = 0; mt < 4; ++mt) red[(w * 16 + nt * 4 + mt) * 64 + lane] = acc[nt][mt];
    __syncthreads();
    STAMP(3, 10);
    f4 s0 = f4{0.f, 0.f, 0.f, 0.f}, s1 = s0;
#pragma unroll
    for (int ww = 0; ww < 8; ++ww) {
      s0 += red[(ww * 16 + (2 * P) * 4 + fm) * 64 + lane];
      s1 += red[(ww * 16 + (2 * P + 1) * 4 + fm) * 64 + lane];
    }
    u4v o;
    {
      f2 t0 = bfr2(f2{g0[0], g0[1]} * bfr2(f2{s0[0], s0[1]})), t1 = bfr2(f2{g0[2], g0[3]} * bfr2(f2{s0[2], s0[3]}));
      f2 t2 = bfr2(f2{g1[0], g1[1]} * bfr2(f2{s1[0], s1[1]})), t3 = bfr2(f2{g1[2], g1[3]} * bfr2(f2{s1[2], s1[3]}));
      o[0] = pack_bf(bf_lo(xv[0]) + t0.x, bf_hi(xv[0]) + t0.y);
      o[1] = pack_bf(bf_lo(xv[1]) + t1.x, bf_hi(xv[1]) + t1.y);
      o[2] = pack_bf(bf_lo(xv[2]) + t2.x, bf_hi(xv[2]) + t2.y);
      o[3] = pack_bf(bf_lo(xv[3]) + t3.x, bf_hi(xv[3]) + t3.y);
    }
    *(u4v*)(out + (size_t)m * D_ + n) = o;
  }
  STAMP(3, 11);
}

extern "C" void kernel_launch(void* const* d_in, const int* in_sizes, int n_in, void* d_out, int out_size, void* d_ws,
                              size_t ws_size, hipStream_t stream) {
  const u16* x = (const u16*)d_in[0];
  const u16* wr = (const u16*)d_in[1];
  const u16* wi = (const u16*)d_in[2];
  const u16* w1 = (const u16*)d_in[4];
  const u16* w2 = (const u16*)d_in[5];
  const u16* c = (const u16*)d_in[6];
  const u16* wmod = (const u16*)d_in[7];
  char* ws = (char*)d_ws;
  u16* out = (u16*)d_out;
  (void)hipFuncSetAttribute((const void*)kmega, hipFuncAttributeMaxDynamicSharedMemorySize, L_TOTAL);
  kz<<<FLAG_BYTES / (512 * 16), 512, 0, stream>>>(ws);
  kmega<<<256, 512, L_TOTAL, stream>>>(x, wr, wi, w1, w2, c, wmod, ws, out);
}
```

```cpp
#include <hip/hip_runtime.h>
#include <hip/hip_bf16.h>
#include <stdint.h>

typedef float f2 __attribute__((ext_vector_type(2)));
typedef float f4 __attribute__((ext_vector_type(4)));
typedef unsigned u2v __attribute__((ext_vector_type(2)));
typedef unsigned u4v __attribute__((ext_vector_type(4)));
typedef short bf16x8 __attribute__((ext_vector_type(8)));
typedef unsigned short u16;

constexpr int S_ = 2048, D_ = 512, F_ = 1792, DF_ = 257;
constexpr int NSPLIT = 4;

constexpr size_t OFF_CNT = 0;
constexpr size_t OFF_FLG = 16384;
constexpr size_t OFF_MODP = 32768;
constexpr size_t OFF_MODF = OFF_MODP + (size_t)NSPLIT * 3072 * 4;
constexpr size_t OFF_MODC = OFF_MODF + 3072 * 4;
constexpr size_t OFF_WT = 1 << 20;
constexpr size_t OFF_XT = 4 << 20;
constexpr size_t OFF_ZT = 8 << 20;
constexpr size_t OFF_H2F = 12 << 20;
constexpr size_t OFF_X1 = 14 << 20;
constexpr size_t OFF_W2F = 18 << 20;
constexpr size_t OFF_HF = 20 << 20;
constexpr int WS_SPAN = 28 << 20;
constexpr float FSC = 1.0f / (2.0f * 2048.0f * 512.0f);

#define STAMP(kid, i)

__device__ __forceinline__ float bf_lo(unsigned u) { return __builtin_bit_cast(float, u << 16); }
__device__ __forceinline__ float bf_hi(unsigned u) { return __builtin_bit_cast(float, u & 0xffff0000u); }
__device__ __forceinline__ float bfr(float x) {
  return (float)(__bf16)x;
}
__device__ __forceinline__ unsigned pack_bf(float lo, float hi) {
  typedef __bf16 b2 __attribute__((ext_vector_type(2)));
  return __builtin_bit_cast(unsigned, __builtin_convertvector(f2{lo, hi}, b2));
}
__device__ __forceinline__ f2 cmul(f2 a, f2 b) {
  f2 t = a * f2{b.x, b.x};
  return __builtin_elementwise_fma(f2{a.y, a.x}, f2{-b.y, b.y}, t);
}
__device__ __forceinline__ f2 cmulc(f2 a, f2 b) {
  f2 t = a * f2{b.x, b.x};
  return __builtin_elementwise_fma(f2{a.y, a.x}, f2{b.y, -b.y}, t);
}
__device__ __forceinline__ f2 bfr2(f2 a) {
  unsigned u = pack_bf(a.x, a.y);
  asm("" : "+v"(u));
  return f2{bf_lo(u), bf_hi(u)};
}
__device__ __forceinline__ float msel(unsigned m, float a, float b) {
  unsigned ua = __builtin_bit_cast(unsigned, a), ub = __builtin_bit_cast(unsigned, b);
  return __builtin_bit_cast(float, (m & ua) | (~m & ub));
}
#define PIN2(x) asm volatile("" : "+v"(x))
constexpr int AUX_SC1 = 16;
#define WS_RSRC(ws) __builtin_amdgcn_make_buffer_rsrc((ws), 0, WS_SPAN, 0x00020000)
#define ST128(v, off) __builtin_amdgcn_raw_buffer_store_b128((v), rs, (int)(off), 0, AUX_SC1)
#define ST64(v, off) __builtin_amdgcn_raw_buffer_store_b64((v), rs, (int)(off), 0, AUX_SC1)
#define LD128(off) __builtin_amdgcn_raw_buffer_load_b128(rs, (int)(off), 0, AUX_SC1)
__device__ __forceinline__ f2 twid(float rev) { return f2{__builtin_amdgcn_cosf(rev), -__builtin_amdgcn_sinf(rev)}; }

template <int DIR>
__device__ __forceinline__ f2 rot90(f2 a) {
  return DIR < 0 ? f2{a.y, -a.x} : f2{-a.y, a.x};
}
template <int DIR>
__device__ __forceinline__ void dft4(f2& c0, f2& c1, f2& c2, f2& c3) {
  f2 d0 = c0 + c2, d2 = c0 - c2, d1 = c1 + c3, e = c1 - c3;
  c0 = d0 + d1;
  c2 = d0 - d1;
  const f2 es = f2{e.y, e.x};
  c1 = __builtin_elementwise_fma(es, DIR < 0 ? f2{1.f, -1.f} : f2{-1.f, 1.f}, d2);
  c3 = __builtin_elementwise_fma(es, DIR < 0 ? f2{-1.f, 1.f} : f2{1.f, -1.f}, d2);
}

__device__ __forceinline__ void sw32(float& a, float& b) {
  auto r = __builtin_amdgcn_permlane32_swap(__builtin_bit_cast(unsigned, a), __builtin_bit_cast(unsigned, b), false, false);
  a = __builtin_bit_cast(float, (unsigned)r[0]);
  b = __builtin_bit_cast(float, (unsigned)r[1]);
}
__device__ __forceinline__ void sw16(float& a, float& b) {
  auto r = __builtin_amdgcn_permlane16_swap(__builtin_bit_cast(unsigned, a), __builtin_bit_cast(unsigned, b), false, false);
  a = __builtin_bit_cast(float, (unsigned)r[0]);
  b = __builtin_bit_cast(float, (unsigned)r[1]);
}
template <int CTRL, int BANK>
__device__ __forceinline__ float udpp(float old, float src) {
  return __builtin_bit_cast(float, __builtin_amdgcn_update_dpp(__builtin_bit_cast(int, old), __builtin_bit_cast(int, src),
                                                               CTRL, 0xf, BANK, false));
}
template <int CTRL>
__device__ __forceinline__ float mdpp(float src) {
  return __builtin_bit_cast(float, __builtin_amdgcn_mov_dpp(__builtin_bit_cast(int, src), CTRL, 0xf, 0xf, false));
}
__device__ __forceinline__ void xp_hi1(float& v0, float& v1, float& v2, float& v3) {
  sw32(v0, v2); sw32(v1, v3); sw16(v0, v1); sw16(v2, v3);
}
__device__ __forceinline__ void xp_mid1(float& v0, float& v1, float& v2, float& v3) {
  float a0 = udpp<0x128, 0xC>(v0, v2), a2 = udpp<0x128, 0x3>(v2, v0);
  float a1 = udpp<0x128, 0xC>(v1, v3), a3 = udpp<0x128, 0x3>(v3, v1);
  v0 = udpp<0x124, 0xA>(a0, a1); v1 = udpp<0x12C, 0x5>(a1, a0);
  v2 = udpp<0x124, 0xA>(a2, a3); v3 = udpp<0x12C, 0x5>(a3, a2);
}
__device__ __forceinline__ void xp_lo1(float& v0, float& v1, float& v2, float& v3, unsigned b1, unsigned b0) {
  float s0 = mdpp<0x4E>(msel(b1, v0, v2)), s1 = mdpp<0x4E>(msel(b1, v1, v3));
  float a0 = msel(b1, s0, v0), a2 = msel(b1, v2, s0), a1 = msel(b1, s1, v1), a3 = msel(b1, v3, s1);
  float t0 = mdpp<0xB1>(msel(b0, a0, a1)), t2 = mdpp<0xB1>(msel(b0, a2, a3));
  v0 = msel(b0, t0, a0); v1 = msel(b0, a1, t0); v2 = msel(b0, t2, a2); v3 = msel(b0, a3, t2);
}
#define XP_CPLX(NAME, FN, ...)                                        \
  {                                                                   \
    float x0 = v[0].x, x1 = v[1].x, x2 = v[2].x, x3 = v[3].x;         \
    float y0 = v[0].y, y1 = v[1].y, y2 = v[2].y, y3 = v[3].y;         \
    FN(x0, x1, x2, x3, ##__VA_ARGS__);                                \
    FN(y0, y1, y2, y3, ##__VA_ARGS__);                                \
    v[0] = f2{x0, y0}; v[1] = f2{x1, y1}; v[2] = f2{x2, y2}; v[3] = f2{x3, y3}; \
  }
__device__ __forceinline__ void xp_hi(f2 (&v)[4]) XP_CPLX(hi, xp_hi1)
__device__ __forceinline__ void xp_mid(f2 (&v)[4]) XP_CPLX(mid, xp_mid1)
__device__ __forceinline__ void xp_lo(f2 (&v)[4], unsigned b1, unsigned b0) XP_CPLX(lo, xp_lo1, b1, b0)

struct Tw256 {
  f2 a[3], b[3], c[3];
  unsigned m1, m0;
};
__device__ __forceinline__ void make_tw256(int lane, Tw256& t) {
#pragma unroll
  for (int r = 1; r < 4; ++r) {
    t.a[r - 1] = twid((float)(lane * r) * (1.0f / 256.0f));
    t.b[r - 1] = twid((float)((lane & 15) * r) * (1.0f / 64.0f));
    t.c[r - 1] = twid((float)((lane & 3) * r) * (1.0f / 16.0f));
  }
  t.m1 = 0u - (unsigned)((lane >> 1) & 1);
  t.m0 = 0u - (unsigned)(lane & 1);
#pragma unroll
  for (int r = 0; r < 3; ++r) {
    PIN2(t.a[r]);
    PIN2(t.b[r]);
    PIN2(t.c[r]);
  }
  PIN2(t.m1);
  PIN2(t.m0);
}
struct NoHook {
  __device__ __forceinline__ void operator()(int) const {}
};
template <class H>
__device__ __forceinline__ void fft256_fwd(f2 (&v)[4], int lane, const Tw256& t, const H& hook) {
  const unsigned b1 = t.m1, b0 = t.m0;
  dft4<-1>(v[0], v[1], v[2], v[3]);
#pragma unroll
  for (int r = 1; r < 4; ++r) v[r] = cmul(v[r], t.a[r - 1]);
  xp_hi(v);
  hook(0);
  dft4<-1>(v[0], v[1], v[2], v[3]);
#pragma unroll
  for (int r = 1; r < 4; ++r) v[r] = cmul(v[r], t.b[r - 1]);
  xp_mid(v);
  hook(1);
  dft4<-1>(v[0], v[1], v[2], v[3]);
#pragma unroll
  for (int r = 1; r < 4; ++r) v[r] = cmul(v[r], t.c[r - 1]);
  xp_lo(v, b1, b0);
  hook(2);
  dft4<-1>(v[0], v[1], v[2], v[3]);
}
template <class H>
__device__ __forceinline__ void fft256_inv(f2 (&v)[4], int lane, const Tw256& t, const H& hook) {
  const unsigned b1 = t.m1, b0 = t.m0;
  dft4<1>(v[0], v[1], v[2], v[3]);
  xp_lo(v, b1, b0);
  hook(0);
#pragma unroll
  for (int r = 1; r < 4; ++r) v[r] = cmulc(v[r], t.c[r - 1]);
  dft4<1>(v[0], v[1], v[2], v[3]);
  xp_mid(v);
  hook(1);
#pragma unroll
  for (int r = 1; r < 4; ++r) v[r] = cmulc(v[r], t.b[r - 1]);
  dft4<1>(v[0], v[1], v[2], v[3]);
  xp_hi(v);
  hook(2);
#pragma unroll
  for (int r = 1; r < 4; ++r) v[r] = cmulc(v[r], t.a[r - 1]);
  dft4<1>(v[0], v[1], v[2], v[3]);
}

__device__ __forceinline__ float wave_sum(float x) {
  x += mdpp<0xB1>(x);
  x += mdpp<0x4E>(x);
  x += mdpp<0x141>(x);
  x += mdpp<0x140>(x);
  float r0 = __builtin_bit_cast(float, __builtin_amdgcn_readlane(__builtin_bit_cast(int, x), 0));
  float r1 = __builtin_bit_cast(float, __builtin_amdgcn_readlane(__builtin_bit_cast(int, x), 16));
  float r2 = __builtin_bit_cast(float, __builtin_amdgcn_readlane(__builtin_bit_cast(int, x), 32));
  float r3 = __builtin_bit_cast(float, __builtin_amdgcn_readlane(__builtin_bit_cast(int, x), 48));
  return (r0 + r1) + (r2 + r3);
}

__device__ __forceinline__ int zpos(int k) { return 17 * (k & 3) + 4 * ((k >> 2) & 3) + ((k >> 4) & 3) + 68 * (k >> 6); }
constexpr int ZRS = 276;

constexpr int K0_GEMV = 48 * NSPLIT, K0_WARM = 240;
__global__ __launch_bounds__(512) void k0_mod(const u16* __restrict__ c, const u16* __restrict__ wmod,
                                              const u16* __restrict__ x, const u16* __restrict__ wr,
                                              const u16* __restrict__ wi, const u16* __restrict__ w1,
                                              const u16* __restrict__ w2, char* __restrict__ ws) {
  __shared__ __attribute__((aligned(16))) char smem[2048];
  const int tid = threadIdx.x, row = tid >> 6, lane = tid & 63;
  const int b = blockIdx.x;
  STAMP(0, 0);
  if (b >= K0_GEMV) {
    int q = b - K0_GEMV;
    const char* src;
    if (q < 64) src = (const char*)x;
    else if ((q -= 64) < 32) src = (const char*)wr;
    else if ((q -= 32) < 32) src = (const char*)wi;
    else if ((q -= 32) < 56) src = (const char*)w2;
    else { q -= 56; src = (const char*)w1; }
    u4v t[4];
#pragma unroll
    for (int i = 0; i < 4; ++i) t[i] = *(const u4v*)(src + (size_t)q * 32768 + i * 8192 + tid * 16);
#pragma unroll
    for (int i = 0; i < 4; ++i) asm volatile("" ::"v"(t[i]));
    STAMP(0, 1);
    return;
  }
  if (b == 0) {
#pragma unroll
    for (int i = 0; i < 4; ++i) ((u4v*)(ws + OFF_CNT))[i * 512 + tid] = u4v{0u, 0u, 0u, 0u};
  }
  float* red = (float*)smem;
  const int cgp = b % 48, ks = b / 48;
  const int cg = tid & 7, r = tid >> 3;
  constexpr int RPS = 512 / NSPLIT;
  float acc[8];
#pragma unroll
  for (int i = 0; i < 8; ++i) acc[i] = 0.f;
  u4v wv[RPS / 64];
  float sc[RPS / 64];
#pragma unroll
  for (int it = 0; it < RPS / 64; ++it) {
    const int rw = ks * RPS + it * 64 + r;
    wv[it] = *(const u4v*)(wmod + (size_t)rw * 3072 + cgp * 64 + cg * 8);
    float cv = bf_lo(c[rw]);
    sc[it] = bfr(cv / (1.0f + __expf(-cv)));
  }
#pragma unroll
  for (int it = 0; it < RPS / 64; ++it) {
#pragma unroll
    for (int i = 0; i < 4; ++i) {
      acc[2 * i] += sc[it] * bf_lo(wv[it][i]);
      acc[2 * i + 1] += sc[it] * bf_hi(wv[it][i]);
    }
  }
#pragma unroll
  for (int i = 0; i < 8; ++i) {
    acc[i] += mdpp<0x128>(acc[i]);
    acc[i] += __shfl_xor(acc[i], 16);
    acc[i] += __shfl_xor(acc[i], 32);
  }
  if (lane < 8) {
#pragma unroll
    for (int i = 0; i < 8; ++i) red[row * 64 + cg * 8 + i] = acc[i];
  }
  __syncthreads();
  if (tid < 64) {
    float s = 0.f;
#pragma unroll
    for (int i = 0; i < 8; ++i) s += red[i * 64 + tid];
    ((float*)(ws + OFF_MODP))[ks * 3072 + cgp * 64 + tid] = s;
  }
  STAMP(0, 1);
}

constexpr int K1_WT = 128, K1_W2 = 112;
__global__ __launch_bounds__(512) void k1_rowfft(const u16* __restrict__ x, const u16* __restrict__ wr,
                                                 const u16* __restrict__ wi, const u16* __restrict__ w2,
                                                 char* __restrict__ ws) {
  __shared__ __attribute__((aligned(16))) char smem[8 * ZRS * 8 + 4096];
  const int tid = threadIdx.x, row = tid >> 6, lane = tid & 63;
  const auto rs = WS_RSRC(ws);
  STAMP(1, 0);
  if (blockIdx.x >= 256) {
    int b = blockIdx.x - 256;
    if (b < K1_WT) {
      char* sr = smem;
      char* si = smem + 16 * 544;
      constexpr int LASTCH = (S_ * DF_ * 2) / 16 - 1;
      for (int it = tid; it < 16 * 34; it += 512) {
        const int i = it / 34, ch = it % 34;
        int gch = ((b + 128 * i) * (DF_ * 2)) / 16 + ch;
        gch = gch > LASTCH ? LASTCH : gch;
        *(u4v*)(sr + i * 544 + ch * 16) = *(const u4v*)((const char*)wr + (size_t)gch * 16);
        *(u4v*)(si + i * 544 + ch * 16) = *(const u4v*)((const char*)wi + (size_t)gch * 16);
      }
      __syncthreads();
      const int tbase = 64 * (b & 7) + 16 * ((b >> 3) & 3) + 4 * ((b >> 5) & 3);
      for (int it = tid; it < DF_ * 4; it += 512) {
        const int k = it >> 2, r3 = it & 3;
        u4v o;
#pragma unroll
        for (int r4 = 0; r4 < 4; ++r4) {
          const int i = r3 + 4 * r4;
          const int off = i * 544 + (((b + 128 * i) * (DF_ * 2)) & 15) + 2 * k;
          o[r4] = (unsigned)*(const u16*)(sr + off) | ((unsigned)*(const u16*)(si + off) << 16);
        }
        ST128(o, OFF_WT + (k * 512 + tbase + r3) * 16);
      }
    } else {
      b -= K1_WT;
      u16* t = (u16*)smem;
      const int g = lane >> 4, r = lane & 15;
      const int ng = b & 3, kp = b >> 2;
#pragma unroll
      for (int it = 0; it < 2; ++it) {
        const int idx = it * 512 + tid;
        const int rr = idx >> 4, ch = idx & 15;
        const int ks2 = kp * 2 + (rr >> 5), gg = (rr >> 3) & 3, j = rr & 7;
        const int e = ks2 / 7, i7 = ks2 % 7;
        int T0, T1;
        if (i7 < 3) { T0 = 14 * e + 2 * i7; T1 = T0 + 1; }
        else if (i7 < 6) { T0 = 14 * e + 7 + 2 * (i7 - 3); T1 = T0 + 1; }
        else { T0 = 14 * e + 6; T1 = 14 * e + 13; }
        const int f = (j < 4) ? (16 * T0 + 4 * gg + j) : (16 * T1 + 4 * gg + (j - 4));
        *(u4v*)(t + rr * 136 + ch * 8) = *(const u4v*)(w2 + (size_t)f * D_ + ng * 128 + ch * 8);
      }
      __syncthreads();
      const int nl = 32 * (row >> 1) + 8 * (r >> 2) + 4 * (row & 1) + (r & 3);
#pragma unroll
      for (int ksl = 0; ksl < 2; ++ksl) {
        u4v o;
#pragma unroll
        for (int jj = 0; jj < 4; ++jj)
          o[jj] = (unsigned)t[(ksl * 32 + g * 8 + 2 * jj) * 136 + nl] |
                  ((unsigned)t[(ksl * 32 + g * 8 + 2 * jj + 1) * 136 + nl] << 16);
        const int nt = ng * 8 + row;
        ST128(o, OFF_W2F + ((nt * 56 + kp * 2 + ksl) * 64 + lane) * 16);
      }
    }
    STAMP(1, 5);
    return;
  }
  f2* zs = (f2*)smem;
  float* mods = (float*)(smem + 8 * ZRS * 8);
  const int row0 = blockIdx.x * 8;
  unsigned xr[4];
  const unsigned* xrow = (const unsigned*)(x + (size_t)(row0 + row) * D_);
#pragma unroll
  for (int j = 0; j < 4; ++j) xr[j] = xrow[lane + 64 * j];
  {
    const float* mp = (const float*)(ws + OFF_MODP);
    f2 s = *(const f2*)(mp + tid * 2);
#pragma unroll
    for (int k = 1; k < NSPLIT; ++k) s += *(const f2*)(mp + k * 3072 + tid * 2);
    f2 o;
#pragma unroll
    for (int i = 0; i < 2; ++i) {
      float v = bfr(s[i]);
      o[i] = (tid >= 256) ? bfr(1.0f + v) : v;
    }
    *(f2*)(mods + tid * 2) = o;
    if (blockIdx.x < 3) {
      const int idx = blockIdx.x * 1024 + tid * 2;
      f2 t = *(const f2*)(mp + idx);
#pragma unroll
      for (int k = 1; k < NSPLIT; ++k) t += *(const f2*)(mp + k * 3072 + idx);
      t = bfr2(t);
      *(f2*)((float*)(ws + OFF_MODF) + idx) = t;
      const int vec = (idx >> 9) - 2, d = idx & 511;
      if (vec >= 0 && vec < 3) {
        if (vec == 2) t += 1.0f;
        ((unsigned*)(ws + OFF_MODC))[(vec * 64 + ((d >> 1) & 63)) * 4 + (d >> 7)] = pack_bf(t.x, t.y);
      }
    }
  }
  Tw256 tw;
  make_tw256(lane, tw);
  const int rp = tid & 3, kk = tid >> 2;
  f2 wpost = twid((float)kk * (1.0f / 512.0f));
  PIN2(wpost);
  f2 v[4];
  float s = 0.f;
#pragma unroll
  for (int j = 0; j < 4; ++j) {
    v[j] = f2{bf_lo(xr[j]), bf_hi(xr[j])};
    s += v[j].x + v[j].y;
  }
  s = wave_sum(s);
  const float meanf = s * (1.0f / D_);
  float q = 0.f;
#pragma unroll
  for (int j = 0; j < 4; ++j) {
    f2 d = v[j] - meanf;
    q += d.x * d.x + d.y * d.y;
  }
  q = wave_sum(q);
  const float mean = bfr(meanf);
  const float den = bfr(sqrtf(bfr(bfr(q * (1.0f / D_)) + 1e-5f)));
  const float rden = 1.0f / den;
  STAMP(1, 1);
  __syncthreads();
  STAMP(1, 2);
#pragma unroll
  for (int j = 0; j < 4; ++j) {
    const int d = 2 * (lane + 64 * j);
    f2 sa = *(const f2*)(mods + d);
    f2 ca1 = *(const f2*)(mods + 512 + d);
    v[j] = bfr2(__builtin_elementwise_fma(v[j] - mean, ca1 * rden, sa));
  }
  fft256_fwd(v, lane, tw, NoHook{});
  {
    f2* buf = zs + row * ZRS + lane + (lane >> 4);
#pragma unroll
    for (int r4 = 0; r4 < 4; ++r4) buf[68 * r4] = v[r4];
  }
  STAMP(1, 3);
  __syncthreads();
  STAMP(1, 4);
  {
    const int k = kk;
    const int k2 = (k == 0) ? 128 : 256 - k;
    const int pa = zpos(k), pb = zpos(k2);
    f4 oa, ob;
#pragma unroll
    for (int h = 0; h < 2; ++h) {
      const f2* zr = zs + (2 * rp + h) * ZRS;
      f2 za = zr[pa], zb = zr[pb];
      f2 E = f2{za.x + zb.x, za.y - zb.y};
      f2 Dm = f2{za.x - zb.x, za.y + zb.y};
      f2 O = f2{Dm.y, -Dm.x};
      f2 wo = cmul(wpost, O);
      f2 ra = E + wo;
      f2 t = E - wo;
      f2 rb = f2{t.x, -t.y};
      if (k == 0) {
        ra = f2{2.f * (za.x + za.y), 2.f * (za.x - za.y)};
        rb = f2{2.f * zb.x, -2.f * zb.y};
      }
      oa[2 * h] = ra.x; oa[2 * h + 1] = ra.y;
      ob[2 * h] = rb.x; ob[2 * h + 1] = rb.y;
    }
    ST128(__builtin_bit_cast(u4v, oa), OFF_XT + (k * S_ + row0 + 2 * rp) * 8);
    ST128(__builtin_bit_cast(u4v, ob), OFF_XT + (k2 * S_ + row0 + 2 * rp) * 8);
  }
  STAMP(1, 5);
}

__device__ __forceinline__ f4 mfma16(u4v a, u4v b, f4 c) {
  return __builtin_amdgcn_mfma_f32_16x16x32_bf16(__builtin_bit_cast(bf16x8, a), __builtin_bit_cast(bf16x8, b), c, 0, 0,
                                                  0);
}
__device__ __forceinline__ f2 silu2(float a, float b) {
  f2 h = f2{a, b};
  f2 t = h * -1.44269504088896340736f;
  f2 d = f2{__builtin_amdgcn_exp2f(t.x), __builtin_amdgcn_exp2f(t.y)} + 1.0f;
  return h * f2{__builtin_amdgcn_rcpf(d.x), __builtin_amdgcn_rcpf(d.y)};
}
__device__ __forceinline__ unsigned silu_pk(float a, float b) {
  f2 r = silu2(a, b);
  return pack_bf(r.x, r.y);
}
typedef __attribute__((address_space(1))) unsigned gu32;
#define DRAIN() asm volatile("s_waitcnt vmcnt(0)" ::: "memory")
typedef __attribute__((address_space(1))) unsigned long long gu64;
constexpr unsigned SPIN_CAP = 1u << 18;
__device__ __forceinline__ void wait_pair(gu64* flags) {
#pragma nounroll
  for (unsigned spin = 0; spin < SPIN_CAP; ++spin) {
    if (__hip_atomic_load(flags, __ATOMIC_RELAXED, __HIP_MEMORY_SCOPE_AGENT) == 0x0000000100000001ull) break;
    __builtin_amdgcn_s_sleep(1);
  }
  __builtin_amdgcn_fence(__ATOMIC_ACQUIRE, "wavefront");
  asm volatile("" ::: "memory");
}

__device__ __forceinline__ void wait_cols(gu64* flags) {
#pragma nounroll
  for (unsigned spin = 0; spin < SPIN_CAP; ++spin) {
    const unsigned long long f0 = __hip_atomic_load(flags, __ATOMIC_RELAXED, __HIP_MEMORY_SCOPE_AGENT);
    const unsigned long long f1 = __hip_atomic_load(flags + 1, __ATOMIC_RELAXED, __HIP_MEMORY_SCOPE_AGENT);
    if (__builtin_amdgcn_ballot_w64((f0 & f1) != 0x0000000100000001ull) == 0ull) break;
    __builtin_amdgcn_s_sleep(2);
  }
}

constexpr int L_W1 = 0;
constexpr int L_T = 112 * 1024;
constexpr int L_ZS = 112 * 1024;
constexpr int L_ST = L_ZS + 8 * ZRS * 8;
constexpr int L_TOTAL = 160 * 1024;
__device__ __forceinline__ constexpr int g1_n(int g) { return g < 13 ? 7 : (g < 16 ? 6 : 3); }
__device__ __forceinline__ constexpr int g1_ft(int g, int j) { return g < 13 ? j : (g < 16 ? 2 * (g - 13) + (j & 1) : 6); }
__device__ __forceinline__ constexpr int g1_ks(int g, int j) { return g < 13 ? g : (g < 16 ? 13 + (j >> 1) : 13 + j); }
#define SCHED() __builtin_amdgcn_sched_barrier(0)
typedef short v4i16 __attribute__((ext_vector_type(4)));
__device__ __forceinline__ u4v w1frag(const char* lw, int ft, int ks) {
  typedef __attribute__((address_space(3))) v4i16* lp;
  const u2v a = __builtin_bit_cast(u2v, __builtin_amdgcn_ds_read_tr16_b64_v4i16((lp)(lw + (2 * ks) * 3584 + ft * 32)));
  const u2v b = __builtin_bit_cast(u2v, __builtin_amdgcn_ds_read_tr16_b64_v4i16((lp)(lw + (2 * ks + 1) * 3584 + ft * 32)));
  return u4v{a[0], a[1], b[0], b[1]};
}
__global__ __launch_bounds__(512, 2) void k23_fused(const u16* __restrict__ x, const u16* __restrict__ w1,
                                                    char* ws, u16* __restrict__ out) {
  extern __shared__ __attribute__((aligned(16))) char dsm[];
  const int tid = threadIdx.x, lane = tid & 63;
  const int w = __builtin_amdgcn_readfirstlane(tid >> 6);
  const int bid = blockIdx.x;
  const int p = (bid & 7) * 2 + (bid >> 7), c = (bid >> 3) & 15;
  const int row0 = 128 * p + 8 * c;
  STAMP(2, 0);
  const auto rs = WS_RSRC(ws);
  gu32* myA = (gu32*)(ws + OFF_CNT) + (p * 8 + (c >> 1)) * 16 + (c & 1);
  gu32* myB = myA + 128 * 16;
  gu64* depA = (gu64*)(ws + OFF_CNT) + (p * 8 + w) * 8;
  gu64* depB = depA + 128 * 8;
  const float* modf_ = (const float*)(ws + OFF_MODF);
  const int mt0 = 8 * p + 4 * (c >> 3), nt0 = 4 * (c & 7);
  const int l56 = lane < 56 ? lane : lane - 56;
  const int r4_ = l56 / 14, ch_ = l56 - 14 * r4_;
  const char* w1g = (const char*)w1 + (size_t)(4 * w + r4_) * (F_ * 2) + c * 224 + ch_ * 16;
  char* w1l = dsm + L_W1 + ((w >> 1) * 32 + (2 * (w & 1) + (r4_ >> 1)) * 4 + (r4_ & 1)) * 224 + ch_ * 16;
  const char* Ab = ws + OFF_W2F + ((size_t)(nt0 * 56 + 7 * w) * 64 + lane) * 16;
  u4v S1[16];
  u4v A2[28];
  auto pf_load = [&](int i) __attribute__((always_inline)) {
    if (i < 16) S1[i] = *(const u4v*)(w1g + (size_t)i * (32 * F_ * 2));
    else if (i < 44) A2[i - 16] = *(const u4v*)(Ab + (size_t)(((i - 16) & 3) * 56 + ((i - 16) >> 2)) * 1024);
  };
  auto pf_put = [&](int i) __attribute__((always_inline)) {
    *(u4v*)(w1l + ((i & 3) * 128 + (i >> 3) * 16 + ((i >> 2) & 1) * 2) * 224) = S1[i];
  };
  auto pf = [&](int i0, int n) __attribute__((always_inline)) {
    SCHED();
#pragma unroll
    for (int i = 0; i < n; ++i) pf_load(i0 + i);
    SCHED();
  };
  Tw256 tw;
  {
    f2* T = (f2*)(dsm + L_T);
    f2* T2 = (f2*)(dsm + L_T + 16384);
    f2* T3 = (f2*)(dsm + L_T + 32768);
    const int k = bid;
    const f2* XT = (const f2*)(ws + OFF_XT) + (size_t)k * S_;
    const unsigned* WT = (const unsigned*)(ws + OFF_WT);
    f2 v[4];
#pragma unroll
    for (int j = 0; j < 4; ++j) v[j] = XT[tid + 512 * j];
    const u4v wv = *(const u4v*)(WT + ((size_t)k * 512 + tid) * 4);
    u4v bv;
    unsigned wn[4], bn[4];
    int mirr[4];
    SCHED();
    make_tw256(lane, tw);
    f2 tw1[4];
#pragma unroll
    for (int j = 0; j < 4; ++j) {
      tw1[j] = twid((float)((lane + 64 * j) * w) * (1.0f / 2048.0f));
      PIN2(tw1[j]);
    }
    pf(0, 6);
    const int hb = w >> 2, a = tid & 255;
    constexpr float R = 0.70710678118654752440f;
    dft4<-1>(v[0], v[1], v[2], v[3]);
    if (hb) {
      v[1] = f2{v[1].x + v[1].y, v[1].y - v[1].x} * R;
      v[2] = f2{v[2].y, -v[2].x};
      v[3] = f2{v[3].y - v[3].x, -v[3].x - v[3].y} * R;
    }
#pragma unroll
    for (int q = 0; q < 4; ++q) T[(hb * 4 + q) * 256 + a] = v[q];
    STAMP(2, 1);
    pf(6, 3);
    if (k == 0) {
      bv = *(const u4v*)(WT + ((size_t)256 * 512 + tid) * 4);
      const int m0 = (lane >> 4) + 4 * ((lane >> 2) & 3) + 16 * (lane & 3);
#pragma unroll
      for (int r4 = 0; r4 < 4; ++r4) {
        const int un = (S_ - (w + 8 * (m0 + 64 * r4))) & (S_ - 1);
        const int mm = un >> 3;
        mirr[r4] = ((un & 7) * 64 + 16 * (mm & 3) + 4 * ((mm >> 2) & 3) + ((mm >> 4) & 3)) * 4 + (mm >> 6);
      }
      if (w != 0) {
        const u4v a4 = *(const u4v*)(WT + (64 * (8 - w) + 63 - lane) * 4);
        const u4v b4 = *(const u4v*)(WT + (size_t)256 * 2048 + (64 * (8 - w) + 63 - lane) * 4);
#pragma unroll
        for (int r4 = 0; r4 < 4; ++r4) { wn[r4] = a4[3 - r4]; bn[r4] = b4[3 - r4]; }
      } else {
#pragma unroll
        for (int r4 = 0; r4 < 4; ++r4) {
          wn[r4] = WT[mirr[r4]];
          bn[r4] = WT[(size_t)256 * 2048 + mirr[r4]];
        }
      }
    }
    __syncthreads();
    {
      const int q = w & 3;
      const float sg = (w < 4) ? 1.f : -1.f;
#pragma unroll
      for (int j = 0; j < 4; ++j) {
        f2 e = T[q * 256 + lane + 64 * j], o = T[(4 + q) * 256 + lane + 64 * j];
        v[j] = cmul(e + o * sg, tw1[j]);
      }
    }
    fft256_fwd(v, lane, tw, [&](int i) __attribute__((always_inline)) { pf(9 + 2 * i, 2); });
    pf(15, 1);
    STAMP(2, 2);
    if (k != 0) {
#pragma unroll
      for (int i = 0; i < 4; ++i) {
        f2 wf = f2{bf_lo(wv[i]), bf_hi(wv[i])} * FSC;
        v[i] = cmul(v[i], wf);
      }
    } else {
#pragma unroll
      for (int r4 = 0; r4 < 4; ++r4) T3[r4 * 512 + tid] = v[r4];
      __syncthreads();
#pragma unroll
      for (int r4 = 0; r4 < 4; ++r4) {
        f2 cm = T3[(mirr[r4] & 3) * 512 + (mirr[r4] >> 2)];
        f2 h0 = f2{bf_lo(wv[r4]) + bf_lo(wn[r4]), bf_hi(wv[r4]) - bf_hi(wn[r4])};
        f2 h1 = f2{bf_lo(bv[r4]) + bf_lo(bn[r4]), bf_hi(bv[r4]) - bf_hi(bn[r4])};
        f2 wp = (h0 + h1) * (0.25f * FSC), wm = (h0 - h1) * (0.25f * FSC);
        f2 cc = f2{cm.x, -cm.y};
        v[r4] = cmul(v[r4], wp) + cmul(cc, wm);
      }
    }
    fft256_inv(v, lane, tw, [&](int i) __attribute__((always_inline)) {
      SCHED();
#pragma unroll
      for (int q = 0; q < 4; ++q) pf_put(4 * i + q);
      SCHED();
    });
#pragma unroll
    for (int j = 0; j < 4; ++j) T2[w * 256 + lane + 64 * j] = cmulc(v[j], tw1[j]);
#pragma unroll
    for (int q = 12; q < 16; ++q) pf_put(q);
    STAMP(2, 3);
    __syncthreads();
    {
      const float sg = hb ? -1.f : 1.f;
#pragma unroll
      for (int q = 0; q < 4; ++q) v[q] = T2[q * 256 + a] + T2[(q + 4) * 256 + a] * sg;
      if (hb) {
        v[1] = f2{v[1].x - v[1].y, v[1].y + v[1].x} * R;
        v[2] = f2{-v[2].y, v[2].x};
        v[3] = f2{-v[3].x - v[3].y, v[3].x - v[3].y} * R;
      }
    }
    dft4<1>(v[0], v[1], v[2], v[3]);
#pragma unroll
    for (int j = 0; j < 4; ++j) ST64(__builtin_bit_cast(u2v, v[j]), OFF_ZT + (k * S_ + tid + 512 * j) * 8);
    STAMP(2, 4);
    DRAIN();
    __syncthreads();
    if (tid < 16)
      __hip_atomic_store((gu32*)(ws + OFF_FLG) + tid * 256 + k, 1u, __ATOMIC_RELAXED, __HIP_MEMORY_SCOPE_AGENT);
  }
  STAMP(2, 5);
  pf(16, 4);
  {
    const char* lw = dsm + L_W1 + (lane >> 2) * 224 + (lane & 3) * 8 + w * (2 * 7168);
    u4v fr[14];
#pragma unroll
    for (int i = 0; i < 14; ++i) fr[i] = w1frag(lw, i % 7, i / 7);
    asm volatile("" ::: "memory");
#pragma unroll
    for (int i = 0; i < 14; ++i) *(u4v*)(dsm + L_W1 + w * (2 * 7168) + i * 1024 + lane * 16) = fr[i];
  }
  STAMP(2, 6);
  {
    f2* zs = (f2*)(dsm + L_ZS);
    const int rp = tid & 3, k = tid >> 2;
    const int k2 = (k == 0) ? 128 : 256 - k;
    u4v xr;
    const unsigned* xrow = (const unsigned*)(x + (size_t)(row0 + w) * D_);
#pragma unroll
    for (int j = 0; j < 4; ++j) xr[j] = xrow[lane + 64 * j];
    const u4v gav = *(const u4v*)(ws + OFF_MODC + lane * 16);
    const u4v sbv = *(const u4v*)(ws + OFF_MODC + 1024 + lane * 16);
    const u4v cbv = *(const u4v*)(ws + OFF_MODC + 2048 + lane * 16);
    if (w != 0) pf(20, 4);
    f2 wq = twid((float)k * (1.0f / 512.0f));
    PIN2(wq);
    const int pa = zpos(k), pb = zpos(k2);
    if (w == 0) wait_cols((gu64*)(ws + OFF_FLG + (bid & 15) * 1024 + lane * 16));
    asm volatile("s_barrier" ::: "memory");
    __builtin_amdgcn_fence(__ATOMIC_ACQUIRE, "wavefront");
    STAMP(3, 0);
    const f4 ya = __builtin_bit_cast(f4, LD128(OFF_ZT + (k * S_ + row0 + 2 * rp) * 8));
    const f4 yb = __builtin_bit_cast(f4, LD128(OFF_ZT + (k2 * S_ + row0 + 2 * rp) * 8));
    if (w == 0) pf(20, 4);
#pragma unroll
    for (int h = 0; h < 2; ++h) {
      f2 a = f2{ya[2 * h], ya[2 * h + 1]}, b = f2{yb[2 * h], yb[2 * h + 1]};
      f2* zr = zs + (2 * rp + h) * ZRS;
      f2 Ze = f2{a.x + b.x, a.y - b.y};
      f2 Dm = f2{a.x - b.x, a.y + b.y};
      f2 Zo = cmulc(Dm, wq);
      f2 za = f2{Ze.x - Zo.y, Ze.y + Zo.x};
      f2 zb = f2{Ze.x + Zo.y, -Ze.y + Zo.x};
      if (k == 0) {
        za = f2{a.x + a.y, a.x - a.y};
        zb = f2{2.f * b.x, -2.f * b.y};
      }
      zr[pa] = za;
      zr[pb] = zb;
    }
    STAMP(3, 1);
    pf(24, 2);
    __syncthreads();
    f2 v[4];
    {
      const f2* buf = zs + w * ZRS + lane + (lane >> 4);
#pragma unroll
      for (int r4 = 0; r4 < 4; ++r4) v[r4] = buf[68 * r4];
    }
    fft256_inv(v, lane, tw, [&](int i) __attribute__((always_inline)) { if (i < 2) pf(26 + 2 * i, 2); });
    STAMP(3, 2);
    float s = 0.f;
    u4v x1p;
#pragma unroll
    for (int j = 0; j < 4; ++j) {
      f2 t = f2{bf_lo(xr[j]), bf_hi(xr[j])} + bfr2(f2{bf_lo(gav[j]), bf_hi(gav[j])} * bfr2(v[j]));
      x1p[j] = pack_bf(t.x, t.y);
      v[j] = f2{bf_lo(x1p[j]), bf_hi(x1p[j])};
      s += v[j].x + v[j].y;
    }
    ST128(x1p, OFF_X1 + ((row0 + w) * 64 + lane) * 16);
    s = wave_sum(s);
    const float meanf = s * (1.0f / D_);
    float q = 0.f;
#pragma unroll
    for (int j = 0; j < 4; ++j) {
      f2 d = v[j] - meanf;
      q += d.x * d.x + d.y * d.y;
    }
    q = wave_sum(q);
    const float mean = bfr(meanf);
    const float den = bfr(sqrtf(bfr(bfr(q * (1.0f / D_)) + 1e-5f)));
    const float rden = 1.0f / den;
    u4v hp;
#pragma unroll
    for (int j = 0; j < 4; ++j) {
      f2 t = __builtin_elementwise_fma(v[j] - mean, f2{bf_lo(cbv[j]), bf_hi(cbv[j])} * rden,
                                       f2{bf_lo(sbv[j]), bf_hi(sbv[j])});
      hp[j] = pack_bf(t.x, t.y);
    }
    *(u4v*)(dsm + L_ST + (lane * 9 + w) * 16) = hp;
    __syncthreads();
    {
      const int ch = tid >> 3, r = tid & 7;
      u4v o = *(const u4v*)(dsm + L_ST + (ch * 9 + r) * 16);
      const int mt = row0 >> 4, r16 = (row0 & 8) + r;
      ST128(o, OFF_H2F + ((mt * 16 + (ch >> 2)) * 64 + (ch & 3) * 16 + r16) * 16);
    }
  }
  STAMP(3, 3);
  DRAIN();
  __syncthreads();
  if (tid == 0) __hip_atomic_store(myA, 1u, __ATOMIC_RELAXED, __HIP_MEMORY_SCOPE_AGENT);
  {
    const int ch = tid >> 3, r = tid & 7;
    u4v t = LD128(OFF_H2F + (((row0 >> 4) * 16 + (ch >> 2)) * 64 + (ch & 3) * 16 + (row0 & 8) + r) * 16);
    asm volatile("" ::"v"(t));
  }
  STAMP(3, 4);
  pf(30, 4);
  wait_pair(depA);
  STAMP(3, 5);
  {
    u4v B[16];
#pragma unroll
    for (int ks = 0; ks < 16; ++ks) B[ks] = LD128(OFF_H2F + (((8 * p + w) * 16 + ks) * 64 + lane) * 16);
    f4 acc[7];
#pragma unroll
    for (int ft = 0; ft < 7; ++ft) acc[ft] = f4{0.f, 0.f, 0.f, 0.f};
    const char* lw = dsm + L_W1 + lane * 16;
    const int e = c >> 1, odd = c & 1;
    const int hb = (int)OFF_HF + (((8 * p + w) * 56 + 7 * e) * 64 + lane) * 16;
    u4v Af[2][7];
#pragma unroll
    for (int j = 0; j < 7; ++j) Af[0][j] = *(const u4v*)(lw + (g1_ks(0, j) * 7 + g1_ft(0, j)) * 1024);
#pragma unroll
    for (int g = 0; g < 17; ++g) {
#pragma unroll
      for (int j = 0; j < 7; ++j) {
        if (j < g1_n(g)) acc[g1_ft(g, j)] = mfma16(Af[g & 1][j], B[g1_ks(g, j)], acc[g1_ft(g, j)]);
        if (g + 1 < 17 && j < g1_n(g + 1))
          Af[(g + 1) & 1][j] = *(const u4v*)(lw + (g1_ks(g + 1, j) * 7 + g1_ft(g + 1, j)) * 1024);
        SCHED();
      }
      if (g >= 2 && g < 12) pf_load(32 + g);
      if (g >= 14) {
        const int i = g - 14;
        f4 a = acc[2 * i], b = acc[2 * i + 1];
        u4v o = u4v{silu_pk(a[0], a[1]), silu_pk(a[2], a[3]), silu_pk(b[0], b[1]), silu_pk(b[2], b[3])};
        ST128(o, hb + (odd * 3 + i) * 1024);
      }
      SCHED();
      if (g == 12) STAMP(3, 6);
    }
    f4 a = acc[6];
    u2v o = u2v{silu_pk(a[0], a[1]), silu_pk(a[2], a[3])};
    ST64(o, hb + 6 * 1024 + odd * 8);
  }
  STAMP(3, 7);
  DRAIN();
  __syncthreads();
  if (tid == 0) __hip_atomic_store(myB, 1u, __ATOMIC_RELAXED, __HIP_MEMORY_SCOPE_AGENT);
  {
    const int hb = (int)OFF_HF + (((8 * p + w) * 56 + 7 * (c >> 1) + 3 * (c & 1)) * 64 + lane) * 16;
    u4v t0 = LD128(hb), t1 = LD128(hb + 1024), t2 = LD128(hb + 2048);
    asm volatile("" ::"v"(t0), "v"(t1), "v"(t2));
  }
  wait_pair(depB);
  STAMP(3, 8);
  {
    const int Bb = (int)OFF_HF + ((mt0 * 56 + 7 * w) * 64 + lane) * 16;
    u4v B[7][4];
#pragma unroll
    for (int ks = 0; ks < 4; ++ks)
#pragma unroll
      for (int mt = 0; mt < 4; ++mt) B[ks][mt] = LD128(Bb + (mt * 56 + ks) * 1024);
    f4 acc[4][4];
#pragma unroll
    for (int i = 0; i < 4; ++i)
#pragma unroll
      for (int j = 0; j < 4; ++j) acc[i][j] = f4{0.f, 0.f, 0.f, 0.f};
#pragma unroll
    for (int ks = 0; ks < 7; ++ks) {
#pragma unroll
      for (int nt = 0; nt < 4; ++nt)
#pragma unroll
        for (int mt = 0; mt < 4; ++mt) acc[nt][mt] = mfma16(A2[ks * 4 + nt], B[ks][mt], acc[nt][mt]);
      if (ks + 4 < 7) {
#pragma unroll
        for (int mt = 0; mt < 4; ++mt) B[ks + 4][mt] = LD128(Bb + (mt * 56 + ks + 4) * 1024);
      }
      SCHED();
    }
    STAMP(3, 9);
    const int g = lane >> 4, mr = lane & 15;
    const int fm = w >> 1, P = w & 1;
    const int m = (mt0 + fm) * 16 + mr;
    const int n = nt0 * 16 + 32 * P + 8 * g;
    unsigned xv[4];
#pragma unroll
    for (int i = 0; i < 4; ++i)
      xv[i] = __builtin_amdgcn_raw_buffer_load_b32(
          rs, (int)OFF_X1 + (m * 64 + (((n >> 1) + i) & 63)) * 16 + (n >> 7) * 4, 0, AUX_SC1);
    const f4 g0 = *(const f4*)(modf_ + 2560 + n), g1 = *(const f4*)(modf_ + 2560 + n + 4);
    f4* red = (f4*)dsm;
#pragma unroll
    for (int nt = 0; nt < 4; ++nt)
#pragma unroll
      for (int mt = 0; mt < 4; ++mt) red[(w * 16 + nt * 4 + mt) * 64 + lane] = acc[nt][mt];
    __syncthreads();
    STAMP(3, 10);
    f4 s0 = f4{0.f, 0.f, 0.f, 0.f}, s1 = s0;
#pragma unroll
    for (int ww = 0; ww < 8; ++ww) {
      s0 += red[(ww * 16 + (2 * P) * 4 + fm) * 64 + lane];
      s1 += red[(ww * 16 + (2 * P + 1) * 4 + fm) * 64 + lane];
    }
    u4v o;
    {
      f2 t0 = bfr2(f2{g0[0], g0[1]} * bfr2(f2{s0[0], s0[1]})), t1 = bfr2(f2{g0[2], g0[3]} * bfr2(f2{s0[2], s0[3]}));
      f2 t2 = bfr2(f2{g1[0], g1[1]} * bfr2(f2{s1[0], s1[1]})), t3 = bfr2(f2{g1[2], g1[3]} * bfr2(f2{s1[2], s1[3]}));
      o[0] = pack_bf(bf_lo(xv[0]) + t0.x, bf_hi(xv[0]) + t0.y);
      o[1] = pack_bf(bf_lo(xv[1]) + t1.x, bf_hi(xv[1]) + t1.y);
      o[2] = pack_bf(bf_lo(xv[2]) + t2.x, bf_hi(xv[2]) + t2.y);
      o[3] = pack_bf(bf_lo(xv[3]) + t3.x, bf_hi(xv[3]) + t3.y);
    }
    *(u4v*)(out + (size_t)m * D_ + n) = o;
  }
  STAMP(3, 11);
}

extern "C" void kernel_launch(void* const* d_in, const int* in_sizes, int n_in, void* d_out, int out_size, void* d_ws,
                              size_t ws_size, hipStream_t stream) {
  const u16* x = (const u16*)d_in[0];
  const u16* wr = (const u16*)d_in[1];
  const u16* wi = (const u16*)d_in[2];
  const u16* w1 = (const u16*)d_in[4];
  const u16* w2 = (const u16*)d_in[5];
  const u16* c = (const u16*)d_in[6];
  const u16* wmod = (const u16*)d_in[7];
  char* ws = (char*)d_ws;
  u16* out = (u16*)d_out;
  (void)hipFuncSetAttribute((const void*)k23_fused, hipFuncAttributeMaxDynamicSharedMemorySize, L_TOTAL);
  k0_mod<<<K0_GEMV + K0_WARM, 512, 0, stream>>>(c, wmod, x, wr, wi, w1, w2, ws);
  k1_rowfft<<<256 + K1_WT + K1_W2, 512, 0, stream>>>(x, wr, wi, w2, ws);
  k23_fused<<<256, 512, L_TOTAL, stream>>>(x, w1, ws, out);
}
```

```cpp
#include <hip/hip_runtime.h>
#include <hip/hip_bf16.h>
#include <stdint.h>

typedef float f2 __attribute__((ext_vector_type(2)));
typedef float f4 __attribute__((ext_vector_type(4)));
typedef unsigned u2v __attribute__((ext_vector_type(2)));
typedef unsigned u4v __attribute__((ext_vector_type(4)));
typedef short bf16x8 __attribute__((ext_vector_type(8)));
typedef unsigned short u16;

constexpr int S_ = 2048, D_ = 512, F_ = 1792, DF_ = 257;
constexpr int NSPLIT = 4;

constexpr size_t OFF_CNT = 0;
#define FSPACE 16
#define FREP 16
constexpr int FSP = FSPACE, NREP = FREP;
constexpr size_t FTAB = (size_t)NREP * 256 * FSP;
constexpr size_t OFF_FLG = 16384;
constexpr size_t OFF_FLR = OFF_FLG + FTAB;
constexpr size_t OFF_FLS = OFF_FLR + FTAB;
constexpr size_t FLAG_BYTES = OFF_FLS + 16384;
constexpr size_t OFF_MODP = 900 << 10;
constexpr size_t OFF_MODF = OFF_MODP + (size_t)NSPLIT * 3072 * 4;
constexpr size_t OFF_MODC = OFF_MODF + 3072 * 4;
constexpr size_t OFF_WT = 1 << 20;
constexpr size_t OFF_XT = 4 << 20;
constexpr size_t OFF_ZT = 28 << 20;
constexpr size_t OFF_H2F = 12 << 20;
constexpr size_t OFF_X1 = 14 << 20;
constexpr size_t OFF_W2F = 18 << 20;
constexpr size_t OFF_HF = 20 << 20;
constexpr int WS_SPAN = 36 << 20;
#define CPITCH (2048 + 32)
constexpr int CP = CPITCH;
constexpr int WT_PITCH = 520;
constexpr float FSC = 1.0f / (2.0f * 2048.0f * 512.0f);

#define STAMP(kid, i)

__device__ __forceinline__ float bf_lo(unsigned u) { return __builtin_bit_cast(float, u << 16); }
__device__ __forceinline__ float bf_hi(unsigned u) { return __builtin_bit_cast(float, u & 0xffff0000u); }
__device__ __forceinline__ float bfr(float x) {
  return (float)(__bf16)x;
}
__device__ __forceinline__ unsigned pack_bf(float lo, float hi) {
  typedef __bf16 b2 __attribute__((ext_vector_type(2)));
  return __builtin_bit_cast(unsigned, __builtin_convertvector(f2{lo, hi}, b2));
}
__device__ __forceinline__ f2 cmul(f2 a, f2 b) {
  f2 t = a * f2{b.x, b.x};
  return __builtin_elementwise_fma(f2{a.y, a.x}, f2{-b.y, b.y}, t);
}
__device__ __forceinline__ f2 cmulc(f2 a, f2 b) {
  f2 t = a * f2{b.x, b.x};
  return __builtin_elementwise_fma(f2{a.y, a.x}, f2{b.y, -b.y}, t);
}
__device__ __forceinline__ f2 bfr2(f2 a) {
  unsigned u = pack_bf(a.x, a.y);
  asm("" : "+v"(u));
  return f2{bf_lo(u), bf_hi(u)};
}
__device__ __forceinline__ float msel(unsigned m, float a, float b) {
  unsigned ua = __builtin_bit_cast(unsigned, a), ub = __builtin_bit_cast(unsigned, b);
  return __builtin_bit_cast(float, (m & ua) | (~m & ub));
}
#define PIN2(x) asm volatile("" : "+v"(x))
constexpr int AUX_SC1 = 16;
#define WS_RSRC(ws) __builtin_amdgcn_make_buffer_rsrc((ws), 0, WS_SPAN, 0x00020000)
#define ST128(v, off) __builtin_amdgcn_raw_buffer_store_b128((v), rs, (int)(off), 0, AUX_SC1)
#define ST64(v, off) __builtin_amdgcn_raw_buffer_store_b64((v), rs, (int)(off), 0, AUX_SC1)
#define ST32(v, off) __builtin_amdgcn_raw_buffer_store_b32((v), rs, (int)(off), 0, AUX_SC1)
#define LD64(off) __builtin_amdgcn_raw_buffer_load_b64(rs, (int)(off), 0, AUX_SC1)
#define LD32(off) __builtin_amdgcn_raw_buffer_load_b32(rs, (int)(off), 0, AUX_SC1)
#define LD128(off) __builtin_amdgcn_raw_buffer_load_b128(rs, (int)(off), 0, AUX_SC1)
__device__ __forceinline__ f2 twid(float rev) { return f2{__builtin_amdgcn_cosf(rev), -__builtin_amdgcn_sinf(rev)}; }

template <int DIR>
__device__ __forceinline__ f2 rot90(f2 a) {
  return DIR < 0 ? f2{a.y, -a.x} : f2{-a.y, a.x};
}
template <int DIR>
__device__ __forceinline__ void dft4(f2& c0, f2& c1, f2& c2, f2& c3) {
  f2 d0 = c0 + c2, d2 = c0 - c2, d1 = c1 + c3, e = c1 - c3;
  c0 = d0 + d1;
  c2 = d0 - d1;
  const f2 es = f2{e.y, e.x};
  c1 = __builtin_elementwise_fma(es, DIR < 0 ? f2{1.f, -1.f} : f2{-1.f, 1.f}, d2);
  c3 = __builtin_elementwise_fma(es, DIR < 0 ? f2{-1.f, 1.f} : f2{1.f, -1.f}, d2);
}

__device__ __forceinline__ void sw32(float& a, float& b) {
  auto r = __builtin_amdgcn_permlane32_swap(__builtin_bit_cast(unsigned, a), __builtin_bit_cast(unsigned, b), false, false);
  a = __builtin_bit_cast(float, (unsigned)r[0]);
  b = __builtin_bit_cast(float, (unsigned)r[1]);
}
__device__ __forceinline__ void sw16(float& a, float& b) {
  auto r = __builtin_amdgcn_permlane16_swap(__builtin_bit_cast(unsigned, a), __builtin_bit_cast(unsigned, b), false, false);
  a = __builtin_bit_cast(float, (unsigned)r[0]);
  b = __builtin_bit_cast(float, (unsigned)r[1]);
}
template <int CTRL, int BANK>
__device__ __forceinline__ float udpp(float old, float src) {
  return __builtin_bit_cast(float, __builtin_amdgcn_update_dpp(__builtin_bit_cast(int, old), __builtin_bit_cast(int, src),
                                                               CTRL, 0xf, BANK, false));
}
template <int CTRL>
__device__ __forceinline__ float mdpp(float src) {
  return __builtin_bit_cast(float, __builtin_amdgcn_mov_dpp(__builtin_bit_cast(int, src), CTRL, 0xf, 0xf, false));
}
__device__ __forceinline__ void xp_hi1(float& v0, float& v1, float& v2, float& v3) {
  sw32(v0, v2); sw32(v1, v3); sw16(v0, v1); sw16(v2, v3);
}
__device__ __forceinline__ void xp_mid1(float& v0, float& v1, float& v2, float& v3) {
  float a0 = udpp<0x128, 0xC>(v0, v2), a2 = udpp<0x128, 0x3>(v2, v0);
  float a1 = udpp<0x128, 0xC>(v1, v3), a3 = udpp<0x128, 0x3>(v3, v1);
  v0 = udpp<0x124, 0xA>(a0, a1); v1 = udpp<0x12C, 0x5>(a1, a0);
  v2 = udpp<0x124, 0xA>(a2, a3); v3 = udpp<0x12C, 0x5>(a3, a2);
}
__device__ __forceinline__ void xp_lo1(float& v0, float& v1, float& v2, float& v3, unsigned b1, unsigned b0) {
  float s0 = mdpp<0x4E>(msel(b1, v0, v2)), s1 = mdpp<0x4E>(msel(b1, v1, v3));
  float a0 = msel(b1, s0, v0), a2 = msel(b1, v2, s0), a1 = msel(b1, s1, v1), a3 = msel(b1, v3, s1);
  float t0 = mdpp<0xB1>(msel(b0, a0, a1)), t2 = mdpp<0xB1>(msel(b0, a2, a3));
  v0 = msel(b0, t0, a0); v1 = msel(b0, a1, t0); v2 = msel(b0, t2, a2); v3 = msel(b0, a3, t2);
}
#define XP_CPLX(NAME, FN, ...)                                        \
  {                                                                   \
    float x0 = v[0].x, x1 = v[1].x, x2 = v[2].x, x3 = v[3].x;         \
    float y0 = v[0].y, y1 = v[1].y, y2 = v[2].y, y3 = v[3].y;         \
    FN(x0, x1, x2, x3, ##__VA_ARGS__);                                \
    FN(y0, y1, y2, y3, ##__VA_ARGS__);                                \
    v[0] = f2{x0, y0}; v[1] = f2{x1, y1}; v[2] = f2{x2, y2}; v[3] = f2{x3, y3}; \
  }
__device__ __forceinline__ void xp_hi(f2 (&v)[4]) XP_CPLX(hi, xp_hi1)
__device__ __forceinline__ void xp_mid(f2 (&v)[4]) XP_CPLX(mid, xp_mid1)
__device__ __forceinline__ void xp_lo(f2 (&v)[4], unsigned b1, unsigned b0) XP_CPLX(lo, xp_lo1, b1, b0)

struct Tw256 {
  f2 a[3], b[3], c[3];
  unsigned m1, m0;
};
__device__ __forceinline__ void make_tw256(int lane, Tw256& t) {
#pragma unroll
  for (int r = 1; r < 4; ++r) {
    t.a[r - 1] = twid((float)(lane * r) * (1.0f / 256.0f));
    t.b[r - 1] = twid((float)((lane & 15) * r) * (1.0f / 64.0f));
    t.c[r - 1] = twid((float)((lane & 3) * r) * (1.0f / 16.0f));
  }
  t.m1 = 0u - (unsigned)((lane >> 1) & 1);
  t.m0 = 0u - (unsigned)(lane & 1);
#pragma unroll
  for (int r = 0; r < 3; ++r) {
    PIN2(t.a[r]);
    PIN2(t.b[r]);
    PIN2(t.c[r]);
  }
  PIN2(t.m1);
  PIN2(t.m0);
}
struct NoHook {
  __device__ __forceinline__ void operator()(int) const {}
};
template <class H>
__device__ __forceinline__ void fft256_fwd(f2 (&v)[4], int lane, const Tw256& t, const H& hook) {
  const unsigned b1 = t.m1, b0 = t.m0;
  dft4<-1>(v[0], v[1], v[2], v[3]);
#pragma unroll
  for (int r = 1; r < 4; ++r) v[r] = cmul(v[r], t.a[r - 1]);
  xp_hi(v);
  hook(0);
  dft4<-1>(v[0], v[1], v[2], v[3]);
#pragma unroll
  for (int r = 1; r < 4; ++r) v[r] = cmul(v[r], t.b[r - 1]);
  xp_mid(v);
  hook(1);
  dft4<-1>(v[0], v[1], v[2], v[3]);
#pragma unroll
  for (int r = 1; r < 4; ++r) v[r] = cmul(v[r], t.c[r - 1]);
  xp_lo(v, b1, b0);
  hook(2);
  dft4<-1>(v[0], v[1], v[2], v[3]);
}
template <class H>
__device__ __forceinline__ void fft256_inv(f2 (&v)[4], int lane, const Tw256& t, const H& hook) {
  const unsigned b1 = t.m1, b0 = t.m0;
  dft4<1>(v[0], v[1], v[2], v[3]);
  xp_lo(v, b1, b0);
  hook(0);
#pragma unroll
  for (int r = 1; r < 4; ++r) v[r] = cmulc(v[r], t.c[r - 1]);
  dft4<1>(v[0], v[1], v[2], v[3]);
  xp_mid(v);
  hook(1);
#pragma unroll
  for (int r = 1; r < 4; ++r) v[r] = cmulc(v[r], t.b[r - 1]);
  dft4<1>(v[0], v[1], v[2], v[3]);
  xp_hi(v);
  hook(2);
#pragma unroll
  for (int r = 1; r < 4; ++r) v[r] = cmulc(v[r], t.a[r - 1]);
  dft4<1>(v[0], v[1], v[2], v[3]);
}

__device__ __forceinline__ float wave_sum(float x) {
  x += mdpp<0xB1>(x);
  x += mdpp<0x4E>(x);
  x += mdpp<0x141>(x);
  x += mdpp<0x140>(x);
  float r0 = __builtin_bit_cast(float, __builtin_amdgcn_readlane(__builtin_bit_cast(int, x), 0));
  float r1 = __builtin_bit_cast(float, __builtin_amdgcn_readlane(__builtin_bit_cast(int, x), 16));
  float r2 = __builtin_bit_cast(float, __builtin_amdgcn_readlane(__builtin_bit_cast(int, x), 32));
  float r3 = __builtin_bit_cast(float, __builtin_amdgcn_readlane(__builtin_bit_cast(int, x), 48));
  return (r0 + r1) + (r2 + r3);
}

__device__ __forceinline__ int zpos(int k) { return 17 * (k & 3) + 4 * ((k >> 2) & 3) + ((k >> 4) & 3) + 68 * (k >> 6); }
constexpr int ZRS = 276;

constexpr int K0_GEMV = 48 * NSPLIT, K0_WT = 128, K0_W2 = 0, K0_WARM = 64, K0_Z = (int)(FLAG_BYTES / 8192);
__global__ __launch_bounds__(512) void k0_mod(const u16* __restrict__ c, const u16* __restrict__ wmod,
                                              const u16* __restrict__ x, const u16* __restrict__ wr,
                                              const u16* __restrict__ wi, char* __restrict__ ws) {
  __shared__ __attribute__((aligned(16))) char smem[17408];
  const int tid = threadIdx.x, row = tid >> 6, lane = tid & 63;
  const auto rs = WS_RSRC(ws);
  int b = blockIdx.x;
  STAMP(0, 0);
  if (b >= K0_GEMV + K0_WT + K0_W2) {
    int q = b - (K0_GEMV + K0_WT + K0_W2);
    if (q < K0_Z) {
      ((u4v*)ws)[q * 512 + tid] = u4v{0u, 0u, 0u, 0u};
      return;
    }
    q -= K0_Z;
    const char* src = (const char*)x + (size_t)q * 32768;
    u4v t[4];
#pragma unroll
    for (int i = 0; i < 4; ++i) t[i] = *(const u4v*)(src + i * 8192 + tid * 16);
#pragma unroll
    for (int i = 0; i < 4; ++i) asm volatile("" ::"v"(t[i]));
    STAMP(0, 1);
    return;
  }
  if (b >= K0_GEMV) {
    b -= K0_GEMV;
    if (b < K0_WT) {
      char* sr = smem;
      char* si = smem + 16 * 544;
      constexpr int LASTCH = (S_ * DF_ * 2) / 16 - 1;
      for (int it = tid; it < 16 * 34; it += 512) {
        const int i = it / 34, ch = it % 34;
        int gch = ((b + 128 * i) * (DF_ * 2)) / 16 + ch;
        gch = gch > LASTCH ? LASTCH : gch;
        *(u4v*)(sr + i * 544 + ch * 16) = *(const u4v*)((const char*)wr + (size_t)gch * 16);
        *(u4v*)(si + i * 544 + ch * 16) = *(const u4v*)((const char*)wi + (size_t)gch * 16);
      }
      __syncthreads();
      const int tbase = 64 * (b & 7) + 16 * ((b >> 3) & 3) + 4 * ((b >> 5) & 3);
      for (int it = tid; it < DF_ * 4; it += 512) {
        const int k = it >> 2, r3 = it & 3;
        u4v o;
#pragma unroll
        for (int r4 = 0; r4 < 4; ++r4) {
          const int i = r3 + 4 * r4;
          const int off = i * 544 + (((b + 128 * i) * (DF_ * 2)) & 15) + 2 * k;
          o[r4] = (unsigned)*(const u16*)(sr + off) | ((unsigned)*(const u16*)(si + off) << 16);
        }
        ST128(o, OFF_WT + (k * 512 + tbase + r3) * 16);
      }
    }
    STAMP(0, 1);
    return;
  }
  float* red = (float*)smem;
  const int cgp = b % 48, ks = b / 48;
  const int cg = tid & 7, r = tid >> 3;
  const int rw = ks * 128 + r;
  const u4v wm0 = *(const u4v*)(wmod + (size_t)rw * 3072 + cgp * 64 + cg * 8);
  const u4v wm1 = *(const u4v*)(wmod + (size_t)(rw + 64) * 3072 + cgp * 64 + cg * 8);
  const float cv0 = bf_lo(c[rw]), cv1 = bf_lo(c[rw + 64]);
  const float s0 = bfr(cv0 / (1.0f + __expf(-cv0))), s1 = bfr(cv1 / (1.0f + __expf(-cv1)));
  float acc[8];
#pragma unroll
  for (int i = 0; i < 4; ++i) {
    acc[2 * i] = s0 * bf_lo(wm0[i]) + s1 * bf_lo(wm1[i]);
    acc[2 * i + 1] = s0 * bf_hi(wm0[i]) + s1 * bf_hi(wm1[i]);
  }
#pragma unroll
  for (int i = 0; i < 8; ++i) {
    acc[i] += mdpp<0x128>(acc[i]);
    acc[i] += __shfl_xor(acc[i], 16);
    acc[i] += __shfl_xor(acc[i], 32);
  }
  if (lane < 8) {
#pragma unroll
    for (int i = 0; i < 8; ++i) red[row * 64 + cg * 8 + i] = acc[i];
  }
  __syncthreads();
  if (tid < 64) {
    float sum = 0.f;
#pragma unroll
    for (int i = 0; i < 8; ++i) sum += red[i * 64 + tid];
    ((float*)(ws + OFF_MODP))[ks * 3072 + cgp * 64 + tid] = sum;
  }
  STAMP(0, 1);
}

__device__ __forceinline__ f4 mfma16(u4v a, u4v b, f4 c) {
  return __builtin_amdgcn_mfma_f32_16x16x32_bf16(__builtin_bit_cast(bf16x8, a), __builtin_bit_cast(bf16x8, b), c, 0, 0,
                                                  0);
}
__device__ __forceinline__ f2 silu2(float a, float b) {
  f2 h = f2{a, b};
  f2 t = h * -1.44269504088896340736f;
  f2 d = f2{__builtin_amdgcn_exp2f(t.x), __builtin_amdgcn_exp2f(t.y)} + 1.0f;
  return h * f2{__builtin_amdgcn_rcpf(d.x), __builtin_amdgcn_rcpf(d.y)};
}
__device__ __forceinline__ unsigned silu_pk(float a, float b) {
  f2 r = silu2(a, b);
  return pack_bf(r.x, r.y);
}
typedef __attribute__((address_space(1))) unsigned gu32;
#define DRAIN() asm volatile("s_waitcnt vmcnt(0)" ::: "memory")
typedef __attribute__((address_space(1))) unsigned long long gu64;
constexpr unsigned SPIN_CAP = 1u << 18;
__device__ __forceinline__ void wait_pair(gu64* flags) {
#pragma nounroll
  for (unsigned spin = 0; spin < SPIN_CAP; ++spin) {
    if (__hip_atomic_load(flags, __ATOMIC_RELAXED, __HIP_MEMORY_SCOPE_AGENT) == 0x0000000100000001ull) break;
    __builtin_amdgcn_s_sleep(1);
  }
  __builtin_amdgcn_fence(__ATOMIC_ACQUIRE, "wavefront");
  asm volatile("" ::: "memory");
}

#define PSLEEP 2
__device__ __forceinline__ void wait_cols(gu32* flags) {
#pragma nounroll
  for (unsigned spin = 0; spin < SPIN_CAP; ++spin) {
    unsigned f = 1u;
#pragma unroll
    for (int j = 0; j < 4; ++j)
      f &= __hip_atomic_load(flags + j * 64 * (FSP / 4), __ATOMIC_RELAXED, __HIP_MEMORY_SCOPE_AGENT);
    if (__builtin_amdgcn_ballot_w64(f != 1u) == 0ull) break;
    __builtin_amdgcn_s_sleep(PSLEEP);
  }
}

__device__ __forceinline__ int wt_off(int k, int t) { return (int)OFF_WT + (k * 512 + t) * 16; }

constexpr int L_W1 = 0;
constexpr int L_T = 112 * 1024;
constexpr int L_ZS = 112 * 1024;
constexpr int L_ST = L_ZS + 8 * ZRS * 8;
constexpr int L_TOTAL = 160 * 1024;
__device__ __forceinline__ constexpr int g1_n(int g) { return g < 13 ? 7 : (g < 16 ? 6 : 3); }
__device__ __forceinline__ constexpr int g1_ft(int g, int j) { return g < 13 ? j : (g < 16 ? 2 * (g - 13) + (j & 1) : 6); }
__device__ __forceinline__ constexpr int g1_ks(int g, int j) { return g < 13 ? g : (g < 16 ? 13 + (j >> 1) : 13 + j); }
#define SCHED() __builtin_amdgcn_sched_barrier(0)
typedef short v4i16 __attribute__((ext_vector_type(4)));
__device__ __forceinline__ u4v w1frag(const char* lw, int ft, int ks) {
  typedef __attribute__((address_space(3))) v4i16* lp;
  const u2v a = __builtin_bit_cast(u2v, __builtin_amdgcn_ds_read_tr16_b64_v4i16((lp)(lw + (2 * ks) * 3584 + ft * 32)));
  const u2v b = __builtin_bit_cast(u2v, __builtin_amdgcn_ds_read_tr16_b64_v4i16((lp)(lw + (2 * ks + 1) * 3584 + ft * 32)));
  return u4v{a[0], a[1], b[0], b[1]};
}
constexpr int L_MODS = L_ZS + 8 * ZRS * 8;
constexpr int L_W2S = L_T + 32768;
__global__ __launch_bounds__(512, 2) void kmega(const u16* __restrict__ x, const u16* __restrict__ w1,
                                                const u16* __restrict__ w2, char* ws, u16* __restrict__ out) {
  extern __shared__ __attribute__((aligned(16))) char dsm[];
  const int tid = threadIdx.x, lane = tid & 63;
  const int w = __builtin_amdgcn_readfirstlane(tid >> 6);
  const int bid = blockIdx.x;
  const int p = (bid & 7) * 2 + (bid >> 7), c = (bid >> 3) & 15;
  const int row0 = 128 * p + 8 * c;
  STAMP(1, 0);
  const auto rs = WS_RSRC(ws);
  gu32* myA = (gu32*)(ws + OFF_CNT) + (p * 8 + (c >> 1)) * 16 + (c & 1);
  gu32* myB = myA + 128 * 16;
  gu64* depA = (gu64*)(ws + OFF_CNT) + (p * 8 + w) * 8;
  gu64* depB = depA + 128 * 8;
  const int mt0 = 8 * p + 4 * (c >> 3), nt0 = 4 * (c & 7);
  const int l56 = lane < 56 ? lane : lane - 56;
  const int r4_ = l56 / 14, ch_ = l56 - 14 * r4_;
  const char* w1g = (const char*)w1 + (size_t)(4 * w + r4_) * (F_ * 2) + c * 224 + ch_ * 16;
  char* w1l = dsm + L_W1 + ((w >> 1) * 32 + (2 * (w & 1) + (r4_ >> 1)) * 4 + (r4_ & 1)) * 224 + ch_ * 16;
  const int Ab = (int)OFF_W2F + ((nt0 * 56 + 7 * w) * 64 + lane) * 16;
  u4v S1[16];
  u4v A2[28];
  auto pf_load = [&](int i) __attribute__((always_inline)) {
    if (i < 16) S1[i] = *(const u4v*)(w1g + (size_t)i * (32 * F_ * 2));
    else if (i < 44) A2[i - 16] = LD128(Ab + (((i - 16) & 3) * 56 + ((i - 16) >> 2)) * 1024);
  };
  auto pf_put = [&](int i) __attribute__((always_inline)) {
    *(u4v*)(w1l + ((i & 3) * 128 + (i >> 3) * 16 + ((i >> 2) & 1) * 2) * 224) = S1[i];
  };
  auto pf = [&](int i0, int n) __attribute__((always_inline)) {
    SCHED();
#pragma unroll
    for (int i = 0; i < n; ++i) pf_load(i0 + i);
    SCHED();
  };
  const int tk = bid < 224 ? bid : bid - 224;
  Tw256 tw;
  {
    f2* zs = (f2*)(dsm + L_ZS);
    float* mods = (float*)(dsm + L_MODS);
    const int r0 = bid * 8;
    unsigned xr[4];
    const unsigned* xrow = (const unsigned*)(x + (size_t)(r0 + w) * D_);
#pragma unroll
    for (int j = 0; j < 4; ++j) xr[j] = xrow[lane + 64 * j];
    const float* mp = (const float*)(ws + OFF_MODP);
    f2 sm = *(const f2*)(mp + tid * 2);
#pragma unroll
    for (int k = 1; k < NSPLIT; ++k) sm += *(const f2*)(mp + k * 3072 + tid * 2);
    const int idx = bid * 12 + (tid < 6 ? tid : 0) * 2;
    f2 fin = *(const f2*)(mp + idx);
#pragma unroll
    for (int k = 1; k < NSPLIT; ++k) fin += *(const f2*)(mp + k * 3072 + idx);
    SCHED();
    make_tw256(lane, tw);
    const int rp = tid & 3, kk = tid >> 2;
    f2 wpost = twid((float)kk * (1.0f / 512.0f));
    PIN2(wpost);
    f2 v[4];
    float s = 0.f;
#pragma unroll
    for (int j = 0; j < 4; ++j) {
      v[j] = f2{bf_lo(xr[j]), bf_hi(xr[j])};
      s += v[j].x + v[j].y;
    }
    s = wave_sum(s);
    const float meanf = s * (1.0f / D_);
    float q = 0.f;
#pragma unroll
    for (int j = 0; j < 4; ++j) {
      f2 d = v[j] - meanf;
      q += d.x * d.x + d.y * d.y;
    }
    q = wave_sum(q);
    const float mean = bfr(meanf);
    const float den = bfr(sqrtf(bfr(bfr(q * (1.0f / D_)) + 1e-5f)));
    const float rden = 1.0f / den;
    sm = bfr2(sm);
    if (tid >= 256) sm = bfr2(sm + 1.0f);
    *(f2*)(mods + tid * 2) = sm;
    if (tid < 6) {
      fin = bfr2(fin);
      ST64(__builtin_bit_cast(u2v, fin), OFF_MODF + idx * 4);
      const int vec = (idx >> 9) - 2, d = idx & 511;
      if (vec >= 0 && vec < 3) {
        if (vec == 2) fin += 1.0f;
        ST32(pack_bf(fin.x, fin.y), OFF_MODC + ((vec * 64 + ((d >> 1) & 63)) * 4 + (d >> 7)) * 4);
      }
    }
    STAMP(1, 3);
    __syncthreads();
    STAMP(1, 4);
    pf(0, 4);
#pragma unroll
    for (int j = 0; j < 4; ++j) {
      const int d = 2 * (lane + 64 * j);
      f2 sa = *(const f2*)(mods + d);
      f2 ca1 = *(const f2*)(mods + 512 + d);
      v[j] = bfr2(__builtin_elementwise_fma(v[j] - mean, ca1 * rden, sa));
    }
    fft256_fwd(v, lane, tw, NoHook{});
    {
      f2* buf = zs + w * ZRS + lane + (lane >> 4);
#pragma unroll
      for (int r4 = 0; r4 < 4; ++r4) buf[68 * r4] = v[r4];
    }
    STAMP(1, 5);
    __syncthreads();
    {
      const int k = kk;
      const int k2 = (k == 0) ? 128 : 256 - k;
      const int pa = zpos(k), pb = zpos(k2);
      f4 oa, ob;
#pragma unroll
      for (int h = 0; h < 2; ++h) {
        const f2* zr = zs + (2 * rp + h) * ZRS;
        f2 za = zr[pa], zb = zr[pb];
        f2 E = f2{za.x + zb.x, za.y - zb.y};
        f2 Dm = f2{za.x - zb.x, za.y + zb.y};
        f2 O = f2{Dm.y, -Dm.x};
        f2 wo = cmul(wpost, O);
        f2 ra = E + wo;
        f2 t = E - wo;
        f2 rb = f2{t.x, -t.y};
        if (k == 0) {
          ra = f2{2.f * (za.x + za.y), 2.f * (za.x - za.y)};
          rb = f2{2.f * zb.x, -2.f * zb.y};
        }
        oa[2 * h] = ra.x; oa[2 * h + 1] = ra.y;
        ob[2 * h] = rb.x; ob[2 * h + 1] = rb.y;
      }
      ST128(__builtin_bit_cast(u4v, oa), OFF_XT + (k * CP + r0 + 2 * rp) * 8);
      ST128(__builtin_bit_cast(u4v, ob), OFF_XT + (k2 * CP + r0 + 2 * rp) * 8);
    }
    STAMP(1, 6);
    DRAIN();
    __syncthreads();
    if (tid < NREP)
      __hip_atomic_store((gu32*)(ws + OFF_FLR + (tid * 256 + bid) * FSP), 1u, __ATOMIC_RELAXED, __HIP_MEMORY_SCOPE_AGENT);
    STAMP(1, 7);
  }
  {
    f2* T = (f2*)(dsm + L_T);
    f2* T2 = (f2*)(dsm + L_T + 16384);
    f2* T3 = (f2*)(dsm + L_T + 32768);
    const int k = bid;
    f2 tw1[4];
#pragma unroll
    for (int j = 0; j < 4; ++j) {
      tw1[j] = twid((float)((lane + 64 * j) * w) * (1.0f / 2048.0f));
      PIN2(tw1[j]);
    }
    if (w == 0) wait_cols((gu32*)(ws + OFF_FLR + ((bid % NREP) * 256 + lane) * FSP));
    asm volatile("s_barrier" ::: "memory");
    __builtin_amdgcn_fence(__ATOMIC_ACQUIRE, "wavefront");
    STAMP(2, 0);
    f2 v[4];
#pragma unroll
    for (int j = 0; j < 4; ++j) v[j] = __builtin_bit_cast(f2, LD64(OFF_XT + (k * CP + tid + 512 * j) * 8));
    u4v bv;
    unsigned wn[4], bn[4];
    int mirr[4];
#pragma unroll
    for (int j = 0; j < 4; ++j) PIN2(v[j]);
    pf(4, 4);
    const int hb = w >> 2, a = tid & 255;
    constexpr float R = 0.70710678118654752440f;
    dft4<-1>(v[0], v[1], v[2], v[3]);
    if (hb) {
      v[1] = f2{v[1].x + v[1].y, v[1].y - v[1].x} * R;
      v[2] = f2{v[2].y, -v[2].x};
      v[3] = f2{v[3].y - v[3].x, -v[3].x - v[3].y} * R;
    }
#pragma unroll
    for (int q = 0; q < 4; ++q) T[(hb * 4 + q) * 256 + a] = v[q];
    STAMP(2, 1);
    const u4v wv = LD128(wt_off(k, tid));
    u4v w2a;
    {
      const int ks2 = tk >> 2, rr = tid >> 4, gg = rr >> 3, j = rr & 7;
      const int e = ks2 / 7, i7 = ks2 - 7 * e;
      const int T0 = 14 * e + (i7 < 3 ? 2 * i7 : (i7 < 6 ? 2 * i7 + 1 : 6));
      const int T1 = i7 < 6 ? T0 + 1 : 14 * e + 13;
      const int f = (j < 4) ? (16 * T0 + 4 * gg + j) : (16 * T1 + 4 * gg + (j - 4));
      w2a = *(const u4v*)(w2 + (size_t)f * D_ + (tk & 3) * 128 + (tid & 15) * 8);
    }
    if (k == 0) {
      bv = LD128(wt_off(256, tid));
      const int m0 = (lane >> 4) + 4 * ((lane >> 2) & 3) + 16 * (lane & 3);
#pragma unroll
      for (int r4 = 0; r4 < 4; ++r4) {
        const int un = (S_ - (w + 8 * (m0 + 64 * r4))) & (S_ - 1);
        const int mm = un >> 3;
        mirr[r4] = ((un & 7) * 64 + 16 * (mm & 3) + 4 * ((mm >> 2) & 3) + ((mm >> 4) & 3)) * 4 + (mm >> 6);
      }
      if (w != 0) {
        const u4v a4 = LD128(wt_off(0, 64 * (8 - w) + 63 - lane));
        const u4v b4 = LD128(wt_off(256, 64 * (8 - w) + 63 - lane));
#pragma unroll
        for (int r4 = 0; r4 < 4; ++r4) { wn[r4] = a4[3 - r4]; bn[r4] = b4[3 - r4]; }
      } else {
#pragma unroll
        for (int r4 = 0; r4 < 4; ++r4) {
          wn[r4] = LD32(wt_off(0, mirr[r4] >> 2) + (mirr[r4] & 3) * 4);
          bn[r4] = LD32(wt_off(256, mirr[r4] >> 2) + (mirr[r4] & 3) * 4);
        }
      }
    }
    __syncthreads();
    {
      const int q = w & 3;
      const float sg = (w < 4) ? 1.f : -1.f;
#pragma unroll
      for (int j = 0; j < 4; ++j) {
        f2 e = T[q * 256 + lane + 64 * j], o = T[(4 + q) * 256 + lane + 64 * j];
        v[j] = cmul(e + o * sg, tw1[j]);
      }
    }
    fft256_fwd(v, lane, tw, [&](int i) __attribute__((always_inline)) { if (i != 1) pf(8 + 2 * i, 4); });
    STAMP(2, 2);
    if (k != 0) {
#pragma unroll
      for (int i = 0; i < 4; ++i) {
        f2 wf = f2{bf_lo(wv[i]), bf_hi(wv[i])} * FSC;
        v[i] = cmul(v[i], wf);
      }
    } else {
#pragma unroll
      for (int r4 = 0; r4 < 4; ++r4) T3[r4 * 512 + tid] = v[r4];
      __syncthreads();
#pragma unroll
      for (int r4 = 0; r4 < 4; ++r4) {
        f2 cm = T3[(mirr[r4] & 3) * 512 + (mirr[r4] >> 2)];
        f2 h0 = f2{bf_lo(wv[r4]) + bf_lo(wn[r4]), bf_hi(wv[r4]) - bf_hi(wn[r4])};
        f2 h1 = f2{bf_lo(bv[r4]) + bf_lo(bn[r4]), bf_hi(bv[r4]) - bf_hi(bn[r4])};
        f2 wp = (h0 + h1) * (0.25f * FSC), wm = (h0 - h1) * (0.25f * FSC);
        f2 cc = f2{cm.x, -cm.y};
        v[r4] = cmul(v[r4], wp) + cmul(cc, wm);
      }
    }
    fft256_inv(v, lane, tw, [&](int i) __attribute__((always_inline)) {
      SCHED();
#pragma unroll
      for (int q = 0; q < 4; ++q) pf_put(4 * i + q);
      SCHED();
    });
#pragma unroll
    for (int j = 0; j < 4; ++j) T2[w * 256 + lane + 64 * j] = cmulc(v[j], tw1[j]);
#pragma unroll
    for (int q = 12; q < 16; ++q) pf_put(q);
    STAMP(2, 3);
    __syncthreads();
    {
      const float sg = hb ? -1.f : 1.f;
#pragma unroll
      for (int q = 0; q < 4; ++q) v[q] = T2[q * 256 + a] + T2[(q + 4) * 256 + a] * sg;
      if (hb) {
        v[1] = f2{v[1].x - v[1].y, v[1].y + v[1].x} * R;
        v[2] = f2{-v[2].y, v[2].x};
        v[3] = f2{-v[3].x - v[3].y, v[3].x - v[3].y} * R;
      }
    }
    dft4<1>(v[0], v[1], v[2], v[3]);
#pragma unroll
    for (int j = 0; j < 4; ++j) ST64(__builtin_bit_cast(u2v, v[j]), OFF_ZT + (k * CP + tid + 512 * j) * 8);
    STAMP(2, 4);
    *(u4v*)(dsm + L_W2S + ((tid >> 4) * 136 + (tid & 15) * 8) * 2) = w2a;
    DRAIN();
    __syncthreads();
    if (tid < NREP)
      __hip_atomic_store((gu32*)(ws + OFF_FLG + (tid * 256 + k) * FSP), 1u, __ATOMIC_RELAXED, __HIP_MEMORY_SCOPE_AGENT);
  }
  STAMP(2, 5);
  {
    const u16* t = (const u16*)(dsm + L_W2S);
    const int g = lane >> 4, r = lane & 15;
    const int nl = 32 * (w >> 1) + 8 * (r >> 2) + 4 * (w & 1) + (r & 3);
    u4v o;
#pragma unroll
    for (int jj = 0; jj < 4; ++jj)
      o[jj] = (unsigned)t[(g * 8 + 2 * jj) * 136 + nl] | ((unsigned)t[(g * 8 + 2 * jj + 1) * 136 + nl] << 16);
    if (bid < 224) ST128(o, OFF_W2F + ((((tk & 3) * 8 + w) * 56 + (tk >> 2)) * 64 + lane) * 16);
  }
  {
    const char* lw = dsm + L_W1 + (lane >> 2) * 224 + (lane & 3) * 8 + w * (2 * 7168);
    u4v fr[14];
#pragma unroll
    for (int i = 0; i < 14; ++i) fr[i] = w1frag(lw, i % 7, i / 7);
    asm volatile("" ::: "memory");
#pragma unroll
    for (int i = 0; i < 14; ++i) *(u4v*)(dsm + L_W1 + w * (2 * 7168) + i * 1024 + lane * 16) = fr[i];
  }
  STAMP(2, 6);
  {
    f2* zs = (f2*)(dsm + L_ZS);
    const int rp = tid & 3, k = tid >> 2;
    const int k2 = (k == 0) ? 128 : 256 - k;
    u4v xr;
    const unsigned* xrow = (const unsigned*)(x + (size_t)(row0 + w) * D_);
#pragma unroll
    for (int j = 0; j < 4; ++j) xr[j] = xrow[lane + 64 * j];
    const u4v gav = LD128(OFF_MODC + lane * 16);
    const u4v sbv = LD128(OFF_MODC + 1024 + lane * 16);
    const u4v cbv = LD128(OFF_MODC + 2048 + lane * 16);
    f2 wq = twid((float)k * (1.0f / 512.0f));
    PIN2(wq);
    const int pa = zpos(k), pb = zpos(k2);
    if (w == 0) wait_cols((gu32*)(ws + OFF_FLG + ((bid % NREP) * 256 + lane) * FSP));
    DRAIN();
    asm volatile("s_barrier" ::: "memory");
    __builtin_amdgcn_fence(__ATOMIC_ACQUIRE, "wavefront");
    if (tid == 0) __hip_atomic_store((gu32*)(ws + OFF_FLS + bid * 64), 1u, __ATOMIC_RELAXED, __HIP_MEMORY_SCOPE_AGENT);
    STAMP(3, 0);
    const f4 ya = __builtin_bit_cast(f4, LD128(OFF_ZT + (k * CP + row0 + 2 * rp) * 8));
    const f4 yb = __builtin_bit_cast(f4, LD128(OFF_ZT + (k2 * CP + row0 + 2 * rp) * 8));
#pragma unroll
    for (int h = 0; h < 2; ++h) {
      f2 a = f2{ya[2 * h], ya[2 * h + 1]}, b = f2{yb[2 * h], yb[2 * h + 1]};
      f2* zr = zs + (2 * rp + h) * ZRS;
      f2 Ze = f2{a.x + b.x, a.y - b.y};
      f2 Dm = f2{a.x - b.x, a.y + b.y};
      f2 Zo = cmulc(Dm, wq);
      f2 za = f2{Ze.x - Zo.y, Ze.y + Zo.x};
      f2 zb = f2{Ze.x + Zo.y, -Ze.y + Zo.x};
      if (k == 0) {
        za = f2{a.x + a.y, a.x - a.y};
        zb = f2{2.f * b.x, -2.f * b.y};
      }
      zr[pa] = za;
      zr[pb] = zb;
    }
    STAMP(3, 1);
    __syncthreads();
    f2 v[4];
    {
      const f2* buf = zs + w * ZRS + lane + (lane >> 4);
#pragma unroll
      for (int r4 = 0; r4 < 4; ++r4) v[r4] = buf[68 * r4];
    }
    fft256_inv(v, lane, tw, [&](int i) __attribute__((always_inline)) {});
    {
      gu32* fs = (gu32*)(ws + OFF_FLS + ((7 * w + (lane < 7 ? lane : 0)) * 4 + (nt0 >> 3)) * 64);
#pragma nounroll
      for (unsigned spin = 0; spin < SPIN_CAP; ++spin) {
        const unsigned f = __hip_atomic_load(fs, __ATOMIC_RELAXED, __HIP_MEMORY_SCOPE_AGENT);
        if (__builtin_amdgcn_ballot_w64(f != 1u) == 0ull) break;
        __builtin_amdgcn_s_sleep(1);
      }
      __builtin_amdgcn_fence(__ATOMIC_ACQUIRE, "wavefront");
      asm volatile("" ::: "memory");
    }
    pf(16, 4);
    STAMP(3, 2);
    float s = 0.f;
    u4v x1p;
#pragma unroll
    for (int j = 0; j < 4; ++j) {
      f2 t = f2{bf_lo(xr[j]), bf_hi(xr[j])} + bfr2(f2{bf_lo(gav[j]), bf_hi(gav[j])} * bfr2(v[j]));
      x1p[j] = pack_bf(t.x, t.y);
      v[j] = f2{bf_lo(x1p[j]), bf_hi(x1p[j])};
      s += v[j].x + v[j].y;
    }
    ST128(x1p, OFF_X1 + ((row0 + w) * 64 + lane) * 16);
    s = wave_sum(s);
    const float meanf = s * (1.0f / D_);
    float q = 0.f;
#pragma unroll
    for (int j = 0; j < 4; ++j) {
      f2 d = v[j] - meanf;
      q += d.x * d.x + d.y * d.y;
    }
    q = wave_sum(q);
    const float mean = bfr(meanf);
    const float den = bfr(sqrtf(bfr(bfr(q * (1.0f / D_)) + 1e-5f)));
    const float rden = 1.0f / den;
    u4v hp;
#pragma unroll
    for (int j = 0; j < 4; ++j) {
      f2 t = __builtin_elementwise_fma(v[j] - mean, f2{bf_lo(cbv[j]), bf_hi(cbv[j])} * rden,
                                       f2{bf_lo(sbv[j]), bf_hi(sbv[j])});
      hp[j] = pack_bf(t.x, t.y);
    }
    *(u4v*)(dsm + L_ST + (lane * 9 + w) * 16) = hp;
    __syncthreads();
    {
      const int ch = tid >> 3, r = tid & 7;
      u4v o = *(const u4v*)(dsm + L_ST + (ch * 9 + r) * 16);
      const int mt = row0 >> 4, r16 = (row0 & 8) + r;
      ST128(o, OFF_H2F + ((mt * 16 + (ch >> 2)) * 64 + (ch & 3) * 16 + r16) * 16);
    }
  }
  STAMP(3, 3);
  DRAIN();
  __syncthreads();
  if (tid == 0) __hip_atomic_store(myA, 1u, __ATOMIC_RELAXED, __HIP_MEMORY_SCOPE_AGENT);
  {
    const int ch = tid >> 3, r = tid & 7;
    u4v t = LD128(OFF_H2F + (((row0 >> 4) * 16 + (ch >> 2)) * 64 + (ch & 3) * 16 + (row0 & 8) + r) * 16);
    asm volatile("" ::"v"(t));
  }
  STAMP(3, 4);
  wait_pair(depA);
  STAMP(3, 5);
  {
    u4v B[16];
#pragma unroll
    for (int ks = 0; ks < 16; ++ks) B[ks] = LD128(OFF_H2F + (((8 * p + w) * 16 + ks) * 64 + lane) * 16);
    f4 acc[7];
#pragma unroll
    for (int ft = 0; ft < 7; ++ft) acc[ft] = f4{0.f, 0.f, 0.f, 0.f};
    const char* lw = dsm + L_W1 + lane * 16;
    const int e = c >> 1, odd = c & 1;
    const int hb = (int)OFF_HF + (((8 * p + w) * 56 + 7 * e) * 64 + lane) * 16;
    u4v Af[2][7];
#pragma unroll
    for (int j = 0; j < 7; ++j) Af[0][j] = *(const u4v*)(lw + (g1_ks(0, j) * 7 + g1_ft(0, j)) * 1024);
#pragma unroll
    for (int g = 0; g < 17; ++g) {
#pragma unroll
      for (int j = 0; j < 7; ++j) {
        if (j < g1_n(g)) acc[g1_ft(g, j)] = mfma16(Af[g & 1][j], B[g1_ks(g, j)], acc[g1_ft(g, j)]);
        if (g + 1 < 17 && j < g1_n(g + 1))
          Af[(g + 1) & 1][j] = *(const u4v*)(lw + (g1_ks(g + 1, j) * 7 + g1_ft(g + 1, j)) * 1024);
        SCHED();
      }
      if (g >= 1 && g < 13) { pf_load(18 + 2 * g); pf_load(19 + 2 * g); }
      if (g >= 14) {
        const int i = g - 14;
        f4 a = acc[2 * i], b = acc[2 * i + 1];
        u4v o = u4v{silu_pk(a[0], a[1]), silu_pk(a[2], a[3]), silu_pk(b[0], b[1]), silu_pk(b[2], b[3])};
        ST128(o, hb + (odd * 3 + i) * 1024);
      }
      SCHED();
      if (g == 12) STAMP(3, 6);
    }
    f4 a = acc[6];
    u2v o = u2v{silu_pk(a[0], a[1]), silu_pk(a[2], a[3])};
    ST64(o, hb + 6 * 1024 + odd * 8);
  }
  STAMP(3, 7);
  DRAIN();
  __syncthreads();
  if (tid == 0) __hip_atomic_store(myB, 1u, __ATOMIC_RELAXED, __HIP_MEMORY_SCOPE_AGENT);
  {
    const int hb = (int)OFF_HF + (((8 * p + w) * 56 + 7 * (c >> 1) + 3 * (c & 1)) * 64 + lane) * 16;
    u4v t0 = LD128(hb), t1 = LD128(hb + 1024), t2 = LD128(hb + 2048);
    asm volatile("" ::"v"(t0), "v"(t1), "v"(t2));
  }
  wait_pair(depB);
  STAMP(3, 8);
  {
    const int Bb = (int)OFF_HF + ((mt0 * 56 + 7 * w) * 64 + lane) * 16;
    u4v B[7][4];
#pragma unroll
    for (int ks = 0; ks < 4; ++ks)
#pragma unroll
      for (int mt = 0; mt < 4; ++mt) B[ks][mt] = LD128(Bb + (mt * 56 + ks) * 1024);
    f4 acc[4][4];
#pragma unroll
    for (int i = 0; i < 4; ++i)
#pragma unroll
      for (int j = 0; j < 4; ++j) acc[i][j] = f4{0.f, 0.f, 0.f, 0.f};
#pragma unroll
    for (int ks = 0; ks < 7; ++ks) {
#pragma unroll
      for (int nt = 0; nt < 4; ++nt)
#pragma unroll
        for (int mt = 0; mt < 4; ++mt) acc[nt][mt] = mfma16(A2[ks * 4 + nt], B[ks][mt], acc[nt][mt]);
      if (ks + 4 < 7) {
#pragma unroll
        for (int mt = 0; mt < 4; ++mt) B[ks + 4][mt] = LD128(Bb + (mt * 56 + ks + 4) * 1024);
      }
      SCHED();
    }
    STAMP(3, 9);
    const int g = lane >> 4, mr = lane & 15;
    const int fm = w >> 1, P = w & 1;
    const int m = (mt0 + fm) * 16 + mr;
    const int n = nt0 * 16 + 32 * P + 8 * g;
    unsigned xv[4];
#pragma unroll
    for (int i = 0; i < 4; ++i)
      xv[i] = __builtin_amdgcn_raw_buffer_load_b32(
          rs, (int)OFF_X1 + (m * 64 + (((n >> 1) + i) & 63)) * 16 + (n >> 7) * 4, 0, AUX_SC1);
    const f4 g0 = __builtin_bit_cast(f4, LD128(OFF_MODF + (2560 + n) * 4));
    const f4 g1 = __builtin_bit_cast(f4, LD128(OFF_MODF + (2560 + n + 4) * 4));
    f4* red = (f4*)dsm;
#pragma unroll
    for (int nt = 0; nt < 4; ++nt)
#pragma unroll
      for (int mt = 0; mt < 4; ++mt) red[(w * 16 + nt * 4 + mt) * 64 + lane] = acc[nt][mt];
    __syncthreads();
    STAMP(3, 10);
    f4 s0 = f4{0.f, 0.f, 0.f, 0.f}, s1 = s0;
#pragma unroll
    for (int ww = 0; ww < 8; ++ww) {
      s0 += red[(ww * 16 + (2 * P) * 4 + fm) * 64 + lane];
      s1 += red[(ww * 16 + (2 * P + 1) * 4 + fm) * 64 + lane];
    }
    u4v o;
    {
      f2 t0 = bfr2(f2{g0[0], g0[1]} * bfr2(f2{s0[0], s0[1]})), t1 = bfr2(f2{g0[2], g0[3]} * bfr2(f2{s0[2], s0[3]}));
      f2 t2 = bfr2(f2{g1[0], g1[1]} * bfr2(f2{s1[0], s1[1]})), t3 = bfr2(f2{g1[2], g1[3]} * bfr2(f2{s1[2], s1[3]}));
      o[0] = pack_bf(bf_lo(xv[0]) + t0.x, bf_hi(xv[0]) + t0.y);
      o[1] = pack_bf(bf_lo(xv[1]) + t1.x, bf_hi(xv[1]) + t1.y);
      o[2] = pack_bf(bf_lo(xv[2]) + t2.x, bf_hi(xv[2]) + t2.y);
      o[3] = pack_bf(bf_lo(xv[3]) + t3.x, bf_hi(xv[3]) + t3.y);
    }
    *(u4v*)(out + (size_t)m * D_ + n) = o;
  }
  STAMP(3, 11);
}

extern "C" void kernel_launch(void* const* d_in, const int* in_sizes, int n_in, void* d_out, int out_size, void* d_ws,
                              size_t ws_size, hipStream_t stream) {
  const u16* x = (const u16*)d_in[0];
  const u16* wr = (const u16*)d_in[1];
  const u16* wi = (const u16*)d_in[2];
  const u16* w1 = (const u16*)d_in[4];
  const u16* w2 = (const u16*)d_in[5];
  const u16* c = (const u16*)d_in[6];
  const u16* wmod = (const u16*)d_in[7];
  char* ws = (char*)d_ws;
  u16* out = (u16*)d_out;
  (void)hipFuncSetAttribute((const void*)kmega, hipFuncAttributeMaxDynamicSharedMemorySize, L_TOTAL);
  k0_mod<<<K0_GEMV + K0_WT + K0_W2 + K0_Z + K0_WARM, 512, 0, stream>>>(c, wmod, x, wr, wi, ws);
  kmega<<<256, 512, L_TOTAL, stream>>>(x, w1, w2, ws, out);
}
```

```cpp
#include <hip/hip_runtime.h>
#include <hip/hip_bf16.h>
#include <stdint.h>

typedef float f2 __attribute__((ext_vector_type(2)));
typedef float f4 __attribute__((ext_vector_type(4)));
typedef unsigned u2v __attribute__((ext_vector_type(2)));
typedef unsigned u4v __attribute__((ext_vector_type(4)));
typedef short bf16x8 __attribute__((ext_vector_type(8)));
typedef unsigned short u16;

constexpr int S_ = 2048, D_ = 512, F_ = 1792, DF_ = 257;
constexpr int NSPLIT = 4;

constexpr size_t OFF_CNT = 0;
#define FSPACE 16
#define FREP 16
constexpr int FSP = FSPACE, NREP = FREP;
constexpr size_t FTAB = (size_t)NREP * 256 * FSP;
constexpr size_t OFF_FLG = 16384;
constexpr size_t OFF_FLR = OFF_FLG + FTAB;
constexpr size_t OFF_FLS = OFF_FLR + FTAB;
constexpr size_t FLAG_BYTES = OFF_FLS + 16384;
constexpr size_t OFF_MODP = 900 << 10;
constexpr size_t OFF_MODF = OFF_MODP + (size_t)NSPLIT * 3072 * 4;
constexpr size_t OFF_MODC = OFF_MODF + 3072 * 4;
constexpr size_t OFF_WT = 1 << 20;
constexpr size_t OFF_XT = 4 << 20;
constexpr size_t OFF_ZT = 28 << 20;
constexpr size_t OFF_H2F = 12 << 20;
constexpr size_t OFF_X1 = 14 << 20;
constexpr size_t OFF_W2F = 18 << 20;
constexpr size_t OFF_HF = 20 << 20;
constexpr int WS_SPAN = 36 << 20;
#define CPITCH (2048 + 32)
constexpr int CP = CPITCH;
constexpr int WT_PITCH = 520;
constexpr float FSC = 1.0f / (2.0f * 2048.0f * 512.0f);

#define STAMP(kid, i)

__device__ __forceinline__ float bf_lo(unsigned u) { return __builtin_bit_cast(float, u << 16); }
__device__ __forceinline__ float bf_hi(unsigned u) { return __builtin_bit_cast(float, u & 0xffff0000u); }
__device__ __forceinline__ float bfr(float x) {
  return (float)(__bf16)x;
}
__device__ __forceinline__ unsigned pack_bf(float lo, float hi) {
  typedef __bf16 b2 __attribute__((ext_vector_type(2)));
  return __builtin_bit_cast(unsigned, __builtin_convertvector(f2{lo, hi}, b2));
}
__device__ __forceinline__ f2 cmul(f2 a, f2 b) {
  f2 t = a * f2{b.x, b.x};
  return __builtin_elementwise_fma(f2{a.y, a.x}, f2{-b.y, b.y}, t);
}
__device__ __forceinline__ f2 cmulc(f2 a, f2 b) {
  f2 t = a * f2{b.x, b.x};
  return __builtin_elementwise_fma(f2{a.y, a.x}, f2{b.y, -b.y}, t);
}
__device__ __forceinline__ f2 bfr2(f2 a) {
  unsigned u = pack_bf(a.x, a.y);
  asm("" : "+v"(u));
  return f2{bf_lo(u), bf_hi(u)};
}
__device__ __forceinline__ float msel(unsigned m, float a, float b) {
  unsigned ua = __builtin_bit_cast(unsigned, a), ub = __builtin_bit_cast(unsigned, b);
  return __builtin_bit_cast(float, (m & ua) | (~m & ub));
}
#define PIN2(x) asm volatile("" : "+v"(x))
constexpr int AUX_SC1 = 16;
#define WS_RSRC(ws) __builtin_amdgcn_make_buffer_rsrc((ws), 0, WS_SPAN, 0x00020000)
#define ST128(v, off) __builtin_amdgcn_raw_buffer_store_b128((v), rs, (int)(off), 0, AUX_SC1)
#define ST64(v, off) __builtin_amdgcn_raw_buffer_store_b64((v), rs, (int)(off), 0, AUX_SC1)
#define ST32(v, off) __builtin_amdgcn_raw_buffer_store_b32((v), rs, (int)(off), 0, AUX_SC1)
#define LD64(off) __builtin_amdgcn_raw_buffer_load_b64(rs, (int)(off), 0, AUX_SC1)
#define LD32(off) __builtin_amdgcn_raw_buffer_load_b32(rs, (int)(off), 0, AUX_SC1)
#define LD128(off) __builtin_amdgcn_raw_buffer_load_b128(rs, (int)(off), 0, AUX_SC1)
__device__ __forceinline__ f2 twid(float rev) { return f2{__builtin_amdgcn_cosf(rev), -__builtin_amdgcn_sinf(rev)}; }

template <int DIR>
__device__ __forceinline__ f2 rot90(f2 a) {
  return DIR < 0 ? f2{a.y, -a.x} : f2{-a.y, a.x};
}
template <int DIR>
__device__ __forceinline__ void dft4(f2& c0, f2& c1, f2& c2, f2& c3) {
  f2 d0 = c0 + c2, d2 = c0 - c2, d1 = c1 + c3, e = c1 - c3;
  c0 = d0 + d1;
  c2 = d0 - d1;
  const f2 es = f2{e.y, e.x};
  c1 = __builtin_elementwise_fma(es, DIR < 0 ? f2{1.f, -1.f} : f2{-1.f, 1.f}, d2);
  c3 = __builtin_elementwise_fma(es, DIR < 0 ? f2{-1.f, 1.f} : f2{1.f, -1.f}, d2);
}

__device__ __forceinline__ void sw32(float& a, float& b) {
  auto r = __builtin_amdgcn_permlane32_swap(__builtin_bit_cast(unsigned, a), __builtin_bit_cast(unsigned, b), false, false);
  a = __builtin_bit_cast(float, (unsigned)r[0]);
  b = __builtin_bit_cast(float, (unsigned)r[1]);
}
__device__ __forceinline__ void sw16(float& a, float& b) {
  auto r = __builtin_amdgcn_permlane16_swap(__builtin_bit_cast(unsigned, a), __builtin_bit_cast(unsigned, b), false, false);
  a = __builtin_bit_cast(float, (unsigned)r[0]);
  b = __builtin_bit_cast(float, (unsigned)r[1]);
}
template <int CTRL, int BANK>
__device__ __forceinline__ float udpp(float old, float src) {
  return __builtin_bit_cast(float, __builtin_amdgcn_update_dpp(__builtin_bit_cast(int, old), __builtin_bit_cast(int, src),
                                                               CTRL, 0xf, BANK, false));
}
template <int CTRL>
__device__ __forceinline__ float mdpp(float src) {
  return __builtin_bit_cast(float, __builtin_amdgcn_mov_dpp(__builtin_bit_cast(int, src), CTRL, 0xf, 0xf, false));
}
__device__ __forceinline__ void xp_hi1(float& v0, float& v1, float& v2, float& v3) {
  sw32(v0, v2); sw32(v1, v3); sw16(v0, v1); sw16(v2, v3);
}
__device__ __forceinline__ void xp_mid1(float& v0, float& v1, float& v2, float& v3) {
  float a0 = udpp<0x128, 0xC>(v0, v2), a2 = udpp<0x128, 0x3>(v2, v0);
  float a1 = udpp<0x128, 0xC>(v1, v3), a3 = udpp<0x128, 0x3>(v3, v1);
  v0 = udpp<0x124, 0xA>(a0, a1); v1 = udpp<0x12C, 0x5>(a1, a0);
  v2 = udpp<0x124, 0xA>(a2, a3); v3 = udpp<0x12C, 0x5>(a3, a2);
}
__device__ __forceinline__ void xp_lo1(float& v0, float& v1, float& v2, float& v3, unsigned b1, unsigned b0) {
  float s0 = mdpp<0x4E>(msel(b1, v0, v2)), s1 = mdpp<0x4E>(msel(b1, v1, v3));
  float a0 = msel(b1, s0, v0), a2 = msel(b1, v2, s0), a1 = msel(b1, s1, v1), a3 = msel(b1, v3, s1);
  float t0 = mdpp<0xB1>(msel(b0, a0, a1)), t2 = mdpp<0xB1>(msel(b0, a2, a3));
  v0 = msel(b0, t0, a0); v1 = msel(b0, a1, t0); v2 = msel(b0, t2, a2); v3 = msel(b0, a3, t2);
}
#define XP_CPLX(NAME, FN, ...)                                        \
  {                                                                   \
    float x0 = v[0].x, x1 = v[1].x, x2 = v[2].x, x3 = v[3].x;         \
    float y0 = v[0].y, y1 = v[1].y, y2 = v[2].y, y3 = v[3].y;         \
    FN(x0, x1, x2, x3, ##__VA_ARGS__);                                \
    FN(y0, y1, y2, y3, ##__VA_ARGS__);                                \
    v[0] = f2{x0, y0}; v[1] = f2{x1, y1}; v[2] = f2{x2, y2}; v[3] = f2{x3, y3}; \
  }
__device__ __forceinline__ void xp_hi(f2 (&v)[4]) XP_CPLX(hi, xp_hi1)
__device__ __forceinline__ void xp_mid(f2 (&v)[4]) XP_CPLX(mid, xp_mid1)
__device__ __forceinline__ void xp_lo(f2 (&v)[4], unsigned b1, unsigned b0) XP_CPLX(lo, xp_lo1, b1, b0)

struct Tw256 {
  f2 a[3], b[3], c[3];
  unsigned m1, m0;
};
__device__ __forceinline__ void make_tw256(int lane, Tw256& t) {
#pragma unroll
  for (int r = 1; r < 4; ++r) {
    t.a[r - 1] = twid((float)(lane * r) * (1.0f / 256.0f));
    t.b[r - 1] = twid((float)((lane & 15) * r) * (1.0f / 64.0f));
    t.c[r - 1] = twid((float)((lane & 3) * r) * (1.0f / 16.0f));
  }
  t.m1 = 0u - (unsigned)((lane >> 1) & 1);
  t.m0 = 0u - (unsigned)(lane & 1);
#pragma unroll
  for (int r = 0; r < 3; ++r) {
    PIN2(t.a[r]);
    PIN2(t.b[r]);
    PIN2(t.c[r]);
  }
  PIN2(t.m1);
  PIN2(t.m0);
}
struct NoHook {
  __device__ __forceinline__ void operator()(int) const {}
};
template <class H>
__device__ __forceinline__ void fft256_fwd(f2 (&v)[4], int lane, const Tw256& t, const H& hook) {
  const unsigned b1 = t.m1, b0 = t.m0;
  dft4<-1>(v[0], v[1], v[2], v[3]);
#pragma unroll
  for (int r = 1; r < 4; ++r) v[r] = cmul(v[r], t.a[r - 1]);
  xp_hi(v);
  hook(0);
  dft4<-1>(v[0], v[1], v[2], v[3]);
#pragma unroll
  for (int r = 1; r < 4; ++r) v[r] = cmul(v[r], t.b[r - 1]);
  xp_mid(v);
  hook(1);
  dft4<-1>(v[0], v[1], v[2], v[3]);
#pragma unroll
  for (int r = 1; r < 4; ++r) v[r] = cmul(v[r], t.c[r - 1]);
  xp_lo(v, b1, b0);
  hook(2);
  dft4<-1>(v[0], v[1], v[2], v[3]);
}
template <class H>
__device__ __forceinline__ void fft256_inv(f2 (&v)[4], int lane, const Tw256& t, const H& hook) {
  const unsigned b1 = t.m1, b0 = t.m0;
  dft4<1>(v[0], v[1], v[2], v[3]);
  xp_lo(v, b1, b0);
  hook(0);
#pragma unroll
  for (int r = 1; r < 4; ++r) v[r] = cmulc(v[r], t.c[r - 1]);
  dft4<1>(v[0], v[1], v[2], v[3]);
  xp_mid(v);
  hook(1);
#pragma unroll
  for (int r = 1; r < 4; ++r) v[r] = cmulc(v[r], t.b[r - 1]);
  dft4<1>(v[0], v[1], v[2], v[3]);
  xp_hi(v);
  hook(2);
#pragma unroll
  for (int r = 1; r < 4; ++r) v[r] = cmulc(v[r], t.a[r - 1]);
  dft4<1>(v[0], v[1], v[2], v[3]);
}

__device__ __forceinline__ float wave_sum(float x) {
  x += mdpp<0xB1>(x);
  x += mdpp<0x4E>(x);
  x += mdpp<0x141>(x);
  x += mdpp<0x140>(x);
  float r0 = __builtin_bit_cast(float, __builtin_amdgcn_readlane(__builtin_bit_cast(int, x), 0));
  float r1 = __builtin_bit_cast(float, __builtin_amdgcn_readlane(__builtin_bit_cast(int, x), 16));
  float r2 = __builtin_bit_cast(float, __builtin_amdgcn_readlane(__builtin_bit_cast(int, x), 32));
  float r3 = __builtin_bit_cast(float, __builtin_amdgcn_readlane(__builtin_bit_cast(int, x), 48));
  return (r0 + r1) + (r2 + r3);
}

__device__ __forceinline__ int zpos(int k) { return 17 * (k & 3) + 4 * ((k >> 2) & 3) + ((k >> 4) & 3) + 68 * (k >> 6); }
constexpr int ZRS = 276;

constexpr int K0_GEMV = 48 * NSPLIT, K0_WT = 128, K0_W2 = 0, K0_WARM = 64, K0_Z = (int)(FLAG_BYTES / 8192);
__global__ __launch_bounds__(512) void k0_mod(const u16* __restrict__ c, const u16* __restrict__ wmod,
                                              const u16* __restrict__ x, const u16* __restrict__ wr,
                                              const u16* __restrict__ wi, const u16* __restrict__ w1,
                                              const u16* __restrict__ w2, const u16* __restrict__ outp,
                                              char* __restrict__ ws) {
  __shared__ __attribute__((aligned(16))) char smem[17408];
  const int tid = threadIdx.x, row = tid >> 6, lane = tid & 63;
  const auto rs = WS_RSRC(ws);
  int b = blockIdx.x;
  STAMP(0, 0);
  if (b >= K0_GEMV + K0_WT + K0_W2) {
    int q = b - (K0_GEMV + K0_WT + K0_W2);
    if (q < K0_Z) {
      ((u4v*)ws)[q * 512 + tid] = u4v{0u, 0u, 0u, 0u};
      return;
    }
    q -= K0_Z;
    if (q >= K0_WARM) {
      const unsigned a0 = *(const unsigned*)(ws + (size_t)(tid + 16) * 65536);
      const char* p1 = tid < 48 ? ws + (size_t)(tid + 528) * 65536
                                : (tid < 80 ? (const char*)outp + (size_t)(tid - 48) * 65536
                                            : (tid < 108 ? (const char*)w1 + (size_t)(tid - 80) * 65536
                                                         : (const char*)w2 + (size_t)(tid < 136 ? tid - 108 : 0) * 65536));
      const unsigned a1 = *(const unsigned*)p1;
      asm volatile("" ::"v"(a0), "v"(a1));
      return;
    }
    const char* src = (const char*)x + (size_t)q * 32768;
    u4v t[4];
#pragma unroll
    for (int i = 0; i < 4; ++i) t[i] = *(const u4v*)(src + i * 8192 + tid * 16);
#pragma unroll
    for (int i = 0; i < 4; ++i) asm volatile("" ::"v"(t[i]));
    STAMP(0, 1);
    return;
  }
  if (b >= K0_GEMV) {
    b -= K0_GEMV;
    if (b < K0_WT) {
      char* sr = smem;
      char* si = smem + 16 * 544;
      constexpr int LASTCH = (S_ * DF_ * 2) / 16 - 1;
      for (int it = tid; it < 16 * 34; it += 512) {
        const int i = it / 34, ch = it % 34;
        int gch = ((b + 128 * i) * (DF_ * 2)) / 16 + ch;
        gch = gch > LASTCH ? LASTCH : gch;
        *(u4v*)(sr + i * 544 + ch * 16) = *(const u4v*)((const char*)wr + (size_t)gch * 16);
        *(u4v*)(si + i * 544 + ch * 16) = *(const u4v*)((const char*)wi + (size_t)gch * 16);
      }
      __syncthreads();
      const int tbase = 64 * (b & 7) + 16 * ((b >> 3) & 3) + 4 * ((b >> 5) & 3);
      for (int it = tid; it < DF_ * 4; it += 512) {
        const int k = it >> 2, r3 = it & 3;
        u4v o;
#pragma unroll
        for (int r4 = 0; r4 < 4; ++r4) {
          const int i = r3 + 4 * r4;
          const int off = i * 544 + (((b + 128 * i) * (DF_ * 2)) & 15) + 2 * k;
          o[r4] = (unsigned)*(const u16*)(sr + off) | ((unsigned)*(const u16*)(si + off) << 16);
        }
        ST128(o, OFF_WT + (k * 512 + tbase + r3) * 16);
      }
    }
    STAMP(0, 1);
    return;
  }
  float* red = (float*)smem;
  const int cgp = b % 48, ks = b / 48;
  const int cg = tid & 7, r = tid >> 3;
  const int rw = ks * 128 + r;
  const u4v wm0 = *(const u4v*)(wmod + (size_t)rw * 3072 + cgp * 64 + cg * 8);
  const u4v wm1 = *(const u4v*)(wmod + (size_t)(rw + 64) * 3072 + cgp * 64 + cg * 8);
  const float cv0 = bf_lo(c[rw]), cv1 = bf_lo(c[rw + 64]);
  const float s0 = bfr(cv0 / (1.0f + __expf(-cv0))), s1 = bfr(cv1 / (1.0f + __expf(-cv1)));
  float acc[8];
#pragma unroll
  for (int i = 0; i < 4; ++i) {
    acc[2 * i] = s0 * bf_lo(wm0[i]) + s1 * bf_lo(wm1[i]);
    acc[2 * i + 1] = s0 * bf_hi(wm0[i]) + s1 * bf_hi(wm1[i]);
  }
#pragma unroll
  for (int i = 0; i < 8; ++i) {
    acc[i] += mdpp<0x128>(acc[i]);
    acc[i] += __shfl_xor(acc[i], 16);
    acc[i] += __shfl_xor(acc[i], 32);
  }
  if (lane < 8) {
#pragma unroll
    for (int i = 0; i < 8; ++i) red[row * 64 + cg * 8 + i] = acc[i];
  }
  __syncthreads();
  if (tid < 64) {
    float sum = 0.f;
#pragma unroll
    for (int i = 0; i < 8; ++i) sum += red[i * 64 + tid];
    ((float*)(ws + OFF_MODP))[ks * 3072 + cgp * 64 + tid] = sum;
  }
  STAMP(0, 1);
}

__device__ __forceinline__ f4 mfma16(u4v a, u4v b, f4 c) {
  return __builtin_amdgcn_mfma_f32_16x16x32_bf16(__builtin_bit_cast(bf16x8, a), __builtin_bit_cast(bf16x8, b), c, 0, 0,
                                                  0);
}
__device__ __forceinline__ f2 silu2(float a, float b) {
  f2 h = f2{a, b};
  f2 t = h * -1.44269504088896340736f;
  f2 d = f2{__builtin_amdgcn_exp2f(t.x), __builtin_amdgcn_exp2f(t.y)} + 1.0f;
  return h * f2{__builtin_amdgcn_rcpf(d.x), __builtin_amdgcn_rcpf(d.y)};
}
__device__ __forceinline__ unsigned silu_pk(float a, float b) {
  f2 r = silu2(a, b);
  return pack_bf(r.x, r.y);
}
typedef __attribute__((address_space(1))) unsigned gu32;
#define DRAIN() asm volatile("s_waitcnt vmcnt(0)" ::: "memory")
typedef __attribute__((address_space(1))) unsigned long long gu64;
constexpr unsigned SPIN_CAP = 1u << 18;
__device__ __forceinline__ void wait_pair(gu64* flags) {
#pragma nounroll
  for (unsigned spin = 0; spin < SPIN_CAP; ++spin) {
    if (__hip_atomic_load(flags, __ATOMIC_RELAXED, __HIP_MEMORY_SCOPE_AGENT) == 0x0000000100000001ull) break;
    __builtin_amdgcn_s_sleep(1);
  }
  __builtin_amdgcn_fence(__ATOMIC_ACQUIRE, "wavefront");
  asm volatile("" ::: "memory");
}

#define PSLEEP 2
__device__ __forceinline__ void wait_cols(gu32* flags) {
#pragma nounroll
  for (unsigned spin = 0; spin < SPIN_CAP; ++spin) {
    unsigned f = 1u;
#pragma unroll
    for (int j = 0; j < 4; ++j)
      f &= __hip_atomic_load(flags + j * 64 * (FSP / 4), __ATOMIC_RELAXED, __HIP_MEMORY_SCOPE_AGENT);
    if (__builtin_amdgcn_ballot_w64(f != 1u) == 0ull) break;
    __builtin_amdgcn_s_sleep(PSLEEP);
  }
}

__device__ __forceinline__ int wt_off(int k, int t) { return (int)OFF_WT + (k * 512 + t) * 16; }

constexpr int L_W1 = 0;
constexpr int L_T = 112 * 1024;
constexpr int L_ZS = 112 * 1024;
constexpr int L_ST = L_ZS + 8 * ZRS * 8;
constexpr int L_TOTAL = 160 * 1024;
__device__ __forceinline__ constexpr int g1_n(int g) { return g < 13 ? 7 : (g < 16 ? 6 : 3); }
__device__ __forceinline__ constexpr int g1_ft(int g, int j) { return g < 13 ? j : (g < 16 ? 2 * (g - 13) + (j & 1) : 6); }
__device__ __forceinline__ constexpr int g1_ks(int g, int j) { return g < 13 ? g : (g < 16 ? 13 + (j >> 1) : 13 + j); }
#define SCHED() __builtin_amdgcn_sched_barrier(0)
typedef short v4i16 __attribute__((ext_vector_type(4)));
__device__ __forceinline__ u4v w1frag(const char* lw, int ft, int ks) {
  typedef __attribute__((address_space(3))) v4i16* lp;
  const u2v a = __builtin_bit_cast(u2v, __builtin_amdgcn_ds_read_tr16_b64_v4i16((lp)(lw + (2 * ks) * 3584 + ft * 32)));
  const u2v b = __builtin_bit_cast(u2v, __builtin_amdgcn_ds_read_tr16_b64_v4i16((lp)(lw + (2 * ks + 1) * 3584 + ft * 32)));
  return u4v{a[0], a[1], b[0], b[1]};
}
constexpr int L_MODS = L_ZS + 8 * ZRS * 8;
constexpr int L_W2S = L_T + 32768;
__global__ __launch_bounds__(512, 2) void kmega(const u16* __restrict__ x, const u16* __restrict__ w1,
                                                const u16* __restrict__ w2, char* ws, u16* __restrict__ out) {
  extern __shared__ __attribute__((aligned(16))) char dsm[];
  const int tid = threadIdx.x, lane = tid & 63;
  const int w = __builtin_amdgcn_readfirstlane(tid >> 6);
  const int bid = blockIdx.x;
  const int p = (bid & 7) * 2 + (bid >> 7), c = (bid >> 3) & 15;
  const int row0 = 128 * p + 8 * c;
  STAMP(1, 0);
  const auto rs = WS_RSRC(ws);
  gu32* myA = (gu32*)(ws + OFF_CNT) + (p * 8 + (c >> 1)) * 16 + (c & 1);
  gu32* myB = myA + 128 * 16;
  gu64* depA = (gu64*)(ws + OFF_CNT) + (p * 8 + w) * 8;
  gu64* depB = depA + 128 * 8;
  const int mt0 = 8 * p + 4 * (c >> 3), nt0 = 4 * (c & 7);
  const int l56 = lane < 56 ? lane : lane - 56;
  const int r4_ = l56 / 14, ch_ = l56 - 14 * r4_;
  const char* w1g = (const char*)w1 + (size_t)(4 * w + r4_) * (F_ * 2) + c * 224 + ch_ * 16;
  char* w1l = dsm + L_W1 + ((w >> 1) * 32 + (2 * (w & 1) + (r4_ >> 1)) * 4 + (r4_ & 1)) * 224 + ch_ * 16;
  const int Ab = (int)OFF_W2F + ((nt0 * 56 + 7 * w) * 64 + lane) * 16;
  u4v S1[16];
  u4v A2[28];
  auto pf_load = [&](int i) __attribute__((always_inline)) {
    if (i < 16) S1[i] = *(const u4v*)(w1g + (size_t)i * (32 * F_ * 2));
    else if (i < 44) A2[i - 16] = LD128(Ab + (((i - 16) & 3) * 56 + ((i - 16) >> 2)) * 1024);
  };
  auto pf_put = [&](int i) __attribute__((always_inline)) {
    *(u4v*)(w1l + ((i & 3) * 128 + (i >> 3) * 16 + ((i >> 2) & 1) * 2) * 224) = S1[i];
  };
  auto pf = [&](int i0, int n) __attribute__((always_inline)) {
    SCHED();
#pragma unroll
    for (int i = 0; i < n; ++i) pf_load(i0 + i);
    SCHED();
  };
  const int tk = bid < 224 ? bid : bid - 224;
  Tw256 tw;
  {
    f2* zs = (f2*)(dsm + L_ZS);
    float* mods = (float*)(dsm + L_MODS);
    const int r0 = bid * 8;
    unsigned xr[4];
    const unsigned* xrow = (const unsigned*)(x + (size_t)(r0 + w) * D_);
#pragma unroll
    for (int j = 0; j < 4; ++j) xr[j] = xrow[lane + 64 * j];
    const float* mp = (const float*)(ws + OFF_MODP);
    f2 sm = *(const f2*)(mp + tid * 2);
#pragma unroll
    for (int k = 1; k < NSPLIT; ++k) sm += *(const f2*)(mp + k * 3072 + tid * 2);
    const int idx = bid * 12 + (tid < 6 ? tid : 0) * 2;
    f2 fin = *(const f2*)(mp + idx);
#pragma unroll
    for (int k = 1; k < NSPLIT; ++k) fin += *(const f2*)(mp + k * 3072 + idx);
    SCHED();
    make_tw256(lane, tw);
    const int rp = tid & 3, kk = tid >> 2;
    f2 wpost = twid((float)kk * (1.0f / 512.0f));
    PIN2(wpost);
    f2 v[4];
    float s = 0.f;
#pragma unroll
    for (int j = 0; j < 4; ++j) {
      v[j] = f2{bf_lo(xr[j]), bf_hi(xr[j])};
      s += v[j].x + v[j].y;
    }
    s = wave_sum(s);
    const float meanf = s * (1.0f / D_);
    float q = 0.f;
#pragma unroll
    for (int j = 0; j < 4; ++j) {
      f2 d = v[j] - meanf;
      q += d.x * d.x + d.y * d.y;
    }
    q = wave_sum(q);
    const float mean = bfr(meanf);
    const float den = bfr(sqrtf(bfr(bfr(q * (1.0f / D_)) + 1e-5f)));
    const float rden = 1.0f / den;
    sm = bfr2(sm);
    if (tid >= 256) sm = bfr2(sm + 1.0f);
    *(f2*)(mods + tid * 2) = sm;
    if (tid < 6) {
      fin = bfr2(fin);
      ST64(__builtin_bit_cast(u2v, fin), OFF_MODF + idx * 4);
      const int vec = (idx >> 9) - 2, d = idx & 511;
      if (vec >= 0 && vec < 3) {
        if (vec == 2) fin += 1.0f;
        ST32(pack_bf(fin.x, fin.y), OFF_MODC + ((vec * 64 + ((d >> 1) & 63)) * 4 + (d >> 7)) * 4);
      }
    }
    STAMP(1, 3);
    __syncthreads();
    STAMP(1, 4);
    pf(0, 4);
#pragma unroll
    for (int j = 0; j < 4; ++j) {
      const int d = 2 * (lane + 64 * j);
      f2 sa = *(const f2*)(mods + d);
      f2 ca1 = *(const f2*)(mods + 512 + d);
      v[j] = bfr2(__builtin_elementwise_fma(v[j] - mean, ca1 * rden, sa));
    }
    fft256_fwd(v, lane, tw, NoHook{});
    {
      f2* buf = zs + w * ZRS + lane + (lane >> 4);
#pragma unroll
      for (int r4 = 0; r4 < 4; ++r4) buf[68 * r4] = v[r4];
    }
    STAMP(1, 5);
    __syncthreads();
    {
      const int k = kk;
      const int k2 = (k == 0) ? 128 : 256 - k;
      const int pa = zpos(k), pb = zpos(k2);
      f4 oa, ob;
#pragma unroll
      for (int h = 0; h < 2; ++h) {
        const f2* zr = zs + (2 * rp + h) * ZRS;
        f2 za = zr[pa], zb = zr[pb];
        f2 E = f2{za.x + zb.x, za.y - zb.y};
        f2 Dm = f2{za.x - zb.x, za.y + zb.y};
        f2 O = f2{Dm.y, -Dm.x};
        f2 wo = cmul(wpost, O);
        f2 ra = E + wo;
        f2 t = E - wo;
        f2 rb = f2{t.x, -t.y};
        if (k == 0) {
          ra = f2{2.f * (za.x + za.y), 2.f * (za.x - za.y)};
          rb = f2{2.f * zb.x, -2.f * zb.y};
        }
        oa[2 * h] = ra.x; oa[2 * h + 1] = ra.y;
        ob[2 * h] = rb.x; ob[2 * h + 1] = rb.y;
      }
      ST128(__builtin_bit_cast(u4v, oa), OFF_XT + (k * CP + r0 + 2 * rp) * 8);
      ST128(__builtin_bit_cast(u4v, ob), OFF_XT + (k2 * CP + r0 + 2 * rp) * 8);
    }
    STAMP(1, 6);
    DRAIN();
    __syncthreads();
    if (tid < NREP)
      __hip_atomic_store((gu32*)(ws + OFF_FLR + (tid * 256 + bid) * FSP), 1u, __ATOMIC_RELAXED, __HIP_MEMORY_SCOPE_AGENT);
    STAMP(1, 7);
  }
  {
    f2* T = (f2*)(dsm + L_T);
    f2* T2 = (f2*)(dsm + L_T + 16384);
    f2* T3 = (f2*)(dsm + L_T + 32768);
    const int k = bid;
    f2 tw1[4];
#pragma unroll
    for (int j = 0; j < 4; ++j) {
      tw1[j] = twid((float)((lane + 64 * j) * w) * (1.0f / 2048.0f));
      PIN2(tw1[j]);
    }
    if (w == 0) wait_cols((gu32*)(ws + OFF_FLR + ((bid % NREP) * 256 + lane) * FSP));
    asm volatile("s_barrier" ::: "memory");
    __builtin_amdgcn_fence(__ATOMIC_ACQUIRE, "wavefront");
    STAMP(2, 0);
    f2 v[4];
#pragma unroll
    for (int j = 0; j < 4; ++j) v[j] = __builtin_bit_cast(f2, LD64(OFF_XT + (k * CP + tid + 512 * j) * 8));
    u4v bv;
    unsigned wn[4], bn[4];
    int mirr[4];
#pragma unroll
    for (int j = 0; j < 4; ++j) PIN2(v[j]);
    pf(4, 4);
    const int hb = w >> 2, a = tid & 255;
    constexpr float R = 0.70710678118654752440f;
    dft4<-1>(v[0], v[1], v[2], v[3]);
    if (hb) {
      v[1] = f2{v[1].x + v[1].y, v[1].y - v[1].x} * R;
      v[2] = f2{v[2].y, -v[2].x};
      v[3] = f2{v[3].y - v[3].x, -v[3].x - v[3].y} * R;
    }
#pragma unroll
    for (int q = 0; q < 4; ++q) T[(hb * 4 + q) * 256 + a] = v[q];
    STAMP(2, 1);
    const u4v wv = LD128(wt_off(k, tid));
    u4v w2a;
    {
      const int ks2 = tk >> 2, rr = tid >> 4, gg = rr >> 3, j = rr & 7;
      const int e = ks2 / 7, i7 = ks2 - 7 * e;
      const int T0 = 14 * e + (i7 < 3 ? 2 * i7 : (i7 < 6 ? 2 * i7 + 1 : 6));
      const int T1 = i7 < 6 ? T0 + 1 : 14 * e + 13;
      const int f = (j < 4) ? (16 * T0 + 4 * gg + j) : (16 * T1 + 4 * gg + (j - 4));
      w2a = *(const u4v*)(w2 + (size_t)f * D_ + (tk & 3) * 128 + (tid & 15) * 8);
    }
    if (k == 0) {
      bv = LD128(wt_off(256, tid));
      const int m0 = (lane >> 4) + 4 * ((lane >> 2) & 3) + 16 * (lane & 3);
#pragma unroll
      for (int r4 = 0; r4 < 4; ++r4) {
        const int un = (S_ - (w + 8 * (m0 + 64 * r4))) & (S_ - 1);
        const int mm = un >> 3;
        mirr[r4] = ((un & 7) * 64 + 16 * (mm & 3) + 4 * ((mm >> 2) & 3) + ((mm >> 4) & 3)) * 4 + (mm >> 6);
      }
      if (w != 0) {
        const u4v a4 = LD128(wt_off(0, 64 * (8 - w) + 63 - lane));
        const u4v b4 = LD128(wt_off(256, 64 * (8 - w) + 63 - lane));
#pragma unroll
        for (int r4 = 0; r4 < 4; ++r4) { wn[r4] = a4[3 - r4]; bn[r4] = b4[3 - r4]; }
      } else {
#pragma unroll
        for (int r4 = 0; r4 < 4; ++r4) {
          wn[r4] = LD32(wt_off(0, mirr[r4] >> 2) + (mirr[r4] & 3) * 4);
          bn[r4] = LD32(wt_off(256, mirr[r4] >> 2) + (mirr[r4] & 3) * 4);
        }
      }
    }
    __syncthreads();
    {
      const int q = w & 3;
      const float sg = (w < 4) ? 1.f : -1.f;
#pragma unroll
      for (int j = 0; j < 4; ++j) {
        f2 e = T[q * 256 + lane + 64 * j], o = T[(4 + q) * 256 + lane + 64 * j];
        v[j] = cmul(e + o * sg, tw1[j]);
      }
    }
    fft256_fwd(v, lane, tw, [&](int i) __attribute__((always_inline)) { if (i != 1) pf(8 + 2 * i, 4); });
    STAMP(2, 2);
    if (k != 0) {
#pragma unroll
      for (int i = 0; i < 4; ++i) {
        f2 wf = f2{bf_lo(wv[i]), bf_hi(wv[i])} * FSC;
        v[i] = cmul(v[i], wf);
      }
    } else {
#pragma unroll
      for (int r4 = 0; r4 < 4; ++r4) T3[r4 * 512 + tid] = v[r4];
      __syncthreads();
#pragma unroll
      for (int r4 = 0; r4 < 4; ++r4) {
        f2 cm = T3[(mirr[r4] & 3) * 512 + (mirr[r4] >> 2)];
        f2 h0 = f2{bf_lo(wv[r4]) + bf_lo(wn[r4]), bf_hi(wv[r4]) - bf_hi(wn[r4])};
        f2 h1 = f2{bf_lo(bv[r4]) + bf_lo(bn[r4]), bf_hi(bv[r4]) - bf_hi(bn[r4])};
        f2 wp = (h0 + h1) * (0.25f * FSC), wm = (h0 - h1) * (0.25f * FSC);
        f2 cc = f2{cm.x, -cm.y};
        v[r4] = cmul(v[r4], wp) + cmul(cc, wm);
      }
    }
    fft256_inv(v, lane, tw, [&](int i) __attribute__((always_inline)) {
      SCHED();
#pragma unroll
      for (int q = 0; q < 4; ++q) pf_put(4 * i + q);
      SCHED();
    });
#pragma unroll
    for (int j = 0; j < 4; ++j) T2[w * 256 + lane + 64 * j] = cmulc(v[j], tw1[j]);
#pragma unroll
    for (int q = 12; q < 16; ++q) pf_put(q);
    STAMP(2, 3);
    __syncthreads();
    {
      const float sg = hb ? -1.f : 1.f;
#pragma unroll
      for (int q = 0; q < 4; ++q) v[q] = T2[q * 256 + a] + T2[(q + 4) * 256 + a] * sg;
      if (hb) {
        v[1] = f2{v[1].x - v[1].y, v[1].y + v[1].x} * R;
        v[2] = f2{-v[2].y, v[2].x};
        v[3] = f2{-v[3].x - v[3].y, v[3].x - v[3].y} * R;
      }
    }
    dft4<1>(v[0], v[1], v[2], v[3]);
#pragma unroll
    for (int j = 0; j < 4; ++j) ST64(__builtin_bit_cast(u2v, v[j]), OFF_ZT + (k * CP + tid + 512 * j) * 8);
    STAMP(2, 4);
    *(u4v*)(dsm + L_W2S + ((tid >> 4) * 136 + (tid & 15) * 8) * 2) = w2a;
    DRAIN();
    __syncthreads();
    if (tid < NREP)
      __hip_atomic_store((gu32*)(ws + OFF_FLG + (tid * 256 + k) * FSP), 1u, __ATOMIC_RELAXED, __HIP_MEMORY_SCOPE_AGENT);
  }
  STAMP(2, 5);
  {
    const u16* t = (const u16*)(dsm + L_W2S);
    const int g = lane >> 4, r = lane & 15;
    const int nl = 32 * (w >> 1) + 8 * (r >> 2) + 4 * (w & 1) + (r & 3);
    u4v o;
#pragma unroll
    for (int jj = 0; jj < 4; ++jj)
      o[jj] = (unsigned)t[(g * 8 + 2 * jj) * 136 + nl] | ((unsigned)t[(g * 8 + 2 * jj + 1) * 136 + nl] << 16);
    if (bid < 224) ST128(o, OFF_W2F + ((((tk & 3) * 8 + w) * 56 + (tk >> 2)) * 64 + lane) * 16);
  }
  {
    const char* lw = dsm + L_W1 + (lane >> 2) * 224 + (lane & 3) * 8 + w * (2 * 7168);
    u4v fr[14];
#pragma unroll
    for (int i = 0; i < 14; ++i) fr[i] = w1frag(lw, i % 7, i / 7);
    asm volatile("" ::: "memory");
#pragma unroll
    for (int i = 0; i < 14; ++i) *(u4v*)(dsm + L_W1 + w * (2 * 7168) + i * 1024 + lane * 16) = fr[i];
  }
  STAMP(2, 6);
  {
    f2* zs = (f2*)(dsm + L_ZS);
    const int rp = tid & 3, k = tid >> 2;
    const int k2 = (k == 0) ? 128 : 256 - k;
    u4v xr;
    const unsigned* xrow = (const unsigned*)(x + (size_t)(row0 + w) * D_);
#pragma unroll
    for (int j = 0; j < 4; ++j) xr[j] = xrow[lane + 64 * j];
    const u4v gav = LD128(OFF_MODC + lane * 16);
    const u4v sbv = LD128(OFF_MODC + 1024 + lane * 16);
    const u4v cbv = LD128(OFF_MODC + 2048 + lane * 16);
    f2 wq = twid((float)k * (1.0f / 512.0f));
    PIN2(wq);
    const int pa = zpos(k), pb = zpos(k2);
    if (w == 0) wait_cols((gu32*)(ws + OFF_FLG + ((bid % NREP) * 256 + lane) * FSP));
    DRAIN();
    asm volatile("s_barrier" ::: "memory");
    __builtin_amdgcn_fence(__ATOMIC_ACQUIRE, "wavefront");
    if (tid == 0) __hip_atomic_store((gu32*)(ws + OFF_FLS + bid * 64), 1u, __ATOMIC_RELAXED, __HIP_MEMORY_SCOPE_AGENT);
    STAMP(3, 0);
    const f4 ya = __builtin_bit_cast(f4, LD128(OFF_ZT + (k * CP + row0 + 2 * rp) * 8));
    const f4 yb = __builtin_bit_cast(f4, LD128(OFF_ZT + (k2 * CP + row0 + 2 * rp) * 8));
#pragma unroll
    for (int h = 0; h < 2; ++h) {
      f2 a = f2{ya[2 * h], ya[2 * h + 1]}, b = f2{yb[2 * h], yb[2 * h + 1]};
      f2* zr = zs + (2 * rp + h) * ZRS;
      f2 Ze = f2{a.x + b.x, a.y - b.y};
      f2 Dm = f2{a.x - b.x, a.y + b.y};
      f2 Zo = cmulc(Dm, wq);
      f2 za = f2{Ze.x - Zo.y, Ze.y + Zo.x};
      f2 zb = f2{Ze.x + Zo.y, -Ze.y + Zo.x};
      if (k == 0) {
        za = f2{a.x + a.y, a.x - a.y};
        zb = f2{2.f * b.x, -2.f * b.y};
      }
      zr[pa] = za;
      zr[pb] = zb;
    }
    STAMP(3, 1);
    __syncthreads();
    f2 v[4];
    {
      const f2* buf = zs + w * ZRS + lane + (lane >> 4);
#pragma unroll
      for (int r4 = 0; r4 < 4; ++r4) v[r4] = buf[68 * r4];
    }
    fft256_inv(v, lane, tw, [&](int i) __attribute__((always_inline)) {});
    {
      gu32* fs = (gu32*)(ws + OFF_FLS + ((7 * w + (lane < 7 ? lane : 0)) * 4 + (nt0 >> 3)) * 64);
#pragma nounroll
      for (unsigned spin = 0; spin < SPIN_CAP; ++spin) {
        const unsigned f = __hip_atomic_load(fs, __ATOMIC_RELAXED, __HIP_MEMORY_SCOPE_AGENT);
        if (__builtin_amdgcn_ballot_w64(f != 1u) == 0ull) break;
        __builtin_amdgcn_s_sleep(1);
      }
      __builtin_amdgcn_fence(__ATOMIC_ACQUIRE, "wavefront");
      asm volatile("" ::: "memory");
    }
    pf(16, 4);
    STAMP(3, 2);
    float s = 0.f;
    u4v x1p;
#pragma unroll
    for (int j = 0; j < 4; ++j) {
      f2 t = f2{bf_lo(xr[j]), bf_hi(xr[j])} + bfr2(f2{bf_lo(gav[j]), bf_hi(gav[j])} * bfr2(v[j]));
      x1p[j] = pack_bf(t.x, t.y);
      v[j] = f2{bf_lo(x1p[j]), bf_hi(x1p[j])};
      s += v[j].x + v[j].y;
    }
    ST128(x1p, OFF_X1 + ((row0 + w) * 64 + lane) * 16);
    s = wave_sum(s);
    const float meanf = s * (1.0f / D_);
    float q = 0.f;
#pragma unroll
    for (int j = 0; j < 4; ++j) {
      f2 d = v[j] - meanf;
      q += d.x * d.x + d.y * d.y;
    }
    q = wave_sum(q);
    const float mean = bfr(meanf);
    const float den = bfr(sqrtf(bfr(bfr(q * (1.0f / D_)) + 1e-5f)));
    const float rden = 1.0f / den;
    u4v hp;
#pragma unroll
    for (int j = 0; j < 4; ++j) {
      f2 t = __builtin_elementwise_fma(v[j] - mean, f2{bf_lo(cbv[j]), bf_hi(cbv[j])} * rden,
                                       f2{bf_lo(sbv[j]), bf_hi(sbv[j])});
      hp[j] = pack_bf(t.x, t.y);
    }
    *(u4v*)(dsm + L_ST + (lane * 9 + w) * 16) = hp;
    __syncthreads();
    {
      const int ch = tid >> 3, r = tid & 7;
      u4v o = *(const u4v*)(dsm + L_ST + (ch * 9 + r) * 16);
      const int mt = row0 >> 4, r16 = (row0 & 8) + r;
      ST128(o, OFF_H2F + ((mt * 16 + (ch >> 2)) * 64 + (ch & 3) * 16 + r16) * 16);
    }
  }
  STAMP(3, 3);
  DRAIN();
  __syncthreads();
  if (tid == 0) __hip_atomic_store(myA, 1u, __ATOMIC_RELAXED, __HIP_MEMORY_SCOPE_AGENT);
  {
    const int ch = tid >> 3, r = tid & 7;
    u4v t = LD128(OFF_H2F + (((row0 >> 4) * 16 + (ch >> 2)) * 64 + (ch & 3) * 16 + (row0 & 8) + r) * 16);
    asm volatile("" ::"v"(t));
  }
  STAMP(3, 4);
  wait_pair(depA);
  STAMP(3, 5);
  {
    u4v B[16];
#pragma unroll
    for (int ks = 0; ks < 16; ++ks) B[ks] = LD128(OFF_H2F + (((8 * p + w) * 16 + ks) * 64 + lane) * 16);
    f4 acc[7];
#pragma unroll
    for (int ft = 0; ft < 7; ++ft) acc[ft] = f4{0.f, 0.f, 0.f, 0.f};
    const char* lw = dsm + L_W1 + lane * 16;
    const int e = c >> 1, odd = c & 1;
    const int hb = (int)OFF_HF + (((8 * p + w) * 56 + 7 * e) * 64 + lane) * 16;
    u4v Af[2][7];
#pragma unroll
    for (int j = 0; j < 7; ++j) Af[0][j] = *(const u4v*)(lw + (g1_ks(0, j) * 7 + g1_ft(0, j)) * 1024);
#pragma unroll
    for (int g = 0; g < 17; ++g) {
#pragma unroll
      for (int j = 0; j < 7; ++j) {
        if (j < g1_n(g)) acc[g1_ft(g, j)] = mfma16(Af[g & 1][j], B[g1_ks(g, j)], acc[g1_ft(g, j)]);
        if (g + 1 < 17 && j < g1_n(g + 1))
          Af[(g + 1) & 1][j] = *(const u4v*)(lw + (g1_ks(g + 1, j) * 7 + g1_ft(g + 1, j)) * 1024);
        SCHED();
      }
      if (g >= 1 && g < 13) { pf_load(18 + 2 * g); pf_load(19 + 2 * g); }
      if (g >= 14) {
        const int i = g - 14;
        f4 a = acc[2 * i], b = acc[2 * i + 1];
        u4v o = u4v{silu_pk(a[0], a[1]), silu_pk(a[2], a[3]), silu_pk(b[0], b[1]), silu_pk(b[2], b[3])};
        ST128(o, hb + (odd * 3 + i) * 1024);
      }
      SCHED();
      if (g == 12) STAMP(3, 6);
    }
    f4 a = acc[6];
    u2v o = u2v{silu_pk(a[0], a[1]), silu_pk(a[2], a[3])};
    ST64(o, hb + 6 * 1024 + odd * 8);
  }
  STAMP(3, 7);
  DRAIN();
  __syncthreads();
  if (tid == 0) __hip_atomic_store(myB, 1u, __ATOMIC_RELAXED, __HIP_MEMORY_SCOPE_AGENT);
  {
    const int hb = (int)OFF_HF + (((8 * p + w) * 56 + 7 * (c >> 1) + 3 * (c & 1)) * 64 + lane) * 16;
    u4v t0 = LD128(hb), t1 = LD128(hb + 1024), t2 = LD128(hb + 2048);
    asm volatile("" ::"v"(t0), "v"(t1), "v"(t2));
  }
  wait_pair(depB);
  STAMP(3, 8);
  {
    const int Bb = (int)OFF_HF + ((mt0 * 56 + 7 * w) * 64 + lane) * 16;
    u4v B[7][4];
#pragma unroll
    for (int ks = 0; ks < 4; ++ks)
#pragma unroll
      for (int mt = 0; mt < 4; ++mt) B[ks][mt] = LD128(Bb + (mt * 56 + ks) * 1024);
    f4 acc[4][4];
#pragma unroll
    for (int i = 0; i < 4; ++i)
#pragma unroll
      for (int j = 0; j < 4; ++j) acc[i][j] = f4{0.f, 0.f, 0.f, 0.f};
#pragma unroll
    for (int ks = 0; ks < 7; ++ks) {
#pragma unroll
      for (int nt = 0; nt < 4; ++nt)
#pragma unroll
        for (int mt = 0; mt < 4; ++mt) acc[nt][mt] = mfma16(A2[ks * 4 + nt], B[ks][mt], acc[nt][mt]);
      if (ks + 4 < 7) {
#pragma unroll
        for (int mt = 0; mt < 4; ++mt) B[ks + 4][mt] = LD128(Bb + (mt * 56 + ks + 4) * 1024);
      }
      SCHED();
    }
    STAMP(3, 9);
    const int g = lane >> 4, mr = lane & 15;
    const int fm = w >> 1, P = w & 1;
    const int m = (mt0 + fm) * 16 + mr;
    const int n = nt0 * 16 + 32 * P + 8 * g;
    unsigned xv[4];
#pragma unroll
    for (int i = 0; i < 4; ++i)
      xv[i] = __builtin_amdgcn_raw_buffer_load_b32(
          rs, (int)OFF_X1 + (m * 64 + (((n >> 1) + i) & 63)) * 16 + (n >> 7) * 4, 0, AUX_SC1);
    const f4 g0 = __builtin_bit_cast(f4, LD128(OFF_MODF + (2560 + n) * 4));
    const f4 g1 = __builtin_bit_cast(f4, LD128(OFF_MODF + (2560 + n + 4) * 4));
    f4* red = (f4*)dsm;
#pragma unroll
    for (int nt = 0; nt < 4; ++nt)
#pragma unroll
      for (int mt = 0; mt < 4; ++mt) red[(w * 16 + nt * 4 + mt) * 64 + lane] = acc[nt][mt];
    __syncthreads();
    STAMP(3, 10);
    f4 s0 = f4{0.f, 0.f, 0.f, 0.f}, s1 = s0;
#pragma unroll
    for (int ww = 0; ww < 8; ++ww) {
      s0 += red[(ww * 16 + (2 * P) * 4 + fm) * 64 + lane];
      s1 += red[(ww * 16 + (2 * P + 1) * 4 + fm) * 64 + lane];
    }
    u4v o;
    {
      f2 t0 = bfr2(f2{g0[0], g0[1]} * bfr2(f2{s0[0], s0[1]})), t1 = bfr2(f2{g0[2], g0[3]} * bfr2(f2{s0[2], s0[3]}));
      f2 t2 = bfr2(f2{g1[0], g1[1]} * bfr2(f2{s1[0], s1[1]})), t3 = bfr2(f2{g1[2], g1[3]} * bfr2(f2{s1[2], s1[3]}));
      o[0] = pack_bf(bf_lo(xv[0]) + t0.x, bf_hi(xv[0]) + t0.y);
      o[1] = pack_bf(bf_lo(xv[1]) + t1.x, bf_hi(xv[1]) + t1.y);
      o[2] = pack_bf(bf_lo(xv[2]) + t2.x, bf_hi(xv[2]) + t2.y);
      o[3] = pack_bf(bf_lo(xv[3]) + t3.x, bf_hi(xv[3]) + t3.y);
    }
    *(u4v*)(out + (size_t)m * D_ + n) = o;
  }
  STAMP(3, 11);
}

extern "C" void kernel_launch(void* const* d_in, const int* in_sizes, int n_in, void* d_out, int out_size, void* d_ws,
                              size_t ws_size, hipStream_t stream) {
  const u16* x = (const u16*)d_in[0];
  const u16* wr = (const u16*)d_in[1];
  const u16* wi = (const u16*)d_in[2];
  const u16* w1 = (const u16*)d_in[4];
  const u16* w2 = (const u16*)d_in[5];
  const u16* c = (const u16*)d_in[6];
  const u16* wmod = (const u16*)d_in[7];
  char* ws = (char*)d_ws;
  u16* out = (u16*)d_out;
  (void)hipFuncSetAttribute((const void*)kmega, hipFuncAttributeMaxDynamicSharedMemorySize, L_TOTAL);
  k0_mod<<<K0_GEMV + K0_WT + K0_W2 + K0_Z + K0_WARM + 16, 512, 0, stream>>>(c, wmod, x, wr, wi, w1, w2, out, ws);
  kmega<<<256, 512, L_TOTAL, stream>>>(x, w1, w2, ws, out);
}
```

```cpp
#include <hip/hip_runtime.h>
#include <hip/hip_bf16.h>
#include <stdint.h>

typedef float f2 __attribute__((ext_vector_type(2)));
typedef float f4 __attribute__((ext_vector_type(4)));
typedef unsigned u2v __attribute__((ext_vector_type(2)));
typedef unsigned u4v __attribute__((ext_vector_type(4)));
typedef short bf16x8 __attribute__((ext_vector_type(8)));
typedef unsigned short u16;

constexpr int S_ = 2048, D_ = 512, F_ = 1792, DF_ = 257;
constexpr int NSPLIT = 4;

constexpr size_t OFF_CNT = 0;
#define FSPACE 16
#define FREP 16
constexpr int FSP = FSPACE, NREP = FREP;
constexpr size_t FTAB = (size_t)NREP * 256 * FSP;
constexpr size_t OFF_FLG = 16384;
constexpr size_t OFF_FLR = OFF_FLG + FTAB;
constexpr size_t OFF_FLS = OFF_FLR + FTAB;
constexpr size_t FLAG_BYTES = OFF_FLS + 16384;
constexpr size_t OFF_MODP = 900 << 10;
constexpr size_t OFF_MODF = OFF_MODP + (size_t)NSPLIT * 3072 * 4;
constexpr size_t OFF_MODC = OFF_MODF + 3072 * 4;
constexpr size_t OFF_WT = 1 << 20;
constexpr size_t OFF_XT = 4 << 20;
constexpr size_t OFF_ZT = 28 << 20;
constexpr size_t OFF_H2F = 12 << 20;
constexpr size_t OFF_X1 = 14 << 20;
constexpr size_t OFF_W2F = 18 << 20;
constexpr size_t OFF_HF = 20 << 20;
constexpr int WS_SPAN = 36 << 20;
#define CPITCH (2048 + 32)
constexpr int CP = CPITCH;
constexpr int WT_PITCH = 520;
constexpr float FSC = 1.0f / (2.0f * 2048.0f * 512.0f);

#define STAMP(kid, i)

__device__ __forceinline__ float bf_lo(unsigned u) { return __builtin_bit_cast(float, u << 16); }
__device__ __forceinline__ float bf_hi(unsigned u) { return __builtin_bit_cast(float, u & 0xffff0000u); }
__device__ __forceinline__ float bfr(float x) {
  return (float)(__bf16)x;
}
__device__ __forceinline__ unsigned pack_bf(float lo, float hi) {
  typedef __bf16 b2 __attribute__((ext_vector_type(2)));
  return __builtin_bit_cast(unsigned, __builtin_convertvector(f2{lo, hi}, b2));
}
__device__ __forceinline__ f2 cmul(f2 a, f2 b) {
  f2 t = a * f2{b.x, b.x};
  return __builtin_elementwise_fma(f2{a.y, a.x}, f2{-b.y, b.y}, t);
}
__device__ __forceinline__ f2 cmulc(f2 a, f2 b) {
  f2 t = a * f2{b.x, b.x};
  return __builtin_elementwise_fma(f2{a.y, a.x}, f2{b.y, -b.y}, t);
}
__device__ __forceinline__ f2 bfr2(f2 a) {
  unsigned u = pack_bf(a.x, a.y);
  asm("" : "+v"(u));
  return f2{bf_lo(u), bf_hi(u)};
}
__device__ __forceinline__ float msel(unsigned m, float a, float b) {
  unsigned ua = __builtin_bit_cast(unsigned, a), ub = __builtin_bit_cast(unsigned, b);
  return __builtin_bit_cast(float, (m & ua) | (~m & ub));
}
#define PIN2(x) asm volatile("" : "+v"(x))
constexpr int AUX_SC1 = 16;
#define WS_RSRC(ws) __builtin_amdgcn_make_buffer_rsrc((ws), 0, WS_SPAN, 0x00020000)
#define ST128(v, off) __builtin_amdgcn_raw_buffer_store_b128((v), rs, (int)(off), 0, AUX_SC1)
#define ST64(v, off) __builtin_amdgcn_raw_buffer_store_b64((v), rs, (int)(off), 0, AUX_SC1)
#define ST32(v, off) __builtin_amdgcn_raw_buffer_store_b32((v), rs, (int)(off), 0, AUX_SC1)
#define LD64(off) __builtin_amdgcn_raw_buffer_load_b64(rs, (int)(off), 0, AUX_SC1)
#define LD32(off) __builtin_amdgcn_raw_buffer_load_b32(rs, (int)(off), 0, AUX_SC1)
#define LD128(off) __builtin_amdgcn_raw_buffer_load_b128(rs, (int)(off), 0, AUX_SC1)
__device__ __forceinline__ f2 twid(float rev) { return f2{__builtin_amdgcn_cosf(rev), -__builtin_amdgcn_sinf(rev)}; }

template <int DIR>
__device__ __forceinline__ f2 rot90(f2 a) {
  return DIR < 0 ? f2{a.y, -a.x} : f2{-a.y, a.x};
}
template <int DIR>
__device__ __forceinline__ void dft4(f2& c0, f2& c1, f2& c2, f2& c3) {
  f2 d0 = c0 + c2, d2 = c0 - c2, d1 = c1 + c3, e = c1 - c3;
  c0 = d0 + d1;
  c2 = d0 - d1;
  const f2 es = f2{e.y, e.x};
  c1 = __builtin_elementwise_fma(es, DIR < 0 ? f2{1.f, -1.f} : f2{-1.f, 1.f}, d2);
  c3 = __builtin_elementwise_fma(es, DIR < 0 ? f2{-1.f, 1.f} : f2{1.f, -1.f}, d2);
}

__device__ __forceinline__ void sw32(float& a, float& b) {
  auto r = __builtin_amdgcn_permlane32_swap(__builtin_bit_cast(unsigned, a), __builtin_bit_cast(unsigned, b), false, false);
  a = __builtin_bit_cast(float, (unsigned)r[0]);
  b = __builtin_bit_cast(float, (unsigned)r[1]);
}
__device__ __forceinline__ void sw16(float& a, float& b) {
  auto r = __builtin_amdgcn_permlane16_swap(__builtin_bit_cast(unsigned, a), __builtin_bit_cast(unsigned, b), false, false);
  a = __builtin_bit_cast(float, (unsigned)r[0]);
  b = __builtin_bit_cast(float, (unsigned)r[1]);
}
template <int CTRL, int BANK>
__device__ __forceinline__ float udpp(float old, float src) {
  return __builtin_bit_cast(float, __builtin_amdgcn_update_dpp(__builtin_bit_cast(int, old), __builtin_bit_cast(int, src),
                                                               CTRL, 0xf, BANK, false));
}
template <int CTRL>
__device__ __forceinline__ float mdpp(float src) {
  return __builtin_bit_cast(float, __builtin_amdgcn_mov_dpp(__builtin_bit_cast(int, src), CTRL, 0xf, 0xf, false));
}
__device__ __forceinline__ void xp_hi1(float& v0, float& v1, float& v2, float& v3) {
  sw32(v0, v2); sw32(v1, v3); sw16(v0, v1); sw16(v2, v3);
}
__device__ __forceinline__ void xp_mid1(float& v0, float& v1, float& v2, float& v3) {
  float a0 = udpp<0x128, 0xC>(v0, v2), a2 = udpp<0x128, 0x3>(v2, v0);
  float a1 = udpp<0x128, 0xC>(v1, v3), a3 = udpp<0x128, 0x3>(v3, v1);
  v0 = udpp<0x124, 0xA>(a0, a1); v1 = udpp<0x12C, 0x5>(a1, a0);
  v2 = udpp<0x124, 0xA>(a2, a3); v3 = udpp<0x12C, 0x5>(a3, a2);
}
__device__ __forceinline__ void xp_lo1(float& v0, float& v1, float& v2, float& v3, unsigned b1, unsigned b0) {
  float s0 = mdpp<0x4E>(msel(b1, v0, v2)), s1 = mdpp<0x4E>(msel(b1, v1, v3));
  float a0 = msel(b1, s0, v0), a2 = msel(b1, v2, s0), a1 = msel(b1, s1, v1), a3 = msel(b1, v3, s1);
  float t0 = mdpp<0xB1>(msel(b0, a0, a1)), t2 = mdpp<0xB1>(msel(b0, a2, a3));
  v0 = msel(b0, t0, a0); v1 = msel(b0, a1, t0); v2 = msel(b0, t2, a2); v3 = msel(b0, a3, t2);
}
#define XP_CPLX(NAME, FN, ...)                                        \
  {                                                                   \
    float x0 = v[0].x, x1 = v[1].x, x2 = v[2].x, x3 = v[3].x;         \
    float y0 = v[0].y, y1 = v[1].y, y2 = v[2].y, y3 = v[3].y;         \
    FN(x0, x1, x2, x3, ##__VA_ARGS__);                                \
    FN(y0, y1, y2, y3, ##__VA_ARGS__);                                \
    v[0] = f2{x0, y0}; v[1] = f2{x1, y1}; v[2] = f2{x2, y2}; v[3] = f2{x3, y3}; \
  }
__device__ __forceinline__ void xp_hi(f2 (&v)[4]) XP_CPLX(hi, xp_hi1)
__device__ __forceinline__ void xp_mid(f2 (&v)[4]) XP_CPLX(mid, xp_mid1)
__device__ __forceinline__ void xp_lo(f2 (&v)[4], unsigned b1, unsigned b0) XP_CPLX(lo, xp_lo1, b1, b0)

struct Tw256 {
  f2 a[3], b[3], c[3];
  unsigned m1, m0;
};
__device__ __forceinline__ void make_tw256(int lane, Tw256& t) {
#pragma unroll
  for (int r = 1; r < 4; ++r) {
    t.a[r - 1] = twid((float)(lane * r) * (1.0f / 256.0f));
    t.b[r - 1] = twid((float)((lane & 15) * r) * (1.0f / 64.0f));
    t.c[r - 1] = twid((float)((lane & 3) * r) * (1.0f / 16.0f));
  }
  t.m1 = 0u - (unsigned)((lane >> 1) & 1);
  t.m0 = 0u - (unsigned)(lane & 1);
#pragma unroll
  for (int r = 0; r < 3; ++r) {
    PIN2(t.a[r]);
    PIN2(t.b[r]);
    PIN2(t.c[r]);
  }
  PIN2(t.m1);
  PIN2(t.m0);
}
struct NoHook {
  __device__ __forceinline__ void operator()(int) const {}
};
template <class H>
__device__ __forceinline__ void fft256_fwd(f2 (&v)[4], int lane, const Tw256& t, const H& hook) {
  const unsigned b1 = t.m1, b0 = t.m0;
  dft4<-1>(v[0], v[1], v[2], v[3]);
#pragma unroll
  for (int r = 1; r < 4; ++r) v[r] = cmul(v[r], t.a[r - 1]);
  xp_hi(v);
  hook(0);
  dft4<-1>(v[0], v[1], v[2], v[3]);
#pragma unroll
  for (int r = 1; r < 4; ++r) v[r] = cmul(v[r], t.b[r - 1]);
  xp_mid(v);
  hook(1);
  dft4<-1>(v[0], v[1], v[2], v[3]);
#pragma unroll
  for (int r = 1; r < 4; ++r) v[r] = cmul(v[r], t.c[r - 1]);
  xp_lo(v, b1, b0);
  hook(2);
  dft4<-1>(v[0], v[1], v[2], v[3]);
}
template <class H>
__device__ __forceinline__ void fft256_inv(f2 (&v)[4], int lane, const Tw256& t, const H& hook) {
  const unsigned b1 = t.m1, b0 = t.m0;
  dft4<1>(v[0], v[1], v[2], v[3]);
  xp_lo(v, b1, b0);
  hook(0);
#pragma unroll
  for (int r = 1; r < 4; ++r) v[r] = cmulc(v[r], t.c[r - 1]);
  dft4<1>(v[0], v[1], v[2], v[3]);
  xp_mid(v);
  hook(1);
#pragma unroll
  for (int r = 1; r < 4; ++r) v[r] = cmulc(v[r], t.b[r - 1]);
  dft4<1>(v[0], v[1], v[2], v[3]);
  xp_hi(v);
  hook(2);
#pragma unroll
  for (int r = 1; r < 4; ++r) v[r] = cmulc(v[r], t.a[r - 1]);
  dft4<1>(v[0], v[1], v[2], v[3]);
}

__device__ __forceinline__ float wave_sum(float x) {
  x += mdpp<0xB1>(x);
  x += mdpp<0x4E>(x);
  x += mdpp<0x141>(x);
  x += mdpp<0x140>(x);
  float r0 = __builtin_bit_cast(float, __builtin_amdgcn_readlane(__builtin_bit_cast(int, x), 0));
  float r1 = __builtin_bit_cast(float, __builtin_amdgcn_readlane(__builtin_bit_cast(int, x), 16));
  float r2 = __builtin_bit_cast(float, __builtin_amdgcn_readlane(__builtin_bit_cast(int, x), 32));
  float r3 = __builtin_bit_cast(float, __builtin_amdgcn_readlane(__builtin_bit_cast(int, x), 48));
  return (r0 + r1) + (r2 + r3);
}

__device__ __forceinline__ int zpos(int k) { return 17 * (k & 3) + 4 * ((k >> 2) & 3) + ((k >> 4) & 3) + 68 * (k >> 6); }
constexpr int ZRS = 276;

constexpr int K0_GEMV = 48 * NSPLIT, K0_WT = 128, K0_W2 = 0, K0_WARM = 120, K0_Z = (int)(FLAG_BYTES / 8192);
__global__ __launch_bounds__(512) void k0_mod(const u16* __restrict__ c, const u16* __restrict__ wmod,
                                              const u16* __restrict__ x, const u16* __restrict__ wr,
                                              const u16* __restrict__ wi, const u16* __restrict__ w1,
                                              const u16* __restrict__ w2, char* __restrict__ ws) {
  __shared__ __attribute__((aligned(16))) char smem[17408];
  const int tid = threadIdx.x, row = tid >> 6, lane = tid & 63;
  const auto rs = WS_RSRC(ws);
  int b = blockIdx.x;
  STAMP(0, 0);
  if (b >= K0_GEMV + K0_WT + K0_W2) {
    int q = b - (K0_GEMV + K0_WT + K0_W2);
    if (q < K0_Z) {
      ((u4v*)ws)[q * 512 + tid] = u4v{0u, 0u, 0u, 0u};
      return;
    }
    q -= K0_Z;
    const char* src = q < 64 ? (const char*)x + (size_t)q * 32768
                             : (q < 120 ? (const char*)w1 + (size_t)(q - 64) * 32768 : (const char*)w2 + (size_t)(q - 120) * 32768);
    u4v t[4];
#pragma unroll
    for (int i = 0; i < 4; ++i) t[i] = *(const u4v*)(src + i * 8192 + tid * 16);
#pragma unroll
    for (int i = 0; i < 4; ++i) asm volatile("" ::"v"(t[i]));
    STAMP(0, 1);
    return;
  }
  if (b >= K0_GEMV) {
    b -= K0_GEMV;
    if (b < K0_WT) {
      char* sr = smem;
      char* si = smem + 16 * 544;
      constexpr int LASTCH = (S_ * DF_ * 2) / 16 - 1;
      for (int it = tid; it < 16 * 34; it += 512) {
        const int i = it / 34, ch = it % 34;
        int gch = ((b + 128 * i) * (DF_ * 2)) / 16 + ch;
        gch = gch > LASTCH ? LASTCH : gch;
        *(u4v*)(sr + i * 544 + ch * 16) = *(const u4v*)((const char*)wr + (size_t)gch * 16);
        *(u4v*)(si + i * 544 + ch * 16) = *(const u4v*)((const char*)wi + (size_t)gch * 16);
      }
      __syncthreads();
      const int tbase = 64 * (b & 7) + 16 * ((b >> 3) & 3) + 4 * ((b >> 5) & 3);
      for (int it = tid; it < DF_ * 4; it += 512) {
        const int k = it >> 2, r3 = it & 3;
        u4v o;
#pragma unroll
        for (int r4 = 0; r4 < 4; ++r4) {
          const int i = r3 + 4 * r4;
          const int off = i * 544 + (((b + 128 * i) * (DF_ * 2)) & 15) + 2 * k;
          o[r4] = (unsigned)*(const u16*)(sr + off) | ((unsigned)*(const u16*)(si + off) << 16);
        }
        ST128(o, OFF_WT + (k * 512 + tbase + r3) * 16);
      }
    }
    STAMP(0, 1);
    return;
  }
  float* red = (float*)smem;
  const int cgp = b % 48, ks = b / 48;
  const int cg = tid & 7, r = tid >> 3;
  const int rw = ks * 128 + r;
  const u4v wm0 = *(const u4v*)(wmod + (size_t)rw * 3072 + cgp * 64 + cg * 8);
  const u4v wm1 = *(const u4v*)(wmod + (size_t)(rw + 64) * 3072 + cgp * 64 + cg * 8);
  const float cv0 = bf_lo(c[rw]), cv1 = bf_lo(c[rw + 64]);
  const float s0 = bfr(cv0 / (1.0f + __expf(-cv0))), s1 = bfr(cv1 / (1.0f + __expf(-cv1)));
  float acc[8];
#pragma unroll
  for (int i = 0; i < 4; ++i) {
    acc[2 * i] = s0 * bf_lo(wm0[i]) + s1 * bf_lo(wm1[i]);
    acc[2 * i + 1] = s0 * bf_hi(wm0[i]) + s1 * bf_hi(wm1[i]);
  }
#pragma unroll
  for (int i = 0; i < 8; ++i) {
    acc[i] += mdpp<0x128>(acc[i]);
    acc[i] += __shfl_xor(acc[i], 16);
    acc[i] += __shfl_xor(acc[i], 32);
  }
  if (lane < 8) {
#pragma unroll
    for (int i = 0; i < 8; ++i) red[row * 64 + cg * 8 + i] = acc[i];
  }
  __syncthreads();
  if (tid < 64) {
    float sum = 0.f;
#pragma unroll
    for (int i = 0; i < 8; ++i) sum += red[i * 64 + tid];
    ((float*)(ws + OFF_MODP))[ks * 3072 + cgp * 64 + tid] = sum;
  }
  STAMP(0, 1);
}

__device__ __forceinline__ f4 mfma16(u4v a, u4v b, f4 c) {
  return __builtin_amdgcn_mfma_f32_16x16x32_bf16(__builtin_bit_cast(bf16x8, a), __builtin_bit_cast(bf16x8, b), c, 0, 0,
                                                  0);
}
__device__ __forceinline__ f2 silu2(float a, float b) {
  f2 h = f2{a, b};
  f2 t = h * -1.44269504088896340736f;
  f2 d = f2{__builtin_amdgcn_exp2f(t.x), __builtin_amdgcn_exp2f(t.y)} + 1.0f;
  return h * f2{__builtin_amdgcn_rcpf(d.x), __builtin_amdgcn_rcpf(d.y)};
}
__device__ __forceinline__ unsigned silu_pk(float a, float b) {
  f2 r = silu2(a, b);
  return pack_bf(r.x, r.y);
}
typedef __attribute__((address_space(1))) unsigned gu32;
#define DRAIN() asm volatile("s_waitcnt vmcnt(0)" ::: "memory")
typedef __attribute__((address_space(1))) unsigned long long gu64;
constexpr unsigned SPIN_CAP = 1u << 18;
__device__ __forceinline__ void wait_pair(gu64* flags) {
#pragma nounroll
  for (unsigned spin = 0; spin < SPIN_CAP; ++spin) {
    if (__hip_atomic_load(flags, __ATOMIC_RELAXED, __HIP_MEMORY_SCOPE_AGENT) == 0x0000000100000001ull) break;
    __builtin_amdgcn_s_sleep(1);
  }
  __builtin_amdgcn_fence(__ATOMIC_ACQUIRE, "wavefront");
  asm volatile("" ::: "memory");
}

#define PSLEEP 2
__device__ __forceinline__ void wait_cols(gu32* flags) {
#pragma nounroll
  for (unsigned spin = 0; spin < SPIN_CAP; ++spin) {
    unsigned f = 1u;
#pragma unroll
    for (int j = 0; j < 4; ++j)
      f &= __hip_atomic_load(flags + j * 64 * (FSP / 4), __ATOMIC_RELAXED, __HIP_MEMORY_SCOPE_AGENT);
    if (__builtin_amdgcn_ballot_w64(f != 1u) == 0ull) break;
    __builtin_amdgcn_s_sleep(PSLEEP);
  }
}

__device__ __forceinline__ int wt_off(int k, int t) { return (int)OFF_WT + (k * 512 + t) * 16; }

constexpr int L_W1 = 0;
constexpr int L_T = 112 * 1024;
constexpr int L_ZS = 112 * 1024;
constexpr int L_ST = L_ZS + 8 * ZRS * 8;
constexpr int L_TOTAL = 160 * 1024;
__device__ __forceinline__ constexpr int g1_n(int g) { return g < 13 ? 7 : (g < 16 ? 6 : 3); }
__device__ __forceinline__ constexpr int g1_ft(int g, int j) { return g < 13 ? j : (g < 16 ? 2 * (g - 13) + (j & 1) : 6); }
__device__ __forceinline__ constexpr int g1_ks(int g, int j) { return g < 13 ? g : (g < 16 ? 13 + (j >> 1) : 13 + j); }
#define SCHED() __builtin_amdgcn_sched_barrier(0)
typedef short v4i16 __attribute__((ext_vector_type(4)));
__device__ __forceinline__ u4v w1frag(const char* lw, int ft, int ks) {
  typedef __attribute__((address_space(3))) v4i16* lp;
  const u2v a = __builtin_bit_cast(u2v, __builtin_amdgcn_ds_read_tr16_b64_v4i16((lp)(lw + (2 * ks) * 3584 + ft * 32)));
  const u2v b = __builtin_bit_cast(u2v, __builtin_amdgcn_ds_read_tr16_b64_v4i16((lp)(lw + (2 * ks + 1) * 3584 + ft * 32)));
  return u4v{a[0], a[1], b[0], b[1]};
}
constexpr int L_MODS = L_ZS + 8 * ZRS * 8;
constexpr int L_W2S = L_T + 32768;
__global__ __launch_bounds__(512, 2) void kmega(const u16* __restrict__ x, const u16* __restrict__ w1,
                                                const u16* __restrict__ w2, char* ws, u16* __restrict__ out) {
  extern __shared__ __attribute__((aligned(16))) char dsm[];
  const int tid = threadIdx.x, lane = tid & 63;
  const int w = __builtin_amdgcn_readfirstlane(tid >> 6);
  const int bid = blockIdx.x;
  const int p = (bid & 7) * 2 + (bid >> 7), c = (bid >> 3) & 15;
  const int row0 = 128 * p + 8 * c;
  STAMP(1, 0);
  const auto rs = WS_RSRC(ws);
  gu32* myA = (gu32*)(ws + OFF_CNT) + (p * 8 + (c >> 1)) * 16 + (c & 1);
  gu32* myB = myA + 128 * 16;
  gu64* depA = (gu64*)(ws + OFF_CNT) + (p * 8 + w) * 8;
  gu64* depB = depA + 128 * 8;
  const int mt0 = 8 * p + 4 * (c >> 3), nt0 = 4 * (c & 7);
  const int l56 = lane < 56 ? lane : lane - 56;
  const int r4_ = l56 / 14, ch_ = l56 - 14 * r4_;
  const char* w1g = (const char*)w1 + (size_t)(4 * w + r4_) * (F_ * 2) + c * 224 + ch_ * 16;
  char* w1l = dsm + L_W1 + ((w >> 1) * 32 + (2 * (w & 1) + (r4_ >> 1)) * 4 + (r4_ & 1)) * 224 + ch_ * 16;
  const int Ab = (int)OFF_W2F + ((nt0 * 56 + 7 * w) * 64 + lane) * 16;
  u4v S1[16];
  u4v A2[28];
  auto pf_load = [&](int i) __attribute__((always_inline)) {
    if (i < 16) S1[i] = *(const u4v*)(w1g + (size_t)i * (32 * F_ * 2));
    else if (i < 44) A2[i - 16] = LD128(Ab + (((i - 16) & 3) * 56 + ((i - 16) >> 2)) * 1024);
  };
  auto pf_put = [&](int i) __attribute__((always_inline)) {
    *(u4v*)(w1l + ((i & 3) * 128 + (i >> 3) * 16 + ((i >> 2) & 1) * 2) * 224) = S1[i];
  };
  auto pf = [&](int i0, int n) __attribute__((always_inline)) {
    SCHED();
#pragma unroll
    for (int i = 0; i < n; ++i) pf_load(i0 + i);
    SCHED();
  };
  const int tk = bid < 224 ? bid : bid - 224;
  Tw256 tw;
  {
    f2* zs = (f2*)(dsm + L_ZS);
    float* mods = (float*)(dsm + L_MODS);
    const int r0 = bid * 8;
    unsigned xr[4];
    const unsigned* xrow = (const unsigned*)(x + (size_t)(r0 + w) * D_);
#pragma unroll
    for (int j = 0; j < 4; ++j) xr[j] = xrow[lane + 64 * j];
    const float* mp = (const float*)(ws + OFF_MODP);
    f2 sm = *(const f2*)(mp + tid * 2);
#pragma unroll
    for (int k = 1; k < NSPLIT; ++k) sm += *(const f2*)(mp + k * 3072 + tid * 2);
    const int idx = bid * 12 + (tid < 6 ? tid : 0) * 2;
    f2 fin = *(const f2*)(mp + idx);
#pragma unroll
    for (int k = 1; k < NSPLIT; ++k) fin += *(const f2*)(mp + k * 3072 + idx);
    SCHED();
    make_tw256(lane, tw);
    const int rp = tid & 3, kk = tid >> 2;
    f2 wpost = twid((float)kk * (1.0f / 512.0f));
    PIN2(wpost);
    f2 v[4];
    float s = 0.f;
#pragma unroll
    for (int j = 0; j < 4; ++j) {
      v[j] = f2{bf_lo(xr[j]), bf_hi(xr[j])};
      s += v[j].x + v[j].y;
    }
    s = wave_sum(s);
    const float meanf = s * (1.0f / D_);
    float q = 0.f;
#pragma unroll
    for (int j = 0; j < 4; ++j) {
      f2 d = v[j] - meanf;
      q += d.x * d.x + d.y * d.y;
    }
    q = wave_sum(q);
    const float mean = bfr(meanf);
    const float den = bfr(sqrtf(bfr(bfr(q * (1.0f / D_)) + 1e-5f)));
    const float rden = 1.0f / den;
    sm = bfr2(sm);
    if (tid >= 256) sm = bfr2(sm + 1.0f);
    *(f2*)(mods + tid * 2) = sm;
    if (tid < 6) {
      fin = bfr2(fin);
      ST64(__builtin_bit_cast(u2v, fin), OFF_MODF + idx * 4);
      const int vec = (idx >> 9) - 2, d = idx & 511;
      if (vec >= 0 && vec < 3) {
        if (vec == 2) fin += 1.0f;
        ST32(pack_bf(fin.x, fin.y), OFF_MODC + ((vec * 64 + ((d >> 1) & 63)) * 4 + (d >> 7)) * 4);
      }
    }
    STAMP(1, 3);
    __syncthreads();
    STAMP(1, 4);
    pf(0, 4);
#pragma unroll
    for (int j = 0; j < 4; ++j) {
      const int d = 2 * (lane + 64 * j);
      f2 sa = *(const f2*)(mods + d);
      f2 ca1 = *(const f2*)(mods + 512 + d);
      v[j] = bfr2(__builtin_elementwise_fma(v[j] - mean, ca1 * rden, sa));
    }
    fft256_fwd(v, lane, tw, NoHook{});
    {
      f2* buf = zs + w * ZRS + lane + (lane >> 4);
#pragma unroll
      for (int r4 = 0; r4 < 4; ++r4) buf[68 * r4] = v[r4];
    }
    STAMP(1, 5);
    __syncthreads();
    {
      const int k = kk;
      const int k2 = (k == 0) ? 128 : 256 - k;
      const int pa = zpos(k), pb = zpos(k2);
      f4 oa, ob;
#pragma unroll
      for (int h = 0; h < 2; ++h) {
        const f2* zr = zs + (2 * rp + h) * ZRS;
        f2 za = zr[pa], zb = zr[pb];
        f2 E = f2{za.x + zb.x, za.y - zb.y};
        f2 Dm = f2{za.x - zb.x, za.y + zb.y};
        f2 O = f2{Dm.y, -Dm.x};
        f2 wo = cmul(wpost, O);
        f2 ra = E + wo;
        f2 t = E - wo;
        f2 rb = f2{t.x, -t.y};
        if (k == 0) {
          ra = f2{2.f * (za.x + za.y), 2.f * (za.x - za.y)};
          rb = f2{2.f * zb.x, -2.f * zb.y};
        }
        oa[2 * h] = ra.x; oa[2 * h + 1] = ra.y;
        ob[2 * h] = rb.x; ob[2 * h + 1] = rb.y;
      }
      ST128(__builtin_bit_cast(u4v, oa), OFF_XT + (k * CP + r0 + 2 * rp) * 8);
      ST128(__builtin_bit_cast(u4v, ob), OFF_XT + (k2 * CP + r0 + 2 * rp) * 8);
    }
    STAMP(1, 6);
    DRAIN();
    __syncthreads();
    if (tid < NREP)
      __hip_atomic_store((gu32*)(ws + OFF_FLR + (tid * 256 + bid) * FSP), 1u, __ATOMIC_RELAXED, __HIP_MEMORY_SCOPE_AGENT);
    STAMP(1, 7);
  }
  {
    f2* T = (f2*)(dsm + L_T);
    f2* T2 = (f2*)(dsm + L_T + 16384);
    f2* T3 = (f2*)(dsm + L_T + 32768);
    const int k = bid;
    f2 tw1[4];
#pragma unroll
    for (int j = 0; j < 4; ++j) {
      tw1[j] = twid((float)((lane + 64 * j) * w) * (1.0f / 2048.0f));
      PIN2(tw1[j]);
    }
    if (w == 0) wait_cols((gu32*)(ws + OFF_FLR + ((bid % NREP) * 256 + lane) * FSP));
    asm volatile("s_barrier" ::: "memory");
    __builtin_amdgcn_fence(__ATOMIC_ACQUIRE, "wavefront");
    STAMP(2, 0);
    f2 v[4];
#pragma unroll
    for (int j = 0; j < 4; ++j) v[j] = __builtin_bit_cast(f2, LD64(OFF_XT + (k * CP + tid + 512 * j) * 8));
    u4v bv;
    unsigned wn[4], bn[4];
    int mirr[4];
#pragma unroll
    for (int j = 0; j < 4; ++j) PIN2(v[j]);
    pf(4, 4);
    const int hb = w >> 2, a = tid & 255;
    constexpr float R = 0.70710678118654752440f;
    dft4<-1>(v[0], v[1], v[2], v[3]);
    if (hb) {
      v[1] = f2{v[1].x + v[1].y, v[1].y - v[1].x} * R;
      v[2] = f2{v[2].y, -v[2].x};
      v[3] = f2{v[3].y - v[3].x, -v[3].x - v[3].y} * R;
    }
#pragma unroll
    for (int q = 0; q < 4; ++q) T[(hb * 4 + q) * 256 + a] = v[q];
    STAMP(2, 1);
    const u4v wv = LD128(wt_off(k, tid));
    u4v w2a;
    {
      const int ks2 = tk >> 2, rr = tid >> 4, gg = rr >> 3, j = rr & 7;
      const int e = ks2 / 7, i7 = ks2 - 7 * e;
      const int T0 = 14 * e + (i7 < 3 ? 2 * i7 : (i7 < 6 ? 2 * i7 + 1 : 6));
      const int T1 = i7 < 6 ? T0 + 1 : 14 * e + 13;
      const int f = (j < 4) ? (16 * T0 + 4 * gg + j) : (16 * T1 + 4 * gg + (j - 4));
      w2a = *(const u4v*)(w2 + (size_t)f * D_ + (tk & 3) * 128 + (tid & 15) * 8);
    }
    if (k == 0) {
      bv = LD128(wt_off(256, tid));
      const int m0 = (lane >> 4) + 4 * ((lane >> 2) & 3) + 16 * (lane & 3);
#pragma unroll
      for (int r4 = 0; r4 < 4; ++r4) {
        const int un = (S_ - (w + 8 * (m0 + 64 * r4))) & (S_ - 1);
        const int mm = un >> 3;
        mirr[r4] = ((un & 7) * 64 + 16 * (mm & 3) + 4 * ((mm >> 2) & 3) + ((mm >> 4) & 3)) * 4 + (mm >> 6);
      }
      if (w != 0) {
        const u4v a4 = LD128(wt_off(0, 64 * (8 - w) + 63 - lane));
        const u4v b4 = LD128(wt_off(256, 64 * (8 - w) + 63 - lane));
#pragma unroll
        for (int r4 = 0; r4 < 4; ++r4) { wn[r4] = a4[3 - r4]; bn[r4] = b4[3 - r4]; }
      } else {
#pragma unroll
        for (int r4 = 0; r4 < 4; ++r4) {
          wn[r4] = LD32(wt_off(0, mirr[r4] >> 2) + (mirr[r4] & 3) * 4);
          bn[r4] = LD32(wt_off(256, mirr[r4] >> 2) + (mirr[r4] & 3) * 4);
        }
      }
    }
    __syncthreads();
    {
      const int q = w & 3;
      const float sg = (w < 4) ? 1.f : -1.f;
#pragma unroll
      for (int j = 0; j < 4; ++j) {
        f2 e = T[q * 256 + lane + 64 * j], o = T[(4 + q) * 256 + lane + 64 * j];
        v[j] = cmul(e + o * sg, tw1[j]);
      }
    }
    fft256_fwd(v, lane, tw, [&](int i) __attribute__((always_inline)) { if (i != 1) pf(8 + 2 * i, 4); });
    STAMP(2, 2);
    if (k != 0) {
#pragma unroll
      for (int i = 0; i < 4; ++i) {
        f2 wf = f2{bf_lo(wv[i]), bf_hi(wv[i])} * FSC;
        v[i] = cmul(v[i], wf);
      }
    } else {
#pragma unroll
      for (int r4 = 0; r4 < 4; ++r4) T3[r4 * 512 + tid] = v[r4];
      __syncthreads();
#pragma unroll
      for (int r4 = 0; r4 < 4; ++r4) {
        f2 cm = T3[(mirr[r4] & 3) * 512 + (mirr[r4] >> 2)];
        f2 h0 = f2{bf_lo(wv[r4]) + bf_lo(wn[r4]), bf_hi(wv[r4]) - bf_hi(wn[r4])};
        f2 h1 = f2{bf_lo(bv[r4]) + bf_lo(bn[r4]), bf_hi(bv[r4]) - bf_hi(bn[r4])};
        f2 wp = (h0 + h1) * (0.25f * FSC), wm = (h0 - h1) * (0.25f * FSC);
        f2 cc = f2{cm.x, -cm.y};
        v[r4] = cmul(v[r4], wp) + cmul(cc, wm);
      }
    }
    fft256_inv(v, lane, tw, [&](int i) __attribute__((always_inline)) {
      SCHED();
#pragma unroll
      for (int q = 0; q < 4; ++q) pf_put(4 * i + q);
      SCHED();
    });
#pragma unroll
    for (int j = 0; j < 4; ++j) T2[w * 256 + lane + 64 * j] = cmulc(v[j], tw1[j]);
#pragma unroll
    for (int q = 12; q < 16; ++q) pf_put(q);
    STAMP(2, 3);
    __syncthreads();
    {
      const float sg = hb ? -1.f : 1.f;
#pragma unroll
      for (int q = 0; q < 4; ++q) v[q] = T2[q * 256 + a] + T2[(q + 4) * 256 + a] * sg;
      if (hb) {
        v[1] = f2{v[1].x - v[1].y, v[1].y + v[1].x} * R;
        v[2] = f2{-v[2].y, v[2].x};
        v[3] = f2{-v[3].x - v[3].y, v[3].x - v[3].y} * R;
      }
    }
    dft4<1>(v[0], v[1], v[2], v[3]);
#pragma unroll
    for (int j = 0; j < 4; ++j) ST64(__builtin_bit_cast(u2v, v[j]), OFF_ZT + (k * CP + tid + 512 * j) * 8);
    STAMP(2, 4);
    *(u4v*)(dsm + L_W2S + ((tid >> 4) * 136 + (tid & 15) * 8) * 2) = w2a;
    DRAIN();
    __syncthreads();
    if (tid < NREP)
      __hip_atomic_store((gu32*)(ws + OFF_FLG + (tid * 256 + k) * FSP), 1u, __ATOMIC_RELAXED, __HIP_MEMORY_SCOPE_AGENT);
  }
  STAMP(2, 5);
  {
    const u16* t = (const u16*)(dsm + L_W2S);
    const int g = lane >> 4, r = lane & 15;
    const int nl = 32 * (w >> 1) + 8 * (r >> 2) + 4 * (w & 1) + (r & 3);
    u4v o;
#pragma unroll
    for (int jj = 0; jj < 4; ++jj)
      o[jj] = (unsigned)t[(g * 8 + 2 * jj) * 136 + nl] | ((unsigned)t[(g * 8 + 2 * jj + 1) * 136 + nl] << 16);
    if (bid < 224) ST128(o, OFF_W2F + ((((tk & 3) * 8 + w) * 56 + (tk >> 2)) * 64 + lane) * 16);
  }
  {
    const char* lw = dsm + L_W1 + (lane >> 2) * 224 + (lane & 3) * 8 + w * (2 * 7168);
    u4v fr[14];
#pragma unroll
    for (int i = 0; i < 14; ++i) fr[i] = w1frag(lw, i % 7, i / 7);
    asm volatile("" ::: "memory");
#pragma unroll
    for (int i = 0; i < 14; ++i) *(u4v*)(dsm + L_W1 + w * (2 * 7168) + i * 1024 + lane * 16) = fr[i];
  }
  STAMP(2, 6);
  {
    f2* zs = (f2*)(dsm + L_ZS);
    const int rp = tid & 3, k = tid >> 2;
    const int k2 = (k == 0) ? 128 : 256 - k;
    u4v xr;
    const unsigned* xrow = (const unsigned*)(x + (size_t)(row0 + w) * D_);
#pragma unroll
    for (int j = 0; j < 4; ++j) xr[j] = xrow[lane + 64 * j];
    const u4v gav = LD128(OFF_MODC + lane * 16);
    const u4v sbv = LD128(OFF_MODC + 1024 + lane * 16);
    const u4v cbv = LD128(OFF_MODC + 2048 + lane * 16);
    f2 wq = twid((float)k * (1.0f / 512.0f));
    PIN2(wq);
    const int pa = zpos(k), pb = zpos(k2);
    if (w == 0) wait_cols((gu32*)(ws + OFF_FLG + ((bid % NREP) * 256 + lane) * FSP));
    DRAIN();
    asm volatile("s_barrier" ::: "memory");
    __builtin_amdgcn_fence(__ATOMIC_ACQUIRE, "wavefront");
    if (tid == 0) __hip_atomic_store((gu32*)(ws + OFF_FLS + bid * 64), 1u, __ATOMIC_RELAXED, __HIP_MEMORY_SCOPE_AGENT);
    STAMP(3, 0);
    const f4 ya = __builtin_bit_cast(f4, LD128(OFF_ZT + (k * CP + row0 + 2 * rp) * 8));
    const f4 yb = __builtin_bit_cast(f4, LD128(OFF_ZT + (k2 * CP + row0 + 2 * rp) * 8));
#pragma unroll
    for (int h = 0; h < 2; ++h) {
      f2 a = f2{ya[2 * h], ya[2 * h + 1]}, b = f2{yb[2 * h], yb[2 * h + 1]};
      f2* zr = zs + (2 * rp + h) * ZRS;
      f2 Ze = f2{a.x + b.x, a.y - b.y};
      f2 Dm = f2{a.x - b.x, a.y + b.y};
      f2 Zo = cmulc(Dm, wq);
      f2 za = f2{Ze.x - Zo.y, Ze.y + Zo.x};
      f2 zb = f2{Ze.x + Zo.y, -Ze.y + Zo.x};
      if (k == 0) {
        za = f2{a.x + a.y, a.x - a.y};
        zb = f2{2.f * b.x, -2.f * b.y};
      }
      zr[pa] = za;
      zr[pb] = zb;
    }
    STAMP(3, 1);
    __syncthreads();
    f2 v[4];
    {
      const f2* buf = zs + w * ZRS + lane + (lane >> 4);
#pragma unroll
      for (int r4 = 0; r4 < 4; ++r4) v[r4] = buf[68 * r4];
    }
    fft256_inv(v, lane, tw, [&](int i) __attribute__((always_inline)) {});
    {
      gu32* fs = (gu32*)(ws + OFF_FLS + ((7 * w + (lane < 7 ? lane : 0)) * 4 + (nt0 >> 3)) * 64);
#pragma nounroll
      for (unsigned spin = 0; spin < SPIN_CAP; ++spin) {
        const unsigned f = __hip_atomic_load(fs, __ATOMIC_RELAXED, __HIP_MEMORY_SCOPE_AGENT);
        if (__builtin_amdgcn_ballot_w64(f != 1u) == 0ull) break;
        __builtin_amdgcn_s_sleep(1);
      }
      __builtin_amdgcn_fence(__ATOMIC_ACQUIRE, "wavefront");
      asm volatile("" ::: "memory");
    }
    pf(16, 4);
    STAMP(3, 2);
    float s = 0.f;
    u4v x1p;
#pragma unroll
    for (int j = 0; j < 4; ++j) {
      f2 t = f2{bf_lo(xr[j]), bf_hi(xr[j])} + bfr2(f2{bf_lo(gav[j]), bf_hi(gav[j])} * bfr2(v[j]));
      x1p[j] = pack_bf(t.x, t.y);
      v[j] = f2{bf_lo(x1p[j]), bf_hi(x1p[j])};
      s += v[j].x + v[j].y;
    }
    ST128(x1p, OFF_X1 + ((row0 + w) * 64 + lane) * 16);
    s = wave_sum(s);
    const float meanf = s * (1.0f / D_);
    float q = 0.f;
#pragma unroll
    for (int j = 0; j < 4; ++j) {
      f2 d = v[j] - meanf;
      q += d.x * d.x + d.y * d.y;
    }
    q = wave_sum(q);
    const float mean = bfr(meanf);
    const float den = bfr(sqrtf(bfr(bfr(q * (1.0f / D_)) + 1e-5f)));
    const float rden = 1.0f / den;
    u4v hp;
#pragma unroll
    for (int j = 0; j < 4; ++j) {
      f2 t = __builtin_elementwise_fma(v[j] - mean, f2{bf_lo(cbv[j]), bf_hi(cbv[j])} * rden,
                                       f2{bf_lo(sbv[j]), bf_hi(sbv[j])});
      hp[j] = pack_bf(t.x, t.y);
    }
    *(u4v*)(dsm + L_ST + (lane * 9 + w) * 16) = hp;
    __syncthreads();
    {
      const int ch = tid >> 3, r = tid & 7;
      u4v o = *(const u4v*)(dsm + L_ST + (ch * 9 + r) * 16);
      const int mt = row0 >> 4, r16 = (row0 & 8) + r;
      ST128(o, OFF_H2F + ((mt * 16 + (ch >> 2)) * 64 + (ch & 3) * 16 + r16) * 16);
    }
  }
  STAMP(3, 3);
  DRAIN();
  __syncthreads();
  if (tid == 0) __hip_atomic_store(myA, 1u, __ATOMIC_RELAXED, __HIP_MEMORY_SCOPE_AGENT);
  {
    const int ch = tid >> 3, r = tid & 7;
    u4v t = LD128(OFF_H2F + (((row0 >> 4) * 16 + (ch >> 2)) * 64 + (ch & 3) * 16 + (row0 & 8) + r) * 16);
    asm volatile("" ::"v"(t));
  }
  STAMP(3, 4);
  wait_pair(depA);
  STAMP(3, 5);
  {
    u4v B[16];
#pragma unroll
    for (int ks = 0; ks < 16; ++ks) B[ks] = LD128(OFF_H2F + (((8 * p + w) * 16 + ks) * 64 + lane) * 16);
    f4 acc[7];
#pragma unroll
    for (int ft = 0; ft < 7; ++ft) acc[ft] = f4{0.f, 0.f, 0.f, 0.f};
    const char* lw = dsm + L_W1 + lane * 16;
    const int e = c >> 1, odd = c & 1;
    const int hb = (int)OFF_HF + (((8 * p + w) * 56 + 7 * e) * 64 + lane) * 16;
    u4v Af[2][7];
#pragma unroll
    for (int j = 0; j < 7; ++j) Af[0][j] = *(const u4v*)(lw + (g1_ks(0, j) * 7 + g1_ft(0, j)) * 1024);
#pragma unroll
    for (int g = 0; g < 17; ++g) {
#pragma unroll
      for (int j = 0; j < 7; ++j) {
        if (j < g1_n(g)) acc[g1_ft(g, j)] = mfma16(Af[g & 1][j], B[g1_ks(g, j)], acc[g1_ft(g, j)]);
        if (g + 1 < 17 && j < g1_n(g + 1))
          Af[(g + 1) & 1][j] = *(const u4v*)(lw + (g1_ks(g + 1, j) * 7 + g1_ft(g + 1, j)) * 1024);
        SCHED();
      }
      if (g >= 1 && g < 13) { pf_load(18 + 2 * g); pf_load(19 + 2 * g); }
      if (g >= 14) {
        const int i = g - 14;
        f4 a = acc[2 * i], b = acc[2 * i + 1];
        u4v o = u4v{silu_pk(a[0], a[1]), silu_pk(a[2], a[3]), silu_pk(b[0], b[1]), silu_pk(b[2], b[3])};
        ST128(o, hb + (odd * 3 + i) * 1024);
      }
      SCHED();
      if (g == 12) STAMP(3, 6);
    }
    f4 a = acc[6];
    u2v o = u2v{silu_pk(a[0], a[1]), silu_pk(a[2], a[3])};
    ST64(o, hb + 6 * 1024 + odd * 8);
  }
  STAMP(3, 7);
  DRAIN();
  __syncthreads();
  if (tid == 0) __hip_atomic_store(myB, 1u, __ATOMIC_RELAXED, __HIP_MEMORY_SCOPE_AGENT);
  {
    const int hb = (int)OFF_HF + (((8 * p + w) * 56 + 7 * (c >> 1) + 3 * (c & 1)) * 64 + lane) * 16;
    u4v t0 = LD128(hb), t1 = LD128(hb + 1024), t2 = LD128(hb + 2048);
    asm volatile("" ::"v"(t0), "v"(t1), "v"(t2));
  }
  wait_pair(depB);
  STAMP(3, 8);
  {
    const int Bb = (int)OFF_HF + ((mt0 * 56 + 7 * w) * 64 + lane) * 16;
    u4v B[7][4];
#pragma unroll
    for (int ks = 0; ks < 4; ++ks)
#pragma unroll
      for (int mt = 0; mt < 4; ++mt) B[ks][mt] = LD128(Bb + (mt * 56 + ks) * 1024);
    f4 acc[4][4];
#pragma unroll
    for (int i = 0; i < 4; ++i)
#pragma unroll
      for (int j = 0; j < 4; ++j) acc[i][j] = f4{0.f, 0.f, 0.f, 0.f};
#pragma unroll
    for (int ks = 0; ks < 7; ++ks) {
#pragma unroll
      for (int nt = 0; nt < 4; ++nt)
#pragma unroll
        for (int mt = 0; mt < 4; ++mt) acc[nt][mt] = mfma16(A2[ks * 4 + nt], B[ks][mt], acc[nt][mt]);
      if (ks + 4 < 7) {
#pragma unroll
        for (int mt = 0; mt < 4; ++mt) B[ks + 4][mt] = LD128(Bb + (mt * 56 + ks + 4) * 1024);
      }
      SCHED();
    }
    STAMP(3, 9);
    const int g = lane >> 4, mr = lane & 15;
    const int fm = w >> 1, P = w & 1;
    const int m = (mt0 + fm) * 16 + mr;
    const int n = nt0 * 16 + 32 * P + 8 * g;
    unsigned xv[4];
#pragma unroll
    for (int i = 0; i < 4; ++i)
      xv[i] = __builtin_amdgcn_raw_buffer_load_b32(
          rs, (int)OFF_X1 + (m * 64 + (((n >> 1) + i) & 63)) * 16 + (n >> 7) * 4, 0, AUX_SC1);
    const f4 g0 = __builtin_bit_cast(f4, LD128(OFF_MODF + (2560 + n) * 4));
    const f4 g1 = __builtin_bit_cast(f4, LD128(OFF_MODF + (2560 + n + 4) * 4));
    f4* red = (f4*)dsm;
#pragma unroll
    for (int nt = 0; nt < 4; ++nt)
#pragma unroll
      for (int mt = 0; mt < 4; ++mt) red[(w * 16 + nt * 4 + mt) * 64 + lane] = acc[nt][mt];
    __syncthreads();
    STAMP(3, 10);
    f4 s0 = f4{0.f, 0.f, 0.f, 0.f}, s1 = s0;
#pragma unroll
    for (int ww = 0; ww < 8; ++ww) {
      s0 += red[(ww * 16 + (2 * P) * 4 + fm) * 64 + lane];
      s1 += red[(ww * 16 + (2 * P + 1) * 4 + fm) * 64 + lane];
    }
    u4v o;
    {
      f2 t0 = bfr2(f2{g0[0], g0[1]} * bfr2(f2{s0[0], s0[1]})), t1 = bfr2(f2{g0[2], g0[3]} * bfr2(f2{s0[2], s0[3]}));
      f2 t2 = bfr2(f2{g1[0], g1[1]} * bfr2(f2{s1[0], s1[1]})), t3 = bfr2(f2{g1[2], g1[3]} * bfr2(f2{s1[2], s1[3]}));
      o[0] = pack_bf(bf_lo(xv[0]) + t0.x, bf_hi(xv[0]) + t0.y);
      o[1] = pack_bf(bf_lo(xv[1]) + t1.x, bf_hi(xv[1]) + t1.y);
      o[2] = pack_bf(bf_lo(xv[2]) + t2.x, bf_hi(xv[2]) + t2.y);
      o[3] = pack_bf(bf_lo(xv[3]) + t3.x, bf_hi(xv[3]) + t3.y);
    }
    *(u4v*)(out + (size_t)m * D_ + n) = o;
  }
  STAMP(3, 11);
}

extern "C" void kernel_launch(void* const* d_in, const int* in_sizes, int n_in, void* d_out, int out_size, void* d_ws,
                              size_t ws_size, hipStream_t stream) {
  const u16* x = (const u16*)d_in[0];
  const u16* wr = (const u16*)d_in[1];
  const u16* wi = (const u16*)d_in[2];
  const u16* w1 = (const u16*)d_in[4];
  const u16* w2 = (const u16*)d_in[5];
  const u16* c = (const u16*)d_in[6];
  const u16* wmod = (const u16*)d_in[7];
  char* ws = (char*)d_ws;
  u16* out = (u16*)d_out;
  (void)hipFuncSetAttribute((const void*)kmega, hipFuncAttributeMaxDynamicSharedMemorySize, L_TOTAL);
  k0_mod<<<K0_GEMV + K0_WT + K0_W2 + K0_Z + K0_WARM, 512, 0, stream>>>(c, wmod, x, wr, wi, w1, w2, ws);
  kmega<<<256, 512, L_TOTAL, stream>>>(x, w1, w2, ws, out);
}
```

```cpp
#include <hip/hip_runtime.h>
#include <hip/hip_bf16.h>
#include <stdint.h>

typedef float f2 __attribute__((ext_vector_type(2)));
typedef float f4 __attribute__((ext_vector_type(4)));
typedef unsigned u2v __attribute__((ext_vector_type(2)));
typedef unsigned u4v __attribute__((ext_vector_type(4)));
typedef short bf16x8 __attribute__((ext_vector_type(8)));
typedef unsigned short u16;

constexpr int S_ = 2048, D_ = 512, F_ = 1792, DF_ = 257;
constexpr int NSPLIT = 4;

constexpr size_t OFF_CNT = 0;
#define FSPACE 16
#define FREP 16
constexpr int FSP = FSPACE, NREP = FREP;
constexpr size_t FTAB = (size_t)NREP * 256 * FSP;
constexpr size_t OFF_FLG = 16384;
constexpr size_t OFF_FLR = OFF_FLG + FTAB;
constexpr size_t OFF_FLS = OFF_FLR + FTAB;
constexpr size_t FLAG_BYTES = OFF_FLS + 16384;
constexpr size_t OFF_MODP = 900 << 10;
constexpr size_t OFF_MODF = OFF_MODP + (size_t)NSPLIT * 3072 * 4;
constexpr size_t OFF_MODC = OFF_MODF + 3072 * 4;
constexpr size_t OFF_WT = 1 << 20;
constexpr size_t OFF_XT = 4 << 20;
constexpr size_t OFF_ZT = 28 << 20;
constexpr size_t OFF_H2F = 12 << 20;
constexpr size_t OFF_X1 = 14 << 20;
constexpr size_t OFF_W2F = 18 << 20;
constexpr size_t OFF_HF = 20 << 20;
constexpr int WS_SPAN = 36 << 20;
#define CPITCH (2048 + 32)
constexpr int CP = CPITCH;
constexpr int WT_PITCH = 520;
constexpr float FSC = 1.0f / (2.0f * 2048.0f * 512.0f);

#define STAMP(kid, i)

__device__ __forceinline__ float bf_lo(unsigned u) { return __builtin_bit_cast(float, u << 16); }
__device__ __forceinline__ float bf_hi(unsigned u) { return __builtin_bit_cast(float, u & 0xffff0000u); }
__device__ __forceinline__ float bfr(float x) {
  return (float)(__bf16)x;
}
__device__ __forceinline__ unsigned pack_bf(float lo, float hi) {
  typedef __bf16 b2 __attribute__((ext_vector_type(2)));
  return __builtin_bit_cast(unsigned, __builtin_convertvector(f2{lo, hi}, b2));
}
__device__ __forceinline__ f2 cmul(f2 a, f2 b) {
  f2 t = a * f2{b.x, b.x};
  return __builtin_elementwise_fma(f2{a.y, a.x}, f2{-b.y, b.y}, t);
}
__device__ __forceinline__ f2 cmulc(f2 a, f2 b) {
  f2 t = a * f2{b.x, b.x};
  return __builtin_elementwise_fma(f2{a.y, a.x}, f2{b.y, -b.y}, t);
}
__device__ __forceinline__ f2 bfr2(f2 a) {
  unsigned u = pack_bf(a.x, a.y);
  asm("" : "+v"(u));
  return f2{bf_lo(u), bf_hi(u)};
}
__device__ __forceinline__ float msel(unsigned m, float a, float b) {
  unsigned ua = __builtin_bit_cast(unsigned, a), ub = __builtin_bit_cast(unsigned, b);
  return __builtin_bit_cast(float, (m & ua) | (~m & ub));
}
#define PIN2(x) asm volatile("" : "+v"(x))
constexpr int AUX_SC1 = 16;
#define WS_RSRC(ws) __builtin_amdgcn_make_buffer_rsrc((ws), 0, WS_SPAN, 0x00020000)
#define ST128(v, off) __builtin_amdgcn_raw_buffer_store_b128((v), rs, (int)(off), 0, AUX_SC1)
#define ST64(v, off) __builtin_amdgcn_raw_buffer_store_b64((v), rs, (int)(off), 0, AUX_SC1)
#define ST32(v, off) __builtin_amdgcn_raw_buffer_store_b32((v), rs, (int)(off), 0, AUX_SC1)
#define LD64(off) __builtin_amdgcn_raw_buffer_load_b64(rs, (int)(off), 0, AUX_SC1)
#define LD32(off) __builtin_amdgcn_raw_buffer_load_b32(rs, (int)(off), 0, AUX_SC1)
#define LD128(off) __builtin_amdgcn_raw_buffer_load_b128(rs, (int)(off), 0, AUX_SC1)
__device__ __forceinline__ f2 twid(float rev) { return f2{__builtin_amdgcn_cosf(rev), -__builtin_amdgcn_sinf(rev)}; }

template <int DIR>
__device__ __forceinline__ f2 rot90(f2 a) {
  return DIR < 0 ? f2{a.y, -a.x} : f2{-a.y, a.x};
}
template <int DIR>
__device__ __forceinline__ void dft4(f2& c0, f2& c1, f2& c2, f2& c3) {
  f2 d0 = c0 + c2, d2 = c0 - c2, d1 = c1 + c3, e = c1 - c3;
  c0 = d0 + d1;
  c2 = d0 - d1;
  const f2 es = f2{e.y, e.x};
  c1 = __builtin_elementwise_fma(es, DIR < 0 ? f2{1.f, -1.f} : f2{-1.f, 1.f}, d2);
  c3 = __builtin_elementwise_fma(es, DIR < 0 ? f2{-1.f, 1.f} : f2{1.f, -1.f}, d2);
}

__device__ __forceinline__ void sw32(float& a, float& b) {
  auto r = __builtin_amdgcn_permlane32_swap(__builtin_bit_cast(unsigned, a), __builtin_bit_cast(unsigned, b), false, false);
  a = __builtin_bit_cast(float, (unsigned)r[0]);
  b = __builtin_bit_cast(float, (unsigned)r[1]);
}
__device__ __forceinline__ void sw16(float& a, float& b) {
  auto r = __builtin_amdgcn_permlane16_swap(__builtin_bit_cast(unsigned, a), __builtin_bit_cast(unsigned, b), false, false);
  a = __builtin_bit_cast(float, (unsigned)r[0]);
  b = __builtin_bit_cast(float, (unsigned)r[1]);
}
template <int CTRL, int BANK>
__device__ __forceinline__ float udpp(float old, float src) {
  return __builtin_bit_cast(float, __builtin_amdgcn_update_dpp(__builtin_bit_cast(int, old), __builtin_bit_cast(int, src),
                                                               CTRL, 0xf, BANK, false));
}
template <int CTRL>
__device__ __forceinline__ float mdpp(float src) {
  return __builtin_bit_cast(float, __builtin_amdgcn_mov_dpp(__builtin_bit_cast(int, src), CTRL, 0xf, 0xf, false));
}
__device__ __forceinline__ void xp_hi1(float& v0, float& v1, float& v2, float& v3) {
  sw32(v0, v2); sw32(v1, v3); sw16(v0, v1); sw16(v2, v3);
}
__device__ __forceinline__ void xp_mid1(float& v0, float& v1, float& v2, float& v3) {
  float a0 = udpp<0x128, 0xC>(v0, v2), a2 = udpp<0x128, 0x3>(v2, v0);
  float a1 = udpp<0x128, 0xC>(v1, v3), a3 = udpp<0x128, 0x3>(v3, v1);
  v0 = udpp<0x124, 0xA>(a0, a1); v1 = udpp<0x12C, 0x5>(a1, a0);
  v2 = udpp<0x124, 0xA>(a2, a3); v3 = udpp<0x12C, 0x5>(a3, a2);
}
__device__ __forceinline__ void xp_lo1(float& v0, float& v1, float& v2, float& v3, unsigned b1, unsigned b0) {
  float s0 = mdpp<0x4E>(msel(b1, v0, v2)), s1 = mdpp<0x4E>(msel(b1, v1, v3));
  float a0 = msel(b1, s0, v0), a2 = msel(b1, v2, s0), a1 = msel(b1, s1, v1), a3 = msel(b1, v3, s1);
  float t0 = mdpp<0xB1>(msel(b0, a0, a1)), t2 = mdpp<0xB1>(msel(b0, a2, a3));
  v0 = msel(b0, t0, a0); v1 = msel(b0, a1, t0); v2 = msel(b0, t2, a2); v3 = msel(b0, a3, t2);
}
#define XP_CPLX(NAME, FN, ...)                                        \
  {                                                                   \
    float x0 = v[0].x, x1 = v[1].x, x2 = v[2].x, x3 = v[3].x;         \
    float y0 = v[0].y, y1 = v[1].y, y2 = v[2].y, y3 = v[3].y;         \
    FN(x0, x1, x2, x3, ##__VA_ARGS__);                                \
    FN(y0, y1, y2, y3, ##__VA_ARGS__);                                \
    v[0] = f2{x0, y0}; v[1] = f2{x1, y1}; v[2] = f2{x2, y2}; v[3] = f2{x3, y3}; \
  }
__device__ __forceinline__ void xp_hi(f2 (&v)[4]) XP_CPLX(hi, xp_hi1)
__device__ __forceinline__ void xp_mid(f2 (&v)[4]) XP_CPLX(mid, xp_mid1)
__device__ __forceinline__ void xp_lo(f2 (&v)[4], unsigned b1, unsigned b0) XP_CPLX(lo, xp_lo1, b1, b0)

template <bool CONJ>
__device__ __forceinline__ void cmul3(f2 (&v)[4], const f2 (&t)[3]) {
  f2 a, b, c;
  if (!CONJ)
    asm("v_pk_mul_f32 %3, %0, %6 op_sel_hi:[1,0]\n\t"
        "v_pk_mul_f32 %4, %1, %7 op_sel_hi:[1,0]\n\t"
        "v_pk_mul_f32 %5, %2, %8 op_sel_hi:[1,0]\n\t"
        "v_pk_fma_f32 %0, %0, %6, %3 op_sel:[1,1,0] op_sel_hi:[0,1,1] neg_lo:[0,1,0]\n\t"
        "v_pk_fma_f32 %1, %1, %7, %4 op_sel:[1,1,0] op_sel_hi:[0,1,1] neg_lo:[0,1,0]\n\t"
        "v_pk_fma_f32 %2, %2, %8, %5 op_sel:[1,1,0] op_sel_hi:[0,1,1] neg_lo:[0,1,0]\n\t"
        "s_nop 1"
        : "+v"(v[1]), "+v"(v[2]), "+v"(v[3]), "=&v"(a), "=&v"(b), "=&v"(c)
        : "v"(t[0]), "v"(t[1]), "v"(t[2]));
  else
    asm("v_pk_mul_f32 %3, %0, %6 op_sel_hi:[1,0]\n\t"
        "v_pk_mul_f32 %4, %1, %7 op_sel_hi:[1,0]\n\t"
        "v_pk_mul_f32 %5, %2, %8 op_sel_hi:[1,0]\n\t"
        "v_pk_fma_f32 %0, %0, %6, %3 op_sel:[1,1,0] op_sel_hi:[0,1,1] neg_hi:[0,1,0]\n\t"
        "v_pk_fma_f32 %1, %1, %7, %4 op_sel:[1,1,0] op_sel_hi:[0,1,1] neg_hi:[0,1,0]\n\t"
        "v_pk_fma_f32 %2, %2, %8, %5 op_sel:[1,1,0] op_sel_hi:[0,1,1] neg_hi:[0,1,0]"
        : "+v"(v[1]), "+v"(v[2]), "+v"(v[3]), "=&v"(a), "=&v"(b), "=&v"(c)
        : "v"(t[0]), "v"(t[1]), "v"(t[2]));
}
template <bool CONJ>
__device__ __forceinline__ f2 cmul1(f2 v, f2 t) {
  f2 a;
  if (!CONJ)
    asm("v_pk_mul_f32 %1, %0, %2 op_sel_hi:[1,0]\n\t"
        "v_pk_fma_f32 %0, %0, %2, %1 op_sel:[1,1,0] op_sel_hi:[0,1,1] neg_lo:[0,1,0]"
        : "+v"(v), "=&v"(a) : "v"(t));
  else
    asm("v_pk_mul_f32 %1, %0, %2 op_sel_hi:[1,0]\n\t"
        "v_pk_fma_f32 %0, %0, %2, %1 op_sel:[1,1,0] op_sel_hi:[0,1,1] neg_hi:[0,1,0]"
        : "+v"(v), "=&v"(a) : "v"(t));
  return v;
}
__device__ __forceinline__ void xp_lo_lds(f2 (&v)[4], f2* patch, int lane) {
  f2* q = patch + (lane >> 2) * 16;
  const int c = lane & 3, sw = (lane >> 2) & 3;
#pragma unroll
  for (int j = 0; j < 4; ++j) q[4 * (j ^ sw) + c] = v[j];
  __builtin_amdgcn_fence(__ATOMIC_RELEASE, "wavefront");
  __builtin_amdgcn_wave_barrier();
  __builtin_amdgcn_fence(__ATOMIC_ACQUIRE, "wavefront");
  const f4 a = *(const f4*)(q + 4 * (c ^ sw)), b = *(const f4*)(q + 4 * (c ^ sw) + 2);
  v[0] = f2{a[0], a[1]}; v[1] = f2{a[2], a[3]}; v[2] = f2{b[0], b[1]}; v[3] = f2{b[2], b[3]};
  __builtin_amdgcn_fence(__ATOMIC_RELEASE, "wavefront");
  __builtin_amdgcn_wave_barrier();
}

struct Tw256 {
  f2 a[3], b[3], c[3];
};
__device__ __forceinline__ void make_tw256(int lane, Tw256& t) {
#pragma unroll
  for (int r = 1; r < 4; ++r) {
    t.a[r - 1] = twid((float)(lane * r) * (1.0f / 256.0f));
    t.b[r - 1] = twid((float)((lane & 15) * r) * (1.0f / 64.0f));
    t.c[r - 1] = twid((float)((lane & 3) * r) * (1.0f / 16.0f));
  }
#pragma unroll
  for (int r = 0; r < 3; ++r) {
    PIN2(t.a[r]);
    PIN2(t.b[r]);
    PIN2(t.c[r]);
  }
}
struct NoHook {
  __device__ __forceinline__ void operator()(int) const {}
};
template <class H>
__device__ __forceinline__ void fft256_fwd(f2 (&v)[4], int lane, const Tw256& t, f2* patch, const H& hook) {
  dft4<-1>(v[0], v[1], v[2], v[3]);
  cmul3<false>(v, t.a);
  xp_hi(v);
  hook(0);
  dft4<-1>(v[0], v[1], v[2], v[3]);
  cmul3<false>(v, t.b);
  xp_mid(v);
  hook(1);
  dft4<-1>(v[0], v[1], v[2], v[3]);
  cmul3<false>(v, t.c);
  xp_lo_lds(v, patch, lane);
  hook(2);
  dft4<-1>(v[0], v[1], v[2], v[3]);
}
template <class H>
__device__ __forceinline__ void fft256_inv(f2 (&v)[4], int lane, const Tw256& t, f2* patch, const H& hook) {
  dft4<1>(v[0], v[1], v[2], v[3]);
  xp_lo_lds(v, patch, lane);
  hook(0);
  cmul3<true>(v, t.c);
  dft4<1>(v[0], v[1], v[2], v[3]);
  xp_mid(v);
  hook(1);
  cmul3<true>(v, t.b);
  dft4<1>(v[0], v[1], v[2], v[3]);
  xp_hi(v);
  hook(2);
  cmul3<true>(v, t.a);
  dft4<1>(v[0], v[1], v[2], v[3]);
}

__device__ __forceinline__ float wave_sum(float x) {
  x += mdpp<0xB1>(x);
  x += mdpp<0x4E>(x);
  x += mdpp<0x141>(x);
  x += mdpp<0x140>(x);
  float r0 = __builtin_bit_cast(float, __builtin_amdgcn_readlane(__builtin_bit_cast(int, x), 0));
  float r1 = __builtin_bit_cast(float, __builtin_amdgcn_readlane(__builtin_bit_cast(int, x), 16));
  float r2 = __builtin_bit_cast(float, __builtin_amdgcn_readlane(__builtin_bit_cast(int, x), 32));
  float r3 = __builtin_bit_cast(float, __builtin_amdgcn_readlane(__builtin_bit_cast(int, x), 48));
  return (r0 + r1) + (r2 + r3);
}

__device__ __forceinline__ int zpos(int k) { return 17 * (k & 3) + 4 * ((k >> 2) & 3) + ((k >> 4) & 3) + 68 * (k >> 6); }
constexpr int ZRS = 276;

constexpr int K0_GEMV = 48 * NSPLIT, K0_WT = 128, K0_W2 = 112, K0_WARM = 0, K0_Z = (int)(FLAG_BYTES / 8192);
__global__ __launch_bounds__(512) void k0_mod(const u16* __restrict__ c, const u16* __restrict__ wmod,
                                              const u16* __restrict__ x, const u16* __restrict__ wr,
                                              const u16* __restrict__ wi, const u16* __restrict__ w1,
                                              const u16* __restrict__ w2, char* __restrict__ ws) {
  __shared__ __attribute__((aligned(16))) char smem[17408];
  const int tid = threadIdx.x, row = tid >> 6, lane = tid & 63;
  const auto rs = WS_RSRC(ws);
  int b = blockIdx.x;
  STAMP(0, 0);
  if (b >= K0_GEMV + K0_WT + K0_W2) {
    int q = b - (K0_GEMV + K0_WT + K0_W2);
    if (q < K0_Z) {
      ((u4v*)ws)[q * 512 + tid] = u4v{0u, 0u, 0u, 0u};
      return;
    }
    q -= K0_Z;
    const char* src = q < 64 ? (const char*)x + (size_t)q * 32768
                             : (q < 120 ? (const char*)w1 + (size_t)(q - 64) * 32768 : (const char*)w2 + (size_t)(q - 120) * 32768);
    u4v t[4];
#pragma unroll
    for (int i = 0; i < 4; ++i) t[i] = *(const u4v*)(src + i * 8192 + tid * 16);
#pragma unroll
    for (int i = 0; i < 4; ++i) asm volatile("" ::"v"(t[i]));
    STAMP(0, 1);
    return;
  }
  if (b >= K0_GEMV) {
    b -= K0_GEMV;
    if (b < K0_WT) {
      char* sr = smem;
      char* si = smem + 16 * 544;
      constexpr int LASTCH = (S_ * DF_ * 2) / 16 - 1;
      for (int it = tid; it < 16 * 34; it += 512) {
        const int i = it / 34, ch = it % 34;
        int gch = ((b + 128 * i) * (DF_ * 2)) / 16 + ch;
        gch = gch > LASTCH ? LASTCH : gch;
        *(u4v*)(sr + i * 544 + ch * 16) = *(const u4v*)((const char*)wr + (size_t)gch * 16);
        *(u4v*)(si + i * 544 + ch * 16) = *(const u4v*)((const char*)wi + (size_t)gch * 16);
      }
      __syncthreads();
      const int tbase = 64 * (b & 7) + 16 * ((b >> 3) & 3) + 4 * ((b >> 5) & 3);
      for (int it = tid; it < DF_ * 4; it += 512) {
        const int k = it >> 2, r3 = it & 3;
        u4v o;
#pragma unroll
        for (int r4 = 0; r4 < 4; ++r4) {
          const int i = r3 + 4 * r4;
          const int off = i * 544 + (((b + 128 * i) * (DF_ * 2)) & 15) + 2 * k;
          o[r4] = (unsigned)*(const u16*)(sr + off) | ((unsigned)*(const u16*)(si + off) << 16);
        }
        ST128(o, OFF_WT + (k * 512 + tbase + r3) * 16);
      }
    } else {
      b -= K0_WT;
      u16* t = (u16*)smem;
      const int g = lane >> 4, r = lane & 15;
      const int ng = b & 3, kp = b >> 2;
#pragma unroll
      for (int it = 0; it < 2; ++it) {
        const int idx = it * 512 + tid;
        const int rr = idx >> 4, ch = idx & 15;
        const int ks2 = kp * 2 + (rr >> 5), gg = (rr >> 3) & 3, j = rr & 7;
        const int e = ks2 / 7, i7 = ks2 % 7;
        int T0, T1;
        if (i7 < 3) { T0 = 14 * e + 2 * i7; T1 = T0 + 1; }
        else if (i7 < 6) { T0 = 14 * e + 7 + 2 * (i7 - 3); T1 = T0 + 1; }
        else { T0 = 14 * e + 6; T1 = 14 * e + 13; }
        const int f = (j < 4) ? (16 * T0 + 4 * gg + j) : (16 * T1 + 4 * gg + (j - 4));
        *(u4v*)(t + rr * 136 + ch * 8) = *(const u4v*)(w2 + (size_t)f * D_ + ng * 128 + ch * 8);
      }
      __syncthreads();
      const int nl = 32 * (row >> 1) + 8 * (r >> 2) + 4 * (row & 1) + (r & 3);
#pragma unroll
      for (int ksl = 0; ksl < 2; ++ksl) {
        u4v o;
#pragma unroll
        for (int jj = 0; jj < 4; ++jj)
          o[jj] = (unsigned)t[(ksl * 32 + g * 8 + 2 * jj) * 136 + nl] |
                  ((unsigned)t[(ksl * 32 + g * 8 + 2 * jj + 1) * 136 + nl] << 16);
        const int nt = ng * 8 + row;
        ST128(o, OFF_W2F + ((nt * 56 + kp * 2 + ksl) * 64 + lane) * 16);
      }
    }
    STAMP(0, 1);
    return;
  }
  float* red = (float*)smem;
  const int cgp = b % 48, ks = b / 48;
  const int cg = tid & 7, r = tid >> 3;
  const int rw = ks * 128 + r;
  const u4v wm0 = *(const u4v*)(wmod + (size_t)rw * 3072 + cgp * 64 + cg * 8);
  const u4v wm1 = *(const u4v*)(wmod + (size_t)(rw + 64) * 3072 + cgp * 64 + cg * 8);
  const float cv0 = bf_lo(c[rw]), cv1 = bf_lo(c[rw + 64]);
  const float s0 = bfr(cv0 / (1.0f + __expf(-cv0))), s1 = bfr(cv1 / (1.0f + __expf(-cv1)));
  float acc[8];
#pragma unroll
  for (int i = 0; i < 4; ++i) {
    acc[2 * i] = s0 * bf_lo(wm0[i]) + s1 * bf_lo(wm1[i]);
    acc[2 * i + 1] = s0 * bf_hi(wm0[i]) + s1 * bf_hi(wm1[i]);
  }
#pragma unroll
  for (int i = 0; i < 8; ++i) {
    acc[i] += mdpp<0x128>(acc[i]);
    acc[i] += __shfl_xor(acc[i], 16);
    acc[i] += __shfl_xor(acc[i], 32);
  }
  if (lane < 8) {
#pragma unroll
    for (int i = 0; i < 8; ++i) red[row * 64 + cg * 8 + i] = acc[i];
  }
  __syncthreads();
  if (tid < 64) {
    float sum = 0.f;
#pragma unroll
    for (int i = 0; i < 8; ++i) sum += red[i * 64 + tid];
    ((float*)(ws + OFF_MODP))[ks * 3072 + cgp * 64 + tid] = sum;
  }
  STAMP(0, 1);
}

__device__ __forceinline__ f4 mfma16(u4v a, u4v b, f4 c) {
  return __builtin_amdgcn_mfma_f32_16x16x32_bf16(__builtin_bit_cast(bf16x8, a), __builtin_bit_cast(bf16x8, b), c, 0, 0,
                                                  0);
}
__device__ __forceinline__ f2 silu2(float a, float b) {
  f2 h = f2{a, b};
  f2 t = h * -1.44269504088896340736f;
  f2 d = f2{__builtin_amdgcn_exp2f(t.x), __builtin_amdgcn_exp2f(t.y)} + 1.0f;
  return h * f2{__builtin_amdgcn_rcpf(d.x), __builtin_amdgcn_rcpf(d.y)};
}
__device__ __forceinline__ unsigned silu_pk(float a, float b) {
  f2 r = silu2(a, b);
  return pack_bf(r.x, r.y);
}
typedef __attribute__((address_space(1))) unsigned gu32;
#define DRAIN() asm volatile("s_waitcnt vmcnt(0)" ::: "memory")
typedef __attribute__((address_space(1))) unsigned long long gu64;
constexpr unsigned SPIN_CAP = 1u << 18;
__device__ __forceinline__ void wait_pair(gu64* flags) {
#pragma nounroll
  for (unsigned spin = 0; spin < SPIN_CAP; ++spin) {
    if (__hip_atomic_load(flags, __ATOMIC_RELAXED, __HIP_MEMORY_SCOPE_AGENT) == 0x0000000100000001ull) break;
    __builtin_amdgcn_s_sleep(1);
  }
  __builtin_amdgcn_fence(__ATOMIC_ACQUIRE, "wavefront");
  asm volatile("" ::: "memory");
}

#define PSLEEP 2
__device__ __forceinline__ void wait_cols(gu32* flags) {
#pragma nounroll
  for (unsigned spin = 0; spin < SPIN_CAP; ++spin) {
    unsigned f = 1u;
#pragma unroll
    for (int j = 0; j < 4; ++j)
      f &= __hip_atomic_load(flags + j * 64 * (FSP / 4), __ATOMIC_RELAXED, __HIP_MEMORY_SCOPE_AGENT);
    if (__builtin_amdgcn_ballot_w64(f != 1u) == 0ull) break;
    __builtin_amdgcn_s_sleep(PSLEEP);
  }
}

__device__ __forceinline__ int wt_off(int k, int t) { return (int)OFF_WT + (k * 512 + t) * 16; }

constexpr int L_W1 = 0;
constexpr int L_T = 112 * 1024;
constexpr int L_ZS = 112 * 1024;
constexpr int L_ST = L_ZS + 8 * ZRS * 8;
constexpr int L_TOTAL = 160 * 1024;
__device__ __forceinline__ constexpr int g1_n(int g) { return g < 13 ? 7 : (g < 16 ? 6 : 3); }
__device__ __forceinline__ constexpr int g1_ft(int g, int j) { return g < 13 ? j : (g < 16 ? 2 * (g - 13) + (j & 1) : 6); }
__device__ __forceinline__ constexpr int g1_ks(int g, int j) { return g < 13 ? g : (g < 16 ? 13 + (j >> 1) : 13 + j); }
#define SCHED() __builtin_amdgcn_sched_barrier(0)
typedef short v4i16 __attribute__((ext_vector_type(4)));
__device__ __forceinline__ u4v w1frag(const char* lw, int ft, int ks) {
  typedef __attribute__((address_space(3))) v4i16* lp;
  const u2v a = __builtin_bit_cast(u2v, __builtin_amdgcn_ds_read_tr16_b64_v4i16((lp)(lw + (2 * ks) * 3584 + ft * 32)));
  const u2v b = __builtin_bit_cast(u2v, __builtin_amdgcn_ds_read_tr16_b64_v4i16((lp)(lw + (2 * ks + 1) * 3584 + ft * 32)));
  return u4v{a[0], a[1], b[0], b[1]};
}
constexpr int L_MODS = L_ZS + 8 * ZRS * 8;
constexpr int L_PX = L_T + 28672;
constexpr int L_W2S = L_T + 32768;
__global__ __launch_bounds__(512, 2) void kmega(const u16* __restrict__ x, const u16* __restrict__ w1,
                                                const u16* __restrict__ w2, char* ws, u16* __restrict__ out) {
  extern __shared__ __attribute__((aligned(16))) char dsm[];
  const int tid = threadIdx.x, lane = tid & 63;
  const int w = __builtin_amdgcn_readfirstlane(tid >> 6);
  const int bid = blockIdx.x;
  const int p = (bid & 7) * 2 + (bid >> 7), c = (bid >> 3) & 15;
  const int row0 = 128 * p + 8 * c;
  STAMP(1, 0);
  const auto rs = WS_RSRC(ws);
  gu32* myA = (gu32*)(ws + OFF_CNT) + (p * 8 + (c >> 1)) * 16 + (c & 1);
  gu32* myB = myA + 128 * 16;
  gu64* depA = (gu64*)(ws + OFF_CNT) + (p * 8 + w) * 8;
  gu64* depB = depA + 128 * 8;
  const int mt0 = 8 * p + 4 * (c >> 3), nt0 = 4 * (c & 7);
  const int l56 = lane < 56 ? lane : lane - 56;
  const int r4_ = l56 / 14, ch_ = l56 - 14 * r4_;
  const char* w1g = (const char*)w1 + (size_t)(4 * w + r4_) * (F_ * 2) + c * 224 + ch_ * 16;
  char* w1l = dsm + L_W1 + ((w >> 1) * 32 + (2 * (w & 1) + (r4_ >> 1)) * 4 + (r4_ & 1)) * 224 + ch_ * 16;
  const int Ab = (int)OFF_W2F + ((nt0 * 56 + 7 * w) * 64 + lane) * 16;
  u4v S1[16];
  u4v A2[28];
  auto pf_load = [&](int i) __attribute__((always_inline)) {
    if (i < 16) S1[i] = *(const u4v*)(w1g + (size_t)i * (32 * F_ * 2));
    else if (i < 44) A2[i - 16] = *(const u4v*)(ws + Ab + (((i - 16) & 3) * 56 + ((i - 16) >> 2)) * 1024);
  };
  auto pf_put = [&](int i) __attribute__((always_inline)) {
    *(u4v*)(w1l + ((i & 3) * 128 + (i >> 3) * 16 + ((i >> 2) & 1) * 2) * 224) = S1[i];
  };
  auto pf = [&](int i0, int n) __attribute__((always_inline)) {
    SCHED();
#pragma unroll
    for (int i = 0; i < n; ++i) pf_load(i0 + i);
    SCHED();
  };
  Tw256 tw;
  {
    f2* zs = (f2*)(dsm + L_ZS);
    float* mods = (float*)(dsm + L_MODS);
    const int r0 = bid * 8;
    unsigned xr[4];
    const unsigned* xrow = (const unsigned*)(x + (size_t)(r0 + w) * D_);
#pragma unroll
    for (int j = 0; j < 4; ++j) xr[j] = xrow[lane + 64 * j];
    const float* mp = (const float*)(ws + OFF_MODP);
    f2 sm = *(const f2*)(mp + tid * 2);
#pragma unroll
    for (int k = 1; k < NSPLIT; ++k) sm += *(const f2*)(mp + k * 3072 + tid * 2);
    const int idx = bid * 12 + (tid < 6 ? tid : 0) * 2;
    f2 fin = *(const f2*)(mp + idx);
#pragma unroll
    for (int k = 1; k < NSPLIT; ++k) fin += *(const f2*)(mp + k * 3072 + idx);
    pf(0, 4);
    make_tw256(lane, tw);
    const int rp = tid & 3, kk = tid >> 2;
    f2 wpost = twid((float)kk * (1.0f / 512.0f));
    PIN2(wpost);
    f2 v[4];
    float s = 0.f;
#pragma unroll
    for (int j = 0; j < 4; ++j) {
      v[j] = f2{bf_lo(xr[j]), bf_hi(xr[j])};
      s += v[j].x + v[j].y;
    }
    s = wave_sum(s);
    const float meanf = s * (1.0f / D_);
    float q = 0.f;
#pragma unroll
    for (int j = 0; j < 4; ++j) {
      f2 d = v[j] - meanf;
      q += d.x * d.x + d.y * d.y;
    }
    q = wave_sum(q);
    const float mean = bfr(meanf);
    const float den = bfr(sqrtf(bfr(bfr(q * (1.0f / D_)) + 1e-5f)));
    const float rden = 1.0f / den;
    sm = bfr2(sm);
    if (tid >= 256) sm = bfr2(sm + 1.0f);
    *(f2*)(mods + tid * 2) = sm;
    if (tid < 6) {
      fin = bfr2(fin);
      ST64(__builtin_bit_cast(u2v, fin), OFF_MODF + idx * 4);
      const int vec = (idx >> 9) - 2, d = idx & 511;
      if (vec >= 0 && vec < 3) {
        if (vec == 2) fin += 1.0f;
        ST32(pack_bf(fin.x, fin.y), OFF_MODC + ((vec * 64 + ((d >> 1) & 63)) * 4 + (d >> 7)) * 4);
      }
    }
    STAMP(1, 3);
    __syncthreads();
    STAMP(1, 4);
    pf(4, 4);
#pragma unroll
    for (int j = 0; j < 4; ++j) {
      const int d = 2 * (lane + 64 * j);
      f2 sa = *(const f2*)(mods + d);
      f2 ca1 = *(const f2*)(mods + 512 + d);
      v[j] = bfr2(__builtin_elementwise_fma(v[j] - mean, ca1 * rden, sa));
    }
    fft256_fwd(v, lane, tw, (f2*)(dsm + L_PX + w * 2048), NoHook{});
    {
      f2* buf = zs + w * ZRS + lane + (lane >> 4);
#pragma unroll
      for (int r4 = 0; r4 < 4; ++r4) buf[68 * r4] = v[r4];
    }
    STAMP(1, 5);
    __syncthreads();
    {
      const int k = kk;
      const int k2 = (k == 0) ? 128 : 256 - k;
      const int pa = zpos(k), pb = zpos(k2);
      f4 oa, ob;
#pragma unroll
      for (int h = 0; h < 2; ++h) {
        const f2* zr = zs + (2 * rp + h) * ZRS;
        f2 za = zr[pa], zb = zr[pb];
        f2 E = f2{za.x + zb.x, za.y - zb.y};
        f2 Dm = f2{za.x - zb.x, za.y + zb.y};
        f2 O = f2{Dm.y, -Dm.x};
        f2 wo = cmul1<false>(O, wpost);
        f2 ra = E + wo;
        f2 t = E - wo;
        f2 rb = f2{t.x, -t.y};
        if (k == 0) {
          ra = f2{2.f * (za.x + za.y), 2.f * (za.x - za.y)};
          rb = f2{2.f * zb.x, -2.f * zb.y};
        }
        oa[2 * h] = ra.x; oa[2 * h + 1] = ra.y;
        ob[2 * h] = rb.x; ob[2 * h + 1] = rb.y;
      }
      ST128(__builtin_bit_cast(u4v, oa), OFF_XT + (k * CP + r0 + 2 * rp) * 8);
      ST128(__builtin_bit_cast(u4v, ob), OFF_XT + (k2 * CP + r0 + 2 * rp) * 8);
    }
    STAMP(1, 6);
    DRAIN();
    __syncthreads();
    if (tid < NREP)
      __hip_atomic_store((gu32*)(ws + OFF_FLR + (tid * 256 + bid) * FSP), 1u, __ATOMIC_RELAXED, __HIP_MEMORY_SCOPE_AGENT);
    STAMP(1, 7);
  }
  {
    f2* T = (f2*)(dsm + L_T);
    f2* T2 = (f2*)(dsm + L_T + 16384);
    f2* T3 = (f2*)(dsm + L_T + 32768);
    const int k = bid;
    f2 tw1[4];
#pragma unroll
    for (int j = 0; j < 4; ++j) {
      tw1[j] = twid((float)((lane + 64 * j) * w) * (1.0f / 2048.0f));
      PIN2(tw1[j]);
    }
    if (w == 0) wait_cols((gu32*)(ws + OFF_FLR + ((bid % NREP) * 256 + lane) * FSP));
    asm volatile("s_barrier" ::: "memory");
    __builtin_amdgcn_fence(__ATOMIC_ACQUIRE, "wavefront");
    STAMP(2, 0);
    f2 v[4];
#pragma unroll
    for (int j = 0; j < 4; ++j) v[j] = __builtin_bit_cast(f2, LD64(OFF_XT + (k * CP + tid + 512 * j) * 8));
    u4v bv;
    unsigned wn[4], bn[4];
    int mirr[4];
#pragma unroll
    for (int j = 0; j < 4; ++j) PIN2(v[j]);
    pf(8, 4);
    const int hb = w >> 2, a = tid & 255;
    constexpr float R = 0.70710678118654752440f;
    dft4<-1>(v[0], v[1], v[2], v[3]);
    if (hb) {
      v[1] = f2{v[1].x + v[1].y, v[1].y - v[1].x} * R;
      v[2] = f2{v[2].y, -v[2].x};
      v[3] = f2{v[3].y - v[3].x, -v[3].x - v[3].y} * R;
    }
#pragma unroll
    for (int q = 0; q < 4; ++q) T[(hb * 4 + q) * 256 + a] = v[q];
    STAMP(2, 1);
    const u4v wv = LD128(wt_off(k, tid));
    if (k == 0) {
      bv = LD128(wt_off(256, tid));
      const int m0 = (lane >> 4) + 4 * ((lane >> 2) & 3) + 16 * (lane & 3);
#pragma unroll
      for (int r4 = 0; r4 < 4; ++r4) {
        const int un = (S_ - (w + 8 * (m0 + 64 * r4))) & (S_ - 1);
        const int mm = un >> 3;
        mirr[r4] = ((un & 7) * 64 + 16 * (mm & 3) + 4 * ((mm >> 2) & 3) + ((mm >> 4) & 3)) * 4 + (mm >> 6);
      }
      if (w != 0) {
        const u4v a4 = LD128(wt_off(0, 64 * (8 - w) + 63 - lane));
        const u4v b4 = LD128(wt_off(256, 64 * (8 - w) + 63 - lane));
#pragma unroll
        for (int r4 = 0; r4 < 4; ++r4) { wn[r4] = a4[3 - r4]; bn[r4] = b4[3 - r4]; }
      } else {
#pragma unroll
        for (int r4 = 0; r4 < 4; ++r4) {
          wn[r4] = LD32(wt_off(0, mirr[r4] >> 2) + (mirr[r4] & 3) * 4);
          bn[r4] = LD32(wt_off(256, mirr[r4] >> 2) + (mirr[r4] & 3) * 4);
        }
      }
    }
    __syncthreads();
    {
      const int q = w & 3;
      const float sg = (w < 4) ? 1.f : -1.f;
#pragma unroll
      for (int j = 0; j < 4; ++j) {
        f2 e = T[q * 256 + lane + 64 * j], o = T[(4 + q) * 256 + lane + 64 * j];
        v[j] = cmul1<false>(e + o * sg, tw1[j]);
      }
    }
    fft256_fwd(v, lane, tw, T2 + w * 256, [&](int i) __attribute__((always_inline)) {
      SCHED();
      if (i < 2) {
#pragma unroll
        for (int q = 0; q < 4; ++q) pf_put(4 * i + q);
      } else {
#pragma unroll
        for (int q = 12; q < 16; ++q) pf_load(q);
      }
      SCHED();
    });
    STAMP(2, 2);
    if (k != 0) {
#pragma unroll
      for (int i = 0; i < 4; ++i) {
        v[i] = cmul1<false>(v[i], f2{bf_lo(wv[i]), bf_hi(wv[i])});
      }
    } else {
#pragma unroll
      for (int r4 = 0; r4 < 4; ++r4) T3[r4 * 512 + tid] = v[r4];
      __syncthreads();
#pragma unroll
      for (int r4 = 0; r4 < 4; ++r4) {
        f2 cm = T3[(mirr[r4] & 3) * 512 + (mirr[r4] >> 2)];
        f2 h0 = f2{bf_lo(wv[r4]) + bf_lo(wn[r4]), bf_hi(wv[r4]) - bf_hi(wn[r4])};
        f2 h1 = f2{bf_lo(bv[r4]) + bf_lo(bn[r4]), bf_hi(bv[r4]) - bf_hi(bn[r4])};
        f2 wp = (h0 + h1) * 0.25f, wm = (h0 - h1) * 0.25f;
        f2 cc = f2{cm.x, -cm.y};
        v[r4] = cmul(v[r4], wp) + cmul(cc, wm);
      }
    }
    fft256_inv(v, lane, tw, T2 + w * 256, [&](int i) __attribute__((always_inline)) {
      SCHED();
      if (i == 0) {
#pragma unroll
        for (int q = 8; q < 12; ++q) pf_put(q);
      }
      if (i == 1) {
#pragma unroll
        for (int q = 16; q < 20; ++q) pf_load(q);
      }
      SCHED();
    });
#pragma unroll
    for (int j = 0; j < 4; ++j) T2[w * 256 + lane + 64 * j] = cmul1<true>(v[j], tw1[j]);
#pragma unroll
    for (int q = 12; q < 16; ++q) pf_put(q);
    STAMP(2, 3);
    __syncthreads();
    {
      const float sg = hb ? -1.f : 1.f;
#pragma unroll
      for (int q = 0; q < 4; ++q) v[q] = T2[q * 256 + a] + T2[(q + 4) * 256 + a] * sg;
      if (hb) {
        v[1] = f2{v[1].x - v[1].y, v[1].y + v[1].x} * R;
        v[2] = f2{-v[2].y, v[2].x};
        v[3] = f2{-v[3].x - v[3].y, v[3].x - v[3].y} * R;
      }
    }
    dft4<1>(v[0], v[1], v[2], v[3]);
#pragma unroll
    for (int j = 0; j < 4; ++j) ST64(__builtin_bit_cast(u2v, v[j]), OFF_ZT + (k * CP + tid + 512 * j) * 8);
    STAMP(2, 4);
    DRAIN();
    __syncthreads();
    if (tid < NREP)
      __hip_atomic_store((gu32*)(ws + OFF_FLG + (tid * 256 + k) * FSP), 1u, __ATOMIC_RELAXED, __HIP_MEMORY_SCOPE_AGENT);
  }
  STAMP(2, 5);
  {
    const char* lw = dsm + L_W1 + (lane >> 2) * 224 + (lane & 3) * 8 + w * (2 * 7168);
    u4v fr[14];
#pragma unroll
    for (int i = 0; i < 14; ++i) fr[i] = w1frag(lw, i % 7, i / 7);
    asm volatile("" ::: "memory");
#pragma unroll
    for (int i = 0; i < 14; ++i) *(u4v*)(dsm + L_W1 + w * (2 * 7168) + i * 1024 + lane * 16) = fr[i];
  }
  STAMP(2, 6);
  {
    f2* zs = (f2*)(dsm + L_ZS);
    const int rp = tid & 3, k = tid >> 2;
    const int k2 = (k == 0) ? 128 : 256 - k;
    u4v xr;
    const unsigned* xrow = (const unsigned*)(x + (size_t)(row0 + w) * D_);
#pragma unroll
    for (int j = 0; j < 4; ++j) xr[j] = xrow[lane + 64 * j];
    const u4v gav = LD128(OFF_MODC + lane * 16);
    const u4v sbv = LD128(OFF_MODC + 1024 + lane * 16);
    const u4v cbv = LD128(OFF_MODC + 2048 + lane * 16);
    f2 wq = twid((float)k * (1.0f / 512.0f));
    PIN2(wq);
    const int pa = zpos(k), pb = zpos(k2);
    if (w == 0) wait_cols((gu32*)(ws + OFF_FLG + ((bid % NREP) * 256 + lane) * FSP));
    asm volatile("s_barrier" ::: "memory");
    __builtin_amdgcn_fence(__ATOMIC_ACQUIRE, "wavefront");
    STAMP(3, 0);
    const f4 ya = __builtin_bit_cast(f4, LD128(OFF_ZT + (k * CP + row0 + 2 * rp) * 8));
    const f4 yb = __builtin_bit_cast(f4, LD128(OFF_ZT + (k2 * CP + row0 + 2 * rp) * 8));
#pragma unroll
    for (int h = 0; h < 2; ++h) {
      f2 a = f2{ya[2 * h], ya[2 * h + 1]}, b = f2{yb[2 * h], yb[2 * h + 1]};
      f2* zr = zs + (2 * rp + h) * ZRS;
      f2 Ze = f2{a.x + b.x, a.y - b.y};
      f2 Dm = f2{a.x - b.x, a.y + b.y};
      f2 Zo = cmul1<true>(Dm, wq);
      f2 za = f2{Ze.x - Zo.y, Ze.y + Zo.x};
      f2 zb = f2{Ze.x + Zo.y, -Ze.y + Zo.x};
      if (k == 0) {
        za = f2{a.x + a.y, a.x - a.y};
        zb = f2{2.f * b.x, -2.f * b.y};
      }
      zr[pa] = za;
      zr[pb] = zb;
    }
    STAMP(3, 1);
    pf(20, 4);
    __syncthreads();
    f2 v[4];
    {
      const f2* buf = zs + w * ZRS + lane + (lane >> 4);
#pragma unroll
      for (int r4 = 0; r4 < 4; ++r4) v[r4] = buf[68 * r4];
    }
    fft256_inv(v, lane, tw, (f2*)(dsm + L_PX + w * 2048), NoHook{});
    pf(24, 4);
    STAMP(3, 2);
    float s = 0.f;
    u4v x1p;
#pragma unroll
    for (int j = 0; j < 4; ++j) {
      f2 t = __builtin_elementwise_fma(f2{bf_lo(gav[j]), bf_hi(gav[j])} * FSC, v[j], f2{bf_lo(xr[j]), bf_hi(xr[j])});
      x1p[j] = pack_bf(t.x, t.y);
      v[j] = f2{bf_lo(x1p[j]), bf_hi(x1p[j])};
      s += v[j].x + v[j].y;
    }
    ST128(x1p, OFF_X1 + ((row0 + w) * 64 + lane) * 16);
    s = wave_sum(s);
    const float meanf = s * (1.0f / D_);
    float q = 0.f;
#pragma unroll
    for (int j = 0; j < 4; ++j) {
      f2 d = v[j] - meanf;
      q += d.x * d.x + d.y * d.y;
    }
    q = wave_sum(q);
    const float mean = bfr(meanf);
    const float den = bfr(sqrtf(bfr(bfr(q * (1.0f / D_)) + 1e-5f)));
    const float rden = 1.0f / den;
    u4v hp;
#pragma unroll
    for (int j = 0; j < 4; ++j) {
      f2 t = __builtin_elementwise_fma(v[j] - mean, f2{bf_lo(cbv[j]), bf_hi(cbv[j])} * rden,
                                       f2{bf_lo(sbv[j]), bf_hi(sbv[j])});
      hp[j] = pack_bf(t.x, t.y);
    }
    *(u4v*)(dsm + L_ST + (lane * 9 + w) * 16) = hp;
    __syncthreads();
    {
      const int ch = tid >> 3, r = tid & 7;
      u4v o = *(const u4v*)(dsm + L_ST + (ch * 9 + r) * 16);
      const int mt = row0 >> 4, r16 = (row0 & 8) + r;
      ST128(o, OFF_H2F + ((mt * 16 + (ch >> 2)) * 64 + (ch & 3) * 16 + r16) * 16);
    }
  }
  STAMP(3, 3);
  DRAIN();
  __syncthreads();
  if (tid == 0) __hip_atomic_store(myA, 1u, __ATOMIC_RELAXED, __HIP_MEMORY_SCOPE_AGENT);
  {
    const int ch = tid >> 3, r = tid & 7;
    u4v t = LD128(OFF_H2F + (((row0 >> 4) * 16 + (ch >> 2)) * 64 + (ch & 3) * 16 + (row0 & 8) + r) * 16);
    asm volatile("" ::"v"(t));
  }
  STAMP(3, 4);
  wait_pair(depA);
  STAMP(3, 5);
  {
    u4v B[16];
#pragma unroll
    for (int ks = 0; ks < 16; ++ks) B[ks] = LD128(OFF_H2F + (((8 * p + w) * 16 + ks) * 64 + lane) * 16);
    f4 acc[7];
#pragma unroll
    for (int ft = 0; ft < 7; ++ft) acc[ft] = f4{0.f, 0.f, 0.f, 0.f};
    const char* lw = dsm + L_W1 + lane * 16;
    unsigned off2 = L_W1 + lane * 16 + 8 * 7168;
    asm volatile("" : "+v"(off2));
    const char* lw2 = dsm + off2;
    const int e = c >> 1, odd = c & 1;
    const int hb = (int)OFF_HF + (((8 * p + w) * 56 + 7 * e) * 64 + lane) * 16;
    u4v Af[2][7];
#pragma unroll
    for (int j = 0; j < 7; ++j) Af[0][j] = *(const u4v*)(lw + (g1_ks(0, j) * 7 + g1_ft(0, j)) * 1024);
#pragma unroll
    for (int g = 0; g < 17; ++g) {
#pragma unroll
      for (int j = 0; j < 7; ++j) {
        if (j < g1_n(g)) acc[g1_ft(g, j)] = mfma16(Af[g & 1][j], B[g1_ks(g, j)], acc[g1_ft(g, j)]);
        if (g + 1 < 17 && j < g1_n(g + 1))
          Af[(g + 1) & 1][j] = g1_ks(g + 1, j) < 8 ? *(const u4v*)(lw + (g1_ks(g + 1, j) * 7 + g1_ft(g + 1, j)) * 1024)
                                                   : *(const u4v*)(lw2 + ((g1_ks(g + 1, j) - 8) * 7 + g1_ft(g + 1, j)) * 1024);
        SCHED();
      }
      if (g >= 4 && g < 12) { pf_load(20 + 2 * g); pf_load(21 + 2 * g); }
      if (g >= 14) {
        const int i = g - 14;
        f4 a = acc[2 * i], b = acc[2 * i + 1];
        u4v o = u4v{silu_pk(a[0], a[1]), silu_pk(a[2], a[3]), silu_pk(b[0], b[1]), silu_pk(b[2], b[3])};
        ST128(o, hb + (odd * 3 + i) * 1024);
      }
      SCHED();
      if (g == 12) STAMP(3, 6);
    }
    f4 a = acc[6];
    u2v o = u2v{silu_pk(a[0], a[1]), silu_pk(a[2], a[3])};
    ST64(o, hb + 6 * 1024 + odd * 8);
  }
  STAMP(3, 7);
  DRAIN();
  __syncthreads();
  if (tid == 0) __hip_atomic_store(myB, 1u, __ATOMIC_RELAXED, __HIP_MEMORY_SCOPE_AGENT);
  {
    const int hb = (int)OFF_HF + (((8 * p + w) * 56 + 7 * (c >> 1) + 3 * (c & 1)) * 64 + lane) * 16;
    u4v t0 = LD128(hb), t1 = LD128(hb + 1024), t2 = LD128(hb + 2048);
    asm volatile("" ::"v"(t0), "v"(t1), "v"(t2));
  }
  wait_pair(depB);
  STAMP(3, 8);
  {
    const int Bb = (int)OFF_HF + ((mt0 * 56 + 7 * w) * 64 + lane) * 16;
    u4v B[7][4];
#pragma unroll
    for (int ks = 0; ks < 4; ++ks)
#pragma unroll
      for (int mt = 0; mt < 4; ++mt) B[ks][mt] = LD128(Bb + (mt * 56 + ks) * 1024);
    f4 acc[4][4];
#pragma unroll
    for (int i = 0; i < 4; ++i)
#pragma unroll
      for (int j = 0; j < 4; ++j) acc[i][j] = f4{0.f, 0.f, 0.f, 0.f};
#pragma unroll
    for (int ks = 0; ks < 7; ++ks) {
#pragma unroll
      for (int nt = 0; nt < 4; ++nt)
#pragma unroll
        for (int mt = 0; mt < 4; ++mt) acc[nt][mt] = mfma16(A2[ks * 4 + nt], B[ks][mt], acc[nt][mt]);
      if (ks + 4 < 7) {
#pragma unroll
        for (int mt = 0; mt < 4; ++mt) B[ks + 4][mt] = LD128(Bb + (mt * 56 + ks + 4) * 1024);
      }
      SCHED();
    }
    STAMP(3, 9);
    const int g = lane >> 4, mr = lane & 15;
    const int fm = w >> 1, P = w & 1;
    const int m = (mt0 + fm) * 16 + mr;
    const int n = nt0 * 16 + 32 * P + 8 * g;
    unsigned xv[4];
#pragma unroll
    for (int i = 0; i < 4; ++i)
      xv[i] = __builtin_amdgcn_raw_buffer_load_b32(
          rs, (int)OFF_X1 + (m * 64 + (((n >> 1) + i) & 63)) * 16 + (n >> 7) * 4, 0, AUX_SC1);
    const f4 g0 = __builtin_bit_cast(f4, LD128(OFF_MODF + (2560 + n) * 4));
    const f4 g1 = __builtin_bit_cast(f4, LD128(OFF_MODF + (2560 + n + 4) * 4));
    f4* red = (f4*)dsm;
#pragma unroll
    for (int nt = 0; nt < 4; ++nt)
#pragma unroll
      for (int mt = 0; mt < 4; ++mt) red[(w * 16 + nt * 4 + mt) * 64 + lane] = acc[nt][mt];
    __syncthreads();
    STAMP(3, 10);
    f4 s0 = f4{0.f, 0.f, 0.f, 0.f}, s1 = s0;
#pragma unroll
    for (int ww = 0; ww < 8; ++ww) {
      s0 += red[(ww * 16 + (2 * P) * 4 + fm) * 64 + lane];
      s1 += red[(ww * 16 + (2 * P + 1) * 4 + fm) * 64 + lane];
    }
    u4v o;
    {
      f2 t0 = bfr2(f2{g0[0], g0[1]} * bfr2(f2{s0[0], s0[1]})), t1 = bfr2(f2{g0[2], g0[3]} * bfr2(f2{s0[2], s0[3]}));
      f2 t2 = bfr2(f2{g1[0], g1[1]} * bfr2(f2{s1[0], s1[1]})), t3 = bfr2(f2{g1[2], g1[3]} * bfr2(f2{s1[2], s1[3]}));
      o[0] = pack_bf(bf_lo(xv[0]) + t0.x, bf_hi(xv[0]) + t0.y);
      o[1] = pack_bf(bf_lo(xv[1]) + t1.x, bf_hi(xv[1]) + t1.y);
      o[2] = pack_bf(bf_lo(xv[2]) + t2.x, bf_hi(xv[2]) + t2.y);
      o[3] = pack_bf(bf_lo(xv[3]) + t3.x, bf_hi(xv[3]) + t3.y);
    }
    *(u4v*)(out + (size_t)m * D_ + n) = o;
  }
  STAMP(3, 11);
}

extern "C" void kernel_launch(void* const* d_in, const int* in_sizes, int n_in, void* d_out, int out_size, void* d_ws,
                              size_t ws_size, hipStream_t stream) {
  const u16* x = (const u16*)d_in[0];
  const u16* wr = (const u16*)d_in[1];
  const u16* wi = (const u16*)d_in[2];
  const u16* w1 = (const u16*)d_in[4];
  const u16* w2 = (const u16*)d_in[5];
  const u16* c = (const u16*)d_in[6];
  const u16* wmod = (const u16*)d_in[7];
  char* ws = (char*)d_ws;
  u16* out = (u16*)d_out;
  (void)hipFuncSetAttribute((const void*)kmega, hipFuncAttributeMaxDynamicSharedMemorySize, L_TOTAL);
  k0_mod<<<K0_GEMV + K0_WT + K0_W2 + K0_Z + K0_WARM, 512, 0, stream>>>(c, wmod, x, wr, wi, w1, w2, ws);
  kmega<<<256, 512, L_TOTAL, stream>>>(x, w1, w2, ws, out);
}
```

```cpp
#include <hip/hip_runtime.h>
#include <hip/hip_bf16.h>
#include <stdint.h>

typedef float f2 __attribute__((ext_vector_type(2)));
typedef float f4 __attribute__((ext_vector_type(4)));
typedef unsigned u2v __attribute__((ext_vector_type(2)));
typedef unsigned u4v __attribute__((ext_vector_type(4)));
typedef short bf16x8 __attribute__((ext_vector_type(8)));
typedef unsigned short u16;

constexpr int S_ = 2048, D_ = 512, F_ = 1792, DF_ = 257;
constexpr int NSPLIT = 4;

constexpr size_t OFF_CNT = 0;
#define FSPACE 16
#define FREP 16
constexpr int FSP = FSPACE, NREP = FREP;
constexpr size_t FTAB = (size_t)NREP * 256 * FSP;
constexpr size_t OFF_FLG = 16384;
constexpr size_t OFF_FLR = OFF_FLG + FTAB;
constexpr size_t OFF_FLS = OFF_FLR + FTAB;
constexpr size_t FLAG_BYTES = OFF_FLS + 16384;
constexpr size_t OFF_MODP = 900 << 10;
constexpr size_t OFF_MODF = OFF_MODP + (size_t)NSPLIT * 3072 * 4;
constexpr size_t OFF_MODC = OFF_MODF + 3072 * 4;
constexpr size_t OFF_WT = 1 << 20;
constexpr size_t OFF_XT = 4 << 20;
constexpr size_t OFF_ZT = 28 << 20;
constexpr size_t OFF_H2F = 12 << 20;
constexpr size_t OFF_X1 = 14 << 20;
constexpr size_t OFF_W2F = 18 << 20;
constexpr size_t OFF_HF = 20 << 20;
constexpr int WS_SPAN = 36 << 20;
#define CPITCH (2048 + 32)
constexpr int CP = CPITCH;
constexpr int WT_PITCH = 520;
constexpr float FSC = 1.0f / (2.0f * 2048.0f * 512.0f);

#define STAMP(kid, i)

__device__ __forceinline__ float bf_lo(unsigned u) { return __builtin_bit_cast(float, u << 16); }
__device__ __forceinline__ float bf_hi(unsigned u) { return __builtin_bit_cast(float, u & 0xffff0000u); }
__device__ __forceinline__ float bfr(float x) {
  return (float)(__bf16)x;
}
__device__ __forceinline__ unsigned pack_bf(float lo, float hi) {
  typedef __bf16 b2 __attribute__((ext_vector_type(2)));
  return __builtin_bit_cast(unsigned, __builtin_convertvector(f2{lo, hi}, b2));
}
__device__ __forceinline__ f2 cmul(f2 a, f2 b) {
  f2 t = a * f2{b.x, b.x};
  return __builtin_elementwise_fma(f2{a.y, a.x}, f2{-b.y, b.y}, t);
}
__device__ __forceinline__ f2 cmulc(f2 a, f2 b) {
  f2 t = a * f2{b.x, b.x};
  return __builtin_elementwise_fma(f2{a.y, a.x}, f2{b.y, -b.y}, t);
}
__device__ __forceinline__ f2 bfr2(f2 a) {
  unsigned u = pack_bf(a.x, a.y);
  asm("" : "+v"(u));
  return f2{bf_lo(u), bf_hi(u)};
}
__device__ __forceinline__ float msel(unsigned m, float a, float b) {
  unsigned ua = __builtin_bit_cast(unsigned, a), ub = __builtin_bit_cast(unsigned, b);
  return __builtin_bit_cast(float, (m & ua) | (~m & ub));
}
#define PIN2(x) asm volatile("" : "+v"(x))
constexpr int AUX_SC1 = 16;
#define WS_RSRC(ws) __builtin_amdgcn_make_buffer_rsrc((ws), 0, WS_SPAN, 0x00020000)
#define ST128(v, off) __builtin_amdgcn_raw_buffer_store_b128((v), rs, (int)(off), 0, AUX_SC1)
#define ST64(v, off) __builtin_amdgcn_raw_buffer_store_b64((v), rs, (int)(off), 0, AUX_SC1)
#define ST32(v, off) __builtin_amdgcn_raw_buffer_store_b32((v), rs, (int)(off), 0, AUX_SC1)
#define LD64(off) __builtin_amdgcn_raw_buffer_load_b64(rs, (int)(off), 0, AUX_SC1)
#define LD32(off) __builtin_amdgcn_raw_buffer_load_b32(rs, (int)(off), 0, AUX_SC1)
#define LD128(off) __builtin_amdgcn_raw_buffer_load_b128(rs, (int)(off), 0, AUX_SC1)
__device__ __forceinline__ f2 twid(float rev) { return f2{__builtin_amdgcn_cosf(rev), -__builtin_amdgcn_sinf(rev)}; }

template <int DIR>
__device__ __forceinline__ f2 rot90(f2 a) {
  return DIR < 0 ? f2{a.y, -a.x} : f2{-a.y, a.x};
}
template <int DIR>
__device__ __forceinline__ void dft4(f2& c0, f2& c1, f2& c2, f2& c3) {
  f2 d0 = c0 + c2, d2 = c0 - c2, d1 = c1 + c3, e = c1 - c3;
  c0 = d0 + d1;
  c2 = d0 - d1;
  const f2 es = f2{e.y, e.x};
  c1 = __builtin_elementwise_fma(es, DIR < 0 ? f2{1.f, -1.f} : f2{-1.f, 1.f}, d2);
  c3 = __builtin_elementwise_fma(es, DIR < 0 ? f2{-1.f, 1.f} : f2{1.f, -1.f}, d2);
}

__device__ __forceinline__ void sw32(float& a, float& b) {
  auto r = __builtin_amdgcn_permlane32_swap(__builtin_bit_cast(unsigned, a), __builtin_bit_cast(unsigned, b), false, false);
  a = __builtin_bit_cast(float, (unsigned)r[0]);
  b = __builtin_bit_cast(float, (unsigned)r[1]);
}
__device__ __forceinline__ void sw16(float& a, float& b) {
  auto r = __builtin_amdgcn_permlane16_swap(__builtin_bit_cast(unsigned, a), __builtin_bit_cast(unsigned, b), false, false);
  a = __builtin_bit_cast(float, (unsigned)r[0]);
  b = __builtin_bit_cast(float, (unsigned)r[1]);
}
template <int CTRL, int BANK>
__device__ __forceinline__ float udpp(float old, float src) {
  return __builtin_bit_cast(float, __builtin_amdgcn_update_dpp(__builtin_bit_cast(int, old), __builtin_bit_cast(int, src),
                                                               CTRL, 0xf, BANK, false));
}
template <int CTRL>
__device__ __forceinline__ float mdpp(float src) {
  return __builtin_bit_cast(float, __builtin_amdgcn_mov_dpp(__builtin_bit_cast(int, src), CTRL, 0xf, 0xf, false));
}
__device__ __forceinline__ void xp_hi1(float& v0, float& v1, float& v2, float& v3) {
  sw32(v0, v2); sw32(v1, v3); sw16(v0, v1); sw16(v2, v3);
}
__device__ __forceinline__ void xp_mid1(float& v0, float& v1, float& v2, float& v3) {
  float a0 = udpp<0x128, 0xC>(v0, v2), a2 = udpp<0x128, 0x3>(v2, v0);
  float a1 = udpp<0x128, 0xC>(v1, v3), a3 = udpp<0x128, 0x3>(v3, v1);
  v0 = udpp<0x124, 0xA>(a0, a1); v1 = udpp<0x12C, 0x5>(a1, a0);
  v2 = udpp<0x124, 0xA>(a2, a3); v3 = udpp<0x12C, 0x5>(a3, a2);
}
__device__ __forceinline__ void xp_lo1(float& v0, float& v1, float& v2, float& v3, unsigned b1, unsigned b0) {
  float s0 = mdpp<0x4E>(msel(b1, v0, v2)), s1 = mdpp<0x4E>(msel(b1, v1, v3));
  float a0 = msel(b1, s0, v0), a2 = msel(b1, v2, s0), a1 = msel(b1, s1, v1), a3 = msel(b1, v3, s1);
  float t0 = mdpp<0xB1>(msel(b0, a0, a1)), t2 = mdpp<0xB1>(msel(b0, a2, a3));
  v0 = msel(b0, t0, a0); v1 = msel(b0, a1, t0); v2 = msel(b0, t2, a2); v3 = msel(b0, a3, t2);
}
#define XP_CPLX(NAME, FN, ...)                                        \
  {                                                                   \
    float x0 = v[0].x, x1 = v[1].x, x2 = v[2].x, x3 = v[3].x;         \
    float y0 = v[0].y, y1 = v[1].y, y2 = v[2].y, y3 = v[3].y;         \
    FN(x0, x1, x2, x3, ##__VA_ARGS__);                                \
    FN(y0, y1, y2, y3, ##__VA_ARGS__);                                \
    v[0] = f2{x0, y0}; v[1] = f2{x1, y1}; v[2] = f2{x2, y2}; v[3] = f2{x3, y3}; \
  }
__device__ __forceinline__ void xp_hi(f2 (&v)[4]) XP_CPLX(hi, xp_hi1)
__device__ __forceinline__ void xp_mid(f2 (&v)[4]) XP_CPLX(mid, xp_mid1)
__device__ __forceinline__ void xp_lo(f2 (&v)[4], unsigned b1, unsigned b0) XP_CPLX(lo, xp_lo1, b1, b0)

template <bool CONJ>
__device__ __forceinline__ void cmul3(f2 (&v)[4], const f2 (&t)[3]) {
  f2 a, b, c;
  if (!CONJ)
    asm("v_pk_mul_f32 %3, %0, %6 op_sel_hi:[1,0]\n\t"
        "v_pk_mul_f32 %4, %1, %7 op_sel_hi:[1,0]\n\t"
        "v_pk_mul_f32 %5, %2, %8 op_sel_hi:[1,0]\n\t"
        "v_pk_fma_f32 %0, %0, %6, %3 op_sel:[1,1,0] op_sel_hi:[0,1,1] neg_lo:[0,1,0]\n\t"
        "v_pk_fma_f32 %1, %1, %7, %4 op_sel:[1,1,0] op_sel_hi:[0,1,1] neg_lo:[0,1,0]\n\t"
        "v_pk_fma_f32 %2, %2, %8, %5 op_sel:[1,1,0] op_sel_hi:[0,1,1] neg_lo:[0,1,0]\n\t"
        "s_nop 1"
        : "+v"(v[1]), "+v"(v[2]), "+v"(v[3]), "=&v"(a), "=&v"(b), "=&v"(c)
        : "v"(t[0]), "v"(t[1]), "v"(t[2]));
  else
    asm("v_pk_mul_f32 %3, %0, %6 op_sel_hi:[1,0]\n\t"
        "v_pk_mul_f32 %4, %1, %7 op_sel_hi:[1,0]\n\t"
        "v_pk_mul_f32 %5, %2, %8 op_sel_hi:[1,0]\n\t"
        "v_pk_fma_f32 %0, %0, %6, %3 op_sel:[1,1,0] op_sel_hi:[0,1,1] neg_hi:[0,1,0]\n\t"
        "v_pk_fma_f32 %1, %1, %7, %4 op_sel:[1,1,0] op_sel_hi:[0,1,1] neg_hi:[0,1,0]\n\t"
        "v_pk_fma_f32 %2, %2, %8, %5 op_sel:[1,1,0] op_sel_hi:[0,1,1] neg_hi:[0,1,0]"
        : "+v"(v[1]), "+v"(v[2]), "+v"(v[3]), "=&v"(a), "=&v"(b), "=&v"(c)
        : "v"(t[0]), "v"(t[1]), "v"(t[2]));
}
template <bool CONJ>
__device__ __forceinline__ f2 cmul1(f2 v, f2 t) {
  f2 a;
  if (!CONJ)
    asm("v_pk_mul_f32 %1, %0, %2 op_sel_hi:[1,0]\n\t"
        "v_pk_fma_f32 %0, %0, %2, %1 op_sel:[1,1,0] op_sel_hi:[0,1,1] neg_lo:[0,1,0]"
        : "+v"(v), "=&v"(a) : "v"(t));
  else
    asm("v_pk_mul_f32 %1, %0, %2 op_sel_hi:[1,0]\n\t"
        "v_pk_fma_f32 %0, %0, %2, %1 op_sel:[1,1,0] op_sel_hi:[0,1,1] neg_hi:[0,1,0]"
        : "+v"(v), "=&v"(a) : "v"(t));
  return v;
}
__device__ __forceinline__ void xp_lo_lds(f2 (&v)[4], f2* patch, int lane) {
  f2* q = patch + (lane >> 2) * 16;
  const int c = lane & 3, sw = (lane >> 2) & 3;
#pragma unroll
  for (int j = 0; j < 4; ++j) q[4 * (j ^ sw) + c] = v[j];
  __builtin_amdgcn_fence(__ATOMIC_RELEASE, "wavefront");
  __builtin_amdgcn_wave_barrier();
  __builtin_amdgcn_fence(__ATOMIC_ACQUIRE, "wavefront");
  const f4 a = *(const f4*)(q + 4 * (c ^ sw)), b = *(const f4*)(q + 4 * (c ^ sw) + 2);
  v[0] = f2{a[0], a[1]}; v[1] = f2{a[2], a[3]}; v[2] = f2{b[0], b[1]}; v[3] = f2{b[2], b[3]};
  __builtin_amdgcn_fence(__ATOMIC_RELEASE, "wavefront");
  __builtin_amdgcn_wave_barrier();
}

struct Tw256 {
  f2 a[3], b[3], c[3];
};
__device__ __forceinline__ void make_tw256(int lane, Tw256& t) {
#pragma unroll
  for (int r = 1; r < 4; ++r) {
    t.a[r - 1] = twid((float)(lane * r) * (1.0f / 256.0f));
    t.b[r - 1] = twid((float)((lane & 15) * r) * (1.0f / 64.0f));
    t.c[r - 1] = twid((float)((lane & 3) * r) * (1.0f / 16.0f));
  }
#pragma unroll
  for (int r = 0; r < 3; ++r) {
    PIN2(t.a[r]);
    PIN2(t.b[r]);
    PIN2(t.c[r]);
  }
}
struct NoHook {
  __device__ __forceinline__ void operator()(int) const {}
};
template <class H>
__device__ __forceinline__ void fft256_fwd(f2 (&v)[4], int lane, const Tw256& t, f2* patch, const H& hook) {
  dft4<-1>(v[0], v[1], v[2], v[3]);
  cmul3<false>(v, t.a);
  xp_hi(v);
  hook(0);
  dft4<-1>(v[0], v[1], v[2], v[3]);
  cmul3<false>(v, t.b);
  xp_mid(v);
  hook(1);
  dft4<-1>(v[0], v[1], v[2], v[3]);
  cmul3<false>(v, t.c);
  xp_lo_lds(v, patch, lane);
  hook(2);
  dft4<-1>(v[0], v[1], v[2], v[3]);
}
template <class H>
__device__ __forceinline__ void fft256_inv(f2 (&v)[4], int lane, const Tw256& t, f2* patch, const H& hook) {
  dft4<1>(v[0], v[1], v[2], v[3]);
  xp_lo_lds(v, patch, lane);
  hook(0);
  cmul3<true>(v, t.c);
  dft4<1>(v[0], v[1], v[2], v[3]);
  xp_mid(v);
  hook(1);
  cmul3<true>(v, t.b);
  dft4<1>(v[0], v[1], v[2], v[3]);
  xp_hi(v);
  hook(2);
  cmul3<true>(v, t.a);
  dft4<1>(v[0], v[1], v[2], v[3]);
}

__device__ __forceinline__ float wave_sum(float x) {
  x += mdpp<0xB1>(x);
  x += mdpp<0x4E>(x);
  x += mdpp<0x141>(x);
  x += mdpp<0x140>(x);
  float r0 = __builtin_bit_cast(float, __builtin_amdgcn_readlane(__builtin_bit_cast(int, x), 0));
  float r1 = __builtin_bit_cast(float, __builtin_amdgcn_readlane(__builtin_bit_cast(int, x), 16));
  float r2 = __builtin_bit_cast(float, __builtin_amdgcn_readlane(__builtin_bit_cast(int, x), 32));
  float r3 = __builtin_bit_cast(float, __builtin_amdgcn_readlane(__builtin_bit_cast(int, x), 48));
  return (r0 + r1) + (r2 + r3);
}

__device__ __forceinline__ int zpos(int k) { return 17 * (k & 3) + 4 * ((k >> 2) & 3) + ((k >> 4) & 3) + 68 * (k >> 6); }
constexpr int ZRS = 276;

constexpr int K0_GEMV = 48 * NSPLIT, K0_WT = 128, K0_W2 = 0, K0_WARM = 0, K0_Z = (int)(FLAG_BYTES / 8192);
__global__ __launch_bounds__(512) void k0_mod(const u16* __restrict__ c, const u16* __restrict__ wmod,
                                              const u16* __restrict__ x, const u16* __restrict__ wr,
                                              const u16* __restrict__ wi, const u16* __restrict__ w1,
                                              const u16* __restrict__ w2, char* __restrict__ ws) {
  __shared__ __attribute__((aligned(16))) char smem[17408];
  const int tid = threadIdx.x, row = tid >> 6, lane = tid & 63;
  const auto rs = WS_RSRC(ws);
  int b = blockIdx.x;
  STAMP(0, 0);
  if (b >= K0_GEMV + K0_WT + K0_W2) {
    int q = b - (K0_GEMV + K0_WT + K0_W2);
    if (q < K0_Z) {
      ((u4v*)ws)[q * 512 + tid] = u4v{0u, 0u, 0u, 0u};
      return;
    }
    q -= K0_Z;
    const char* src = q < 64 ? (const char*)x + (size_t)q * 32768
                             : (q < 120 ? (const char*)w1 + (size_t)(q - 64) * 32768 : (const char*)w2 + (size_t)(q - 120) * 32768);
    u4v t[4];
#pragma unroll
    for (int i = 0; i < 4; ++i) t[i] = *(const u4v*)(src + i * 8192 + tid * 16);
#pragma unroll
    for (int i = 0; i < 4; ++i) asm volatile("" ::"v"(t[i]));
    STAMP(0, 1);
    return;
  }
  if (b >= K0_GEMV) {
    b -= K0_GEMV;
    if (b < K0_WT) {
      char* sr = smem;
      char* si = smem + 16 * 544;
      constexpr int LASTCH = (S_ * DF_ * 2) / 16 - 1;
      for (int it = tid; it < 16 * 34; it += 512) {
        const int i = it / 34, ch = it % 34;
        int gch = ((b + 128 * i) * (DF_ * 2)) / 16 + ch;
        gch = gch > LASTCH ? LASTCH : gch;
        *(u4v*)(sr + i * 544 + ch * 16) = *(const u4v*)((const char*)wr + (size_t)gch * 16);
        *(u4v*)(si + i * 544 + ch * 16) = *(const u4v*)((const char*)wi + (size_t)gch * 16);
      }
      __syncthreads();
      const int tbase = 64 * (b & 7) + 16 * ((b >> 3) & 3) + 4 * ((b >> 5) & 3);
      for (int it = tid; it < DF_ * 4; it += 512) {
        const int k = it >> 2, r3 = it & 3;
        u4v o;
#pragma unroll
        for (int r4 = 0; r4 < 4; ++r4) {
          const int i = r3 + 4 * r4;
          const int off = i * 544 + (((b + 128 * i) * (DF_ * 2)) & 15) + 2 * k;
          o[r4] = (unsigned)*(const u16*)(sr + off) | ((unsigned)*(const u16*)(si + off) << 16);
        }
        ST128(o, OFF_WT + (k * 512 + tbase + r3) * 16);
      }
    }
    STAMP(0, 1);
    return;
  }
  float* red = (float*)smem;
  const int cgp = b % 48, ks = b / 48;
  const int cg = tid & 7, r = tid >> 3;
  const int rw = ks * 128 + r;
  const u4v wm0 = *(const u4v*)(wmod + (size_t)rw * 3072 + cgp * 64 + cg * 8);
  const u4v wm1 = *(const u4v*)(wmod + (size_t)(rw + 64) * 3072 + cgp * 64 + cg * 8);
  const float cv0 = bf_lo(c[rw]), cv1 = bf_lo(c[rw + 64]);
  const float s0 = bfr(cv0 / (1.0f + __expf(-cv0))), s1 = bfr(cv1 / (1.0f + __expf(-cv1)));
  float acc[8];
#pragma unroll
  for (int i = 0; i < 4; ++i) {
    acc[2 * i] = s0 * bf_lo(wm0[i]) + s1 * bf_lo(wm1[i]);
    acc[2 * i + 1] = s0 * bf_hi(wm0[i]) + s1 * bf_hi(wm1[i]);
  }
#pragma unroll
  for (int i = 0; i < 8; ++i) {
    acc[i] += mdpp<0x128>(acc[i]);
    acc[i] += __shfl_xor(acc[i], 16);
    acc[i] += __shfl_xor(acc[i], 32);
  }
  if (lane < 8) {
#pragma unroll
    for (int i = 0; i < 8; ++i) red[row * 64 + cg * 8 + i] = acc[i];
  }
  __syncthreads();
  if (tid < 64) {
    float sum = 0.f;
#pragma unroll
    for (int i = 0; i < 8; ++i) sum += red[i * 64 + tid];
    ((float*)(ws + OFF_MODP))[ks * 3072 + cgp * 64 + tid] = sum;
  }
  STAMP(0, 1);
}

__device__ __forceinline__ f4 mfma16(u4v a, u4v b, f4 c) {
  return __builtin_amdgcn_mfma_f32_16x16x32_bf16(__builtin_bit_cast(bf16x8, a), __builtin_bit_cast(bf16x8, b), c, 0, 0,
                                                  0);
}
__device__ __forceinline__ f2 silu2(float a, float b) {
  f2 h = f2{a, b};
  f2 t = h * -1.44269504088896340736f;
  f2 d = f2{__builtin_amdgcn_exp2f(t.x), __builtin_amdgcn_exp2f(t.y)} + 1.0f;
  return h * f2{__builtin_amdgcn_rcpf(d.x), __builtin_amdgcn_rcpf(d.y)};
}
__device__ __forceinline__ unsigned silu_pk(float a, float b) {
  f2 r = silu2(a, b);
  return pack_bf(r.x, r.y);
}
typedef __attribute__((address_space(1))) unsigned gu32;
#define DRAIN() asm volatile("s_waitcnt vmcnt(0)" ::: "memory")
typedef __attribute__((address_space(1))) unsigned long long gu64;
constexpr unsigned SPIN_CAP = 1u << 18;
__device__ __forceinline__ void wait_pair(gu64* flags) {
#pragma nounroll
  for (unsigned spin = 0; spin < SPIN_CAP; ++spin) {
    if (__hip_atomic_load(flags, __ATOMIC_RELAXED, __HIP_MEMORY_SCOPE_AGENT) == 0x0000000100000001ull) break;
    __builtin_amdgcn_s_sleep(1);
  }
  __builtin_amdgcn_fence(__ATOMIC_ACQUIRE, "wavefront");
  asm volatile("" ::: "memory");
}

#define PSLEEP 2
__device__ __forceinline__ void wait_cols(gu32* flags) {
#pragma nounroll
  for (unsigned spin = 0; spin < SPIN_CAP; ++spin) {
    unsigned f = 1u;
#pragma unroll
    for (int j = 0; j < 4; ++j)
      f &= __hip_atomic_load(flags + j * 64 * (FSP / 4), __ATOMIC_RELAXED, __HIP_MEMORY_SCOPE_AGENT);
    if (__builtin_amdgcn_ballot_w64(f != 1u) == 0ull) break;
    __builtin_amdgcn_s_sleep(PSLEEP);
  }
}

__device__ __forceinline__ int wt_off(int k, int t) { return (int)OFF_WT + (k * 512 + t) * 16; }

constexpr int L_W1 = 0;
constexpr int L_T = 112 * 1024;
constexpr int L_ZS = 112 * 1024;
constexpr int L_ST = L_ZS + 8 * ZRS * 8;
constexpr int L_TOTAL = 160 * 1024;
__device__ __forceinline__ constexpr int g1_n(int g) { return g < 13 ? 7 : (g < 16 ? 6 : 3); }
__device__ __forceinline__ constexpr int g1_ft(int g, int j) { return g < 13 ? j : (g < 16 ? 2 * (g - 13) + (j & 1) : 6); }
__device__ __forceinline__ constexpr int g1_ks(int g, int j) { return g < 13 ? g : (g < 16 ? 13 + (j >> 1) : 13 + j); }
#define SCHED() __builtin_amdgcn_sched_barrier(0)
typedef short v4i16 __attribute__((ext_vector_type(4)));
__device__ __forceinline__ u4v w1frag(const char* lw, int ft, int ks) {
  typedef __attribute__((address_space(3))) v4i16* lp;
  const u2v a = __builtin_bit_cast(u2v, __builtin_amdgcn_ds_read_tr16_b64_v4i16((lp)(lw + (2 * ks) * 3584 + ft * 32)));
  const u2v b = __builtin_bit_cast(u2v, __builtin_amdgcn_ds_read_tr16_b64_v4i16((lp)(lw + (2 * ks + 1) * 3584 + ft * 32)));
  return u4v{a[0], a[1], b[0], b[1]};
}
constexpr int L_MODS = L_ZS + 8 * ZRS * 8;
constexpr int L_PX = L_T + 28672;
constexpr int L_W2S = L_T + 32768;
__global__ __launch_bounds__(512, 2) void kmega(const u16* __restrict__ x, const u16* __restrict__ w1,
                                                const u16* __restrict__ w2, char* ws, u16* __restrict__ out) {
  extern __shared__ __attribute__((aligned(16))) char dsm[];
  const int tid = threadIdx.x, lane = tid & 63;
  const int w = __builtin_amdgcn_readfirstlane(tid >> 6);
  const int bid = blockIdx.x;
  const int p = (bid & 7) * 2 + (bid >> 7), c = (bid >> 3) & 15;
  const int row0 = 128 * p + 8 * c;
  STAMP(1, 0);
  const auto rs = WS_RSRC(ws);
  gu32* myA = (gu32*)(ws + OFF_CNT) + (p * 8 + (c >> 1)) * 16 + (c & 1);
  gu32* myB = myA + 128 * 16;
  gu64* depA = (gu64*)(ws + OFF_CNT) + (p * 8 + w) * 8;
  gu64* depB = depA + 128 * 8;
  const int mt0 = 8 * p + 4 * (c >> 3), nt0 = 4 * (c & 7);
  const int l56 = lane < 56 ? lane : lane - 56;
  const int r4_ = l56 / 14, ch_ = l56 - 14 * r4_;
  const char* w1g = (const char*)w1 + (size_t)(4 * w + r4_) * (F_ * 2) + c * 224 + ch_ * 16;
  char* w1l = dsm + L_W1 + ((w >> 1) * 32 + (2 * (w & 1) + (r4_ >> 1)) * 4 + (r4_ & 1)) * 224 + ch_ * 16;
  const int Ab = (int)OFF_W2F + ((nt0 * 56 + 7 * w) * 64 + lane) * 16;
  u4v S1[16];
  u4v A2[28];
  auto pf_load = [&](int i) __attribute__((always_inline)) {
    if (i < 16) S1[i] = *(const u4v*)(w1g + (size_t)i * (32 * F_ * 2));
    else if (i < 44) A2[i - 16] = LD128(Ab + (((i - 16) & 3) * 56 + ((i - 16) >> 2)) * 1024);
  };
  auto pf_put = [&](int i) __attribute__((always_inline)) {
    *(u4v*)(w1l + ((i & 3) * 128 + (i >> 3) * 16 + ((i >> 2) & 1) * 2) * 224) = S1[i];
  };
  auto pf = [&](int i0, int n) __attribute__((always_inline)) {
    SCHED();
#pragma unroll
    for (int i = 0; i < n; ++i) pf_load(i0 + i);
    SCHED();
  };
  const int tk = bid < 224 ? bid : bid - 224;
  Tw256 tw;
  {
    f2* zs = (f2*)(dsm + L_ZS);
    float* mods = (float*)(dsm + L_MODS);
    const int r0 = bid * 8;
    unsigned xr[4];
    const unsigned* xrow = (const unsigned*)(x + (size_t)(r0 + w) * D_);
#pragma unroll
    for (int j = 0; j < 4; ++j) xr[j] = xrow[lane + 64 * j];
    const float* mp = (const float*)(ws + OFF_MODP);
    f2 sm = *(const f2*)(mp + tid * 2);
#pragma unroll
    for (int k = 1; k < NSPLIT; ++k) sm += *(const f2*)(mp + k * 3072 + tid * 2);
    const int idx = bid * 12 + (tid < 6 ? tid : 0) * 2;
    f2 fin = *(const f2*)(mp + idx);
#pragma unroll
    for (int k = 1; k < NSPLIT; ++k) fin += *(const f2*)(mp + k * 3072 + idx);
    pf(0, 4);
    make_tw256(lane, tw);
    const int rp = tid & 3, kk = tid >> 2;
    f2 wpost = twid((float)kk * (1.0f / 512.0f));
    PIN2(wpost);
    f2 v[4];
    float s = 0.f;
#pragma unroll
    for (int j = 0; j < 4; ++j) {
      v[j] = f2{bf_lo(xr[j]), bf_hi(xr[j])};
      s += v[j].x + v[j].y;
    }
    s = wave_sum(s);
    const float meanf = s * (1.0f / D_);
    float q = 0.f;
#pragma unroll
    for (int j = 0; j < 4; ++j) {
      f2 d = v[j] - meanf;
      q += d.x * d.x + d.y * d.y;
    }
    q = wave_sum(q);
    const float mean = bfr(meanf);
    const float den = bfr(sqrtf(bfr(bfr(q * (1.0f / D_)) + 1e-5f)));
    const float rden = 1.0f / den;
    sm = bfr2(sm);
    if (tid >= 256) sm = bfr2(sm + 1.0f);
    *(f2*)(mods + tid * 2) = sm;
    if (tid < 6) {
      fin = bfr2(fin);
      ST64(__builtin_bit_cast(u2v, fin), OFF_MODF + idx * 4);
      const int vec = (idx >> 9) - 2, d = idx & 511;
      if (vec >= 0 && vec < 3) {
        if (vec == 2) fin += 1.0f;
        ST32(pack_bf(fin.x, fin.y), OFF_MODC + ((vec * 64 + ((d >> 1) & 63)) * 4 + (d >> 7)) * 4);
      }
    }
    STAMP(1, 3);
    __syncthreads();
    STAMP(1, 4);
    pf(4, 4);
#pragma unroll
    for (int j = 0; j < 4; ++j) {
      const int d = 2 * (lane + 64 * j);
      f2 sa = *(const f2*)(mods + d);
      f2 ca1 = *(const f2*)(mods + 512 + d);
      v[j] = bfr2(__builtin_elementwise_fma(v[j] - mean, ca1 * rden, sa));
    }
    fft256_fwd(v, lane, tw, (f2*)(dsm + L_PX + w * 2048), NoHook{});
    {
      f2* buf = zs + w * ZRS + lane + (lane >> 4);
#pragma unroll
      for (int r4 = 0; r4 < 4; ++r4) buf[68 * r4] = v[r4];
    }
    STAMP(1, 5);
    __syncthreads();
    {
      const int k = kk;
      const int k2 = (k == 0) ? 128 : 256 - k;
      const int pa = zpos(k), pb = zpos(k2);
      f4 oa, ob;
#pragma unroll
      for (int h = 0; h < 2; ++h) {
        const f2* zr = zs + (2 * rp + h) * ZRS;
        f2 za = zr[pa], zb = zr[pb];
        f2 E = f2{za.x + zb.x, za.y - zb.y};
        f2 Dm = f2{za.x - zb.x, za.y + zb.y};
        f2 O = f2{Dm.y, -Dm.x};
        f2 wo = cmul1<false>(O, wpost);
        f2 ra = E + wo;
        f2 t = E - wo;
        f2 rb = f2{t.x, -t.y};
        if (k == 0) {
          ra = f2{2.f * (za.x + za.y), 2.f * (za.x - za.y)};
          rb = f2{2.f * zb.x, -2.f * zb.y};
        }
        oa[2 * h] = ra.x; oa[2 * h + 1] = ra.y;
        ob[2 * h] = rb.x; ob[2 * h + 1] = rb.y;
      }
      ST128(__builtin_bit_cast(u4v, oa), OFF_XT + (k * CP + r0 + 2 * rp) * 8);
      ST128(__builtin_bit_cast(u4v, ob), OFF_XT + (k2 * CP + r0 + 2 * rp) * 8);
    }
    STAMP(1, 6);
    DRAIN();
    __syncthreads();
    if (tid < NREP)
      __hip_atomic_store((gu32*)(ws + OFF_FLR + (tid * 256 + bid) * FSP), 1u, __ATOMIC_RELAXED, __HIP_MEMORY_SCOPE_AGENT);
    STAMP(1, 7);
  }
  {
    f2* T = (f2*)(dsm + L_T);
    f2* T2 = (f2*)(dsm + L_T + 16384);
    f2* T3 = (f2*)(dsm + L_T + 32768);
    const int k = bid;
    f2 tw1[4];
#pragma unroll
    for (int j = 0; j < 4; ++j) {
      tw1[j] = twid((float)((lane + 64 * j) * w) * (1.0f / 2048.0f));
      PIN2(tw1[j]);
    }
    if (w == 0) wait_cols((gu32*)(ws + OFF_FLR + ((bid % NREP) * 256 + lane) * FSP));
    asm volatile("s_barrier" ::: "memory");
    __builtin_amdgcn_fence(__ATOMIC_ACQUIRE, "wavefront");
    STAMP(2, 0);
    f2 v[4];
#pragma unroll
    for (int j = 0; j < 4; ++j) v[j] = __builtin_bit_cast(f2, LD64(OFF_XT + (k * CP + tid + 512 * j) * 8));
    u4v bv;
    unsigned wn[4], bn[4];
    int mirr[4];
#pragma unroll
    for (int j = 0; j < 4; ++j) PIN2(v[j]);
    pf(8, 4);
    const int hb = w >> 2, a = tid & 255;
    constexpr float R = 0.70710678118654752440f;
    dft4<-1>(v[0], v[1], v[2], v[3]);
    if (hb) {
      v[1] = f2{v[1].x + v[1].y, v[1].y - v[1].x} * R;
      v[2] = f2{v[2].y, -v[2].x};
      v[3] = f2{v[3].y - v[3].x, -v[3].x - v[3].y} * R;
    }
#pragma unroll
    for (int q = 0; q < 4; ++q) T[(hb * 4 + q) * 256 + a] = v[q];
    STAMP(2, 1);
    const u4v wv = LD128(wt_off(k, tid));
    u4v w2a;
    {
      const int ks2 = tk >> 2, rr = tid >> 4, gg = rr >> 3, j = rr & 7;
      const int e = ks2 / 7, i7 = ks2 - 7 * e;
      const int T0 = 14 * e + (i7 < 3 ? 2 * i7 : (i7 < 6 ? 2 * i7 + 1 : 6));
      const int T1 = i7 < 6 ? T0 + 1 : 14 * e + 13;
      const int f = (j < 4) ? (16 * T0 + 4 * gg + j) : (16 * T1 + 4 * gg + (j - 4));
      w2a = *(const u4v*)(w2 + (size_t)f * D_ + (tk & 3) * 128 + (tid & 15) * 8);
    }
    if (k == 0) {
      bv = LD128(wt_off(256, tid));
      const int m0 = (lane >> 4) + 4 * ((lane >> 2) & 3) + 16 * (lane & 3);
#pragma unroll
      for (int r4 = 0; r4 < 4; ++r4) {
        const int un = (S_ - (w + 8 * (m0 + 64 * r4))) & (S_ - 1);
        const int mm = un >> 3;
        mirr[r4] = ((un & 7) * 64 + 16 * (mm & 3) + 4 * ((mm >> 2) & 3) + ((mm >> 4) & 3)) * 4 + (mm >> 6);
      }
      if (w != 0) {
        const u4v a4 = LD128(wt_off(0, 64 * (8 - w) + 63 - lane));
        const u4v b4 = LD128(wt_off(256, 64 * (8 - w) + 63 - lane));
#pragma unroll
        for (int r4 = 0; r4 < 4; ++r4) { wn[r4] = a4[3 - r4]; bn[r4] = b4[3 - r4]; }
      } else {
#pragma unroll
        for (int r4 = 0; r4 < 4; ++r4) {
          wn[r4] = LD32(wt_off(0, mirr[r4] >> 2) + (mirr[r4] & 3) * 4);
          bn[r4] = LD32(wt_off(256, mirr[r4] >> 2) + (mirr[r4] & 3) * 4);
        }
      }
    }
    __syncthreads();
    {
      const int q = w & 3;
      const float sg = (w < 4) ? 1.f : -1.f;
#pragma unroll
      for (int j = 0; j < 4; ++j) {
        f2 e = T[q * 256 + lane + 64 * j], o = T[(4 + q) * 256 + lane + 64 * j];
        v[j] = cmul1<false>(e + o * sg, tw1[j]);
      }
    }
    fft256_fwd(v, lane, tw, T2 + w * 256, [&](int i) __attribute__((always_inline)) {
      SCHED();
      if (i < 2) {
#pragma unroll
        for (int q = 0; q < 4; ++q) pf_put(4 * i + q);
      } else {
#pragma unroll
        for (int q = 12; q < 16; ++q) pf_load(q);
      }
      SCHED();
    });
    STAMP(2, 2);
    if (k != 0) {
#pragma unroll
      for (int i = 0; i < 4; ++i) {
        v[i] = cmul1<false>(v[i], f2{bf_lo(wv[i]), bf_hi(wv[i])});
      }
    } else {
#pragma unroll
      for (int r4 = 0; r4 < 4; ++r4) T3[r4 * 512 + tid] = v[r4];
      __syncthreads();
#pragma unroll
      for (int r4 = 0; r4 < 4; ++r4) {
        f2 cm = T3[(mirr[r4] & 3) * 512 + (mirr[r4] >> 2)];
        f2 h0 = f2{bf_lo(wv[r4]) + bf_lo(wn[r4]), bf_hi(wv[r4]) - bf_hi(wn[r4])};
        f2 h1 = f2{bf_lo(bv[r4]) + bf_lo(bn[r4]), bf_hi(bv[r4]) - bf_hi(bn[r4])};
        f2 wp = (h0 + h1) * 0.25f, wm = (h0 - h1) * 0.25f;
        f2 cc = f2{cm.x, -cm.y};
        v[r4] = cmul(v[r4], wp) + cmul(cc, wm);
      }
    }
    fft256_inv(v, lane, tw, T2 + w * 256, [&](int i) __attribute__((always_inline)) {
      SCHED();
      if (i == 0) {
#pragma unroll
        for (int q = 8; q < 12; ++q) pf_put(q);
      }
      SCHED();
    });
#pragma unroll
    for (int j = 0; j < 4; ++j) T2[w * 256 + lane + 64 * j] = cmul1<true>(v[j], tw1[j]);
#pragma unroll
    for (int q = 12; q < 16; ++q) pf_put(q);
    STAMP(2, 3);
    __syncthreads();
    {
      const float sg = hb ? -1.f : 1.f;
#pragma unroll
      for (int q = 0; q < 4; ++q) v[q] = T2[q * 256 + a] + T2[(q + 4) * 256 + a] * sg;
      if (hb) {
        v[1] = f2{v[1].x - v[1].y, v[1].y + v[1].x} * R;
        v[2] = f2{-v[2].y, v[2].x};
        v[3] = f2{-v[3].x - v[3].y, v[3].x - v[3].y} * R;
      }
    }
    dft4<1>(v[0], v[1], v[2], v[3]);
#pragma unroll
    for (int j = 0; j < 4; ++j) ST64(__builtin_bit_cast(u2v, v[j]), OFF_ZT + (k * CP + tid + 512 * j) * 8);
    STAMP(2, 4);
    *(u4v*)(dsm + L_W2S + ((tid >> 4) * 136 + (tid & 15) * 8) * 2) = w2a;
    DRAIN();
    __syncthreads();
    if (tid < NREP)
      __hip_atomic_store((gu32*)(ws + OFF_FLG + (tid * 256 + k) * FSP), 1u, __ATOMIC_RELAXED, __HIP_MEMORY_SCOPE_AGENT);
  }
  STAMP(2, 5);
  {
    const u16* t = (const u16*)(dsm + L_W2S);
    const int g = lane >> 4, r = lane & 15;
    const int nl = 32 * (w >> 1) + 8 * (r >> 2) + 4 * (w & 1) + (r & 3);
    u4v o;
#pragma unroll
    for (int jj = 0; jj < 4; ++jj)
      o[jj] = (unsigned)t[(g * 8 + 2 * jj) * 136 + nl] | ((unsigned)t[(g * 8 + 2 * jj + 1) * 136 + nl] << 16);
    if (bid < 224) ST128(o, OFF_W2F + ((((tk & 3) * 8 + w) * 56 + (tk >> 2)) * 64 + lane) * 16);
  }
  {
    const char* lw = dsm + L_W1 + (lane >> 2) * 224 + (lane & 3) * 8 + w * (2 * 7168);
    u4v fr[14];
#pragma unroll
    for (int i = 0; i < 14; ++i) fr[i] = w1frag(lw, i % 7, i / 7);
    asm volatile("" ::: "memory");
#pragma unroll
    for (int i = 0; i < 14; ++i) *(u4v*)(dsm + L_W1 + w * (2 * 7168) + i * 1024 + lane * 16) = fr[i];
  }
  STAMP(2, 6);
  {
    f2* zs = (f2*)(dsm + L_ZS);
    const int rp = tid & 3, k = tid >> 2;
    const int k2 = (k == 0) ? 128 : 256 - k;
    u4v xr;
    const unsigned* xrow = (const unsigned*)(x + (size_t)(row0 + w) * D_);
#pragma unroll
    for (int j = 0; j < 4; ++j) xr[j] = xrow[lane + 64 * j];
    const u4v gav = LD128(OFF_MODC + lane * 16);
    const u4v sbv = LD128(OFF_MODC + 1024 + lane * 16);
    const u4v cbv = LD128(OFF_MODC + 2048 + lane * 16);
    f2 wq = twid((float)k * (1.0f / 512.0f));
    PIN2(wq);
    const int pa = zpos(k), pb = zpos(k2);
    if (w == 0) wait_cols((gu32*)(ws + OFF_FLG + ((bid % NREP) * 256 + lane) * FSP));
    DRAIN();
    asm volatile("s_barrier" ::: "memory");
    __builtin_amdgcn_fence(__ATOMIC_ACQUIRE, "wavefront");
    if (tid == 0) __hip_atomic_store((gu32*)(ws + OFF_FLS + bid * 64), 1u, __ATOMIC_RELAXED, __HIP_MEMORY_SCOPE_AGENT);
    STAMP(3, 0);
    const f4 ya = __builtin_bit_cast(f4, LD128(OFF_ZT + (k * CP + row0 + 2 * rp) * 8));
    const f4 yb = __builtin_bit_cast(f4, LD128(OFF_ZT + (k2 * CP + row0 + 2 * rp) * 8));
#pragma unroll
    for (int h = 0; h < 2; ++h) {
      f2 a = f2{ya[2 * h], ya[2 * h + 1]}, b = f2{yb[2 * h], yb[2 * h + 1]};
      f2* zr = zs + (2 * rp + h) * ZRS;
      f2 Ze = f2{a.x + b.x, a.y - b.y};
      f2 Dm = f2{a.x - b.x, a.y + b.y};
      f2 Zo = cmul1<true>(Dm, wq);
      f2 za = f2{Ze.x - Zo.y, Ze.y + Zo.x};
      f2 zb = f2{Ze.x + Zo.y, -Ze.y + Zo.x};
      if (k == 0) {
        za = f2{a.x + a.y, a.x - a.y};
        zb = f2{2.f * b.x, -2.f * b.y};
      }
      zr[pa] = za;
      zr[pb] = zb;
    }
    STAMP(3, 1);
    __syncthreads();
    f2 v[4];
    {
      const f2* buf = zs + w * ZRS + lane + (lane >> 4);
#pragma unroll
      for (int r4 = 0; r4 < 4; ++r4) v[r4] = buf[68 * r4];
    }
    fft256_inv(v, lane, tw, (f2*)(dsm + L_PX + w * 2048), NoHook{});
    {
      gu32* fs = (gu32*)(ws + OFF_FLS + ((7 * w + (lane < 7 ? lane : 0)) * 4 + (nt0 >> 3)) * 64);
#pragma nounroll
      for (unsigned spin = 0; spin < SPIN_CAP; ++spin) {
        const unsigned f = __hip_atomic_load(fs, __ATOMIC_RELAXED, __HIP_MEMORY_SCOPE_AGENT);
        if (__builtin_amdgcn_ballot_w64(f != 1u) == 0ull) break;
        __builtin_amdgcn_s_sleep(1);
      }
      __builtin_amdgcn_fence(__ATOMIC_ACQUIRE, "wavefront");
      asm volatile("" ::: "memory");
    }
    pf(16, 4);
    STAMP(3, 2);
    float s = 0.f;
    u4v x1p;
#pragma unroll
    for (int j = 0; j < 4; ++j) {
      f2 t = __builtin_elementwise_fma(f2{bf_lo(gav[j]), bf_hi(gav[j])} * FSC, v[j], f2{bf_lo(xr[j]), bf_hi(xr[j])});
      x1p[j] = pack_bf(t.x, t.y);
      v[j] = f2{bf_lo(x1p[j]), bf_hi(x1p[j])};
      s += v[j].x + v[j].y;
    }
    ST128(x1p, OFF_X1 + ((row0 + w) * 64 + lane) * 16);
    s = wave_sum(s);
    const float meanf = s * (1.0f / D_);
    float q = 0.f;
#pragma unroll
    for (int j = 0; j < 4; ++j) {
      f2 d = v[j] - meanf;
      q += d.x * d.x + d.y * d.y;
    }
    q = wave_sum(q);
    const float mean = bfr(meanf);
    const float den = bfr(sqrtf(bfr(bfr(q * (1.0f / D_)) + 1e-5f)));
    const float rden = 1.0f / den;
    u4v hp;
#pragma unroll
    for (int j = 0; j < 4; ++j) {
      f2 t = __builtin_elementwise_fma(v[j] - mean, f2{bf_lo(cbv[j]), bf_hi(cbv[j])} * rden,
                                       f2{bf_lo(sbv[j]), bf_hi(sbv[j])});
      hp[j] = pack_bf(t.x, t.y);
    }
    *(u4v*)(dsm + L_ST + (lane * 9 + w) * 16) = hp;
    __syncthreads();
    {
      const int ch = tid >> 3, r = tid & 7;
      u4v o = *(const u4v*)(dsm + L_ST + (ch * 9 + r) * 16);
      const int mt = row0 >> 4, r16 = (row0 & 8) + r;
      ST128(o, OFF_H2F + ((mt * 16 + (ch >> 2)) * 64 + (ch & 3) * 16 + r16) * 16);
    }
  }
  STAMP(3, 3);
  DRAIN();
  __syncthreads();
  if (tid == 0) __hip_atomic_store(myA, 1u, __ATOMIC_RELAXED, __HIP_MEMORY_SCOPE_AGENT);
  {
    const int ch = tid >> 3, r = tid & 7;
    u4v t = LD128(OFF_H2F + (((row0 >> 4) * 16 + (ch >> 2)) * 64 + (ch & 3) * 16 + (row0 & 8) + r) * 16);
    asm volatile("" ::"v"(t));
  }
  STAMP(3, 4);
  wait_pair(depA);
  STAMP(3, 5);
  {
    u4v B[16];
#pragma unroll
    for (int ks = 0; ks < 16; ++ks) B[ks] = LD128(OFF_H2F + (((8 * p + w) * 16 + ks) * 64 + lane) * 16);
    f4 acc[7];
#pragma unroll
    for (int ft = 0; ft < 7; ++ft) acc[ft] = f4{0.f, 0.f, 0.f, 0.f};
    const char* lw = dsm + L_W1 + lane * 16;
    unsigned off2 = L_W1 + lane * 16 + 8 * 7168;
    asm volatile("" : "+v"(off2));
    const char* lw2 = dsm + off2;
    const int e = c >> 1, odd = c & 1;
    const int hb = (int)OFF_HF + (((8 * p + w) * 56 + 7 * e) * 64 + lane) * 16;
    u4v Af[2][7];
#pragma unroll
    for (int j = 0; j < 7; ++j) Af[0][j] = *(const u4v*)(lw + (g1_ks(0, j) * 7 + g1_ft(0, j)) * 1024);
#pragma unroll
    for (int g = 0; g < 17; ++g) {
#pragma unroll
      for (int j = 0; j < 7; ++j) {
        if (j < g1_n(g)) acc[g1_ft(g, j)] = mfma16(Af[g & 1][j], B[g1_ks(g, j)], acc[g1_ft(g, j)]);
        if (g + 1 < 17 && j < g1_n(g + 1))
          Af[(g + 1) & 1][j] = g1_ks(g + 1, j) < 8 ? *(const u4v*)(lw + (g1_ks(g + 1, j) * 7 + g1_ft(g + 1, j)) * 1024)
                                                   : *(const u4v*)(lw2 + ((g1_ks(g + 1, j) - 8) * 7 + g1_ft(g + 1, j)) * 1024);
        SCHED();
      }
      if (g >= 1 && g < 13) { pf_load(18 + 2 * g); pf_load(19 + 2 * g); }
      if (g >= 14) {
        const int i = g - 14;
        f4 a = acc[2 * i], b = acc[2 * i + 1];
        u4v o = u4v{silu_pk(a[0], a[1]), silu_pk(a[2], a[3]), silu_pk(b[0], b[1]), silu_pk(b[2], b[3])};
        ST128(o, hb + (odd * 3 + i) * 1024);
      }
      SCHED();
      if (g == 12) STAMP(3, 6);
    }
    f4 a = acc[6];
    u2v o = u2v{silu_pk(a[0], a[1]), silu_pk(a[2], a[3])};
    ST64(o, hb + 6 * 1024 + odd * 8);
  }
  STAMP(3, 7);
  DRAIN();
  __syncthreads();
  if (tid == 0) __hip_atomic_store(myB, 1u, __ATOMIC_RELAXED, __HIP_MEMORY_SCOPE_AGENT);
  {
    const int hb = (int)OFF_HF + (((8 * p + w) * 56 + 7 * (c >> 1) + 3 * (c & 1)) * 64 + lane) * 16;
    u4v t0 = LD128(hb), t1 = LD128(hb + 1024), t2 = LD128(hb + 2048);
    asm volatile("" ::"v"(t0), "v"(t1), "v"(t2));
  }
  wait_pair(depB);
  STAMP(3, 8);
  {
    const int Bb = (int)OFF_HF + ((mt0 * 56 + 7 * w) * 64 + lane) * 16;
    u4v B[7][4];
#pragma unroll
    for (int ks = 0; ks < 4; ++ks)
#pragma unroll
      for (int mt = 0; mt < 4; ++mt) B[ks][mt] = LD128(Bb + (mt * 56 + ks) * 1024);
    f4 acc[4][4];
#pragma unroll
    for (int i = 0; i < 4; ++i)
#pragma unroll
      for (int j = 0; j < 4; ++j) acc[i][j] = f4{0.f, 0.f, 0.f, 0.f};
#pragma unroll
    for (int ks = 0; ks < 7; ++ks) {
#pragma unroll
      for (int nt = 0; nt < 4; ++nt)
#pragma unroll
        for (int mt = 0; mt < 4; ++mt) acc[nt][mt] = mfma16(A2[ks * 4 + nt], B[ks][mt], acc[nt][mt]);
      if (ks + 4 < 7) {
#pragma unroll
        for (int mt = 0; mt < 4; ++mt) B[ks + 4][mt] = LD128(Bb + (mt * 56 + ks + 4) * 1024);
      }
      SCHED();
    }
    STAMP(3, 9);
    const int g = lane >> 4, mr = lane & 15;
    const int fm = w >> 1, P = w & 1;
    const int m = (mt0 + fm) * 16 + mr;
    const int n = nt0 * 16 + 32 * P + 8 * g;
    unsigned xv[4];
#pragma unroll
    for (int i = 0; i < 4; ++i)
      xv[i] = __builtin_amdgcn_raw_buffer_load_b32(
          rs, (int)OFF_X1 + (m * 64 + (((n >> 1) + i) & 63)) * 16 + (n >> 7) * 4, 0, AUX_SC1);
    const f4 g0 = __builtin_bit_cast(f4, LD128(OFF_MODF + (2560 + n) * 4));
    const f4 g1 = __builtin_bit_cast(f4, LD128(OFF_MODF + (2560 + n + 4) * 4));
    f4* red = (f4*)dsm;
#pragma unroll
    for (int nt = 0; nt < 4; ++nt)
#pragma unroll
      for (int mt = 0; mt < 4; ++mt) red[(w * 16 + nt * 4 + mt) * 64 + lane] = acc[nt][mt];
    __syncthreads();
    STAMP(3, 10);
    f4 s0 = f4{0.f, 0.f, 0.f, 0.f}, s1 = s0;
#pragma unroll
    for (int ww = 0; ww < 8; ++ww) {
      s0 += red[(ww * 16 + (2 * P) * 4 + fm) * 64 + lane];
      s1 += red[(ww * 16 + (2 * P + 1) * 4 + fm) * 64 + lane];
    }
    u4v o;
    {
      f2 t0 = bfr2(f2{g0[0], g0[1]} * bfr2(f2{s0[0], s0[1]})), t1 = bfr2(f2{g0[2], g0[3]} * bfr2(f2{s0[2], s0[3]}));
      f2 t2 = bfr2(f2{g1[0], g1[1]} * bfr2(f2{s1[0], s1[1]})), t3 = bfr2(f2{g1[2], g1[3]} * bfr2(f2{s1[2], s1[3]}));
      o[0] = pack_bf(bf_lo(xv[0]) + t0.x, bf_hi(xv[0]) + t0.y);
      o[1] = pack_bf(bf_lo(xv[1]) + t1.x, bf_hi(xv[1]) + t1.y);
      o[2] = pack_bf(bf_lo(xv[2]) + t2.x, bf_hi(xv[2]) + t2.y);
      o[3] = pack_bf(bf_lo(xv[3]) + t3.x, bf_hi(xv[3]) + t3.y);
    }
    *(u4v*)(out + (size_t)m * D_ + n) = o;
  }
  STAMP(3, 11);
}

extern "C" void kernel_launch(void* const* d_in, const int* in_sizes, int n_in, void* d_out, int out_size, void* d_ws,
                              size_t ws_size, hipStream_t stream) {
  const u16* x = (const u16*)d_in[0];
  const u16* wr = (const u16*)d_in[1];
  const u16* wi = (const u16*)d_in[2];
  const u16* w1 = (const u16*)d_in[4];
  const u16* w2 = (const u16*)d_in[5];
  const u16* c = (const u16*)d_in[6];
  const u16* wmod = (const u16*)d_in[7];
  char* ws = (char*)d_ws;
  u16* out = (u16*)d_out;
  (void)hipFuncSetAttribute((const void*)kmega, hipFuncAttributeMaxDynamicSharedMemorySize, L_TOTAL);
  k0_mod<<<K0_GEMV + K0_WT + K0_W2 + K0_Z + K0_WARM, 512, 0, stream>>>(c, wmod, x, wr, wi, w1, w2, ws);
  kmega<<<256, 512, L_TOTAL, stream>>>(x, w1, w2, ws, out);
}
```

```cpp
#include <hip/hip_runtime.h>
#include <hip/hip_bf16.h>
#include <stdint.h>

typedef float f2 __attribute__((ext_vector_type(2)));
typedef float f4 __attribute__((ext_vector_type(4)));
typedef unsigned u2v __attribute__((ext_vector_type(2)));
typedef unsigned u4v __attribute__((ext_vector_type(4)));
typedef short bf16x8 __attribute__((ext_vector_type(8)));
typedef unsigned short u16;

constexpr int S_ = 2048, D_ = 512, F_ = 1792, DF_ = 257;
constexpr int NSPLIT = 4;

constexpr size_t OFF_CNT = 0;
#define FSPACE 16
#define FREP 16
constexpr int FSP = FSPACE, NREP = FREP;
constexpr size_t FTAB = (size_t)NREP * 256 * FSP;
constexpr size_t OFF_FLG = 16384;
constexpr size_t OFF_FLR = OFF_FLG + FTAB;
constexpr size_t OFF_FLS = OFF_FLR + FTAB;
constexpr size_t FLAG_BYTES = OFF_FLS + 16384;
constexpr size_t OFF_MODP = 900 << 10;
constexpr size_t OFF_MODF = OFF_MODP + (size_t)NSPLIT * 3072 * 4;
constexpr size_t OFF_MODC = OFF_MODF + 3072 * 4;
constexpr size_t OFF_WT = 1 << 20;
constexpr size_t OFF_XT = 4 << 20;
constexpr size_t OFF_ZT = 28 << 20;
constexpr size_t OFF_H2F = 12 << 20;
constexpr size_t OFF_X1 = 14 << 20;
constexpr size_t OFF_W2F = 18 << 20;
constexpr size_t OFF_HF = 20 << 20;
constexpr int WS_SPAN = 36 << 20;
#define CPITCH (2048 + 32)
constexpr int CP = CPITCH;
constexpr int WT_PITCH = 520;
constexpr float FSC = 1.0f / (2.0f * 2048.0f * 512.0f);

#define STAMP(kid, i)

__device__ __forceinline__ float bf_lo(unsigned u) { return __builtin_bit_cast(float, u << 16); }
__device__ __forceinline__ float bf_hi(unsigned u) { return __builtin_bit_cast(float, u & 0xffff0000u); }
__device__ __forceinline__ float bfr(float x) {
  return (float)(__bf16)x;
}
__device__ __forceinline__ unsigned pack_bf(float lo, float hi) {
  typedef __bf16 b2 __attribute__((ext_vector_type(2)));
  return __builtin_bit_cast(unsigned, __builtin_convertvector(f2{lo, hi}, b2));
}
__device__ __forceinline__ f2 cmul(f2 a, f2 b) {
  f2 t = a * f2{b.x, b.x};
  return __builtin_elementwise_fma(f2{a.y, a.x}, f2{-b.y, b.y}, t);
}
__device__ __forceinline__ f2 cmulc(f2 a, f2 b) {
  f2 t = a * f2{b.x, b.x};
  return __builtin_elementwise_fma(f2{a.y, a.x}, f2{b.y, -b.y}, t);
}
__device__ __forceinline__ f2 bfr2(f2 a) {
  unsigned u = pack_bf(a.x, a.y);
  asm("" : "+v"(u));
  return f2{bf_lo(u), bf_hi(u)};
}
__device__ __forceinline__ float msel(unsigned m, float a, float b) {
  unsigned ua = __builtin_bit_cast(unsigned, a), ub = __builtin_bit_cast(unsigned, b);
  return __builtin_bit_cast(float, (m & ua) | (~m & ub));
}
#define PIN2(x) asm volatile("" : "+v"(x))
constexpr int AUX_SC1 = 16;
#define WS_RSRC(ws) __builtin_amdgcn_make_buffer_rsrc((ws), 0, WS_SPAN, 0x00020000)
#define ST128(v, off) __builtin_amdgcn_raw_buffer_store_b128((v), rs, (int)(off), 0, AUX_SC1)
#define ST64(v, off) __builtin_amdgcn_raw_buffer_store_b64((v), rs, (int)(off), 0, AUX_SC1)
#define ST32(v, off) __builtin_amdgcn_raw_buffer_store_b32((v), rs, (int)(off), 0, AUX_SC1)
#define LD64(off) __builtin_amdgcn_raw_buffer_load_b64(rs, (int)(off), 0, AUX_SC1)
#define LD32(off) __builtin_amdgcn_raw_buffer_load_b32(rs, (int)(off), 0, AUX_SC1)
#define LD128(off) __builtin_amdgcn_raw_buffer_load_b128(rs, (int)(off), 0, AUX_SC1)
__device__ __forceinline__ f2 twid(float rev) { return f2{__builtin_amdgcn_cosf(rev), -__builtin_amdgcn_sinf(rev)}; }

template <int DIR>
__device__ __forceinline__ f2 rot90(f2 a) {
  return DIR < 0 ? f2{a.y, -a.x} : f2{-a.y, a.x};
}
template <int DIR>
__device__ __forceinline__ void dft4(f2& c0, f2& c1, f2& c2, f2& c3) {
  f2 d0 = c0 + c2, d2 = c0 - c2, d1 = c1 + c3, e = c1 - c3;
  c0 = d0 + d1;
  c2 = d0 - d1;
  const f2 es = f2{e.y, e.x};
  c1 = __builtin_elementwise_fma(es, DIR < 0 ? f2{1.f, -1.f} : f2{-1.f, 1.f}, d2);
  c3 = __builtin_elementwise_fma(es, DIR < 0 ? f2{-1.f, 1.f} : f2{1.f, -1.f}, d2);
}

__device__ __forceinline__ void sw32(float& a, float& b) {
  auto r = __builtin_amdgcn_permlane32_swap(__builtin_bit_cast(unsigned, a), __builtin_bit_cast(unsigned, b), false, false);
  a = __builtin_bit_cast(float, (unsigned)r[0]);
  b = __builtin_bit_cast(float, (unsigned)r[1]);
}
__device__ __forceinline__ void sw16(float& a, float& b) {
  auto r = __builtin_amdgcn_permlane16_swap(__builtin_bit_cast(unsigned, a), __builtin_bit_cast(unsigned, b), false, false);
  a = __builtin_bit_cast(float, (unsigned)r[0]);
  b = __builtin_bit_cast(float, (unsigned)r[1]);
}
template <int CTRL, int BANK>
__device__ __forceinline__ float udpp(float old, float src) {
  return __builtin_bit_cast(float, __builtin_amdgcn_update_dpp(__builtin_bit_cast(int, old), __builtin_bit_cast(int, src),
                                                               CTRL, 0xf, BANK, false));
}
template <int CTRL>
__device__ __forceinline__ float mdpp(float src) {
  return __builtin_bit_cast(float, __builtin_amdgcn_mov_dpp(__builtin_bit_cast(int, src), CTRL, 0xf, 0xf, false));
}
__device__ __forceinline__ void xp_hi1(float& v0, float& v1, float& v2, float& v3) {
  sw32(v0, v2); sw32(v1, v3); sw16(v0, v1); sw16(v2, v3);
}
__device__ __forceinline__ void xp_mid1(float& v0, float& v1, float& v2, float& v3) {
  float a0 = udpp<0x128, 0xC>(v0, v2), a2 = udpp<0x128, 0x3>(v2, v0);
  float a1 = udpp<0x128, 0xC>(v1, v3), a3 = udpp<0x128, 0x3>(v3, v1);
  v0 = udpp<0x124, 0xA>(a0, a1); v1 = udpp<0x12C, 0x5>(a1, a0);
  v2 = udpp<0x124, 0xA>(a2, a3); v3 = udpp<0x12C, 0x5>(a3, a2);
}
__device__ __forceinline__ void xp_lo1(float& v0, float& v1, float& v2, float& v3, unsigned b1, unsigned b0) {
  float s0 = mdpp<0x4E>(msel(b1, v0, v2)), s1 = mdpp<0x4E>(msel(b1, v1, v3));
  float a0 = msel(b1, s0, v0), a2 = msel(b1, v2, s0), a1 = msel(b1, s1, v1), a3 = msel(b1, v3, s1);
  float t0 = mdpp<0xB1>(msel(b0, a0, a1)), t2 = mdpp<0xB1>(msel(b0, a2, a3));
  v0 = msel(b0, t0, a0); v1 = msel(b0, a1, t0); v2 = msel(b0, t2, a2); v3 = msel(b0, a3, t2);
}
#define XP_CPLX(NAME, FN, ...)                                        \
  {                                                                   \
    float x0 = v[0].x, x1 = v[1].x, x2 = v[2].x, x3 = v[3].x;         \
    float y0 = v[0].y, y1 = v[1].y, y2 = v[2].y, y3 = v[3].y;         \
    FN(x0, x1, x2, x3, ##__VA_ARGS__);                                \
    FN(y0, y1, y2, y3, ##__VA_ARGS__);                                \
    v[0] = f2{x0, y0}; v[1] = f2{x1, y1}; v[2] = f2{x2, y2}; v[3] = f2{x3, y3}; \
  }
__device__ __forceinline__ void xp_hi(f2 (&v)[4]) XP_CPLX(hi, xp_hi1)
__device__ __forceinline__ void xp_mid(f2 (&v)[4]) XP_CPLX(mid, xp_mid1)
__device__ __forceinline__ void xp_lo(f2 (&v)[4], unsigned b1, unsigned b0) XP_CPLX(lo, xp_lo1, b1, b0)

template <bool CONJ>
__device__ __forceinline__ void cmul3(f2 (&v)[4], const f2 (&t)[3]) {
  f2 a, b, c;
  if (!CONJ)
    asm("v_pk_mul_f32 %3, %0, %6 op_sel_hi:[1,0]\n\t"
        "v_pk_mul_f32 %4, %1, %7 op_sel_hi:[1,0]\n\t"
        "v_pk_mul_f32 %5, %2, %8 op_sel_hi:[1,0]\n\t"
        "v_pk_fma_f32 %0, %0, %6, %3 op_sel:[1,1,0] op_sel_hi:[0,1,1] neg_lo:[0,1,0]\n\t"
        "v_pk_fma_f32 %1, %1, %7, %4 op_sel:[1,1,0] op_sel_hi:[0,1,1] neg_lo:[0,1,0]\n\t"
        "v_pk_fma_f32 %2, %2, %8, %5 op_sel:[1,1,0] op_sel_hi:[0,1,1] neg_lo:[0,1,0]\n\t"
        "s_nop 1"
        : "+v"(v[1]), "+v"(v[2]), "+v"(v[3]), "=&v"(a), "=&v"(b), "=&v"(c)
        : "v"(t[0]), "v"(t[1]), "v"(t[2]));
  else
    asm("v_pk_mul_f32 %3, %0, %6 op_sel_hi:[1,0]\n\t"
        "v_pk_mul_f32 %4, %1, %7 op_sel_hi:[1,0]\n\t"
        "v_pk_mul_f32 %5, %2, %8 op_sel_hi:[1,0]\n\t"
        "v_pk_fma_f32 %0, %0, %6, %3 op_sel:[1,1,0] op_sel_hi:[0,1,1] neg_hi:[0,1,0]\n\t"
        "v_pk_fma_f32 %1, %1, %7, %4 op_sel:[1,1,0] op_sel_hi:[0,1,1] neg_hi:[0,1,0]\n\t"
        "v_pk_fma_f32 %2, %2, %8, %5 op_sel:[1,1,0] op_sel_hi:[0,1,1] neg_hi:[0,1,0]"
        : "+v"(v[1]), "+v"(v[2]), "+v"(v[3]), "=&v"(a), "=&v"(b), "=&v"(c)
        : "v"(t[0]), "v"(t[1]), "v"(t[2]));
}
template <bool CONJ>
__device__ __forceinline__ f2 cmul1(f2 v, f2 t) {
  f2 a;
  if (!CONJ)
    asm("v_pk_mul_f32 %1, %0, %2 op_sel_hi:[1,0]\n\t"
        "v_pk_fma_f32 %0, %0, %2, %1 op_sel:[1,1,0] op_sel_hi:[0,1,1] neg_lo:[0,1,0]"
        : "+v"(v), "=&v"(a) : "v"(t));
  else
    asm("v_pk_mul_f32 %1, %0, %2 op_sel_hi:[1,0]\n\t"
        "v_pk_fma_f32 %0, %0, %2, %1 op_sel:[1,1,0] op_sel_hi:[0,1,1] neg_hi:[0,1,0]"
        : "+v"(v), "=&v"(a) : "v"(t));
  return v;
}
__device__ __forceinline__ void xp_lo_lds(f2 (&v)[4], f2* patch, int lane) {
  f2* q = patch + (lane >> 2) * 16;
  const int c = lane & 3, sw = (lane >> 2) & 3;
#pragma unroll
  for (int j = 0; j < 4; ++j) q[4 * (j ^ sw) + c] = v[j];
  __builtin_amdgcn_fence(__ATOMIC_RELEASE, "wavefront");
  __builtin_amdgcn_wave_barrier();
  __builtin_amdgcn_fence(__ATOMIC_ACQUIRE, "wavefront");
  const f4 a = *(const f4*)(q + 4 * (c ^ sw)), b = *(const f4*)(q + 4 * (c ^ sw) + 2);
  v[0] = f2{a[0], a[1]}; v[1] = f2{a[2], a[3]}; v[2] = f2{b[0], b[1]}; v[3] = f2{b[2], b[3]};
  __builtin_amdgcn_fence(__ATOMIC_RELEASE, "wavefront");
  __builtin_amdgcn_wave_barrier();
}

struct Tw256 {
  f2 a[3], b[3], c[3];
};
__device__ __forceinline__ void make_tw256(int lane, Tw256& t) {
#pragma unroll
  for (int r = 1; r < 4; ++r) {
    t.a[r - 1] = twid((float)(lane * r) * (1.0f / 256.0f));
    t.b[r - 1] = twid((float)((lane & 15) * r) * (1.0f / 64.0f));
    t.c[r - 1] = twid((float)((lane & 3) * r) * (1.0f / 16.0f));
  }
#pragma unroll
  for (int r = 0; r < 3; ++r) {
    PIN2(t.a[r]);
    PIN2(t.b[r]);
    PIN2(t.c[r]);
  }
}
struct NoHook {
  __device__ __forceinline__ void operator()(int) const {}
};
template <class H>
__device__ __forceinline__ void fft256_fwd(f2 (&v)[4], int lane, const Tw256& t, f2* patch, const H& hook) {
  dft4<-1>(v[0], v[1], v[2], v[3]);
  cmul3<false>(v, t.a);
  xp_hi(v);
  hook(0);
  dft4<-1>(v[0], v[1], v[2], v[3]);
  cmul3<false>(v, t.b);
  xp_mid(v);
  hook(1);
  dft4<-1>(v[0], v[1], v[2], v[3]);
  cmul3<false>(v, t.c);
  xp_lo_lds(v, patch, lane);
  hook(2);
  dft4<-1>(v[0], v[1], v[2], v[3]);
}
template <class H>
__device__ __forceinline__ void fft256_inv(f2 (&v)[4], int lane, const Tw256& t, f2* patch, const H& hook) {
  dft4<1>(v[0], v[1], v[2], v[3]);
  xp_lo_lds(v, patch, lane);
  hook(0);
  cmul3<true>(v, t.c);
  dft4<1>(v[0], v[1], v[2], v[3]);
  xp_mid(v);
  hook(1);
  cmul3<true>(v, t.b);
  dft4<1>(v[0], v[1], v[2], v[3]);
  xp_hi(v);
  hook(2);
  cmul3<true>(v, t.a);
  dft4<1>(v[0], v[1], v[2], v[3]);
}

__device__ __forceinline__ float wave_sum(float x) {
  x += mdpp<0xB1>(x);
  x += mdpp<0x4E>(x);
  x += mdpp<0x141>(x);
  x += mdpp<0x140>(x);
  float r0 = __builtin_bit_cast(float, __builtin_amdgcn_readlane(__builtin_bit_cast(int, x), 0));
  float r1 = __builtin_bit_cast(float, __builtin_amdgcn_readlane(__builtin_bit_cast(int, x), 16));
  float r2 = __builtin_bit_cast(float, __builtin_amdgcn_readlane(__builtin_bit_cast(int, x), 32));
  float r3 = __builtin_bit_cast(float, __builtin_amdgcn_readlane(__builtin_bit_cast(int, x), 48));
  return (r0 + r1) + (r2 + r3);
}

__device__ __forceinline__ int zpos(int k) { return 17 * (k & 3) + 4 * ((k >> 2) & 3) + ((k >> 4) & 3) + 68 * (k >> 6); }
constexpr int ZRS = 276;

constexpr int K0_GEMV = 48 * NSPLIT, K0_WT = 128, K0_W2 = 112, K0_WARM = 0, K0_Z = (int)(FLAG_BYTES / 8192);
__global__ __launch_bounds__(512) void k0_mod(const u16* __restrict__ c, const u16* __restrict__ wmod,
                                              const u16* __restrict__ x, const u16* __restrict__ wr,
                                              const u16* __restrict__ wi, const u16* __restrict__ w1,
                                              const u16* __restrict__ w2, char* __restrict__ ws) {
  __shared__ __attribute__((aligned(16))) char smem[17408];
  const int tid = threadIdx.x, row = tid >> 6, lane = tid & 63;
  const auto rs = WS_RSRC(ws);
  int b = blockIdx.x;
  STAMP(0, 0);
  if (b >= K0_GEMV + K0_WT + K0_W2) {
    int q = b - (K0_GEMV + K0_WT + K0_W2);
    if (q < K0_Z) {
      ((u4v*)ws)[q * 512 + tid] = u4v{0u, 0u, 0u, 0u};
      return;
    }
    q -= K0_Z;
    const char* src = q < 64 ? (const char*)x + (size_t)q * 32768
                             : (q < 120 ? (const char*)w1 + (size_t)(q - 64) * 32768 : (const char*)w2 + (size_t)(q - 120) * 32768);
    u4v t[4];
#pragma unroll
    for (int i = 0; i < 4; ++i) t[i] = *(const u4v*)(src + i * 8192 + tid * 16);
#pragma unroll
    for (int i = 0; i < 4; ++i) asm volatile("" ::"v"(t[i]));
    STAMP(0, 1);
    return;
  }
  if (b >= K0_GEMV) {
    b -= K0_GEMV;
    if (b < K0_WT) {
      char* sr = smem;
      char* si = smem + 16 * 544;
      constexpr int LASTCH = (S_ * DF_ * 2) / 16 - 1;
      for (int it = tid; it < 16 * 34; it += 512) {
        const int i = it / 34, ch = it % 34;
        int gch = ((b + 128 * i) * (DF_ * 2)) / 16 + ch;
        gch = gch > LASTCH ? LASTCH : gch;
        *(u4v*)(sr + i * 544 + ch * 16) = *(const u4v*)((const char*)wr + (size_t)gch * 16);
        *(u4v*)(si + i * 544 + ch * 16) = *(const u4v*)((const char*)wi + (size_t)gch * 16);
      }
      __syncthreads();
      const int tbase = 64 * (b & 7) + 16 * ((b >> 3) & 3) + 4 * ((b >> 5) & 3);
      for (int it = tid; it < DF_ * 4; it += 512) {
        const int k = it >> 2, r3 = it & 3;
        u4v o;
#pragma unroll
        for (int r4 = 0; r4 < 4; ++r4) {
          const int i = r3 + 4 * r4;
          const int off = i * 544 + (((b + 128 * i) * (DF_ * 2)) & 15) + 2 * k;
          o[r4] = (unsigned)*(const u16*)(sr + off) | ((unsigned)*(const u16*)(si + off) << 16);
        }
        ST128(o, OFF_WT + (k * 512 + tbase + r3) * 16);
      }
    } else {
      b -= K0_WT;
      u16* t = (u16*)smem;
      const int g = lane >> 4, r = lane & 15;
      const int ng = b & 3, kp = b >> 2;
#pragma unroll
      for (int it = 0; it < 2; ++it) {
        const int idx = it * 512 + tid;
        const int rr = idx >> 4, ch = idx & 15;
        const int ks2 = kp * 2 + (rr >> 5), gg = (rr >> 3) & 3, j = rr & 7;
        const int e = ks2 / 7, i7 = ks2 % 7;
        int T0, T1;
        if (i7 < 3) { T0 = 14 * e + 2 * i7; T1 = T0 + 1; }
        else if (i7 < 6) { T0 = 14 * e + 7 + 2 * (i7 - 3); T1 = T0 + 1; }
        else { T0 = 14 * e + 6; T1 = 14 * e + 13; }
        const int f = (j < 4) ? (16 * T0 + 4 * gg + j) : (16 * T1 + 4 * gg + (j - 4));
        *(u4v*)(t + rr * 136 + ch * 8) = *(const u4v*)(w2 + (size_t)f * D_ + ng * 128 + ch * 8);
      }
      __syncthreads();
      const int nl = 32 * (row >> 1) + 8 * (r >> 2) + 4 * (row & 1) + (r & 3);
#pragma unroll
      for (int ksl = 0; ksl < 2; ++ksl) {
        u4v o;
#pragma unroll
        for (int jj = 0; jj < 4; ++jj)
          o[jj] = (unsigned)t[(ksl * 32 + g * 8 + 2 * jj) * 136 + nl] |
                  ((unsigned)t[(ksl * 32 + g * 8 + 2 * jj + 1) * 136 + nl] << 16);
        const int nt = ng * 8 + row;
        ST128(o, OFF_W2F + ((nt * 56 + kp * 2 + ksl) * 64 + lane) * 16);
      }
    }
    STAMP(0, 1);
    return;
  }
  float* red = (float*)smem;
  const int cgp = b % 48, ks = b / 48;
  const int cg = tid & 7, r = tid >> 3;
  const int rw = ks * 128 + r;
  const u4v wm0 = *(const u4v*)(wmod + (size_t)rw * 3072 + cgp * 64 + cg * 8);
  const u4v wm1 = *(const u4v*)(wmod + (size_t)(rw + 64) * 3072 + cgp * 64 + cg * 8);
  const float cv0 = bf_lo(c[rw]), cv1 = bf_lo(c[rw + 64]);
  const float s0 = bfr(cv0 / (1.0f + __expf(-cv0))), s1 = bfr(cv1 / (1.0f + __expf(-cv1)));
  float acc[8];
#pragma unroll
  for (int i = 0; i < 4; ++i) {
    acc[2 * i] = s0 * bf_lo(wm0[i]) + s1 * bf_lo(wm1[i]);
    acc[2 * i + 1] = s0 * bf_hi(wm0[i]) + s1 * bf_hi(wm1[i]);
  }
#pragma unroll
  for (int i = 0; i < 8; ++i) {
    acc[i] += mdpp<0x128>(acc[i]);
    acc[i] += __shfl_xor(acc[i], 16);
    acc[i] += __shfl_xor(acc[i], 32);
  }
  if (lane < 8) {
#pragma unroll
    for (int i = 0; i < 8; ++i) red[row * 64 + cg * 8 + i] = acc[i];
  }
  __syncthreads();
  if (tid < 64) {
    float sum = 0.f;
#pragma unroll
    for (int i = 0; i < 8; ++i) sum += red[i * 64 + tid];
    ((float*)(ws + OFF_MODP))[ks * 3072 + cgp * 64 + tid] = sum;
  }
  STAMP(0, 1);
}

__device__ __forceinline__ f4 mfma16(u4v a, u4v b, f4 c) {
  return __builtin_amdgcn_mfma_f32_16x16x32_bf16(__builtin_bit_cast(bf16x8, a), __builtin_bit_cast(bf16x8, b), c, 0, 0,
                                                  0);
}
__device__ __forceinline__ f2 silu2(float a, float b) {
  f2 h = f2{a, b};
  f2 t = h * -1.44269504088896340736f;
  f2 d = f2{__builtin_amdgcn_exp2f(t.x), __builtin_amdgcn_exp2f(t.y)} + 1.0f;
  return h * f2{__builtin_amdgcn_rcpf(d.x), __builtin_amdgcn_rcpf(d.y)};
}
__device__ __forceinline__ unsigned silu_pk(float a, float b) {
  f2 r = silu2(a, b);
  return pack_bf(r.x, r.y);
}
typedef __attribute__((address_space(1))) unsigned gu32;
#define DRAIN() asm volatile("s_waitcnt vmcnt(0)" ::: "memory")
typedef __attribute__((address_space(1))) unsigned long long gu64;
constexpr unsigned SPIN_CAP = 1u << 18;
__device__ __forceinline__ void wait_pair(gu64* flags) {
#pragma nounroll
  for (unsigned spin = 0; spin < SPIN_CAP; ++spin) {
    if (__hip_atomic_load(flags, __ATOMIC_RELAXED, __HIP_MEMORY_SCOPE_AGENT) == 0x0000000100000001ull) break;
    __builtin_amdgcn_s_sleep(1);
  }
  __builtin_amdgcn_fence(__ATOMIC_ACQUIRE, "wavefront");
  asm volatile("" ::: "memory");
}

#define PSLEEP 2
__device__ __forceinline__ void wait_cols(gu32* flags) {
#pragma nounroll
  for (unsigned spin = 0; spin < SPIN_CAP; ++spin) {
    unsigned f = 1u;
#pragma unroll
    for (int j = 0; j < 4; ++j)
      f &= __hip_atomic_load(flags + j * 64 * (FSP / 4), __ATOMIC_RELAXED, __HIP_MEMORY_SCOPE_AGENT);
    if (__builtin_amdgcn_ballot_w64(f != 1u) == 0ull) break;
    __builtin_amdgcn_s_sleep(PSLEEP);
  }
}

__device__ __forceinline__ int wt_off(int k, int t) { return (int)OFF_WT + (k * 512 + t) * 16; }

constexpr int L_W1 = 0;
constexpr int L_T = 112 * 1024;
constexpr int L_ZS = 112 * 1024;
constexpr int L_ST = L_ZS + 8 * ZRS * 8;
constexpr int L_TOTAL = 160 * 1024;
__device__ __forceinline__ constexpr int g1_n(int g) { return g < 13 ? 7 : (g < 16 ? 6 : 3); }
__device__ __forceinline__ constexpr int g1_ft(int g, int j) { return g < 13 ? j : (g < 16 ? 2 * (g - 13) + (j & 1) : 6); }
__device__ __forceinline__ constexpr int g1_ks(int g, int j) { return g < 13 ? g : (g < 16 ? 13 + (j >> 1) : 13 + j); }
#define SCHED() __builtin_amdgcn_sched_barrier(0)
typedef short v4i16 __attribute__((ext_vector_type(4)));
__device__ __forceinline__ u4v w1frag(const char* lw, int ft, int ks) {
  typedef __attribute__((address_space(3))) v4i16* lp;
  const u2v a = __builtin_bit_cast(u2v, __builtin_amdgcn_ds_read_tr16_b64_v4i16((lp)(lw + (2 * ks) * 3584 + ft * 32)));
  const u2v b = __builtin_bit_cast(u2v, __builtin_amdgcn_ds_read_tr16_b64_v4i16((lp)(lw + (2 * ks + 1) * 3584 + ft * 32)));
  return u4v{a[0], a[1], b[0], b[1]};
}
constexpr int L_MODS = L_ZS + 8 * ZRS * 8;
constexpr int L_PX = L_T + 28672;
constexpr int L_W2S = L_T + 32768;
__global__ __launch_bounds__(512, 2) void kmega(const u16* __restrict__ x, const u16* __restrict__ w1,
                                                const u16* __restrict__ w2, char* ws, u16* __restrict__ out) {
  extern __shared__ __attribute__((aligned(16))) char dsm[];
  const int tid = threadIdx.x, lane = tid & 63;
  const int w = __builtin_amdgcn_readfirstlane(tid >> 6);
  const int bid = blockIdx.x;
  const int p = (bid & 7) * 2 + (bid >> 7), c = (bid >> 3) & 15;
  const int row0 = 128 * p + 8 * c;
  STAMP(1, 0);
  const auto rs = WS_RSRC(ws);
  gu32* myA = (gu32*)(ws + OFF_CNT) + (p * 8 + (c >> 1)) * 16 + (c & 1);
  gu32* myB = myA + 128 * 16;
  gu64* depA = (gu64*)(ws + OFF_CNT) + (p * 8 + w) * 8;
  gu64* depB = depA + 128 * 8;
  const int mt0 = 8 * p + 4 * (c >> 3), nt0 = 4 * (c & 7);
  const int l56 = lane < 56 ? lane : lane - 56;
  const int r4_ = l56 / 14, ch_ = l56 - 14 * r4_;
  const char* w1g = (const char*)w1 + (size_t)(4 * w + r4_) * (F_ * 2) + c * 224 + ch_ * 16;
  char* w1l = dsm + L_W1 + ((w >> 1) * 32 + (2 * (w & 1) + (r4_ >> 1)) * 4 + (r4_ & 1)) * 224 + ch_ * 16;
  const int Ab = (int)OFF_W2F + ((nt0 * 56 + 7 * w) * 64 + lane) * 16;
  u4v S1[16];
  u4v A2[28];
  auto pf_load = [&](int i) __attribute__((always_inline)) {
    if (i < 16) S1[i] = *(const u4v*)(w1g + (size_t)i * (32 * F_ * 2));
    else if (i < 44) A2[i - 16] = *(const u4v*)(ws + Ab + (((i - 16) & 3) * 56 + ((i - 16) >> 2)) * 1024);
  };
  auto pf_put = [&](int i) __attribute__((always_inline)) {
    *(u4v*)(w1l + ((i & 3) * 128 + (i >> 3) * 16 + ((i >> 2) & 1) * 2) * 224) = S1[i];
  };
  auto pf = [&](int i0, int n) __attribute__((always_inline)) {
    SCHED();
#pragma unroll
    for (int i = 0; i < n; ++i) pf_load(i0 + i);
    SCHED();
  };
  Tw256 tw;
  {
    f2* zs = (f2*)(dsm + L_ZS);
    float* mods = (float*)(dsm + L_MODS);
    const int r0 = bid * 8;
    unsigned xr[4];
    const unsigned* xrow = (const unsigned*)(x + (size_t)(r0 + w) * D_);
#pragma unroll
    for (int j = 0; j < 4; ++j) xr[j] = xrow[lane + 64 * j];
    const float* mp = (const float*)(ws + OFF_MODP);
    f2 sm = *(const f2*)(mp + tid * 2);
#pragma unroll
    for (int k = 1; k < NSPLIT; ++k) sm += *(const f2*)(mp + k * 3072 + tid * 2);
    const int idx = bid * 12 + (tid < 6 ? tid : 0) * 2;
    f2 fin = *(const f2*)(mp + idx);
#pragma unroll
    for (int k = 1; k < NSPLIT; ++k) fin += *(const f2*)(mp + k * 3072 + idx);
    pf(0, 4);
    make_tw256(lane, tw);
    const int rp = tid & 3, kk = tid >> 2;
    f2 wpost = twid((float)kk * (1.0f / 512.0f));
    PIN2(wpost);
    f2 v[4];
    float s = 0.f;
#pragma unroll
    for (int j = 0; j < 4; ++j) {
      v[j] = f2{bf_lo(xr[j]), bf_hi(xr[j])};
      s += v[j].x + v[j].y;
    }
    s = wave_sum(s);
    const float meanf = s * (1.0f / D_);
    float q = 0.f;
#pragma unroll
    for (int j = 0; j < 4; ++j) {
      f2 d = v[j] - meanf;
      q += d.x * d.x + d.y * d.y;
    }
    q = wave_sum(q);
    const float mean = bfr(meanf);
    const float den = bfr(sqrtf(bfr(bfr(q * (1.0f / D_)) + 1e-5f)));
    const float rden = 1.0f / den;
    sm = bfr2(sm);
    if (tid >= 256) sm = bfr2(sm + 1.0f);
    *(f2*)(mods + tid * 2) = sm;
    if (tid < 6) {
      fin = bfr2(fin);
      ST64(__builtin_bit_cast(u2v, fin), OFF_MODF + idx * 4);
      const int vec = (idx >> 9) - 2, d = idx & 511;
      if (vec >= 0 && vec < 3) {
        if (vec == 2) fin += 1.0f;
        ST32(pack_bf(fin.x, fin.y), OFF_MODC + ((vec * 64 + ((d >> 1) & 63)) * 4 + (d >> 7)) * 4);
      }
    }
    STAMP(1, 3);
    __syncthreads();
    STAMP(1, 4);
    pf(4, 4);
#pragma unroll
    for (int j = 0; j < 4; ++j) {
      const int d = 2 * (lane + 64 * j);
      f2 sa = *(const f2*)(mods + d);
      f2 ca1 = *(const f2*)(mods + 512 + d);
      v[j] = bfr2(__builtin_elementwise_fma(v[j] - mean, ca1 * rden, sa));
    }
    fft256_fwd(v, lane, tw, (f2*)(dsm + L_PX + w * 2048), NoHook{});
    {
      f2* buf = zs + w * ZRS + lane + (lane >> 4);
#pragma unroll
      for (int r4 = 0; r4 < 4; ++r4) buf[68 * r4] = v[r4];
    }
    STAMP(1, 5);
    __syncthreads();
    {
      const int k = kk;
      const int k2 = (k == 0) ? 128 : 256 - k;
      const int pa = zpos(k), pb = zpos(k2);
      f4 oa, ob;
#pragma unroll
      for (int h = 0; h < 2; ++h) {
        const f2* zr = zs + (2 * rp + h) * ZRS;
        f2 za = zr[pa], zb = zr[pb];
        f2 E = f2{za.x + zb.x, za.y - zb.y};
        f2 Dm = f2{za.x - zb.x, za.y + zb.y};
        f2 O = f2{Dm.y, -Dm.x};
        f2 wo = cmul1<false>(O, wpost);
        f2 ra = E + wo;
        f2 t = E - wo;
        f2 rb = f2{t.x, -t.y};
        if (k == 0) {
          ra = f2{2.f * (za.x + za.y), 2.f * (za.x - za.y)};
          rb = f2{2.f * zb.x, -2.f * zb.y};
        }
        oa[2 * h] = ra.x; oa[2 * h + 1] = ra.y;
        ob[2 * h] = rb.x; ob[2 * h + 1] = rb.y;
      }
      ST128(__builtin_bit_cast(u4v, oa), OFF_XT + (k * CP + r0 + 2 * rp) * 8);
      ST128(__builtin_bit_cast(u4v, ob), OFF_XT + (k2 * CP + r0 + 2 * rp) * 8);
    }
    STAMP(1, 6);
    DRAIN();
    __syncthreads();
    if (tid < NREP)
      __hip_atomic_store((gu32*)(ws + OFF_FLR + (tid * 256 + bid) * FSP), 1u, __ATOMIC_RELAXED, __HIP_MEMORY_SCOPE_AGENT);
    STAMP(1, 7);
  }
  {
    f2* T = (f2*)(dsm + L_T);
    f2* T2 = (f2*)(dsm + L_T + 16384);
    f2* T3 = (f2*)(dsm + L_T + 32768);
    const int k = bid;
    f2 tw1[4];
#pragma unroll
    for (int j = 0; j < 4; ++j) {
      tw1[j] = twid((float)((lane + 64 * j) * w) * (1.0f / 2048.0f));
      PIN2(tw1[j]);
    }
    if (w == 0) wait_cols((gu32*)(ws + OFF_FLR + ((bid % NREP) * 256 + lane) * FSP));
    asm volatile("s_barrier" ::: "memory");
    __builtin_amdgcn_fence(__ATOMIC_ACQUIRE, "wavefront");
    STAMP(2, 0);
    f2 v[4];
#pragma unroll
    for (int j = 0; j < 4; ++j) v[j] = __builtin_bit_cast(f2, LD64(OFF_XT + (k * CP + tid + 512 * j) * 8));
    u4v bv;
    unsigned wn[4], bn[4];
    int mirr[4];
#pragma unroll
    for (int j = 0; j < 4; ++j) PIN2(v[j]);
    pf(8, 4);
    const int hb = w >> 2, a = tid & 255;
    constexpr float R = 0.70710678118654752440f;
    dft4<-1>(v[0], v[1], v[2], v[3]);
    if (hb) {
      v[1] = f2{v[1].x + v[1].y, v[1].y - v[1].x} * R;
      v[2] = f2{v[2].y, -v[2].x};
      v[3] = f2{v[3].y - v[3].x, -v[3].x - v[3].y} * R;
    }
#pragma unroll
    for (int q = 0; q < 4; ++q) T[(hb * 4 + q) * 256 + a] = v[q];
    STAMP(2, 1);
    const u4v wv = LD128(wt_off(k, tid));
    if (k == 0) {
      bv = LD128(wt_off(256, tid));
      const int m0 = (lane >> 4) + 4 * ((lane >> 2) & 3) + 16 * (lane & 3);
#pragma unroll
      for (int r4 = 0; r4 < 4; ++r4) {
        const int un = (S_ - (w + 8 * (m0 + 64 * r4))) & (S_ - 1);
        const int mm = un >> 3;
        mirr[r4] = ((un & 7) * 64 + 16 * (mm & 3) + 4 * ((mm >> 2) & 3) + ((mm >> 4) & 3)) * 4 + (mm >> 6);
      }
      if (w != 0) {
        const u4v a4 = LD128(wt_off(0, 64 * (8 - w) + 63 - lane));
        const u4v b4 = LD128(wt_off(256, 64 * (8 - w) + 63 - lane));
#pragma unroll
        for (int r4 = 0; r4 < 4; ++r4) { wn[r4] = a4[3 - r4]; bn[r4] = b4[3 - r4]; }
      } else {
#pragma unroll
        for (int r4 = 0; r4 < 4; ++r4) {
          wn[r4] = LD32(wt_off(0, mirr[r4] >> 2) + (mirr[r4] & 3) * 4);
          bn[r4] = LD32(wt_off(256, mirr[r4] >> 2) + (mirr[r4] & 3) * 4);
        }
      }
    }
    __syncthreads();
    {
      const int q = w & 3;
      const float sg = (w < 4) ? 1.f : -1.f;
#pragma unroll
      for (int j = 0; j < 4; ++j) {
        f2 e = T[q * 256 + lane + 64 * j], o = T[(4 + q) * 256 + lane + 64 * j];
        v[j] = cmul1<false>(e + o * sg, tw1[j]);
      }
    }
    fft256_fwd(v, lane, tw, T2 + w * 256, [&](int i) __attribute__((always_inline)) {
      SCHED();
      if (i < 2) {
#pragma unroll
        for (int q = 0; q < 4; ++q) pf_put(4 * i + q);
      } else {
#pragma unroll
        for (int q = 12; q < 16; ++q) pf_load(q);
      }
      SCHED();
    });
    STAMP(2, 2);
    if (k != 0) {
#pragma unroll
      for (int i = 0; i < 4; ++i) {
        v[i] = cmul1<false>(v[i], f2{bf_lo(wv[i]), bf_hi(wv[i])});
      }
    } else {
#pragma unroll
      for (int r4 = 0; r4 < 4; ++r4) T3[r4 * 512 + tid] = v[r4];
      __syncthreads();
#pragma unroll
      for (int r4 = 0; r4 < 4; ++r4) {
        f2 cm = T3[(mirr[r4] & 3) * 512 + (mirr[r4] >> 2)];
        f2 h0 = f2{bf_lo(wv[r4]) + bf_lo(wn[r4]), bf_hi(wv[r4]) - bf_hi(wn[r4])};
        f2 h1 = f2{bf_lo(bv[r4]) + bf_lo(bn[r4]), bf_hi(bv[r4]) - bf_hi(bn[r4])};
        f2 wp = (h0 + h1) * 0.25f, wm = (h0 - h1) * 0.25f;
        f2 cc = f2{cm.x, -cm.y};
        v[r4] = cmul(v[r4], wp) + cmul(cc, wm);
      }
    }
    fft256_inv(v, lane, tw, T2 + w * 256, [&](int i) __attribute__((always_inline)) {
      SCHED();
      if (i == 0) {
#pragma unroll
        for (int q = 8; q < 12; ++q) pf_put(q);
      }
      if (i == 1) {
#pragma unroll
        for (int q = 16; q < 20; ++q) pf_load(q);
      }
      SCHED();
    });
#pragma unroll
    for (int j = 0; j < 4; ++j) T2[w * 256 + lane + 64 * j] = cmul1<true>(v[j], tw1[j]);
#pragma unroll
    for (int q = 12; q < 16; ++q) pf_put(q);
    STAMP(2, 3);
    __syncthreads();
    {
      const float sg = hb ? -1.f : 1.f;
#pragma unroll
      for (int q = 0; q < 4; ++q) v[q] = T2[q * 256 + a] + T2[(q + 4) * 256 + a] * sg;
      if (hb) {
        v[1] = f2{v[1].x - v[1].y, v[1].y + v[1].x} * R;
        v[2] = f2{-v[2].y, v[2].x};
        v[3] = f2{-v[3].x - v[3].y, v[3].x - v[3].y} * R;
      }
    }
    dft4<1>(v[0], v[1], v[2], v[3]);
#pragma unroll
    for (int j = 0; j < 4; ++j) ST64(__builtin_bit_cast(u2v, v[j]), OFF_ZT + (k * CP + tid + 512 * j) * 8);
    STAMP(2, 4);
    DRAIN();
    __syncthreads();
    if (tid < NREP)
      __hip_atomic_store((gu32*)(ws + OFF_FLG + (tid * 256 + k) * FSP), 1u, __ATOMIC_RELAXED, __HIP_MEMORY_SCOPE_AGENT);
  }
  STAMP(2, 5);
  {
    const char* lw = dsm + L_W1 + (lane >> 2) * 224 + (lane & 3) * 8 + w * (2 * 7168);
    u4v fr[14];
#pragma unroll
    for (int i = 0; i < 14; ++i) fr[i] = w1frag(lw, i % 7, i / 7);
    asm volatile("" ::: "memory");
#pragma unroll
    for (int i = 0; i < 14; ++i) *(u4v*)(dsm + L_W1 + w * (2 * 7168) + i * 1024 + lane * 16) = fr[i];
  }
  STAMP(2, 6);
  {
    f2* zs = (f2*)(dsm + L_ZS);
    const int rp = tid & 3, k = tid >> 2;
    const int k2 = (k == 0) ? 128 : 256 - k;
    u4v xr;
    const unsigned* xrow = (const unsigned*)(x + (size_t)(row0 + w) * D_);
#pragma unroll
    for (int j = 0; j < 4; ++j) xr[j] = xrow[lane + 64 * j];
    const u4v gav = LD128(OFF_MODC + lane * 16);
    const u4v sbv = LD128(OFF_MODC + 1024 + lane * 16);
    const u4v cbv = LD128(OFF_MODC + 2048 + lane * 16);
    f2 wq = twid((float)k * (1.0f / 512.0f));
    PIN2(wq);
    const int pa = zpos(k), pb = zpos(k2);
    if (w == 0) wait_cols((gu32*)(ws + OFF_FLG + ((bid % NREP) * 256 + lane) * FSP));
    asm volatile("s_barrier" ::: "memory");
    __builtin_amdgcn_fence(__ATOMIC_ACQUIRE, "wavefront");
    STAMP(3, 0);
    const f4 ya = __builtin_bit_cast(f4, LD128(OFF_ZT + (k * CP + row0 + 2 * rp) * 8));
    const f4 yb = __builtin_bit_cast(f4, LD128(OFF_ZT + (k2 * CP + row0 + 2 * rp) * 8));
#pragma unroll
    for (int h = 0; h < 2; ++h) {
      f2 a = f2{ya[2 * h], ya[2 * h + 1]}, b = f2{yb[2 * h], yb[2 * h + 1]};
      f2* zr = zs + (2 * rp + h) * ZRS;
      f2 Ze = f2{a.x + b.x, a.y - b.y};
      f2 Dm = f2{a.x - b.x, a.y + b.y};
      f2 Zo = cmul1<true>(Dm, wq);
      f2 za = f2{Ze.x - Zo.y, Ze.y + Zo.x};
      f2 zb = f2{Ze.x + Zo.y, -Ze.y + Zo.x};
      if (k == 0) {
        za = f2{a.x + a.y, a.x - a.y};
        zb = f2{2.f * b.x, -2.f * b.y};
      }
      zr[pa] = za;
      zr[pb] = zb;
    }
    STAMP(3, 1);
    pf(20, 4);
    __syncthreads();
    f2 v[4];
    {
      const f2* buf = zs + w * ZRS + lane + (lane >> 4);
#pragma unroll
      for (int r4 = 0; r4 < 4; ++r4) v[r4] = buf[68 * r4];
    }
    fft256_inv(v, lane, tw, (f2*)(dsm + L_PX + w * 2048), NoHook{});
    pf(24, 4);
    STAMP(3, 2);
    float s = 0.f;
    u4v x1p;
#pragma unroll
    for (int j = 0; j < 4; ++j) {
      f2 t = __builtin_elementwise_fma(f2{bf_lo(gav[j]), bf_hi(gav[j])} * FSC, v[j], f2{bf_lo(xr[j]), bf_hi(xr[j])});
      x1p[j] = pack_bf(t.x, t.y);
      v[j] = f2{bf_lo(x1p[j]), bf_hi(x1p[j])};
      s += v[j].x + v[j].y;
    }
    ST128(x1p, OFF_X1 + ((row0 + w) * 64 + lane) * 16);
    s = wave_sum(s);
    const float meanf = s * (1.0f / D_);
    float q = 0.f;
#pragma unroll
    for (int j = 0; j < 4; ++j) {
      f2 d = v[j] - meanf;
      q += d.x * d.x + d.y * d.y;
    }
    q = wave_sum(q);
    const float mean = bfr(meanf);
    const float den = bfr(sqrtf(bfr(bfr(q * (1.0f / D_)) + 1e-5f)));
    const float rden = 1.0f / den;
    u4v hp;
#pragma unroll
    for (int j = 0; j < 4; ++j) {
      f2 t = __builtin_elementwise_fma(v[j] - mean, f2{bf_lo(cbv[j]), bf_hi(cbv[j])} * rden,
                                       f2{bf_lo(sbv[j]), bf_hi(sbv[j])});
      hp[j] = pack_bf(t.x, t.y);
    }
    *(u4v*)(dsm + L_ST + (lane * 9 + w) * 16) = hp;
    __syncthreads();
    {
      const int ch = tid >> 3, r = tid & 7;
      u4v o = *(const u4v*)(dsm + L_ST + (ch * 9 + r) * 16);
      const int mt = row0 >> 4, r16 = (row0 & 8) + r;
      ST128(o, OFF_H2F + ((mt * 16 + (ch >> 2)) * 64 + (ch & 3) * 16 + r16) * 16);
    }
  }
  STAMP(3, 3);
  DRAIN();
  __syncthreads();
  if (tid == 0) __hip_atomic_store(myA, 1u, __ATOMIC_RELAXED, __HIP_MEMORY_SCOPE_AGENT);
  {
    const int ch = tid >> 3, r = tid & 7;
    u4v t = LD128(OFF_H2F + (((row0 >> 4) * 16 + (ch >> 2)) * 64 + (ch & 3) * 16 + (row0 & 8) + r) * 16);
    asm volatile("" ::"v"(t));
  }
  STAMP(3, 4);
  wait_pair(depA);
  STAMP(3, 5);
  {
    u4v B[16];
#pragma unroll
    for (int ks = 0; ks < 16; ++ks) B[ks] = LD128(OFF_H2F + (((8 * p + w) * 16 + ks) * 64 + lane) * 16);
    f4 acc[7];
#pragma unroll
    for (int ft = 0; ft < 7; ++ft) acc[ft] = f4{0.f, 0.f, 0.f, 0.f};
    const char* lw = dsm + L_W1 + lane * 16;
    unsigned off2 = L_W1 + lane * 16 + 8 * 7168;
    asm volatile("" : "+v"(off2));
    const char* lw2 = dsm + off2;
    const int e = c >> 1, odd = c & 1;
    const int hb = (int)OFF_HF + (((8 * p + w) * 56 + 7 * e) * 64 + lane) * 16;
    u4v Af[2][7];
#pragma unroll
    for (int j = 0; j < 7; ++j) Af[0][j] = *(const u4v*)(lw + (g1_ks(0, j) * 7 + g1_ft(0, j)) * 1024);
#pragma unroll
    for (int g = 0; g < 17; ++g) {
#pragma unroll
      for (int j = 0; j < 7; ++j) {
        if (j < g1_n(g)) acc[g1_ft(g, j)] = mfma16(Af[g & 1][j], B[g1_ks(g, j)], acc[g1_ft(g, j)]);
        if (g + 1 < 17 && j < g1_n(g + 1))
          Af[(g + 1) & 1][j] = g1_ks(g + 1, j) < 8 ? *(const u4v*)(lw + (g1_ks(g + 1, j) * 7 + g1_ft(g + 1, j)) * 1024)
                                                   : *(const u4v*)(lw2 + ((g1_ks(g + 1, j) - 8) * 7 + g1_ft(g + 1, j)) * 1024);
        SCHED();
      }
      if (g >= 4 && g < 12) { pf_load(20 + 2 * g); pf_load(21 + 2 * g); }
      if (g >= 14) {
        const int i = g - 14;
        f4 a = acc[2 * i], b = acc[2 * i + 1];
        u4v o = u4v{silu_pk(a[0], a[1]), silu_pk(a[2], a[3]), silu_pk(b[0], b[1]), silu_pk(b[2], b[3])};
        ST128(o, hb + (odd * 3 + i) * 1024);
      }
      SCHED();
      if (g == 12) STAMP(3, 6);
    }
    f4 a = acc[6];
    u2v o = u2v{silu_pk(a[0], a[1]), silu_pk(a[2], a[3])};
    ST64(o, hb + 6 * 1024 + odd * 8);
  }
  STAMP(3, 7);
  DRAIN();
  __syncthreads();
  if (tid == 0) __hip_atomic_store(myB, 1u, __ATOMIC_RELAXED, __HIP_MEMORY_SCOPE_AGENT);
  {
    const int hb = (int)OFF_HF + (((8 * p + w) * 56 + 7 * (c >> 1) + 3 * (c & 1)) * 64 + lane) * 16;
    u4v t0 = LD128(hb), t1 = LD128(hb + 1024), t2 = LD128(hb + 2048);
    asm volatile("" ::"v"(t0), "v"(t1), "v"(t2));
  }
  wait_pair(depB);
  STAMP(3, 8);
  {
    const int Bb = (int)OFF_HF + ((mt0 * 56 + 7 * w) * 64 + lane) * 16;
    u4v B[7][4];
#pragma unroll
    for (int ks = 0; ks < 4; ++ks)
#pragma unroll
      for (int mt = 0; mt < 4; ++mt) B[ks][mt] = LD128(Bb + (mt * 56 + ks) * 1024);
    f4 acc[4][4];
#pragma unroll
    for (int i = 0; i < 4; ++i)
#pragma unroll
      for (int j = 0; j < 4; ++j) acc[i][j] = f4{0.f, 0.f, 0.f, 0.f};
#pragma unroll
    for (int ks = 0; ks < 7; ++ks) {
#pragma unroll
      for (int nt = 0; nt < 4; ++nt)
#pragma unroll
        for (int mt = 0; mt < 4; ++mt) acc[nt][mt] = mfma16(A2[ks * 4 + nt], B[ks][mt], acc[nt][mt]);
      if (ks + 4 < 7) {
#pragma unroll
        for (int mt = 0; mt < 4; ++mt) B[ks + 4][mt] = LD128(Bb + (mt * 56 + ks + 4) * 1024);
      }
      SCHED();
    }
    STAMP(3, 9);
    const int g = lane >> 4, mr = lane & 15;
    const int fm = w >> 1, P = w & 1;
    const int m = (mt0 + fm) * 16 + mr;
    const int n = nt0 * 16 + 32 * P + 8 * g;
    unsigned xv[4];
#pragma unroll
    for (int i = 0; i < 4; ++i)
      xv[i] = __builtin_amdgcn_raw_buffer_load_b32(
          rs, (int)OFF_X1 + (m * 64 + (((n >> 1) + i) & 63)) * 16 + (n >> 7) * 4, 0, AUX_SC1);
    const f4 g0 = __builtin_bit_cast(f4, LD128(OFF_MODF + (2560 + n) * 4));
    const f4 g1 = __builtin_bit_cast(f4, LD128(OFF_MODF + (2560 + n + 4) * 4));
    f4* red = (f4*)dsm;
#pragma unroll
    for (int nt = 0; nt < 4; ++nt)
#pragma unroll
      for (int mt = 0; mt < 4; ++mt) red[(w * 16 + nt * 4 + mt) * 64 + lane] = acc[nt][mt];
    __syncthreads();
    STAMP(3, 10);
    f4 s0 = f4{0.f, 0.f, 0.f, 0.f}, s1 = s0;
#pragma unroll
    for (int ww = 0; ww < 8; ++ww) {
      s0 += red[(ww * 16 + (2 * P) * 4 + fm) * 64 + lane];
      s1 += red[(ww * 16 + (2 * P + 1) * 4 + fm) * 64 + lane];
    }
    u4v o;
    {
      f2 t0 = bfr2(f2{g0[0], g0[1]} * bfr2(f2{s0[0], s0[1]})), t1 = bfr2(f2{g0[2], g0[3]} * bfr2(f2{s0[2], s0[3]}));
      f2 t2 = bfr2(f2{g1[0], g1[1]} * bfr2(f2{s1[0], s1[1]})), t3 = bfr2(f2{g1[2], g1[3]} * bfr2(f2{s1[2], s1[3]}));
      o[0] = pack_bf(bf_lo(xv[0]) + t0.x, bf_hi(xv[0]) + t0.y);
      o[1] = pack_bf(bf_lo(xv[1]) + t1.x, bf_hi(xv[1]) + t1.y);
      o[2] = pack_bf(bf_lo(xv[2]) + t2.x, bf_hi(xv[2]) + t2.y);
      o[3] = pack_bf(bf_lo(xv[3]) + t3.x, bf_hi(xv[3]) + t3.y);
    }
    const auto ro = __builtin_amdgcn_make_buffer_rsrc(out, 0, S_ * D_ * 2, 0x00020000);
    __builtin_amdgcn_raw_buffer_store_b128(o, ro, (m * D_ + n) * 2, 0, AUX_SC1);
  }
  STAMP(3, 11);
}

extern "C" void kernel_launch(void* const* d_in, const int* in_sizes, int n_in, void* d_out, int out_size, void* d_ws,
                              size_t ws_size, hipStream_t stream) {
  const u16* x = (const u16*)d_in[0];
  const u16* wr = (const u16*)d_in[1];
  const u16* wi = (const u16*)d_in[2];
  const u16* w1 = (const u16*)d_in[4];
  const u16* w2 = (const u16*)d_in[5];
  const u16* c = (const u16*)d_in[6];
  const u16* wmod = (const u16*)d_in[7];
  char* ws = (char*)d_ws;
  u16* out = (u16*)d_out;
  (void)hipFuncSetAttribute((const void*)kmega, hipFuncAttributeMaxDynamicSharedMemorySize, L_TOTAL);
  k0_mod<<<K0_GEMV + K0_WT + K0_W2 + K0_Z + K0_WARM, 512, 0, stream>>>(c, wmod, x, wr, wi, w1, w2, ws);
  kmega<<<256, 512, L_TOTAL, stream>>>(x, w1, w2, ws, out);
}
```

```cpp
#include <hip/hip_runtime.h>
#include <hip/hip_bf16.h>
#include <stdint.h>

typedef float f2 __attribute__((ext_vector_type(2)));
typedef float f4 __attribute__((ext_vector_type(4)));
typedef unsigned u2v __attribute__((ext_vector_type(2)));
typedef unsigned u4v __attribute__((ext_vector_type(4)));
typedef short bf16x8 __attribute__((ext_vector_type(8)));
typedef unsigned short u16;

constexpr int S_ = 2048, D_ = 512, F_ = 1792, DF_ = 257;
constexpr int NSPLIT = 4;

constexpr size_t OFF_CNT = 0;
#define FSPACE 16
#define FREP 16
constexpr int FSP = FSPACE, NREP = FREP;
constexpr size_t FTAB = (size_t)NREP * 256 * FSP;
constexpr size_t OFF_FLG = 16384;
constexpr size_t OFF_FLR = OFF_FLG + FTAB;
constexpr size_t OFF_FLS = OFF_FLR + FTAB;
constexpr size_t FLAG_BYTES = OFF_FLS + 16384;
constexpr size_t OFF_MODP = 900 << 10;
constexpr size_t OFF_MODF = OFF_MODP + (size_t)NSPLIT * 3072 * 4;
constexpr size_t OFF_MODC = OFF_MODF + 3072 * 4;
constexpr size_t OFF_WT = 1 << 20;
constexpr size_t OFF_XT = 4 << 20;
constexpr size_t OFF_ZT = 28 << 20;
constexpr size_t OFF_H2F = 12 << 20;
constexpr size_t OFF_X1 = 14 << 20;
constexpr size_t OFF_W2F = 18 << 20;
constexpr size_t OFF_HF = 20 << 20;
constexpr int WS_SPAN = 36 << 20;
#define CPITCH (2048 + 32)
constexpr int CP = CPITCH;
constexpr int WT_PITCH = 520;
constexpr float FSC = 1.0f / (2.0f * 2048.0f * 512.0f);

#define STAMP(kid, i)

__device__ __forceinline__ float bf_lo(unsigned u) { return __builtin_bit_cast(float, u << 16); }
__device__ __forceinline__ float bf_hi(unsigned u) { return __builtin_bit_cast(float, u & 0xffff0000u); }
__device__ __forceinline__ float bfr(float x) {
  return (float)(__bf16)x;
}
__device__ __forceinline__ unsigned pack_bf(float lo, float hi) {
  typedef __bf16 b2 __attribute__((ext_vector_type(2)));
  return __builtin_bit_cast(unsigned, __builtin_convertvector(f2{lo, hi}, b2));
}
__device__ __forceinline__ f2 cmul(f2 a, f2 b) {
  f2 t = a * f2{b.x, b.x};
  return __builtin_elementwise_fma(f2{a.y, a.x}, f2{-b.y, b.y}, t);
}
__device__ __forceinline__ f2 cmulc(f2 a, f2 b) {
  f2 t = a * f2{b.x, b.x};
  return __builtin_elementwise_fma(f2{a.y, a.x}, f2{b.y, -b.y}, t);
}
__device__ __forceinline__ f2 bfr2(f2 a) {
  unsigned u = pack_bf(a.x, a.y);
  asm("" : "+v"(u));
  return f2{bf_lo(u), bf_hi(u)};
}
__device__ __forceinline__ float msel(unsigned m, float a, float b) {
  unsigned ua = __builtin_bit_cast(unsigned, a), ub = __builtin_bit_cast(unsigned, b);
  return __builtin_bit_cast(float, (m & ua) | (~m & ub));
}
#define PIN2(x) asm volatile("" : "+v"(x))
constexpr int AUX_SC1 = 16;
#define WS_RSRC(ws) __builtin_amdgcn_make_buffer_rsrc((ws), 0, WS_SPAN, 0x00020000)
#define ST128(v, off) __builtin_amdgcn_raw_buffer_store_b128((v), rs, (int)(off), 0, AUX_SC1)
#define ST64(v, off) __builtin_amdgcn_raw_buffer_store_b64((v), rs, (int)(off), 0, AUX_SC1)
#define ST32(v, off) __builtin_amdgcn_raw_buffer_store_b32((v), rs, (int)(off), 0, AUX_SC1)
#define LD64(off) __builtin_amdgcn_raw_buffer_load_b64(rs, (int)(off), 0, AUX_SC1)
#define LD32(off) __builtin_amdgcn_raw_buffer_load_b32(rs, (int)(off), 0, AUX_SC1)
#define LD128(off) __builtin_amdgcn_raw_buffer_load_b128(rs, (int)(off), 0, AUX_SC1)
__device__ __forceinline__ f2 twid(float rev) { return f2{__builtin_amdgcn_cosf(rev), -__builtin_amdgcn_sinf(rev)}; }

template <int DIR>
__device__ __forceinline__ f2 rot90(f2 a) {
  return DIR < 0 ? f2{a.y, -a.x} : f2{-a.y, a.x};
}
template <int DIR>
__device__ __forceinline__ void dft4(f2& c0, f2& c1, f2& c2, f2& c3) {
  f2 d0 = c0 + c2, d2 = c0 - c2, d1 = c1 + c3, e = c1 - c3;
  c0 = d0 + d1;
  c2 = d0 - d1;
  const f2 es = f2{e.y, e.x};
  c1 = __builtin_elementwise_fma(es, DIR < 0 ? f2{1.f, -1.f} : f2{-1.f, 1.f}, d2);
  c3 = __builtin_elementwise_fma(es, DIR < 0 ? f2{-1.f, 1.f} : f2{1.f, -1.f}, d2);
}

__device__ __forceinline__ void sw32(float& a, float& b) {
  auto r = __builtin_amdgcn_permlane32_swap(__builtin_bit_cast(unsigned, a), __builtin_bit_cast(unsigned, b), false, false);
  a = __builtin_bit_cast(float, (unsigned)r[0]);
  b = __builtin_bit_cast(float, (unsigned)r[1]);
}
__device__ __forceinline__ void sw16(float& a, float& b) {
  auto r = __builtin_amdgcn_permlane16_swap(__builtin_bit_cast(unsigned, a), __builtin_bit_cast(unsigned, b), false, false);
  a = __builtin_bit_cast(float, (unsigned)r[0]);
  b = __builtin_bit_cast(float, (unsigned)r[1]);
}
template <int CTRL, int BANK>
__device__ __forceinline__ float udpp(float old, float src) {
  return __builtin_bit_cast(float, __builtin_amdgcn_update_dpp(__builtin_bit_cast(int, old), __builtin_bit_cast(int, src),
                                                               CTRL, 0xf, BANK, false));
}
template <int CTRL>
__device__ __forceinline__ float mdpp(float src) {
  return __builtin_bit_cast(float, __builtin_amdgcn_mov_dpp(__builtin_bit_cast(int, src), CTRL, 0xf, 0xf, false));
}
__device__ __forceinline__ void xp_hi1(float& v0, float& v1, float& v2, float& v3) {
  sw32(v0, v2); sw32(v1, v3); sw16(v0, v1); sw16(v2, v3);
}
__device__ __forceinline__ void xp_mid1(float& v0, float& v1, float& v2, float& v3) {
  float a0 = udpp<0x128, 0xC>(v0, v2), a2 = udpp<0x128, 0x3>(v2, v0);
  float a1 = udpp<0x128, 0xC>(v1, v3), a3 = udpp<0x128, 0x3>(v3, v1);
  v0 = udpp<0x124, 0xA>(a0, a1); v1 = udpp<0x12C, 0x5>(a1, a0);
  v2 = udpp<0x124, 0xA>(a2, a3); v3 = udpp<0x12C, 0x5>(a3, a2);
}
__device__ __forceinline__ void xp_lo1(float& v0, float& v1, float& v2, float& v3, unsigned b1, unsigned b0) {
  float s0 = mdpp<0x4E>(msel(b1, v0, v2)), s1 = mdpp<0x4E>(msel(b1, v1, v3));
  float a0 = msel(b1, s0, v0), a2 = msel(b1, v2, s0), a1 = msel(b1, s1, v1), a3 = msel(b1, v3, s1);
  float t0 = mdpp<0xB1>(msel(b0, a0, a1)), t2 = mdpp<0xB1>(msel(b0, a2, a3));
  v0 = msel(b0, t0, a0); v1 = msel(b0, a1, t0); v2 = msel(b0, t2, a2); v3 = msel(b0, a3, t2);
}
#define XP_CPLX(NAME, FN, ...)                                        \
  {                                                                   \
    float x0 = v[0].x, x1 = v[1].x, x2 = v[2].x, x3 = v[3].x;         \
    float y0 = v[0].y, y1 = v[1].y, y2 = v[2].y, y3 = v[3].y;         \
    FN(x0, x1, x2, x3, ##__VA_ARGS__);                                \
    FN(y0, y1, y2, y3, ##__VA_ARGS__);                                \
    v[0] = f2{x0, y0}; v[1] = f2{x1, y1}; v[2] = f2{x2, y2}; v[3] = f2{x3, y3}; \
  }
__device__ __forceinline__ void xp_hi(f2 (&v)[4]) XP_CPLX(hi, xp_hi1)
__device__ __forceinline__ void xp_mid(f2 (&v)[4]) XP_CPLX(mid, xp_mid1)
__device__ __forceinline__ void xp_lo(f2 (&v)[4], unsigned b1, unsigned b0) XP_CPLX(lo, xp_lo1, b1, b0)

template <bool CONJ>
__device__ __forceinline__ void cmul3(f2 (&v)[4], const f2 (&t)[3]) {
  f2 a, b, c;
  if (!CONJ)
    asm("v_pk_mul_f32 %3, %0, %6 op_sel_hi:[1,0]\n\t"
        "v_pk_mul_f32 %4, %1, %7 op_sel_hi:[1,0]\n\t"
        "v_pk_mul_f32 %5, %2, %8 op_sel_hi:[1,0]\n\t"
        "v_pk_fma_f32 %0, %0, %6, %3 op_sel:[1,1,0] op_sel_hi:[0,1,1] neg_lo:[0,1,0]\n\t"
        "v_pk_fma_f32 %1, %1, %7, %4 op_sel:[1,1,0] op_sel_hi:[0,1,1] neg_lo:[0,1,0]\n\t"
        "v_pk_fma_f32 %2, %2, %8, %5 op_sel:[1,1,0] op_sel_hi:[0,1,1] neg_lo:[0,1,0]\n\t"
        "s_nop 1"
        : "+v"(v[1]), "+v"(v[2]), "+v"(v[3]), "=&v"(a), "=&v"(b), "=&v"(c)
        : "v"(t[0]), "v"(t[1]), "v"(t[2]));
  else
    asm("v_pk_mul_f32 %3, %0, %6 op_sel_hi:[1,0]\n\t"
        "v_pk_mul_f32 %4, %1, %7 op_sel_hi:[1,0]\n\t"
        "v_pk_mul_f32 %5, %2, %8 op_sel_hi:[1,0]\n\t"
        "v_pk_fma_f32 %0, %0, %6, %3 op_sel:[1,1,0] op_sel_hi:[0,1,1] neg_hi:[0,1,0]\n\t"
        "v_pk_fma_f32 %1, %1, %7, %4 op_sel:[1,1,0] op_sel_hi:[0,1,1] neg_hi:[0,1,0]\n\t"
        "v_pk_fma_f32 %2, %2, %8, %5 op_sel:[1,1,0] op_sel_hi:[0,1,1] neg_hi:[0,1,0]"
        : "+v"(v[1]), "+v"(v[2]), "+v"(v[3]), "=&v"(a), "=&v"(b), "=&v"(c)
        : "v"(t[0]), "v"(t[1]), "v"(t[2]));
}
template <bool CONJ>
__device__ __forceinline__ f2 cmul1(f2 v, f2 t) {
  f2 a;
  if (!CONJ)
    asm("v_pk_mul_f32 %1, %0, %2 op_sel_hi:[1,0]\n\t"
        "v_pk_fma_f32 %0, %0, %2, %1 op_sel:[1,1,0] op_sel_hi:[0,1,1] neg_lo:[0,1,0]"
        : "+v"(v), "=&v"(a) : "v"(t));
  else
    asm("v_pk_mul_f32 %1, %0, %2 op_sel_hi:[1,0]\n\t"
        "v_pk_fma_f32 %0, %0, %2, %1 op_sel:[1,1,0] op_sel_hi:[0,1,1] neg_hi:[0,1,0]"
        : "+v"(v), "=&v"(a) : "v"(t));
  return v;
}
__device__ __forceinline__ void xp_lo_lds(f2 (&v)[4], f2* patch, int lane) {
  f2* q = patch + (lane >> 2) * 16;
  const int c = lane & 3, sw = (lane >> 2) & 3;
#pragma unroll
  for (int j = 0; j < 4; ++j) q[4 * (j ^ sw) + c] = v[j];
  __builtin_amdgcn_fence(__ATOMIC_RELEASE, "wavefront");
  __builtin_amdgcn_wave_barrier();
  __builtin_amdgcn_fence(__ATOMIC_ACQUIRE, "wavefront");
  const f4 a = *(const f4*)(q + 4 * (c ^ sw)), b = *(const f4*)(q + 4 * (c ^ sw) + 2);
  v[0] = f2{a[0], a[1]}; v[1] = f2{a[2], a[3]}; v[2] = f2{b[0], b[1]}; v[3] = f2{b[2], b[3]};
  __builtin_amdgcn_fence(__ATOMIC_RELEASE, "wavefront");
  __builtin_amdgcn_wave_barrier();
}

struct Tw256 {
  f2 a[3], b[3], c[3];
};
__device__ __forceinline__ void make_tw256(int lane, Tw256& t) {
#pragma unroll
  for (int r = 1; r < 4; ++r) {
    t.a[r - 1] = twid((float)(lane * r) * (1.0f / 256.0f));
    t.b[r - 1] = twid((float)((lane & 15) * r) * (1.0f / 64.0f));
    t.c[r - 1] = twid((float)((lane & 3) * r) * (1.0f / 16.0f));
  }
#pragma unroll
  for (int r = 0; r < 3; ++r) {
    PIN2(t.a[r]);
    PIN2(t.b[r]);
    PIN2(t.c[r]);
  }
}
struct NoHook {
  __device__ __forceinline__ void operator()(int) const {}
};
template <class H>
__device__ __forceinline__ void fft256_fwd(f2 (&v)[4], int lane, const Tw256& t, f2* patch, const H& hook) {
  dft4<-1>(v[0], v[1], v[2], v[3]);
  cmul3<false>(v, t.a);
  xp_hi(v);
  hook(0);
  dft4<-1>(v[0], v[1], v[2], v[3]);
  cmul3<false>(v, t.b);
  xp_mid(v);
  hook(1);
  dft4<-1>(v[0], v[1], v[2], v[3]);
  cmul3<false>(v, t.c);
  xp_lo_lds(v, patch, lane);
  hook(2);
  dft4<-1>(v[0], v[1], v[2], v[3]);
}
template <class H>
__device__ __forceinline__ void fft256_inv(f2 (&v)[4], int lane, const Tw256& t, f2* patch, const H& hook) {
  dft4<1>(v[0], v[1], v[2], v[3]);
  xp_lo_lds(v, patch, lane);
  hook(0);
  cmul3<true>(v, t.c);
  dft4<1>(v[0], v[1], v[2], v[3]);
  xp_mid(v);
  hook(1);
  cmul3<true>(v, t.b);
  dft4<1>(v[0], v[1], v[2], v[3]);
  xp_hi(v);
  hook(2);
  cmul3<true>(v, t.a);
  dft4<1>(v[0], v[1], v[2], v[3]);
}

__device__ __forceinline__ float wave_sum(float x) {
  x += mdpp<0xB1>(x);
  x += mdpp<0x4E>(x);
  x += mdpp<0x141>(x);
  x += mdpp<0x140>(x);
  float r0 = __builtin_bit_cast(float, __builtin_amdgcn_readlane(__builtin_bit_cast(int, x), 0));
  float r1 = __builtin_bit_cast(float, __builtin_amdgcn_readlane(__builtin_bit_cast(int, x), 16));
  float r2 = __builtin_bit_cast(float, __builtin_amdgcn_readlane(__builtin_bit_cast(int, x), 32));
  float r3 = __builtin_bit_cast(float, __builtin_amdgcn_readlane(__builtin_bit_cast(int, x), 48));
  return (r0 + r1) + (r2 + r3);
}

__device__ __forceinline__ void ln_stats(const f2 (&v)[4], float& mean, float& rden) {
  const float K = __builtin_bit_cast(float, __builtin_amdgcn_readfirstlane(__builtin_bit_cast(int, v[0].x)));
  f2 s2 = f2{0.f, 0.f}, q2 = f2{0.f, 0.f};
#pragma unroll
  for (int j = 0; j < 4; ++j) {
    const f2 d = v[j] - K;
    s2 += d;
    q2 = __builtin_elementwise_fma(d, d, q2);
  }
  const float sm = wave_sum(s2.x + s2.y) * (1.0f / D_), qm = wave_sum(q2.x + q2.y) * (1.0f / D_);
  const float var = fmaxf(qm - sm * sm, 0.f);
  mean = bfr(K + sm);
  rden = __builtin_amdgcn_rcpf(bfr(__builtin_amdgcn_sqrtf(bfr(bfr(var) + 1e-5f))));
}

__device__ __forceinline__ int zpos(int k) { return 17 * (k & 3) + 4 * ((k >> 2) & 3) + ((k >> 4) & 3) + 68 * (k >> 6); }
constexpr int ZRS = 276;

constexpr int K0_GEMV = 48 * NSPLIT, K0_WT = 128, K0_W2 = 112, K0_WARM = 0, K0_Z = (int)(FLAG_BYTES / 8192);
__global__ __launch_bounds__(512) void k0_mod(const u16* __restrict__ c, const u16* __restrict__ wmod,
                                              const u16* __restrict__ x, const u16* __restrict__ wr,
                                              const u16* __restrict__ wi, const u16* __restrict__ w1,
                                              const u16* __restrict__ w2, char* __restrict__ ws) {
  __shared__ __attribute__((aligned(16))) char smem[17408];
  const int tid = threadIdx.x, row = tid >> 6, lane = tid & 63;
  const auto rs = WS_RSRC(ws);
  int b = blockIdx.x;
  STAMP(0, 0);
  if (b >= K0_GEMV + K0_WT + K0_W2) {
    int q = b - (K0_GEMV + K0_WT + K0_W2);
    if (q < K0_Z) {
      ((u4v*)ws)[q * 512 + tid] = u4v{0u, 0u, 0u, 0u};
      return;
    }
    q -= K0_Z;
    const char* src = q < 64 ? (const char*)x + (size_t)q * 32768
                             : (q < 120 ? (const char*)w1 + (size_t)(q - 64) * 32768 : (const char*)w2 + (size_t)(q - 120) * 32768);
    u4v t[4];
#pragma unroll
    for (int i = 0; i < 4; ++i) t[i] = *(const u4v*)(src + i * 8192 + tid * 16);
#pragma unroll
    for (int i = 0; i < 4; ++i) asm volatile("" ::"v"(t[i]));
    STAMP(0, 1);
    return;
  }
  if (b >= K0_GEMV) {
    b -= K0_GEMV;
    if (b < K0_WT) {
      char* sr = smem;
      char* si = smem + 16 * 544;
      constexpr int LASTCH = (S_ * DF_ * 2) / 16 - 1;
      for (int it = tid; it < 16 * 34; it += 512) {
        const int i = it / 34, ch = it % 34;
        int gch = ((b + 128 * i) * (DF_ * 2)) / 16 + ch;
        gch = gch > LASTCH ? LASTCH : gch;
        *(u4v*)(sr + i * 544 + ch * 16) = *(const u4v*)((const char*)wr + (size_t)gch * 16);
        *(u4v*)(si + i * 544 + ch * 16) = *(const u4v*)((const char*)wi + (size_t)gch * 16);
      }
      __syncthreads();
      const int tbase = 64 * (b & 7) + 16 * ((b >> 3) & 3) + 4 * ((b >> 5) & 3);
      for (int it = tid; it < DF_ * 4; it += 512) {
        const int k = it >> 2, r3 = it & 3;
        u4v o;
#pragma unroll
        for (int r4 = 0; r4 < 4; ++r4) {
          const int i = r3 + 4 * r4;
          const int off = i * 544 + (((b + 128 * i) * (DF_ * 2)) & 15) + 2 * k;
          o[r4] = (unsigned)*(const u16*)(sr + off) | ((unsigned)*(const u16*)(si + off) << 16);
        }
        ST128(o, OFF_WT + (k * 512 + tbase + r3) * 16);
      }
    } else {
      b -= K0_WT;
      u16* t = (u16*)smem;
      const int g = lane >> 4, r = lane & 15;
      const int ng = b & 3, kp = b >> 2;
#pragma unroll
      for (int it = 0; it < 2; ++it) {
        const int idx = it * 512 + tid;
        const int rr = idx >> 4, ch = idx & 15;
        const int ks2 = kp * 2 + (rr >> 5), gg = (rr >> 3) & 3, j = rr & 7;
        const int e = ks2 / 7, i7 = ks2 % 7;
        int T0, T1;
        if (i7 < 3) { T0 = 14 * e + 2 * i7; T1 = T0 + 1; }
        else if (i7 < 6) { T0 = 14 * e + 7 + 2 * (i7 - 3); T1 = T0 + 1; }
        else { T0 = 14 * e + 6; T1 = 14 * e + 13; }
        const int f = (j < 4) ? (16 * T0 + 4 * gg + j) : (16 * T1 + 4 * gg + (j - 4));
        *(u4v*)(t + rr * 136 + ch * 8) = *(const u4v*)(w2 + (size_t)f * D_ + ng * 128 + ch * 8);
      }
      __syncthreads();
      const int nl = 32 * (row >> 1) + 8 * (r >> 2) + 4 * (row & 1) + (r & 3);
#pragma unroll
      for (int ksl = 0; ksl < 2; ++ksl) {
        u4v o;
#pragma unroll
        for (int jj = 0; jj < 4; ++jj)
          o[jj] = (unsigned)t[(ksl * 32 + g * 8 + 2 * jj) * 136 + nl] |
                  ((unsigned)t[(ksl * 32 + g * 8 + 2 * jj + 1) * 136 + nl] << 16);
        const int nt = ng * 8 + row;
        ST128(o, OFF_W2F + ((nt * 56 + kp * 2 + ksl) * 64 + lane) * 16);
      }
    }
    STAMP(0, 1);
    return;
  }
  float* red = (float*)smem;
  const int cgp = b % 48, ks = b / 48;
  const int cg = tid & 7, r = tid >> 3;
  const int rw = ks * 128 + r;
  const u4v wm0 = *(const u4v*)(wmod + (size_t)rw * 3072 + cgp * 64 + cg * 8);
  const u4v wm1 = *(const u4v*)(wmod + (size_t)(rw + 64) * 3072 + cgp * 64 + cg * 8);
  const float cv0 = bf_lo(c[rw]), cv1 = bf_lo(c[rw + 64]);
  const float s0 = bfr(cv0 / (1.0f + __expf(-cv0))), s1 = bfr(cv1 / (1.0f + __expf(-cv1)));
  float acc[8];
#pragma unroll
  for (int i = 0; i < 4; ++i) {
    acc[2 * i] = s0 * bf_lo(wm0[i]) + s1 * bf_lo(wm1[i]);
    acc[2 * i + 1] = s0 * bf_hi(wm0[i]) + s1 * bf_hi(wm1[i]);
  }
#pragma unroll
  for (int i = 0; i < 8; ++i) {
    acc[i] += mdpp<0x128>(acc[i]);
    acc[i] += __shfl_xor(acc[i], 16);
    acc[i] += __shfl_xor(acc[i], 32);
  }
  if (lane < 8) {
#pragma unroll
    for (int i = 0; i < 8; ++i) red[row * 64 + cg * 8 + i] = acc[i];
  }
  __syncthreads();
  if (tid < 64) {
    float sum = 0.f;
#pragma unroll
    for (int i = 0; i < 8; ++i) sum += red[i * 64 + tid];
    ((float*)(ws + OFF_MODP))[ks * 3072 + cgp * 64 + tid] = sum;
  }
  STAMP(0, 1);
}

__device__ __forceinline__ f4 mfma16(u4v a, u4v b, f4 c) {
  return __builtin_amdgcn_mfma_f32_16x16x32_bf16(__builtin_bit_cast(bf16x8, a), __builtin_bit_cast(bf16x8, b), c, 0, 0,
                                                  0);
}
__device__ __forceinline__ f2 silu2(float a, float b) {
  f2 h = f2{a, b};
  f2 t = h * -1.44269504088896340736f;
  f2 d = f2{__builtin_amdgcn_exp2f(t.x), __builtin_amdgcn_exp2f(t.y)} + 1.0f;
  return h * f2{__builtin_amdgcn_rcpf(d.x), __builtin_amdgcn_rcpf(d.y)};
}
__device__ __forceinline__ unsigned silu_pk(float a, float b) {
  f2 r = silu2(a, b);
  return pack_bf(r.x, r.y);
}
typedef __attribute__((address_space(1))) unsigned gu32;
#define DRAIN() asm volatile("s_waitcnt vmcnt(0)" ::: "memory")
typedef __attribute__((address_space(1))) unsigned long long gu64;
constexpr unsigned SPIN_CAP = 1u << 18;
__device__ __forceinline__ void wait_pair(gu64* flags) {
#pragma nounroll
  for (unsigned spin = 0; spin < SPIN_CAP; ++spin) {
    if (__hip_atomic_load(flags, __ATOMIC_RELAXED, __HIP_MEMORY_SCOPE_AGENT) == 0x0000000100000001ull) break;
    __builtin_amdgcn_s_sleep(1);
  }
  __builtin_amdgcn_fence(__ATOMIC_ACQUIRE, "wavefront");
  asm volatile("" ::: "memory");
}

#define PSLEEP 2
__device__ __forceinline__ void wait_cols(gu32* flags) {
#pragma nounroll
  for (unsigned spin = 0; spin < SPIN_CAP; ++spin) {
    unsigned f = 1u;
#pragma unroll
    for (int j = 0; j < 4; ++j)
      f &= __hip_atomic_load(flags + j * 64 * (FSP / 4), __ATOMIC_RELAXED, __HIP_MEMORY_SCOPE_AGENT);
    if (__builtin_amdgcn_ballot_w64(f != 1u) == 0ull) break;
    __builtin_amdgcn_s_sleep(PSLEEP);
  }
}

__device__ __forceinline__ int wt_off(int k, int t) { return (int)OFF_WT + (k * 512 + t) * 16; }

constexpr int L_W1 = 0;
constexpr int L_T = 112 * 1024;
constexpr int L_ZS = 112 * 1024;
constexpr int L_ST = L_ZS + 8 * ZRS * 8;
constexpr int L_TOTAL = 160 * 1024;
__device__ __forceinline__ constexpr int g1_n(int g) { return g < 13 ? 7 : (g < 16 ? 6 : 3); }
__device__ __forceinline__ constexpr int g1_ft(int g, int j) { return g < 13 ? j : (g < 16 ? 2 * (g - 13) + (j & 1) : 6); }
__device__ __forceinline__ constexpr int g1_ks(int g, int j) { return g < 13 ? g : (g < 16 ? 13 + (j >> 1) : 13 + j); }
#define G1WIN 5
#define G2WIN 2
#define SCHED() __builtin_amdgcn_sched_barrier(0)
typedef short v4i16 __attribute__((ext_vector_type(4)));
__device__ __forceinline__ u4v w1frag(const char* lw, int ft, int ks) {
  typedef __attribute__((address_space(3))) v4i16* lp;
  const u2v a = __builtin_bit_cast(u2v, __builtin_amdgcn_ds_read_tr16_b64_v4i16((lp)(lw + (2 * ks) * 3584 + ft * 32)));
  const u2v b = __builtin_bit_cast(u2v, __builtin_amdgcn_ds_read_tr16_b64_v4i16((lp)(lw + (2 * ks + 1) * 3584 + ft * 32)));
  return u4v{a[0], a[1], b[0], b[1]};
}
constexpr int L_MODS = L_ZS + 8 * ZRS * 8;
constexpr int L_PX = L_T + 28672;
constexpr int L_W2S = L_T + 32768;
__global__ __launch_bounds__(512, 2) void kmega(const u16* __restrict__ x, const u16* __restrict__ w1,
                                                const u16* __restrict__ w2, char* ws, u16* __restrict__ out) {
  extern __shared__ __attribute__((aligned(16))) char dsm[];
  const int tid = threadIdx.x, lane = tid & 63;
  const int w = __builtin_amdgcn_readfirstlane(tid >> 6);
  const int bid = blockIdx.x;
  const int p = (bid & 7) * 2 + (bid >> 7), c = (bid >> 3) & 15;
  const int row0 = 128 * p + 8 * c;
  STAMP(1, 0);
  unsigned cw0, cw1, cw2;
  {
    unsigned long long pc;
    asm volatile("s_getpc_b64 %0" : "=s"(pc));
    const char* code = (const char*)(pc & ~255ull) + (tid < 286 ? tid : 0) * 64;
    cw0 = *(const unsigned*)(code);
    cw1 = cw0; cw2 = cw0;
  }
  const auto rs = WS_RSRC(ws);
  gu32* myA = (gu32*)(ws + OFF_CNT) + (p * 8 + (c >> 1)) * 16 + (c & 1);
  gu32* myB = myA + 128 * 16;
  gu64* depA = (gu64*)(ws + OFF_CNT) + (p * 8 + w) * 8;
  gu64* depB = depA + 128 * 8;
  const int mt0 = 8 * p + 4 * (c >> 3), nt0 = 4 * (c & 7);
  const int l56 = lane < 56 ? lane : lane - 56;
  const int r4_ = l56 / 14, ch_ = l56 - 14 * r4_;
  const char* w1g = (const char*)w1 + (size_t)(4 * w + r4_) * (F_ * 2) + c * 224 + ch_ * 16;
  char* w1l = dsm + L_W1 + ((w >> 1) * 32 + (2 * (w & 1) + (r4_ >> 1)) * 4 + (r4_ & 1)) * 224 + ch_ * 16;
  const int Ab = (int)OFF_W2F + ((nt0 * 56 + 7 * w) * 64 + lane) * 16;
  u4v S1[16];
  u4v A2[28];
  auto pf_load = [&](int i) __attribute__((always_inline)) {
    if (i < 16) S1[i] = *(const u4v*)(w1g + (size_t)i * (32 * F_ * 2));
    else if (i < 44) A2[i - 16] = *(const u4v*)(ws + Ab + (((i - 16) & 3) * 56 + ((i - 16) >> 2)) * 1024);
  };
  auto pf_put = [&](int i) __attribute__((always_inline)) {
    *(u4v*)(w1l + ((i & 3) * 128 + (i >> 3) * 16 + ((i >> 2) & 1) * 2) * 224) = S1[i];
  };
  auto pf = [&](int i0, int n) __attribute__((always_inline)) {
    SCHED();
#pragma unroll
    for (int i = 0; i < n; ++i) pf_load(i0 + i);
    SCHED();
  };
  Tw256 tw;
  {
    f2* zs = (f2*)(dsm + L_ZS);
    float* mods = (float*)(dsm + L_MODS);
    const int r0 = bid * 8;
    unsigned xr[4];
    const unsigned* xrow = (const unsigned*)(x + (size_t)(r0 + w) * D_);
#pragma unroll
    for (int j = 0; j < 4; ++j) xr[j] = xrow[lane + 64 * j];
    const float* mp = (const float*)(ws + OFF_MODP);
    f2 sm = *(const f2*)(mp + tid * 2);
#pragma unroll
    for (int k = 1; k < NSPLIT; ++k) sm += *(const f2*)(mp + k * 3072 + tid * 2);
    const int idx = bid * 12 + (tid < 6 ? tid : 0) * 2;
    f2 fin = *(const f2*)(mp + idx);
#pragma unroll
    for (int k = 1; k < NSPLIT; ++k) fin += *(const f2*)(mp + k * 3072 + idx);
    pf(0, 4);
    make_tw256(lane, tw);
    const int rp = tid & 3, kk = tid >> 2;
    f2 wpost = twid((float)kk * (1.0f / 512.0f));
    PIN2(wpost);
    f2 v[4];
#pragma unroll
    for (int j = 0; j < 4; ++j) v[j] = f2{bf_lo(xr[j]), bf_hi(xr[j])};
    float mean, rden;
    ln_stats(v, mean, rden);
    asm volatile("" ::"v"(cw0), "v"(cw1), "v"(cw2));
    sm = bfr2(sm);
    if (tid >= 256) sm = bfr2(sm + 1.0f);
    *(f2*)(mods + tid * 2) = sm;
    if (tid < 6) {
      fin = bfr2(fin);
      ST64(__builtin_bit_cast(u2v, fin), OFF_MODF + idx * 4);
      const int vec = (idx >> 9) - 2, d = idx & 511;
      if (vec >= 0 && vec < 3) {
        if (vec == 2) fin += 1.0f;
        ST32(pack_bf(fin.x, fin.y), OFF_MODC + ((vec * 64 + ((d >> 1) & 63)) * 4 + (d >> 7)) * 4);
      }
    }
    STAMP(1, 3);
    __syncthreads();
    STAMP(1, 4);
    pf(4, 4);
#pragma unroll
    for (int j = 0; j < 4; ++j) {
      const int d = 2 * (lane + 64 * j);
      f2 sa = *(const f2*)(mods + d);
      f2 ca1 = *(const f2*)(mods + 512 + d);
      v[j] = bfr2(__builtin_elementwise_fma(v[j] - mean, ca1 * rden, sa));
    }
    fft256_fwd(v, lane, tw, (f2*)(dsm + L_PX + w * 2048), NoHook{});
    {
      f2* buf = zs + w * ZRS + lane + (lane >> 4);
#pragma unroll
      for (int r4 = 0; r4 < 4; ++r4) buf[68 * r4] = v[r4];
    }
    STAMP(1, 5);
    __syncthreads();
    {
      const int k = kk;
      const int k2 = (k == 0) ? 128 : 256 - k;
      const int pa = zpos(k), pb = zpos(k2);
      f4 oa, ob;
#pragma unroll
      for (int h = 0; h < 2; ++h) {
        const f2* zr = zs + (2 * rp + h) * ZRS;
        f2 za = zr[pa], zb = zr[pb];
        f2 E = f2{za.x + zb.x, za.y - zb.y};
        f2 Dm = f2{za.x - zb.x, za.y + zb.y};
        f2 O = f2{Dm.y, -Dm.x};
        f2 wo = cmul1<false>(O, wpost);
        f2 ra = E + wo;
        f2 t = E - wo;
        f2 rb = f2{t.x, -t.y};
        if (k == 0) {
          ra = f2{2.f * (za.x + za.y), 2.f * (za.x - za.y)};
          rb = f2{2.f * zb.x, -2.f * zb.y};
        }
        oa[2 * h] = ra.x; oa[2 * h + 1] = ra.y;
        ob[2 * h] = rb.x; ob[2 * h + 1] = rb.y;
      }
      ST128(__builtin_bit_cast(u4v, oa), OFF_XT + (k * CP + r0 + 2 * rp) * 8);
      ST128(__builtin_bit_cast(u4v, ob), OFF_XT + (k2 * CP + r0 + 2 * rp) * 8);
    }
    STAMP(1, 6);
    DRAIN();
    __syncthreads();
    if (tid < NREP)
      __hip_atomic_store((gu32*)(ws + OFF_FLR + (tid * 256 + bid) * FSP), 1u, __ATOMIC_RELAXED, __HIP_MEMORY_SCOPE_AGENT);
    STAMP(1, 7);
  }
  {
    f2* T = (f2*)(dsm + L_T);
    f2* T2 = (f2*)(dsm + L_T + 16384);
    f2* T3 = (f2*)(dsm + L_T + 32768);
    const int k = bid;
    f2 tw1[4];
#pragma unroll
    for (int j = 0; j < 4; ++j) {
      tw1[j] = twid((float)((lane + 64 * j) * w) * (1.0f / 2048.0f));
      PIN2(tw1[j]);
    }
    if (w == 0) wait_cols((gu32*)(ws + OFF_FLR + ((bid % NREP) * 256 + lane) * FSP));
    asm volatile("s_barrier" ::: "memory");
    __builtin_amdgcn_fence(__ATOMIC_ACQUIRE, "wavefront");
    STAMP(2, 0);
    f2 v[4];
#pragma unroll
    for (int j = 0; j < 4; ++j) v[j] = __builtin_bit_cast(f2, LD64(OFF_XT + (k * CP + tid + 512 * j) * 8));
    u4v bv;
    unsigned wn[4], bn[4];
    int mirr[4];
#pragma unroll
    for (int j = 0; j < 4; ++j) PIN2(v[j]);
    pf(8, 4);
    const int hb = w >> 2, a = tid & 255;
    constexpr float R = 0.70710678118654752440f;
    dft4<-1>(v[0], v[1], v[2], v[3]);
    if (hb) {
      v[1] = f2{v[1].x + v[1].y, v[1].y - v[1].x} * R;
      v[2] = f2{v[2].y, -v[2].x};
      v[3] = f2{v[3].y - v[3].x, -v[3].x - v[3].y} * R;
    }
#pragma unroll
    for (int q = 0; q < 4; ++q) T[(hb * 4 + q) * 256 + a] = v[q];
    STAMP(2, 1);
    const u4v wv = LD128(wt_off(k, tid));
    if (k == 0) {
      bv = LD128(wt_off(256, tid));
      const int m0 = (lane >> 4) + 4 * ((lane >> 2) & 3) + 16 * (lane & 3);
#pragma unroll
      for (int r4 = 0; r4 < 4; ++r4) {
        const int un = (S_ - (w + 8 * (m0 + 64 * r4))) & (S_ - 1);
        const int mm = un >> 3;
        mirr[r4] = ((un & 7) * 64 + 16 * (mm & 3) + 4 * ((mm >> 2) & 3) + ((mm >> 4) & 3)) * 4 + (mm >> 6);
      }
      if (w != 0) {
        const u4v a4 = LD128(wt_off(0, 64 * (8 - w) + 63 - lane));
        const u4v b4 = LD128(wt_off(256, 64 * (8 - w) + 63 - lane));
#pragma unroll
        for (int r4 = 0; r4 < 4; ++r4) { wn[r4] = a4[3 - r4]; bn[r4] = b4[3 - r4]; }
      } else {
#pragma unroll
        for (int r4 = 0; r4 < 4; ++r4) {
          wn[r4] = LD32(wt_off(0, mirr[r4] >> 2) + (mirr[r4] & 3) * 4);
          bn[r4] = LD32(wt_off(256, mirr[r4] >> 2) + (mirr[r4] & 3) * 4);
        }
      }
    }
    __syncthreads();
    {
      const int q = w & 3;
      const float sg = (w < 4) ? 1.f : -1.f;
#pragma unroll
      for (int j = 0; j < 4; ++j) {
        f2 e = T[q * 256 + lane + 64 * j], o = T[(4 + q) * 256 + lane + 64 * j];
        v[j] = cmul1<false>(e + o * sg, tw1[j]);
      }
    }
    fft256_fwd(v, lane, tw, T2 + w * 256, [&](int i) __attribute__((always_inline)) {
      SCHED();
      if (i < 2) {
#pragma unroll
        for (int q = 0; q < 4; ++q) pf_put(4 * i + q);
      } else {
#pragma unroll
        for (int q = 12; q < 16; ++q) pf_load(q);
      }
      SCHED();
    });
    STAMP(2, 2);
    if (k != 0) {
#pragma unroll
      for (int i = 0; i < 4; ++i) {
        v[i] = cmul1<false>(v[i], f2{bf_lo(wv[i]), bf_hi(wv[i])});
      }
    } else {
#pragma unroll
      for (int r4 = 0; r4 < 4; ++r4) T3[r4 * 512 + tid] = v[r4];
      __syncthreads();
#pragma unroll
      for (int r4 = 0; r4 < 4; ++r4) {
        f2 cm = T3[(mirr[r4] & 3) * 512 + (mirr[r4] >> 2)];
        f2 h0 = f2{bf_lo(wv[r4]) + bf_lo(wn[r4]), bf_hi(wv[r4]) - bf_hi(wn[r4])};
        f2 h1 = f2{bf_lo(bv[r4]) + bf_lo(bn[r4]), bf_hi(bv[r4]) - bf_hi(bn[r4])};
        f2 wp = (h0 + h1) * 0.25f, wm = (h0 - h1) * 0.25f;
        f2 cc = f2{cm.x, -cm.y};
        v[r4] = cmul(v[r4], wp) + cmul(cc, wm);
      }
    }
    fft256_inv(v, lane, tw, T2 + w * 256, [&](int i) __attribute__((always_inline)) {
      SCHED();
      if (i == 0) {
#pragma unroll
        for (int q = 8; q < 12; ++q) pf_put(q);
      }
      if (i == 1) {
#pragma unroll
        for (int q = 16; q < 20; ++q) pf_load(q);
      }
      SCHED();
    });
#pragma unroll
    for (int j = 0; j < 4; ++j) T2[w * 256 + lane + 64 * j] = cmul1<true>(v[j], tw1[j]);
#pragma unroll
    for (int q = 12; q < 16; ++q) pf_put(q);
    STAMP(2, 3);
    __syncthreads();
    {
      const float sg = hb ? -1.f : 1.f;
#pragma unroll
      for (int q = 0; q < 4; ++q) v[q] = T2[q * 256 + a] + T2[(q + 4) * 256 + a] * sg;
      if (hb) {
        v[1] = f2{v[1].x - v[1].y, v[1].y + v[1].x} * R;
        v[2] = f2{-v[2].y, v[2].x};
        v[3] = f2{-v[3].x - v[3].y, v[3].x - v[3].y} * R;
      }
    }
    dft4<1>(v[0], v[1], v[2], v[3]);
#pragma unroll
    for (int j = 0; j < 4; ++j) ST64(__builtin_bit_cast(u2v, v[j]), OFF_ZT + (k * CP + tid + 512 * j) * 8);
    STAMP(2, 4);
    DRAIN();
    __syncthreads();
    if (tid < NREP)
      __hip_atomic_store((gu32*)(ws + OFF_FLG + (tid * 256 + k) * FSP), 1u, __ATOMIC_RELAXED, __HIP_MEMORY_SCOPE_AGENT);
  }
  STAMP(2, 5);
  {
    const char* lw = dsm + L_W1 + (lane >> 2) * 224 + (lane & 3) * 8 + w * (2 * 7168);
    u4v fr[14];
#pragma unroll
    for (int i = 0; i < 14; ++i) fr[i] = w1frag(lw, i % 7, i / 7);
    asm volatile("" ::: "memory");
#pragma unroll
    for (int i = 0; i < 14; ++i) *(u4v*)(dsm + L_W1 + w * (2 * 7168) + i * 1024 + lane * 16) = fr[i];
  }
  STAMP(2, 6);
  {
    f2* zs = (f2*)(dsm + L_ZS);
    const int rp = tid & 3, k = tid >> 2;
    const int k2 = (k == 0) ? 128 : 256 - k;
    u4v xr;
    const unsigned* xrow = (const unsigned*)(x + (size_t)(row0 + w) * D_);
#pragma unroll
    for (int j = 0; j < 4; ++j) xr[j] = xrow[lane + 64 * j];
    const u4v gav = LD128(OFF_MODC + lane * 16);
    const u4v sbv = LD128(OFF_MODC + 1024 + lane * 16);
    const u4v cbv = LD128(OFF_MODC + 2048 + lane * 16);
    f2 wq = twid((float)k * (1.0f / 512.0f));
    PIN2(wq);
    const int pa = zpos(k), pb = zpos(k2);
    if (w == 0) wait_cols((gu32*)(ws + OFF_FLG + ((bid % NREP) * 256 + lane) * FSP));
    asm volatile("s_barrier" ::: "memory");
    __builtin_amdgcn_fence(__ATOMIC_ACQUIRE, "wavefront");
    STAMP(3, 0);
    const f4 ya = __builtin_bit_cast(f4, LD128(OFF_ZT + (k * CP + row0 + 2 * rp) * 8));
    const f4 yb = __builtin_bit_cast(f4, LD128(OFF_ZT + (k2 * CP + row0 + 2 * rp) * 8));
#pragma unroll
    for (int h = 0; h < 2; ++h) {
      f2 a = f2{ya[2 * h], ya[2 * h + 1]}, b = f2{yb[2 * h], yb[2 * h + 1]};
      f2* zr = zs + (2 * rp + h) * ZRS;
      f2 Ze = f2{a.x + b.x, a.y - b.y};
      f2 Dm = f2{a.x - b.x, a.y + b.y};
      f2 Zo = cmul1<true>(Dm, wq);
      f2 za = f2{Ze.x - Zo.y, Ze.y + Zo.x};
      f2 zb = f2{Ze.x + Zo.y, -Ze.y + Zo.x};
      if (k == 0) {
        za = f2{a.x + a.y, a.x - a.y};
        zb = f2{2.f * b.x, -2.f * b.y};
      }
      zr[pa] = za;
      zr[pb] = zb;
    }
    STAMP(3, 1);
    pf(20, 4);
    __syncthreads();
    f2 v[4];
    {
      const f2* buf = zs + w * ZRS + lane + (lane >> 4);
#pragma unroll
      for (int r4 = 0; r4 < 4; ++r4) v[r4] = buf[68 * r4];
    }
    fft256_inv(v, lane, tw, (f2*)(dsm + L_PX + w * 2048), NoHook{});
    pf(24, 4);
    STAMP(3, 2);
    u4v x1p;
#pragma unroll
    for (int j = 0; j < 4; ++j) {
      f2 t = __builtin_elementwise_fma(f2{bf_lo(gav[j]), bf_hi(gav[j])} * FSC, v[j], f2{bf_lo(xr[j]), bf_hi(xr[j])});
      x1p[j] = pack_bf(t.x, t.y);
      v[j] = f2{bf_lo(x1p[j]), bf_hi(x1p[j])};
    }
    ST128(x1p, OFF_X1 + ((row0 + w) * 64 + lane) * 16);
    float mean, rden;
    ln_stats(v, mean, rden);
    u4v hp;
#pragma unroll
    for (int j = 0; j < 4; ++j) {
      f2 t = __builtin_elementwise_fma(v[j] - mean, f2{bf_lo(cbv[j]), bf_hi(cbv[j])} * rden,
                                       f2{bf_lo(sbv[j]), bf_hi(sbv[j])});
      hp[j] = pack_bf(t.x, t.y);
    }
    *(u4v*)(dsm + L_ST + (lane * 9 + w) * 16) = hp;
    __syncthreads();
    {
      const int ch = tid >> 3, r = tid & 7;
      u4v o = *(const u4v*)(dsm + L_ST + (ch * 9 + r) * 16);
      const int mt = row0 >> 4, r16 = (row0 & 8) + r;
      ST128(o, OFF_H2F + ((mt * 16 + (ch >> 2)) * 64 + (ch & 3) * 16 + r16) * 16);
    }
  }
  STAMP(3, 3);
  DRAIN();
  __syncthreads();
  if (tid == 0) __hip_atomic_store(myA, 1u, __ATOMIC_RELAXED, __HIP_MEMORY_SCOPE_AGENT);
  {
    const int ch = tid >> 3, r = tid & 7;
    u4v t = LD128(OFF_H2F + (((row0 >> 4) * 16 + (ch >> 2)) * 64 + (ch & 3) * 16 + (row0 & 8) + r) * 16);
    asm volatile("" ::"v"(t));
  }
  STAMP(3, 4);
  wait_pair(depA);
  STAMP(3, 5);
  {
    u4v B[16];
#pragma unroll
    for (int ks = 0; ks < G1WIN; ++ks) B[ks] = LD128(OFF_H2F + (((8 * p + w) * 16 + ks) * 64 + lane) * 16);
    f4 acc[7];
#pragma unroll
    for (int ft = 0; ft < 7; ++ft) acc[ft] = f4{0.f, 0.f, 0.f, 0.f};
    const char* lw = dsm + L_W1 + lane * 16;
    unsigned off2 = L_W1 + lane * 16 + 8 * 7168;
    asm volatile("" : "+v"(off2));
    const char* lw2 = dsm + off2;
    const int e = c >> 1, odd = c & 1;
    const int hb = (int)OFF_HF + (((8 * p + w) * 56 + 7 * e) * 64 + lane) * 16;
    u4v Af[2][7];
#pragma unroll
    for (int j = 0; j < 7; ++j) Af[0][j] = *(const u4v*)(lw + (g1_ks(0, j) * 7 + g1_ft(0, j)) * 1024);
#pragma unroll
    for (int g = 0; g < 17; ++g) {
#pragma unroll
      for (int j = 0; j < 7; ++j) {
        if (j < g1_n(g)) acc[g1_ft(g, j)] = mfma16(Af[g & 1][j], B[g1_ks(g, j)], acc[g1_ft(g, j)]);
        if (g + 1 < 17 && j < g1_n(g + 1))
          Af[(g + 1) & 1][j] = g1_ks(g + 1, j) < 8 ? *(const u4v*)(lw + (g1_ks(g + 1, j) * 7 + g1_ft(g + 1, j)) * 1024)
                                                   : *(const u4v*)(lw2 + ((g1_ks(g + 1, j) - 8) * 7 + g1_ft(g + 1, j)) * 1024);
        SCHED();
      }
      if (g + G1WIN < 16) B[g + G1WIN] = LD128(OFF_H2F + (((8 * p + w) * 16 + g + G1WIN) * 64 + lane) * 16);
      else if (g + G1WIN < 20) {
#pragma unroll
        for (int q = 0; q < 4; ++q) pf_load(28 + 4 * (g + G1WIN - 16) + q);
      }
      if (g >= 14) {
        const int i = g - 14;
        f4 a = acc[2 * i], b = acc[2 * i + 1];
        u4v o = u4v{silu_pk(a[0], a[1]), silu_pk(a[2], a[3]), silu_pk(b[0], b[1]), silu_pk(b[2], b[3])};
        ST128(o, hb + (odd * 3 + i) * 1024);
      }
      SCHED();
      if (g == 12) STAMP(3, 6);
    }
    f4 a = acc[6];
    u2v o = u2v{silu_pk(a[0], a[1]), silu_pk(a[2], a[3])};
    ST64(o, hb + 6 * 1024 + odd * 8);
  }
  STAMP(3, 7);
  DRAIN();
  __syncthreads();
  if (tid == 0) __hip_atomic_store(myB, 1u, __ATOMIC_RELAXED, __HIP_MEMORY_SCOPE_AGENT);
  {
    const int hb = (int)OFF_HF + (((8 * p + w) * 56 + 7 * (c >> 1) + 3 * (c & 1)) * 64 + lane) * 16;
    u4v t0 = LD128(hb), t1 = LD128(hb + 1024), t2 = LD128(hb + 2048);
    asm volatile("" ::"v"(t0), "v"(t1), "v"(t2));
  }
  wait_pair(depB);
  STAMP(3, 8);
  {
    const int Bb = (int)OFF_HF + ((mt0 * 56 + 7 * w) * 64 + lane) * 16;
    u4v B[7][4];
#pragma unroll
    for (int ks = 0; ks < G2WIN; ++ks)
#pragma unroll
      for (int mt = 0; mt < 4; ++mt) B[ks][mt] = LD128(Bb + (mt * 56 + ks) * 1024);
    f4 acc[4][4];
#pragma unroll
    for (int i = 0; i < 4; ++i)
#pragma unroll
      for (int j = 0; j < 4; ++j) acc[i][j] = f4{0.f, 0.f, 0.f, 0.f};
#pragma unroll
    for (int ks = 0; ks < 7; ++ks) {
#pragma unroll
      for (int nt = 0; nt < 4; ++nt)
#pragma unroll
        for (int mt = 0; mt < 4; ++mt) acc[nt][mt] = mfma16(A2[ks * 4 + nt], B[ks][mt], acc[nt][mt]);
      if (ks + G2WIN < 7) {
#pragma unroll
        for (int mt = 0; mt < 4; ++mt) B[ks + G2WIN][mt] = LD128(Bb + (mt * 56 + ks + G2WIN) * 1024);
      }
      SCHED();
    }
    STAMP(3, 9);
    const int g = lane >> 4, mr = lane & 15;
    const int fm = w >> 1, P = w & 1;
    const int m = (mt0 + fm) * 16 + mr;
    const int n = nt0 * 16 + 32 * P + 8 * g;
    unsigned xv[4];
#pragma unroll
    for (int i = 0; i < 4; ++i)
      xv[i] = __builtin_amdgcn_raw_buffer_load_b32(
          rs, (int)OFF_X1 + (m * 64 + (((n >> 1) + i) & 63)) * 16 + (n >> 7) * 4, 0, AUX_SC1);
    const f4 g0 = __builtin_bit_cast(f4, LD128(OFF_MODF + (2560 + n) * 4));
    const f4 g1 = __builtin_bit_cast(f4, LD128(OFF_MODF + (2560 + n + 4) * 4));
    f4* red = (f4*)dsm;
#pragma unroll
    for (int nt = 0; nt < 4; ++nt)
#pragma unroll
      for (int mt = 0; mt < 4; ++mt) red[(w * 16 + nt * 4 + mt) * 64 + lane] = acc[nt][mt];
    __syncthreads();
    STAMP(3, 10);
    f4 s0 = f4{0.f, 0.f, 0.f, 0.f}, s1 = s0;
#pragma unroll
    for (int ww = 0; ww < 8; ++ww) {
      s0 += red[(ww * 16 + (2 * P) * 4 + fm) * 64 + lane];
      s1 += red[(ww * 16 + (2 * P + 1) * 4 + fm) * 64 + lane];
    }
    u4v o;
    {
      f2 t0 = bfr2(f2{g0[0], g0[1]} * bfr2(f2{s0[0], s0[1]})), t1 = bfr2(f2{g0[2], g0[3]} * bfr2(f2{s0[2], s0[3]}));
      f2 t2 = bfr2(f2{g1[0], g1[1]} * bfr2(f2{s1[0], s1[1]})), t3 = bfr2(f2{g1[2], g1[3]} * bfr2(f2{s1[2], s1[3]}));
      o[0] = pack_bf(bf_lo(xv[0]) + t0.x, bf_hi(xv[0]) + t0.y);
      o[1] = pack_bf(bf_lo(xv[1]) + t1.x, bf_hi(xv[1]) + t1.y);
      o[2] = pack_bf(bf_lo(xv[2]) + t2.x, bf_hi(xv[2]) + t2.y);
      o[3] = pack_bf(bf_lo(xv[3]) + t3.x, bf_hi(xv[3]) + t3.y);
    }
    const auto ro = __builtin_amdgcn_make_buffer_rsrc(out, 0, S_ * D_ * 2, 0x00020000);
    __builtin_amdgcn_raw_buffer_store_b128(o, ro, (m * D_ + n) * 2, 0, AUX_SC1);
  }
  STAMP(3, 11);
}

extern "C" void kernel_launch(void* const* d_in, const int* in_sizes, int n_in, void* d_out, int out_size, void* d_ws,
                              size_t ws_size, hipStream_t stream) {
  const u16* x = (const u16*)d_in[0];
  const u16* wr = (const u16*)d_in[1];
  const u16* wi = (const u16*)d_in[2];
  const u16* w1 = (const u16*)d_in[4];
  const u16* w2 = (const u16*)d_in[5];
  const u16* c = (const u16*)d_in[6];
  const u16* wmod = (const u16*)d_in[7];
  char* ws = (char*)d_ws;
  u16* out = (u16*)d_out;
  (void)hipFuncSetAttribute((const void*)kmega, hipFuncAttributeMaxDynamicSharedMemorySize, L_TOTAL);
  k0_mod<<<K0_GEMV + K0_WT + K0_W2 + K0_Z + K0_WARM, 512, 0, stream>>>(c, wmod, x, wr, wi, w1, w2, ws);
  kmega<<<256, 512, L_TOTAL, stream>>>(x, w1, w2, ws, out);
}
```

```cpp
#include <hip/hip_runtime.h>
#include <hip/hip_bf16.h>
#include <stdint.h>

typedef float f2 __attribute__((ext_vector_type(2)));
typedef float f4 __attribute__((ext_vector_type(4)));
typedef unsigned u2v __attribute__((ext_vector_type(2)));
typedef unsigned u4v __attribute__((ext_vector_type(4)));
typedef short bf16x8 __attribute__((ext_vector_type(8)));
typedef unsigned short u16;

constexpr int S_ = 2048, D_ = 512, F_ = 1792, DF_ = 257;
constexpr int NSPLIT = 4;

constexpr size_t OFF_CNT = 0;
#define FSPACE 16
#define FREP 16
constexpr int FSP = FSPACE, NREP = FREP;
constexpr size_t FTAB = (size_t)NREP * 256 * FSP;
constexpr size_t OFF_FLG = 16384;
constexpr size_t OFF_FLR = OFF_FLG + FTAB;
constexpr size_t OFF_FLS = OFF_FLR + FTAB;
constexpr size_t FLAG_BYTES = OFF_FLS + 16384;
constexpr size_t OFF_MODP = 900 << 10;
constexpr size_t OFF_MODF = OFF_MODP + (size_t)NSPLIT * 3072 * 4;
constexpr size_t OFF_MODC = OFF_MODF + 3072 * 4;
constexpr size_t OFF_WT = 1 << 20;
constexpr size_t OFF_XT = 4 << 20;
constexpr size_t OFF_ZT = 28 << 20;
constexpr size_t OFF_H2F = 12 << 20;
constexpr size_t OFF_X1 = 14 << 20;
constexpr size_t OFF_W2F = 18 << 20;
constexpr size_t OFF_HF = 20 << 20;
constexpr int WS_SPAN = 36 << 20;
#define CPITCH (2048 + 32)
constexpr int CP = CPITCH;
constexpr int WT_PITCH = 520;
constexpr float FSC = 1.0f / (2.0f * 2048.0f * 512.0f);

#define STAMP(kid, i)

__device__ __forceinline__ float bf_lo(unsigned u) { return __builtin_bit_cast(float, u << 16); }
__device__ __forceinline__ float bf_hi(unsigned u) { return __builtin_bit_cast(float, u & 0xffff0000u); }
__device__ __forceinline__ float bfr(float x) {
  return (float)(__bf16)x;
}
__device__ __forceinline__ unsigned pack_bf(float lo, float hi) {
  typedef __bf16 b2 __attribute__((ext_vector_type(2)));
  return __builtin_bit_cast(unsigned, __builtin_convertvector(f2{lo, hi}, b2));
}
__device__ __forceinline__ f2 cmul(f2 a, f2 b) {
  f2 t = a * f2{b.x, b.x};
  return __builtin_elementwise_fma(f2{a.y, a.x}, f2{-b.y, b.y}, t);
}
__device__ __forceinline__ f2 cmulc(f2 a, f2 b) {
  f2 t = a * f2{b.x, b.x};
  return __builtin_elementwise_fma(f2{a.y, a.x}, f2{b.y, -b.y}, t);
}
__device__ __forceinline__ f2 bfr2(f2 a) {
  unsigned u = pack_bf(a.x, a.y);
  asm("" : "+v"(u));
  return f2{bf_lo(u), bf_hi(u)};
}
__device__ __forceinline__ float msel(unsigned m, float a, float b) {
  unsigned ua = __builtin_bit_cast(unsigned, a), ub = __builtin_bit_cast(unsigned, b);
  return __builtin_bit_cast(float, (m & ua) | (~m & ub));
}
#define PIN2(x) asm volatile("" : "+v"(x))
constexpr int AUX_SC1 = 16;
#define WS_RSRC(ws) __builtin_amdgcn_make_buffer_rsrc((ws), 0, WS_SPAN, 0x00020000)
#define ST128(v, off) __builtin_amdgcn_raw_buffer_store_b128((v), rs, (int)(off), 0, AUX_SC1)
#define ST64(v, off) __builtin_amdgcn_raw_buffer_store_b64((v), rs, (int)(off), 0, AUX_SC1)
#define ST32(v, off) __builtin_amdgcn_raw_buffer_store_b32((v), rs, (int)(off), 0, AUX_SC1)
#define LD64(off) __builtin_amdgcn_raw_buffer_load_b64(rs, (int)(off), 0, AUX_SC1)
#define LD32(off) __builtin_amdgcn_raw_buffer_load_b32(rs, (int)(off), 0, AUX_SC1)
#define LD128(off) __builtin_amdgcn_raw_buffer_load_b128(rs, (int)(off), 0, AUX_SC1)
__device__ __forceinline__ f2 twid(float rev) { return f2{__builtin_amdgcn_cosf(rev), -__builtin_amdgcn_sinf(rev)}; }

template <int DIR>
__device__ __forceinline__ f2 rot90(f2 a) {
  return DIR < 0 ? f2{a.y, -a.x} : f2{-a.y, a.x};
}
template <int DIR>
__device__ __forceinline__ void dft4(f2& c0, f2& c1, f2& c2, f2& c3) {
  f2 d0 = c0 + c2, d2 = c0 - c2, d1 = c1 + c3, e = c1 - c3;
  c0 = d0 + d1;
  c2 = d0 - d1;
  const f2 es = f2{e.y, e.x};
  c1 = __builtin_elementwise_fma(es, DIR < 0 ? f2{1.f, -1.f} : f2{-1.f, 1.f}, d2);
  c3 = __builtin_elementwise_fma(es, DIR < 0 ? f2{-1.f, 1.f} : f2{1.f, -1.f}, d2);
}

__device__ __forceinline__ void sw32(float& a, float& b) {
  auto r = __builtin_amdgcn_permlane32_swap(__builtin_bit_cast(unsigned, a), __builtin_bit_cast(unsigned, b), false, false);
  a = __builtin_bit_cast(float, (unsigned)r[0]);
  b = __builtin_bit_cast(float, (unsigned)r[1]);
}
__device__ __forceinline__ void sw16(float& a, float& b) {
  auto r = __builtin_amdgcn_permlane16_swap(__builtin_bit_cast(unsigned, a), __builtin_bit_cast(unsigned, b), false, false);
  a = __builtin_bit_cast(float, (unsigned)r[0]);
  b = __builtin_bit_cast(float, (unsigned)r[1]);
}
template <int CTRL, int BANK>
__device__ __forceinline__ float udpp(float old, float src) {
  return __builtin_bit_cast(float, __builtin_amdgcn_update_dpp(__builtin_bit_cast(int, old), __builtin_bit_cast(int, src),
                                                               CTRL, 0xf, BANK, false));
}
template <int CTRL>
__device__ __forceinline__ float mdpp(float src) {
  return __builtin_bit_cast(float, __builtin_amdgcn_mov_dpp(__builtin_bit_cast(int, src), CTRL, 0xf, 0xf, false));
}
__device__ __forceinline__ void xp_hi1(float& v0, float& v1, float& v2, float& v3) {
  sw32(v0, v2); sw32(v1, v3); sw16(v0, v1); sw16(v2, v3);
}
__device__ __forceinline__ void xp_mid1(float& v0, float& v1, float& v2, float& v3) {
  float a0 = udpp<0x128, 0xC>(v0, v2), a2 = udpp<0x128, 0x3>(v2, v0);
  float a1 = udpp<0x128, 0xC>(v1, v3), a3 = udpp<0x128, 0x3>(v3, v1);
  v0 = udpp<0x124, 0xA>(a0, a1); v1 = udpp<0x12C, 0x5>(a1, a0);
  v2 = udpp<0x124, 0xA>(a2, a3); v3 = udpp<0x12C, 0x5>(a3, a2);
}
__device__ __forceinline__ void xp_lo1(float& v0, float& v1, float& v2, float& v3, unsigned b1, unsigned b0) {
  float s0 = mdpp<0x4E>(msel(b1, v0, v2)), s1 = mdpp<0x4E>(msel(b1, v1, v3));
  float a0 = msel(b1, s0, v0), a2 = msel(b1, v2, s0), a1 = msel(b1, s1, v1), a3 = msel(b1, v3, s1);
  float t0 = mdpp<0xB1>(msel(b0, a0, a1)), t2 = mdpp<0xB1>(msel(b0, a2, a3));
  v0 = msel(b0, t0, a0); v1 = msel(b0, a1, t0); v2 = msel(b0, t2, a2); v3 = msel(b0, a3, t2);
}
#define XP_CPLX(NAME, FN, ...)                                        \
  {                                                                   \
    float x0 = v[0].x, x1 = v[1].x, x2 = v[2].x, x3 = v[3].x;         \
    float y0 = v[0].y, y1 = v[1].y, y2 = v[2].y, y3 = v[3].y;         \
    FN(x0, x1, x2, x3, ##__VA_ARGS__);                                \
    FN(y0, y1, y2, y3, ##__VA_ARGS__);                                \
    v[0] = f2{x0, y0}; v[1] = f2{x1, y1}; v[2] = f2{x2, y2}; v[3] = f2{x3, y3}; \
  }
__device__ __forceinline__ void xp_hi(f2 (&v)[4]) XP_CPLX(hi, xp_hi1)
__device__ __forceinline__ void xp_mid(f2 (&v)[4]) XP_CPLX(mid, xp_mid1)
__device__ __forceinline__ void xp_lo(f2 (&v)[4], unsigned b1, unsigned b0) XP_CPLX(lo, xp_lo1, b1, b0)

template <bool CONJ>
__device__ __forceinline__ void cmul3(f2 (&v)[4], const f2 (&t)[3]) {
  f2 a, b, c;
  if (!CONJ)
    asm("v_pk_mul_f32 %3, %0, %6 op_sel_hi:[1,0]\n\t"
        "v_pk_mul_f32 %4, %1, %7 op_sel_hi:[1,0]\n\t"
        "v_pk_mul_f32 %5, %2, %8 op_sel_hi:[1,0]\n\t"
        "v_pk_fma_f32 %0, %0, %6, %3 op_sel:[1,1,0] op_sel_hi:[0,1,1] neg_lo:[0,1,0]\n\t"
        "v_pk_fma_f32 %1, %1, %7, %4 op_sel:[1,1,0] op_sel_hi:[0,1,1] neg_lo:[0,1,0]\n\t"
        "v_pk_fma_f32 %2, %2, %8, %5 op_sel:[1,1,0] op_sel_hi:[0,1,1] neg_lo:[0,1,0]\n\t"
        "s_nop 1"
        : "+v"(v[1]), "+v"(v[2]), "+v"(v[3]), "=&v"(a), "=&v"(b), "=&v"(c)
        : "v"(t[0]), "v"(t[1]), "v"(t[2]));
  else
    asm("v_pk_mul_f32 %3, %0, %6 op_sel_hi:[1,0]\n\t"
        "v_pk_mul_f32 %4, %1, %7 op_sel_hi:[1,0]\n\t"
        "v_pk_mul_f32 %5, %2, %8 op_sel_hi:[1,0]\n\t"
        "v_pk_fma_f32 %0, %0, %6, %3 op_sel:[1,1,0] op_sel_hi:[0,1,1] neg_hi:[0,1,0]\n\t"
        "v_pk_fma_f32 %1, %1, %7, %4 op_sel:[1,1,0] op_sel_hi:[0,1,1] neg_hi:[0,1,0]\n\t"
        "v_pk_fma_f32 %2, %2, %8, %5 op_sel:[1,1,0] op_sel_hi:[0,1,1] neg_hi:[0,1,0]"
        : "+v"(v[1]), "+v"(v[2]), "+v"(v[3]), "=&v"(a), "=&v"(b), "=&v"(c)
        : "v"(t[0]), "v"(t[1]), "v"(t[2]));
}
template <bool CONJ>
__device__ __forceinline__ f2 cmul1(f2 v, f2 t) {
  f2 a;
  if (!CONJ)
    asm("v_pk_mul_f32 %1, %0, %2 op_sel_hi:[1,0]\n\t"
        "v_pk_fma_f32 %0, %0, %2, %1 op_sel:[1,1,0] op_sel_hi:[0,1,1] neg_lo:[0,1,0]"
        : "+v"(v), "=&v"(a) : "v"(t));
  else
    asm("v_pk_mul_f32 %1, %0, %2 op_sel_hi:[1,0]\n\t"
        "v_pk_fma_f32 %0, %0, %2, %1 op_sel:[1,1,0] op_sel_hi:[0,1,1] neg_hi:[0,1,0]"
        : "+v"(v), "=&v"(a) : "v"(t));
  return v;
}
__device__ __forceinline__ void xp_lo_lds(f2 (&v)[4], f2* patch, int lane) {
  f2* q = patch + (lane >> 2) * 16;
  const int c = lane & 3, sw = (lane >> 2) & 3;
#pragma unroll
  for (int j = 0; j < 4; ++j) q[4 * (j ^ sw) + c] = v[j];
  __builtin_amdgcn_fence(__ATOMIC_RELEASE, "wavefront");
  __builtin_amdgcn_wave_barrier();
  __builtin_amdgcn_fence(__ATOMIC_ACQUIRE, "wavefront");
  const f4 a = *(const f4*)(q + 4 * (c ^ sw)), b = *(const f4*)(q + 4 * (c ^ sw) + 2);
  v[0] = f2{a[0], a[1]}; v[1] = f2{a[2], a[3]}; v[2] = f2{b[0], b[1]}; v[3] = f2{b[2], b[3]};
  __builtin_amdgcn_fence(__ATOMIC_RELEASE, "wavefront");
  __builtin_amdgcn_wave_barrier();
}

struct Tw256 {
  f2 a[3], b[3], c[3];
};
__device__ __forceinline__ void make_tw256(int lane, Tw256& t) {
#pragma unroll
  for (int r = 1; r < 4; ++r) {
    t.a[r - 1] = twid((float)(lane * r) * (1.0f / 256.0f));
    t.b[r - 1] = twid((float)((lane & 15) * r) * (1.0f / 64.0f));
    t.c[r - 1] = twid((float)((lane & 3) * r) * (1.0f / 16.0f));
  }
#pragma unroll
  for (int r = 0; r < 3; ++r) {
    PIN2(t.a[r]);
    PIN2(t.b[r]);
    PIN2(t.c[r]);
  }
}
struct NoHook {
  __device__ __forceinline__ void operator()(int) const {}
};
template <class H>
__device__ __forceinline__ void fft256_fwd(f2 (&v)[4], int lane, const Tw256& t, f2* patch, const H& hook) {
  dft4<-1>(v[0], v[1], v[2], v[3]);
  cmul3<false>(v, t.a);
  xp_hi(v);
  hook(0);
  dft4<-1>(v[0], v[1], v[2], v[3]);
  cmul3<false>(v, t.b);
  xp_mid(v);
  hook(1);
  dft4<-1>(v[0], v[1], v[2], v[3]);
  cmul3<false>(v, t.c);
  xp_lo_lds(v, patch, lane);
  hook(2);
  dft4<-1>(v[0], v[1], v[2], v[3]);
}
template <class H>
__device__ __forceinline__ void fft256_inv(f2 (&v)[4], int lane, const Tw256& t, f2* patch, const H& hook) {
  dft4<1>(v[0], v[1], v[2], v[3]);
  xp_lo_lds(v, patch, lane);
  hook(0);
  cmul3<true>(v, t.c);
  dft4<1>(v[0], v[1], v[2], v[3]);
  xp_mid(v);
  hook(1);
  cmul3<true>(v, t.b);
  dft4<1>(v[0], v[1], v[2], v[3]);
  xp_hi(v);
  hook(2);
  cmul3<true>(v, t.a);
  dft4<1>(v[0], v[1], v[2], v[3]);
}

__device__ __forceinline__ float wave_sum(float x) {
  x += mdpp<0xB1>(x);
  x += mdpp<0x4E>(x);
  x += mdpp<0x141>(x);
  x += mdpp<0x140>(x);
  float r0 = __builtin_bit_cast(float, __builtin_amdgcn_readlane(__builtin_bit_cast(int, x), 0));
  float r1 = __builtin_bit_cast(float, __builtin_amdgcn_readlane(__builtin_bit_cast(int, x), 16));
  float r2 = __builtin_bit_cast(float, __builtin_amdgcn_readlane(__builtin_bit_cast(int, x), 32));
  float r3 = __builtin_bit_cast(float, __builtin_amdgcn_readlane(__builtin_bit_cast(int, x), 48));
  return (r0 + r1) + (r2 + r3);
}

__device__ __forceinline__ void ln_stats(const f2 (&v)[4], float& mean, float& rden) {
  const float K = __builtin_bit_cast(float, __builtin_amdgcn_readfirstlane(__builtin_bit_cast(int, v[0].x)));
  f2 s2 = f2{0.f, 0.f}, q2 = f2{0.f, 0.f};
#pragma unroll
  for (int j = 0; j < 4; ++j) {
    const f2 d = v[j] - K;
    s2 += d;
    q2 = __builtin_elementwise_fma(d, d, q2);
  }
  const float sm = wave_sum(s2.x + s2.y) * (1.0f / D_), qm = wave_sum(q2.x + q2.y) * (1.0f / D_);
  const float var = fmaxf(qm - sm * sm, 0.f);
  mean = bfr(K + sm);
  rden = __builtin_amdgcn_rcpf(bfr(__builtin_amdgcn_sqrtf(bfr(bfr(var) + 1e-5f))));
}

__device__ __forceinline__ int zpos(int k) { return 17 * (k & 3) + 4 * ((k >> 2) & 3) + ((k >> 4) & 3) + 68 * (k >> 6); }
constexpr int ZRS = 276;

constexpr int K0_GEMV = 48 * NSPLIT, K0_WT = 128, K0_W2 = 112, K0_WARM = 0, K0_Z = (int)(FLAG_BYTES / 8192);
__global__ __launch_bounds__(512) void k0_mod(const u16* __restrict__ c, const u16* __restrict__ wmod,
                                              const u16* __restrict__ x, const u16* __restrict__ wr,
                                              const u16* __restrict__ wi, const u16* __restrict__ w1,
                                              const u16* __restrict__ w2, char* __restrict__ ws) {
  __shared__ __attribute__((aligned(16))) char smem[17408];
  const int tid = threadIdx.x, row = tid >> 6, lane = tid & 63;
  const auto rs = WS_RSRC(ws);
  int b = blockIdx.x;
  STAMP(0, 0);
  if (b >= K0_GEMV + K0_WT + K0_W2) {
    int q = b - (K0_GEMV + K0_WT + K0_W2);
    if (q < K0_Z) {
      ((u4v*)ws)[q * 512 + tid] = u4v{0u, 0u, 0u, 0u};
      return;
    }
    q -= K0_Z;
    const char* src = q < 64 ? (const char*)x + (size_t)q * 32768
                             : (q < 120 ? (const char*)w1 + (size_t)(q - 64) * 32768 : (const char*)w2 + (size_t)(q - 120) * 32768);
    u4v t[4];
#pragma unroll
    for (int i = 0; i < 4; ++i) t[i] = *(const u4v*)(src + i * 8192 + tid * 16);
#pragma unroll
    for (int i = 0; i < 4; ++i) asm volatile("" ::"v"(t[i]));
    STAMP(0, 1);
    return;
  }
  if (b >= K0_GEMV) {
    b -= K0_GEMV;
    if (b < K0_WT) {
      char* sr = smem;
      char* si = smem + 16 * 544;
      constexpr int LASTCH = (S_ * DF_ * 2) / 16 - 1;
      for (int it = tid; it < 16 * 34; it += 512) {
        const int i = it / 34, ch = it % 34;
        int gch = ((b + 128 * i) * (DF_ * 2)) / 16 + ch;
        gch = gch > LASTCH ? LASTCH : gch;
        *(u4v*)(sr + i * 544 + ch * 16) = *(const u4v*)((const char*)wr + (size_t)gch * 16);
        *(u4v*)(si + i * 544 + ch * 16) = *(const u4v*)((const char*)wi + (size_t)gch * 16);
      }
      __syncthreads();
      const int tbase = 64 * (b & 7) + 16 * ((b >> 3) & 3) + 4 * ((b >> 5) & 3);
      for (int it = tid; it < DF_ * 4; it += 512) {
        const int k = it >> 2, r3 = it & 3;
        u4v o;
#pragma unroll
        for (int r4 = 0; r4 < 4; ++r4) {
          const int i = r3 + 4 * r4;
          const int off = i * 544 + (((b + 128 * i) * (DF_ * 2)) & 15) + 2 * k;
          o[r4] = (unsigned)*(const u16*)(sr + off) | ((unsigned)*(const u16*)(si + off) << 16);
        }
        ST128(o, OFF_WT + (k * 512 + tbase + r3) * 16);
      }
    } else {
      b -= K0_WT;
      u16* t = (u16*)smem;
      const int g = lane >> 4, r = lane & 15;
      const int ng = b & 3, kp = b >> 2;
#pragma unroll
      for (int it = 0; it < 2; ++it) {
        const int idx = it * 512 + tid;
        const int rr = idx >> 4, ch = idx & 15;
        const int ks2 = kp * 2 + (rr >> 5), gg = (rr >> 3) & 3, j = rr & 7;
        const int e = ks2 / 7, i7 = ks2 % 7;
        int T0, T1;
        if (i7 < 3) { T0 = 14 * e + 2 * i7; T1 = T0 + 1; }
        else if (i7 < 6) { T0 = 14 * e + 7 + 2 * (i7 - 3); T1 = T0 + 1; }
        else { T0 = 14 * e + 6; T1 = 14 * e + 13; }
        const int f = (j < 4) ? (16 * T0 + 4 * gg + j) : (16 * T1 + 4 * gg + (j - 4));
        *(u4v*)(t + rr * 136 + ch * 8) = *(const u4v*)(w2 + (size_t)f * D_ + ng * 128 + ch * 8);
      }
      __syncthreads();
      const int nl = 32 * (row >> 1) + 8 * (r >> 2) + 4 * (row & 1) + (r & 3);
#pragma unroll
      for (int ksl = 0; ksl < 2; ++ksl) {
        u4v o;
#pragma unroll
        for (int jj = 0; jj < 4; ++jj)
          o[jj] = (unsigned)t[(ksl * 32 + g * 8 + 2 * jj) * 136 + nl] |
                  ((unsigned)t[(ksl * 32 + g * 8 + 2 * jj + 1) * 136 + nl] << 16);
        const int nt = ng * 8 + row;
        ST128(o, OFF_W2F + ((nt * 56 + kp * 2 + ksl) * 64 + lane) * 16);
      }
    }
    STAMP(0, 1);
    return;
  }
  float* red = (float*)smem;
  const int cgp = b % 48, ks = b / 48;
  const int cg = tid & 7, r = tid >> 3;
  const int rw = ks * 128 + r;
  const u4v wm0 = *(const u4v*)(wmod + (size_t)rw * 3072 + cgp * 64 + cg * 8);
  const u4v wm1 = *(const u4v*)(wmod + (size_t)(rw + 64) * 3072 + cgp * 64 + cg * 8);
  const float cv0 = bf_lo(c[rw]), cv1 = bf_lo(c[rw + 64]);
  const float s0 = bfr(cv0 / (1.0f + __expf(-cv0))), s1 = bfr(cv1 / (1.0f + __expf(-cv1)));
  float acc[8];
#pragma unroll
  for (int i = 0; i < 4; ++i) {
    acc[2 * i] = s0 * bf_lo(wm0[i]) + s1 * bf_lo(wm1[i]);
    acc[2 * i + 1] = s0 * bf_hi(wm0[i]) + s1 * bf_hi(wm1[i]);
  }
#pragma unroll
  for (int i = 0; i < 8; ++i) {
    acc[i] += mdpp<0x128>(acc[i]);
    acc[i] += __shfl_xor(acc[i], 16);
    acc[i] += __shfl_xor(acc[i], 32);
  }
  if (lane < 8) {
#pragma unroll
    for (int i = 0; i < 8; ++i) red[row * 64 + cg * 8 + i] = acc[i];
  }
  __syncthreads();
  if (tid < 64) {
    float sum = 0.f;
#pragma unroll
    for (int i = 0; i < 8; ++i) sum += red[i * 64 + tid];
    ((float*)(ws + OFF_MODP))[ks * 3072 + cgp * 64 + tid] = sum;
  }
  STAMP(0, 1);
}

__device__ __forceinline__ f4 mfma16(u4v a, u4v b, f4 c) {
  return __builtin_amdgcn_mfma_f32_16x16x32_bf16(__builtin_bit_cast(bf16x8, a), __builtin_bit_cast(bf16x8, b), c, 0, 0,
                                                  0);
}
__device__ __forceinline__ f2 silu2(float a, float b) {
  f2 h = f2{a, b};
  f2 t = h * -1.44269504088896340736f;
  f2 d = f2{__builtin_amdgcn_exp2f(t.x), __builtin_amdgcn_exp2f(t.y)} + 1.0f;
  return h * f2{__builtin_amdgcn_rcpf(d.x), __builtin_amdgcn_rcpf(d.y)};
}
__device__ __forceinline__ unsigned silu_pk(float a, float b) {
  f2 r = silu2(a, b);
  return pack_bf(r.x, r.y);
}
typedef __attribute__((address_space(1))) unsigned gu32;
#define DRAIN() asm volatile("s_waitcnt vmcnt(0)" ::: "memory")
typedef __attribute__((address_space(1))) unsigned long long gu64;
constexpr unsigned SPIN_CAP = 1u << 18;
__device__ __forceinline__ void wait_pair(gu64* flags) {
#pragma nounroll
  for (unsigned spin = 0; spin < SPIN_CAP; ++spin) {
    if (__hip_atomic_load(flags, __ATOMIC_RELAXED, __HIP_MEMORY_SCOPE_AGENT) == 0x0000000100000001ull) break;
    __builtin_amdgcn_s_sleep(1);
  }
  __builtin_amdgcn_fence(__ATOMIC_ACQUIRE, "wavefront");
  asm volatile("" ::: "memory");
}

#define PSLEEP 2
__device__ __forceinline__ void wait_cols(gu32* flags) {
#pragma nounroll
  for (unsigned spin = 0; spin < SPIN_CAP; ++spin) {
    unsigned f = 1u;
#pragma unroll
    for (int j = 0; j < 4; ++j)
      f &= __hip_atomic_load(flags + j * 64 * (FSP / 4), __ATOMIC_RELAXED, __HIP_MEMORY_SCOPE_AGENT);
    if (__builtin_amdgcn_ballot_w64(f != 1u) == 0ull) break;
    __builtin_amdgcn_s_sleep(PSLEEP);
  }
}

__device__ __forceinline__ int wt_off(int k, int t) { return (int)OFF_WT + (k * 512 + t) * 16; }

constexpr int L_W1 = 0;
constexpr int L_T = 112 * 1024;
constexpr int L_ZS = 112 * 1024;
constexpr int L_ST = L_ZS + 8 * ZRS * 8;
constexpr int L_TOTAL = 160 * 1024;
__device__ __forceinline__ constexpr int g1_ft(int g, int j) { return g < 8 ? (j & 3) : (g < 12 ? 4 + (j & 1) : 6); }
__device__ __forceinline__ constexpr int g1_ks(int g, int j) {
  return g < 8 ? 2 * g + (j >> 2) : (g < 12 ? 4 * (g - 8) + (j >> 1) : 8 * (g - 12) + j);
}
#define G1WIN 6
#define G2WIN 2
#define SCHED() __builtin_amdgcn_sched_barrier(0)
typedef short v4i16 __attribute__((ext_vector_type(4)));
__device__ __forceinline__ u4v w1frag(const char* lw, int ft, int ks) {
  typedef __attribute__((address_space(3))) v4i16* lp;
  const u2v a = __builtin_bit_cast(u2v, __builtin_amdgcn_ds_read_tr16_b64_v4i16((lp)(lw + (2 * ks) * 3584 + ft * 32)));
  const u2v b = __builtin_bit_cast(u2v, __builtin_amdgcn_ds_read_tr16_b64_v4i16((lp)(lw + (2 * ks + 1) * 3584 + ft * 32)));
  return u4v{a[0], a[1], b[0], b[1]};
}
constexpr int L_MODS = L_ZS + 8 * ZRS * 8;
constexpr int L_PX = L_T + 28672;
constexpr int L_W2S = L_T + 32768;
__global__ __launch_bounds__(512, 2) void kmega(const u16* __restrict__ x, const u16* __restrict__ w1,
                                                const u16* __restrict__ w2, char* ws, u16* __restrict__ out) {
  extern __shared__ __attribute__((aligned(16))) char dsm[];
  const int tid = threadIdx.x, lane = tid & 63;
  const int w = __builtin_amdgcn_readfirstlane(tid >> 6);
  const int bid = blockIdx.x;
  const int p = (bid & 7) * 2 + (bid >> 7), c = (bid >> 3) & 15;
  const int row0 = 128 * p + 8 * c;
  STAMP(1, 0);
  unsigned cw0, cw1, cw2;
  {
    unsigned long long pc;
    asm volatile("s_getpc_b64 %0" : "=s"(pc));
    const char* code = (const char*)(pc & ~255ull) + (tid < 272 ? tid : 0) * 64;
    cw0 = *(const unsigned*)(code);
    cw1 = cw0; cw2 = cw0;
  }
  const auto rs = WS_RSRC(ws);
  gu32* myA = (gu32*)(ws + OFF_CNT) + (p * 8 + (c >> 1)) * 16 + (c & 1);
  gu32* myB = myA + 128 * 16;
  gu64* depA = (gu64*)(ws + OFF_CNT) + (p * 8 + w) * 8;
  gu64* depB = depA + 128 * 8;
  gu32* myE = myB + 8;
  gu64* depE = depB + 4;
  const int mt0 = 8 * p + 4 * (c >> 3), nt0 = 4 * (c & 7);
  const int l56 = lane < 56 ? lane : lane - 56;
  const int r4_ = l56 / 14, ch_ = l56 - 14 * r4_;
  const char* w1g = (const char*)w1 + (size_t)(4 * w + r4_) * (F_ * 2) + c * 224 + ch_ * 16;
  char* w1l = dsm + L_W1 + ((w >> 1) * 32 + (2 * (w & 1) + (r4_ >> 1)) * 4 + (r4_ & 1)) * 224 + ch_ * 16;
  const int Ab = (int)OFF_W2F + ((nt0 * 56 + 7 * w) * 64 + lane) * 16;
  u4v S1[16];
  u4v A2[28];
  auto pf_load = [&](int i) __attribute__((always_inline)) {
    if (i < 16) S1[i] = *(const u4v*)(w1g + (size_t)i * (32 * F_ * 2));
    else if (i < 44) A2[i - 16] = *(const u4v*)(ws + Ab + (((i - 16) & 3) * 56 + ((i - 16) >> 2)) * 1024);
  };
  auto pf_put = [&](int i) __attribute__((always_inline)) {
    *(u4v*)(w1l + ((i & 3) * 128 + (i >> 3) * 16 + ((i >> 2) & 1) * 2) * 224) = S1[i];
  };
  auto pf = [&](int i0, int n) __attribute__((always_inline)) {
    SCHED();
#pragma unroll
    for (int i = 0; i < n; ++i) pf_load(i0 + i);
    SCHED();
  };
  Tw256 tw;
  {
    f2* zs = (f2*)(dsm + L_ZS);
    float* mods = (float*)(dsm + L_MODS);
    const int r0 = bid * 8;
    unsigned xr[4];
    const unsigned* xrow = (const unsigned*)(x + (size_t)(r0 + w) * D_);
#pragma unroll
    for (int j = 0; j < 4; ++j) xr[j] = xrow[lane + 64 * j];
    const float* mp = (const float*)(ws + OFF_MODP);
    f2 sm = *(const f2*)(mp + tid * 2);
#pragma unroll
    for (int k = 1; k < NSPLIT; ++k) sm += *(const f2*)(mp + k * 3072 + tid * 2);
    const int idx = bid * 12 + (tid < 6 ? tid : 0) * 2;
    f2 fin = *(const f2*)(mp + idx);
#pragma unroll
    for (int k = 1; k < NSPLIT; ++k) fin += *(const f2*)(mp + k * 3072 + idx);
    pf(0, 4);
    make_tw256(lane, tw);
    const int rp = tid & 3, kk = tid >> 2;
    f2 wpost = twid((float)kk * (1.0f / 512.0f));
    PIN2(wpost);
    f2 v[4];
#pragma unroll
    for (int j = 0; j < 4; ++j) v[j] = f2{bf_lo(xr[j]), bf_hi(xr[j])};
    float mean, rden;
    ln_stats(v, mean, rden);
    asm volatile("" ::"v"(cw0), "v"(cw1), "v"(cw2));
    sm = bfr2(sm);
    if (tid >= 256) sm = bfr2(sm + 1.0f);
    *(f2*)(mods + tid * 2) = sm;
    if (tid < 6) {
      fin = bfr2(fin);
      ST64(__builtin_bit_cast(u2v, fin), OFF_MODF + idx * 4);
      const int vec = (idx >> 9) - 2, d = idx & 511;
      if (vec >= 0 && vec < 3) {
        if (vec == 2) fin += 1.0f;
        ST32(pack_bf(fin.x, fin.y), OFF_MODC + ((vec * 64 + ((d >> 1) & 63)) * 4 + (d >> 7)) * 4);
      }
    }
    STAMP(1, 3);
    __syncthreads();
    STAMP(1, 4);
    pf(4, 4);
#pragma unroll
    for (int j = 0; j < 4; ++j) {
      const int d = 2 * (lane + 64 * j);
      f2 sa = *(const f2*)(mods + d);
      f2 ca1 = *(const f2*)(mods + 512 + d);
      v[j] = bfr2(__builtin_elementwise_fma(v[j] - mean, ca1 * rden, sa));
    }
    fft256_fwd(v, lane, tw, (f2*)(dsm + L_PX + w * 2048), NoHook{});
    {
      f2* buf = zs + w * ZRS + lane + (lane >> 4);
#pragma unroll
      for (int r4 = 0; r4 < 4; ++r4) buf[68 * r4] = v[r4];
    }
    STAMP(1, 5);
    __syncthreads();
    {
      const int k = kk;
      const int k2 = (k == 0) ? 128 : 256 - k;
      const int pa = zpos(k), pb = zpos(k2);
      f4 oa, ob;
#pragma unroll
      for (int h = 0; h < 2; ++h) {
        const f2* zr = zs + (2 * rp + h) * ZRS;
        f2 za = zr[pa], zb = zr[pb];
        f2 E = f2{za.x + zb.x, za.y - zb.y};
        f2 Dm = f2{za.x - zb.x, za.y + zb.y};
        f2 O = f2{Dm.y, -Dm.x};
        f2 wo = cmul1<false>(O, wpost);
        f2 ra = E + wo;
        f2 t = E - wo;
        f2 rb = f2{t.x, -t.y};
        if (k == 0) {
          ra = f2{2.f * (za.x + za.y), 2.f * (za.x - za.y)};
          rb = f2{2.f * zb.x, -2.f * zb.y};
        }
        oa[2 * h] = ra.x; oa[2 * h + 1] = ra.y;
        ob[2 * h] = rb.x; ob[2 * h + 1] = rb.y;
      }
      ST128(__builtin_bit_cast(u4v, oa), OFF_XT + (k * CP + r0 + 2 * rp) * 8);
      ST128(__builtin_bit_cast(u4v, ob), OFF_XT + (k2 * CP + r0 + 2 * rp) * 8);
    }
    STAMP(1, 6);
    DRAIN();
    __syncthreads();
    if (tid < NREP)
      __hip_atomic_store((gu32*)(ws + OFF_FLR + (tid * 256 + bid) * FSP), 1u, __ATOMIC_RELAXED, __HIP_MEMORY_SCOPE_AGENT);
    STAMP(1, 7);
  }
  {
    f2* T = (f2*)(dsm + L_T);
    f2* T2 = (f2*)(dsm + L_T + 16384);
    f2* T3 = (f2*)(dsm + L_T + 32768);
    const int k = bid;
    f2 tw1[4];
#pragma unroll
    for (int j = 0; j < 4; ++j) {
      tw1[j] = twid((float)((lane + 64 * j) * w) * (1.0f / 2048.0f));
      PIN2(tw1[j]);
    }
    pf(28, 3);
    if (w == 0) wait_cols((gu32*)(ws + OFF_FLR + ((bid % NREP) * 256 + lane) * FSP));
    asm volatile("s_barrier" ::: "memory");
    __builtin_amdgcn_fence(__ATOMIC_ACQUIRE, "wavefront");
    STAMP(2, 0);
    f2 v[4];
#pragma unroll
    for (int j = 0; j < 4; ++j) v[j] = __builtin_bit_cast(f2, LD64(OFF_XT + (k * CP + tid + 512 * j) * 8));
    u4v bv;
    unsigned wn[4], bn[4];
    int mirr[4];
#pragma unroll
    for (int j = 0; j < 4; ++j) PIN2(v[j]);
    pf(8, 4);
    const int hb = w >> 2, a = tid & 255;
    constexpr float R = 0.70710678118654752440f;
    dft4<-1>(v[0], v[1], v[2], v[3]);
    if (hb) {
      v[1] = f2{v[1].x + v[1].y, v[1].y - v[1].x} * R;
      v[2] = f2{v[2].y, -v[2].x};
      v[3] = f2{v[3].y - v[3].x, -v[3].x - v[3].y} * R;
    }
#pragma unroll
    for (int q = 0; q < 4; ++q) T[(hb * 4 + q) * 256 + a] = v[q];
    STAMP(2, 1);
    const u4v wv = LD128(wt_off(k, tid));
    if (k == 0) {
      bv = LD128(wt_off(256, tid));
      const int m0 = (lane >> 4) + 4 * ((lane >> 2) & 3) + 16 * (lane & 3);
#pragma unroll
      for (int r4 = 0; r4 < 4; ++r4) {
        const int un = (S_ - (w + 8 * (m0 + 64 * r4))) & (S_ - 1);
        const int mm = un >> 3;
        mirr[r4] = ((un & 7) * 64 + 16 * (mm & 3) + 4 * ((mm >> 2) & 3) + ((mm >> 4) & 3)) * 4 + (mm >> 6);
      }
      if (w != 0) {
        const u4v a4 = LD128(wt_off(0, 64 * (8 - w) + 63 - lane));
        const u4v b4 = LD128(wt_off(256, 64 * (8 - w) + 63 - lane));
#pragma unroll
        for (int r4 = 0; r4 < 4; ++r4) { wn[r4] = a4[3 - r4]; bn[r4] = b4[3 - r4]; }
      } else {
#pragma unroll
        for (int r4 = 0; r4 < 4; ++r4) {
          wn[r4] = LD32(wt_off(0, mirr[r4] >> 2) + (mirr[r4] & 3) * 4);
          bn[r4] = LD32(wt_off(256, mirr[r4] >> 2) + (mirr[r4] & 3) * 4);
        }
      }
    }
    __syncthreads();
    {
      const int q = w & 3;
      const float sg = (w < 4) ? 1.f : -1.f;
#pragma unroll
      for (int j = 0; j < 4; ++j) {
        f2 e = T[q * 256 + lane + 64 * j], o = T[(4 + q) * 256 + lane + 64 * j];
        v[j] = cmul1<false>(e + o * sg, tw1[j]);
      }
    }
    fft256_fwd(v, lane, tw, T2 + w * 256, [&](int i) __attribute__((always_inline)) {
      SCHED();
      if (i < 2) {
#pragma unroll
        for (int q = 0; q < 4; ++q) pf_put(4 * i + q);
      } else {
#pragma unroll
        for (int q = 12; q < 16; ++q) pf_load(q);
      }
      SCHED();
    });
    STAMP(2, 2);
    if (k != 0) {
#pragma unroll
      for (int i = 0; i < 4; ++i) {
        v[i] = cmul1<false>(v[i], f2{bf_lo(wv[i]), bf_hi(wv[i])});
      }
    } else {
#pragma unroll
      for (int r4 = 0; r4 < 4; ++r4) T3[r4 * 512 + tid] = v[r4];
      __syncthreads();
#pragma unroll
      for (int r4 = 0; r4 < 4; ++r4) {
        f2 cm = T3[(mirr[r4] & 3) * 512 + (mirr[r4] >> 2)];
        f2 h0 = f2{bf_lo(wv[r4]) + bf_lo(wn[r4]), bf_hi(wv[r4]) - bf_hi(wn[r4])};
        f2 h1 = f2{bf_lo(bv[r4]) + bf_lo(bn[r4]), bf_hi(bv[r4]) - bf_hi(bn[r4])};
        f2 wp = (h0 + h1) * 0.25f, wm = (h0 - h1) * 0.25f;
        f2 cc = f2{cm.x, -cm.y};
        v[r4] = cmul(v[r4], wp) + cmul(cc, wm);
      }
    }
    fft256_inv(v, lane, tw, T2 + w * 256, [&](int i) __attribute__((always_inline)) {
      SCHED();
      if (i == 0) {
#pragma unroll
        for (int q = 8; q < 12; ++q) pf_put(q);
      }
      if (i == 1) {
#pragma unroll
        for (int q = 16; q < 20; ++q) pf_load(q);
      }
      SCHED();
    });
#pragma unroll
    for (int j = 0; j < 4; ++j) T2[w * 256 + lane + 64 * j] = cmul1<true>(v[j], tw1[j]);
#pragma unroll
    for (int q = 12; q < 16; ++q) pf_put(q);
    STAMP(2, 3);
    __syncthreads();
    {
      const float sg = hb ? -1.f : 1.f;
#pragma unroll
      for (int q = 0; q < 4; ++q) v[q] = T2[q * 256 + a] + T2[(q + 4) * 256 + a] * sg;
      if (hb) {
        v[1] = f2{v[1].x - v[1].y, v[1].y + v[1].x} * R;
        v[2] = f2{-v[2].y, v[2].x};
        v[3] = f2{-v[3].x - v[3].y, v[3].x - v[3].y} * R;
      }
    }
    dft4<1>(v[0], v[1], v[2], v[3]);
#pragma unroll
    for (int j = 0; j < 4; ++j) ST64(__builtin_bit_cast(u2v, v[j]), OFF_ZT + (k * CP + tid + 512 * j) * 8);
    STAMP(2, 4);
    DRAIN();
    __syncthreads();
    if (tid < NREP)
      __hip_atomic_store((gu32*)(ws + OFF_FLG + (tid * 256 + k) * FSP), 1u, __ATOMIC_RELAXED, __HIP_MEMORY_SCOPE_AGENT);
  }
  STAMP(2, 5);
  {
    const char* lw = dsm + L_W1 + (lane >> 2) * 224 + (lane & 3) * 8 + w * (2 * 7168);
    u4v fr[14];
#pragma unroll
    for (int i = 0; i < 14; ++i) fr[i] = w1frag(lw, i % 7, i / 7);
    asm volatile("" ::: "memory");
#pragma unroll
    for (int i = 0; i < 14; ++i) *(u4v*)(dsm + L_W1 + w * (2 * 7168) + i * 1024 + lane * 16) = fr[i];
  }
  STAMP(2, 6);
  {
    f2* zs = (f2*)(dsm + L_ZS);
    const int rp = tid & 3, k = tid >> 2;
    const int k2 = (k == 0) ? 128 : 256 - k;
    u4v xr;
    const unsigned* xrow = (const unsigned*)(x + (size_t)(row0 + w) * D_);
#pragma unroll
    for (int j = 0; j < 4; ++j) xr[j] = xrow[lane + 64 * j];
    const u4v gav = LD128(OFF_MODC + lane * 16);
    const u4v sbv = LD128(OFF_MODC + 1024 + lane * 16);
    const u4v cbv = LD128(OFF_MODC + 2048 + lane * 16);
    f2 wq = twid((float)k * (1.0f / 512.0f));
    PIN2(wq);
    const int pa = zpos(k), pb = zpos(k2);
    pf(31, 3);
    if (w == 0) wait_cols((gu32*)(ws + OFF_FLG + ((bid % NREP) * 256 + lane) * FSP));
    asm volatile("s_barrier" ::: "memory");
    __builtin_amdgcn_fence(__ATOMIC_ACQUIRE, "wavefront");
    STAMP(3, 0);
    const f4 ya = __builtin_bit_cast(f4, LD128(OFF_ZT + (k * CP + row0 + 2 * rp) * 8));
    const f4 yb = __builtin_bit_cast(f4, LD128(OFF_ZT + (k2 * CP + row0 + 2 * rp) * 8));
#pragma unroll
    for (int h = 0; h < 2; ++h) {
      f2 a = f2{ya[2 * h], ya[2 * h + 1]}, b = f2{yb[2 * h], yb[2 * h + 1]};
      f2* zr = zs + (2 * rp + h) * ZRS;
      f2 Ze = f2{a.x + b.x, a.y - b.y};
      f2 Dm = f2{a.x - b.x, a.y + b.y};
      f2 Zo = cmul1<true>(Dm, wq);
      f2 za = f2{Ze.x - Zo.y, Ze.y + Zo.x};
      f2 zb = f2{Ze.x + Zo.y, -Ze.y + Zo.x};
      if (k == 0) {
        za = f2{a.x + a.y, a.x - a.y};
        zb = f2{2.f * b.x, -2.f * b.y};
      }
      zr[pa] = za;
      zr[pb] = zb;
    }
    STAMP(3, 1);
    pf(20, 4);
    __syncthreads();
    f2 v[4];
    {
      const f2* buf = zs + w * ZRS + lane + (lane >> 4);
#pragma unroll
      for (int r4 = 0; r4 < 4; ++r4) v[r4] = buf[68 * r4];
    }
    fft256_inv(v, lane, tw, (f2*)(dsm + L_PX + w * 2048), NoHook{});
    pf(24, 4);
    STAMP(3, 2);
    u4v x1p;
#pragma unroll
    for (int j = 0; j < 4; ++j) {
      f2 t = __builtin_elementwise_fma(f2{bf_lo(gav[j]), bf_hi(gav[j])} * FSC, v[j], f2{bf_lo(xr[j]), bf_hi(xr[j])});
      x1p[j] = pack_bf(t.x, t.y);
      v[j] = f2{bf_lo(x1p[j]), bf_hi(x1p[j])};
    }
    ST128(x1p, OFF_X1 + ((row0 + w) * 64 + lane) * 16);
    float mean, rden;
    ln_stats(v, mean, rden);
    u4v hp;
#pragma unroll
    for (int j = 0; j < 4; ++j) {
      f2 t = __builtin_elementwise_fma(v[j] - mean, f2{bf_lo(cbv[j]), bf_hi(cbv[j])} * rden,
                                       f2{bf_lo(sbv[j]), bf_hi(sbv[j])});
      hp[j] = pack_bf(t.x, t.y);
    }
    *(u4v*)(dsm + L_ST + (lane * 9 + w) * 16) = hp;
    __syncthreads();
    {
      const int ch = tid >> 3, r = tid & 7;
      u4v o = *(const u4v*)(dsm + L_ST + (ch * 9 + r) * 16);
      const int mt = row0 >> 4, r16 = (row0 & 8) + r;
      ST128(o, OFF_H2F + ((mt * 16 + (ch >> 2)) * 64 + (ch & 3) * 16 + r16) * 16);
    }
  }
  STAMP(3, 3);
  DRAIN();
  __syncthreads();
  if (tid == 0) __hip_atomic_store(myA, 1u, __ATOMIC_RELAXED, __HIP_MEMORY_SCOPE_AGENT);
  {
    const int ch = tid >> 3, r = tid & 7;
    u4v t = LD128(OFF_H2F + (((row0 >> 4) * 16 + (ch >> 2)) * 64 + (ch & 3) * 16 + (row0 & 8) + r) * 16);
    asm volatile("" ::"v"(t));
  }
  STAMP(3, 4);
  wait_pair(depA);
  STAMP(3, 5);
  u4v rb0, rb1;
  {
    u4v B[16];
#pragma unroll
    for (int ks = 0; ks < G1WIN; ++ks) B[ks] = LD128(OFF_H2F + (((8 * p + w) * 16 + ks) * 64 + lane) * 16);
    f4 acc[7];
#pragma unroll
    for (int ft = 0; ft < 7; ++ft) acc[ft] = f4{0.f, 0.f, 0.f, 0.f};
    const char* lw = dsm + L_W1 + lane * 16;
    unsigned off2 = L_W1 + lane * 16 + 8 * 7168;
    asm volatile("" : "+v"(off2));
    const char* lw2 = dsm + off2;
    const int e = c >> 1, odd = c & 1;
    const int hb = (int)OFF_HF + (((8 * p + w) * 56 + 7 * e) * 64 + lane) * 16;
    u4v Af[2][8];
#pragma unroll
    for (int j = 0; j < 8; ++j) Af[0][j] = *(const u4v*)(lw + (g1_ks(0, j) * 7 + g1_ft(0, j)) * 1024);
#pragma unroll
    for (int g = 0; g < 14; ++g) {
#pragma unroll
      for (int j = 0; j < 8; ++j) {
        acc[g1_ft(g, j)] = mfma16(Af[g & 1][j], B[g1_ks(g, j)], acc[g1_ft(g, j)]);
        if (g + 1 < 14)
          Af[(g + 1) & 1][j] = g1_ks(g + 1, j) < 8 ? *(const u4v*)(lw + (g1_ks(g + 1, j) * 7 + g1_ft(g + 1, j)) * 1024)
                                                   : *(const u4v*)(lw2 + ((g1_ks(g + 1, j) - 8) * 7 + g1_ft(g + 1, j)) * 1024);
        SCHED();
      }
#pragma unroll
      for (int q = 0; q < 2; ++q)
        if (2 * g + G1WIN + q < 16)
          B[2 * g + G1WIN + q] = LD128(OFF_H2F + (((8 * p + w) * 16 + 2 * g + G1WIN + q) * 64 + lane) * 16);
      if (g >= 5 && g < 10) { pf_load(24 + 2 * g); pf_load(25 + 2 * g); }
      if (g == 8 || g == 9 || g == 12) {
        const int i = g == 12 ? 2 : g - 8;
        f4 a = acc[2 * i], b = acc[2 * i + 1];
        u4v o = u4v{silu_pk(a[0], a[1]), silu_pk(a[2], a[3]), silu_pk(b[0], b[1]), silu_pk(b[2], b[3])};
        ST128(o, hb + (odd * 3 + i) * 1024);
      }
      if (g == 11) {
        DRAIN();
        asm volatile("s_barrier" ::: "memory");
        if (tid == 0) __hip_atomic_store(myE, 1u, __ATOMIC_RELAXED, __HIP_MEMORY_SCOPE_AGENT);
        rb0 = LD128(hb + odd * 3072);
        rb1 = LD128(hb + odd * 3072 + 1024);
      }
      SCHED();
      if (g == 7) STAMP(3, 6);
    }
    wait_pair(depE);
    asm volatile("" ::"v"(rb0), "v"(rb1));
    f4 a = acc[6];
    u2v o = u2v{silu_pk(a[0], a[1]), silu_pk(a[2], a[3])};
    ST64(o, hb + 6 * 1024 + odd * 8);
  }
  STAMP(3, 7);
  {
    const int Bb = (int)OFF_HF + ((mt0 * 56 + 7 * w) * 64 + lane) * 16;
    u4v B[7][4];
    constexpr int KO[7] = {0, 1, 3, 4, 2, 5, 6};
    SCHED();
#pragma unroll
    for (int i = 0; i < 2; ++i)
#pragma unroll
      for (int mt = 0; mt < 4; ++mt) B[i][mt] = LD128(Bb + (mt * 56 + KO[i]) * 1024);
    SCHED();
    asm volatile("s_waitcnt vmcnt(8) lgkmcnt(0)\n\ts_barrier" ::: "memory");
    if (tid == 0) __hip_atomic_store(myB, 1u, __ATOMIC_RELAXED, __HIP_MEMORY_SCOPE_AGENT);
    u4v rb2 = LD128((int)OFF_HF + (((8 * p + w) * 56 + 7 * (c >> 1) + 3 * (c & 1) + 2) * 64 + lane) * 16);
    STAMP(3, 8);
    f4 acc[4][4];
#pragma unroll
    for (int i = 0; i < 4; ++i)
#pragma unroll
      for (int j = 0; j < 4; ++j) acc[i][j] = f4{0.f, 0.f, 0.f, 0.f};
#pragma unroll
    for (int i = 2; i < 4; ++i)
#pragma unroll
      for (int mt = 0; mt < 4; ++mt) B[i][mt] = LD128(Bb + (mt * 56 + KO[i]) * 1024);
    SCHED();
#pragma unroll
    for (int i = 0; i < 7; ++i) {
      if (i == 2) {
        wait_pair(depB);
        asm volatile("" ::"v"(rb2));
#pragma unroll
        for (int q = 4; q < 7; ++q)
#pragma unroll
          for (int mt = 0; mt < 4; ++mt) B[q][mt] = LD128(Bb + (mt * 56 + KO[q]) * 1024);
        SCHED();
      }
#pragma unroll
      for (int nt = 0; nt < 4; ++nt)
#pragma unroll
        for (int mt = 0; mt < 4; ++mt) acc[nt][mt] = mfma16(A2[KO[i] * 4 + nt], B[i][mt], acc[nt][mt]);
      SCHED();
    }
    STAMP(3, 9);
    const int g = lane >> 4, mr = lane & 15;
    const int fm = w >> 1, P = w & 1;
    const int m = (mt0 + fm) * 16 + mr;
    const int n = nt0 * 16 + 32 * P + 8 * g;
    unsigned xv[4];
#pragma unroll
    for (int i = 0; i < 4; ++i)
      xv[i] = __builtin_amdgcn_raw_buffer_load_b32(
          rs, (int)OFF_X1 + (m * 64 + (((n >> 1) + i) & 63)) * 16 + (n >> 7) * 4, 0, AUX_SC1);
    const f4 g0 = __builtin_bit_cast(f4, LD128(OFF_MODF + (2560 + n) * 4));
    const f4 g1 = __builtin_bit_cast(f4, LD128(OFF_MODF + (2560 + n + 4) * 4));
    f4* red = (f4*)dsm;
#pragma unroll
    for (int nt = 0; nt < 4; ++nt)
#pragma unroll
      for (int mt = 0; mt < 4; ++mt) red[(w * 16 + nt * 4 + mt) * 64 + lane] = acc[nt][mt];
    __syncthreads();
    STAMP(3, 10);
    f4 s0 = f4{0.f, 0.f, 0.f, 0.f}, s1 = s0;
#pragma unroll
    for (int ww = 0; ww < 8; ++ww) {
      s0 += red[(ww * 16 + (2 * P) * 4 + fm) * 64 + lane];
      s1 += red[(ww * 16 + (2 * P + 1) * 4 + fm) * 64 + lane];
    }
    u4v o;
    {
      f2 t0 = bfr2(f2{g0[0], g0[1]} * bfr2(f2{s0[0], s0[1]})), t1 = bfr2(f2{g0[2], g0[3]} * bfr2(f2{s0[2], s0[3]}));
      f2 t2 = bfr2(f2{g1[0], g1[1]} * bfr2(f2{s1[0], s1[1]})), t3 = bfr2(f2{g1[2], g1[3]} * bfr2(f2{s1[2], s1[3]}));
      o[0] = pack_bf(bf_lo(xv[0]) + t0.x, bf_hi(xv[0]) + t0.y);
      o[1] = pack_bf(bf_lo(xv[1]) + t1.x, bf_hi(xv[1]) + t1.y);
      o[2] = pack_bf(bf_lo(xv[2]) + t2.x, bf_hi(xv[2]) + t2.y);
      o[3] = pack_bf(bf_lo(xv[3]) + t3.x, bf_hi(xv[3]) + t3.y);
    }
    const auto ro = __builtin_amdgcn_make_buffer_rsrc(out, 0, S_ * D_ * 2, 0x00020000);
    __builtin_amdgcn_raw_buffer_store_b128(o, ro, (m * D_ + n) * 2, 0, AUX_SC1);
  }
  STAMP(3, 11);
}

extern "C" void kernel_launch(void* const* d_in, const int* in_sizes, int n_in, void* d_out, int out_size, void* d_ws,
                              size_t ws_size, hipStream_t stream) {
  const u16* x = (const u16*)d_in[0];
  const u16* wr = (const u16*)d_in[1];
  const u16* wi = (const u16*)d_in[2];
  const u16* w1 = (const u16*)d_in[4];
  const u16* w2 = (const u16*)d_in[5];
  const u16* c = (const u16*)d_in[6];
  const u16* wmod = (const u16*)d_in[7];
  char* ws = (char*)d_ws + ((ws_size - (size_t)WS_SPAN) & ~(size_t)((1 << 21) - 1));
  u16* out = (u16*)d_out;
  (void)hipFuncSetAttribute((const void*)kmega, hipFuncAttributeMaxDynamicSharedMemorySize, L_TOTAL);
  k0_mod<<<K0_GEMV + K0_WT + K0_W2 + K0_Z + K0_WARM, 512, 0, stream>>>(c, wmod, x, wr, wi, w1, w2, ws);
  kmega<<<256, 512, L_TOTAL, stream>>>(x, w1, w2, ws, out);
}
```

```cpp
#include <hip/hip_runtime.h>
#include <hip/hip_bf16.h>
#include <stdint.h>

typedef float f2 __attribute__((ext_vector_type(2)));
typedef float f4 __attribute__((ext_vector_type(4)));
typedef unsigned u2v __attribute__((ext_vector_type(2)));
typedef unsigned u4v __attribute__((ext_vector_type(4)));
typedef short bf16x8 __attribute__((ext_vector_type(8)));
typedef unsigned short u16;

constexpr int S_ = 2048, D_ = 512, F_ = 1792, DF_ = 257;
constexpr int NSPLIT = 4;

constexpr size_t OFF_CNT = 0;
#define FSPACE 16
#define FREP 16
constexpr int FSP = FSPACE, NREP = FREP;
constexpr size_t FTAB = (size_t)NREP * 256 * FSP;
constexpr size_t OFF_FLG = 16384;
constexpr size_t OFF_FLR = OFF_FLG + FTAB;
constexpr size_t FLAG_BYTES = OFF_FLR + FTAB;
constexpr size_t OFF_MODP = 900 << 10;
constexpr size_t OFF_MODF = OFF_MODP + (size_t)NSPLIT * 3072 * 4;
constexpr size_t OFF_MODC = OFF_MODF + 3072 * 4;
constexpr size_t OFF_WT = 1 << 20;
constexpr size_t OFF_XT = 4 << 20;
constexpr size_t OFF_ZT = 28 << 20;
constexpr size_t OFF_H2F = 12 << 20;
constexpr size_t OFF_X1 = 14 << 20;
constexpr size_t OFF_W2F = 18 << 20;
constexpr size_t OFF_HF = 20 << 20;
constexpr int WS_SPAN = 36 << 20;
#define CPITCH (2048 + 32)
constexpr int CP = CPITCH;
constexpr float FSC = 1.0f / (2.0f * 2048.0f * 512.0f);

__device__ __forceinline__ float bf_lo(unsigned u) { return __builtin_bit_cast(float, u << 16); }
__device__ __forceinline__ float bf_hi(unsigned u) { return __builtin_bit_cast(float, u & 0xffff0000u); }
__device__ __forceinline__ float bfr(float x) {
  return (float)(__bf16)x;
}
__device__ __forceinline__ unsigned pack_bf(float lo, float hi) {
  typedef __bf16 b2 __attribute__((ext_vector_type(2)));
  return __builtin_bit_cast(unsigned, __builtin_convertvector(f2{lo, hi}, b2));
}
__device__ __forceinline__ f2 cmul(f2 a, f2 b) {
  f2 t = a * f2{b.x, b.x};
  return __builtin_elementwise_fma(f2{a.y, a.x}, f2{-b.y, b.y}, t);
}
__device__ __forceinline__ f2 bfr2(f2 a) {
  unsigned u = pack_bf(a.x, a.y);
  asm("" : "+v"(u));
  return f2{bf_lo(u), bf_hi(u)};
}
#define PIN2(x) asm volatile("" : "+v"(x))
constexpr int AUX_SC1 = 16;
#define WS_RSRC(ws) __builtin_amdgcn_make_buffer_rsrc((ws), 0, WS_SPAN, 0x00020000)
#define ST128(v, off) __builtin_amdgcn_raw_buffer_store_b128((v), rs, (int)(off), 0, AUX_SC1)
#define ST64(v, off) __builtin_amdgcn_raw_buffer_store_b64((v), rs, (int)(off), 0, AUX_SC1)
#define ST32(v, off) __builtin_amdgcn_raw_buffer_store_b32((v), rs, (int)(off), 0, AUX_SC1)
#define LD64(off) __builtin_amdgcn_raw_buffer_load_b64(rs, (int)(off), 0, AUX_SC1)
#define LD32(off) __builtin_amdgcn_raw_buffer_load_b32(rs, (int)(off), 0, AUX_SC1)
#define LD128(off) __builtin_amdgcn_raw_buffer_load_b128(rs, (int)(off), 0, AUX_SC1)
__device__ __forceinline__ f2 twid(float rev) { return f2{__builtin_amdgcn_cosf(rev), -__builtin_amdgcn_sinf(rev)}; }

template <int DIR>
__device__ __forceinline__ void dft4(f2& c0, f2& c1, f2& c2, f2& c3) {
  f2 d0 = c0 + c2, d2 = c0 - c2, d1 = c1 + c3, e = c1 - c3;
  c0 = d0 + d1;
  c2 = d0 - d1;
  const f2 es = f2{e.y, e.x};
  c1 = __builtin_elementwise_fma(es, DIR < 0 ? f2{1.f, -1.f} : f2{-1.f, 1.f}, d2);
  c3 = __builtin_elementwise_fma(es, DIR < 0 ? f2{-1.f, 1.f} : f2{1.f, -1.f}, d2);
}

__device__ __forceinline__ void sw32(float& a, float& b) {
  auto r = __builtin_amdgcn_permlane32_swap(__builtin_bit_cast(unsigned, a), __builtin_bit_cast(unsigned, b), false, false);
  a = __builtin_bit_cast(float, (unsigned)r[0]);
  b = __builtin_bit_cast(float, (unsigned)r[1]);
}
__device__ __forceinline__ void sw16(float& a, float& b) {
  auto r = __builtin_amdgcn_permlane16_swap(__builtin_bit_cast(unsigned, a), __builtin_bit_cast(unsigned, b), false, false);
  a = __builtin_bit_cast(float, (unsigned)r[0]);
  b = __builtin_bit_cast(float, (unsigned)r[1]);
}
template <int CTRL, int BANK>
__device__ __forceinline__ float udpp(float old, float src) {
  return __builtin_bit_cast(float, __builtin_amdgcn_update_dpp(__builtin_bit_cast(int, old), __builtin_bit_cast(int, src),
                                                               CTRL, 0xf, BANK, false));
}
template <int CTRL>
__device__ __forceinline__ float mdpp(float src) {
  return __builtin_bit_cast(float, __builtin_amdgcn_mov_dpp(__builtin_bit_cast(int, src), CTRL, 0xf, 0xf, false));
}
__device__ __forceinline__ void xp_hi1(float& v0, float& v1, float& v2, float& v3) {
  sw32(v0, v2); sw32(v1, v3); sw16(v0, v1); sw16(v2, v3);
}
__device__ __forceinline__ void xp_mid1(float& v0, float& v1, float& v2, float& v3) {
  float a0 = udpp<0x128, 0xC>(v0, v2), a2 = udpp<0x128, 0x3>(v2, v0);
  float a1 = udpp<0x128, 0xC>(v1, v3), a3 = udpp<0x128, 0x3>(v3, v1);
  v0 = udpp<0x124, 0xA>(a0, a1); v1 = udpp<0x12C, 0x5>(a1, a0);
  v2 = udpp<0x124, 0xA>(a2, a3); v3 = udpp<0x12C, 0x5>(a3, a2);
}
#define XP_CPLX(NAME, FN, ...)                                        \
  {                                                                   \
    float x0 = v[0].x, x1 = v[1].x, x2 = v[2].x, x3 = v[3].x;         \
    float y0 = v[0].y, y1 = v[1].y, y2 = v[2].y, y3 = v[3].y;         \
    FN(x0, x1, x2, x3, ##__VA_ARGS__);                                \
    FN(y0, y1, y2, y3, ##__VA_ARGS__);                                \
    v[0] = f2{x0, y0}; v[1] = f2{x1, y1}; v[2] = f2{x2, y2}; v[3] = f2{x3, y3}; \
  }
__device__ __forceinline__ void xp_hi(f2 (&v)[4]) XP_CPLX(hi, xp_hi1)
__device__ __forceinline__ void xp_mid(f2 (&v)[4]) XP_CPLX(mid, xp_mid1)

template <bool CONJ>
__device__ __forceinline__ void cmul3(f2 (&v)[4], const f2 (&t)[3]) {
  f2 a, b, c;
  if (!CONJ)
    asm("v_pk_mul_f32 %3, %0, %6 op_sel_hi:[1,0]\n\t"
        "v_pk_mul_f32 %4, %1, %7 op_sel_hi:[1,0]\n\t"
        "v_pk_mul_f32 %5, %2, %8 op_sel_hi:[1,0]\n\t"
        "v_pk_fma_f32 %0, %0, %6, %3 op_sel:[1,1,0] op_sel_hi:[0,1,1] neg_lo:[0,1,0]\n\t"
        "v_pk_fma_f32 %1, %1, %7, %4 op_sel:[1,1,0] op_sel_hi:[0,1,1] neg_lo:[0,1,0]\n\t"
        "v_pk_fma_f32 %2, %2, %8, %5 op_sel:[1,1,0] op_sel_hi:[0,1,1] neg_lo:[0,1,0]\n\t"
        "s_nop 1"
        : "+v"(v[1]), "+v"(v[2]), "+v"(v[3]), "=&v"(a), "=&v"(b), "=&v"(c)
        : "v"(t[0]), "v"(t[1]), "v"(t[2]));
  else
    asm("v_pk_mul_f32 %3, %0, %6 op_sel_hi:[1,0]\n\t"
        "v_pk_mul_f32 %4, %1, %7 op_sel_hi:[1,0]\n\t"
        "v_pk_mul_f32 %5, %2, %8 op_sel_hi:[1,0]\n\t"
        "v_pk_fma_f32 %0, %0, %6, %3 op_sel:[1,1,0] op_sel_hi:[0,1,1] neg_hi:[0,1,0]\n\t"
        "v_pk_fma_f32 %1, %1, %7, %4 op_sel:[1,1,0] op_sel_hi:[0,1,1] neg_hi:[0,1,0]\n\t"
        "v_pk_fma_f32 %2, %2, %8, %5 op_sel:[1,1,0] op_sel_hi:[0,1,1] neg_hi:[0,1,0]"
        : "+v"(v[1]), "+v"(v[2]), "+v"(v[3]), "=&v"(a), "=&v"(b), "=&v"(c)
        : "v"(t[0]), "v"(t[1]), "v"(t[2]));
}
template <bool CONJ>
__device__ __forceinline__ f2 cmul1(f2 v, f2 t) {
  f2 a;
  if (!CONJ)
    asm("v_pk_mul_f32 %1, %0, %2 op_sel_hi:[1,0]\n\t"
        "v_pk_fma_f32 %0, %0, %2, %1 op_sel:[1,1,0] op_sel_hi:[0,1,1] neg_lo:[0,1,0]"
        : "+v"(v), "=&v"(a) : "v"(t));
  else
    asm("v_pk_mul_f32 %1, %0, %2 op_sel_hi:[1,0]\n\t"
        "v_pk_fma_f32 %0, %0, %2, %1 op_sel:[1,1,0] op_sel_hi:[0,1,1] neg_hi:[0,1,0]"
        : "+v"(v), "=&v"(a) : "v"(t));
  return v;
}
__device__ __forceinline__ void xp_lo_lds(f2 (&v)[4], f2* patch, int lane) {
  f2* q = patch + (lane >> 2) * 16;
  const int c = lane & 3, sw = (lane >> 2) & 3;
#pragma unroll
  for (int j = 0; j < 4; ++j) q[4 * (j ^ sw) + c] = v[j];
  __builtin_amdgcn_fence(__ATOMIC_RELEASE, "wavefront");
  __builtin_amdgcn_wave_barrier();
  __builtin_amdgcn_fence(__ATOMIC_ACQUIRE, "wavefront");
  const f4 a = *(const f4*)(q + 4 * (c ^ sw)), b = *(const f4*)(q + 4 * (c ^ sw) + 2);
  v[0] = f2{a[0], a[1]}; v[1] = f2{a[2], a[3]}; v[2] = f2{b[0], b[1]}; v[3] = f2{b[2], b[3]};
  __builtin_amdgcn_fence(__ATOMIC_RELEASE, "wavefront");
  __builtin_amdgcn_wave_barrier();
}

struct Tw256 {
  f2 a[3], b[3], c[3];
};
__device__ __forceinline__ void make_tw256(int lane, Tw256& t) {
#pragma unroll
  for (int r = 1; r < 4; ++r) {
    t.a[r - 1] = twid((float)(lane * r) * (1.0f / 256.0f));
    t.b[r - 1] = twid((float)((lane & 15) * r) * (1.0f / 64.0f));
    t.c[r - 1] = twid((float)((lane & 3) * r) * (1.0f / 16.0f));
  }
#pragma unroll
  for (int r = 0; r < 3; ++r) {
    PIN2(t.a[r]);
    PIN2(t.b[r]);
    PIN2(t.c[r]);
  }
}
struct NoHook {
  __device__ __forceinline__ void operator()(int) const {}
};
template <class H>
__device__ __forceinline__ void fft256_fwd(f2 (&v)[4], int lane, const Tw256& t, f2* patch, const H& hook) {
  dft4<-1>(v[0], v[1], v[2], v[3]);
  cmul3<false>(v, t.a);
  xp_hi(v);
  hook(0);
  dft4<-1>(v[0], v[1], v[2], v[3]);
  cmul3<false>(v, t.b);
  xp_mid(v);
  hook(1);
  dft4<-1>(v[0], v[1], v[2], v[3]);
  cmul3<false>(v, t.c);
  xp_lo_lds(v, patch, lane);
  hook(2);
  dft4<-1>(v[0], v[1], v[2], v[3]);
}
template <class H>
__device__ __forceinline__ void fft256_inv(f2 (&v)[4], int lane, const Tw256& t, f2* patch, const H& hook) {
  dft4<1>(v[0], v[1], v[2], v[3]);
  xp_lo_lds(v, patch, lane);
  hook(0);
  cmul3<true>(v, t.c);
  dft4<1>(v[0], v[1], v[2], v[3]);
  xp_mid(v);
  hook(1);
  cmul3<true>(v, t.b);
  dft4<1>(v[0], v[1], v[2], v[3]);
  xp_hi(v);
  hook(2);
  cmul3<true>(v, t.a);
  dft4<1>(v[0], v[1], v[2], v[3]);
}

__device__ __forceinline__ float wave_sum(float x) {
  x += mdpp<0xB1>(x);
  x += mdpp<0x4E>(x);
  x += mdpp<0x141>(x);
  x += mdpp<0x140>(x);
  float r0 = __builtin_bit_cast(float, __builtin_amdgcn_readlane(__builtin_bit_cast(int, x), 0));
  float r1 = __builtin_bit_cast(float, __builtin_amdgcn_readlane(__builtin_bit_cast(int, x), 16));
  float r2 = __builtin_bit_cast(float, __builtin_amdgcn_readlane(__builtin_bit_cast(int, x), 32));
  float r3 = __builtin_bit_cast(float, __builtin_amdgcn_readlane(__builtin_bit_cast(int, x), 48));
  return (r0 + r1) + (r2 + r3);
}

__device__ __forceinline__ void ln_stats(const f2 (&v)[4], float& mean, float& rden) {
  const float K = __builtin_bit_cast(float, __builtin_amdgcn_readfirstlane(__builtin_bit_cast(int, v[0].x)));
  f2 s2 = f2{0.f, 0.f}, q2 = f2{0.f, 0.f};
#pragma unroll
  for (int j = 0; j < 4; ++j) {
    const f2 d = v[j] - K;
    s2 += d;
    q2 = __builtin_elementwise_fma(d, d, q2);
  }
  const float sm = wave_sum(s2.x + s2.y) * (1.0f / D_), qm = wave_sum(q2.x + q2.y) * (1.0f / D_);
  const float var = fmaxf(qm - sm * sm, 0.f);
  mean = bfr(K + sm);
  rden = __builtin_amdgcn_rcpf(bfr(__builtin_amdgcn_sqrtf(bfr(bfr(var) + 1e-5f))));
}

__device__ __forceinline__ int zpos(int k) { return 17 * (k & 3) + 4 * ((k >> 2) & 3) + ((k >> 4) & 3) + 68 * (k >> 6); }
constexpr int ZRS = 276;

constexpr int K0_GEMV = 48 * NSPLIT, K0_WT = 128, K0_W2 = 112, K0_Z = (int)(FLAG_BYTES / 8192);
__global__ __launch_bounds__(512) void k0_mod(const u16* __restrict__ c, const u16* __restrict__ wmod,
                                              const u16* __restrict__ x, const u16* __restrict__ wr,
                                              const u16* __restrict__ wi, const u16* __restrict__ w1,
                                              const u16* __restrict__ w2, char* __restrict__ ws) {
  __shared__ __attribute__((aligned(16))) char smem[17408];
  const int tid = threadIdx.x, row = tid >> 6, lane = tid & 63;
  const auto rs = WS_RSRC(ws);
  int b = blockIdx.x;
  if (b >= K0_GEMV + K0_WT + K0_W2) {
    int q = b - (K0_GEMV + K0_WT + K0_W2);
    if (q < K0_Z) {
      ((u4v*)ws)[q * 512 + tid] = u4v{0u, 0u, 0u, 0u};
      return;
    }
    return;
  }
  if (b >= K0_GEMV) {
    b -= K0_GEMV;
    if (b < K0_WT) {
      char* sr = smem;
      char* si = smem + 16 * 544;
      constexpr int LASTCH = (S_ * DF_ * 2) / 16 - 1;
      for (int it = tid; it < 16 * 34; it += 512) {
        const int i = it / 34, ch = it % 34;
        int gch = ((b + 128 * i) * (DF_ * 2)) / 16 + ch;
        gch = gch > LASTCH ? LASTCH : gch;
        *(u4v*)(sr + i * 544 + ch * 16) = *(const u4v*)((const char*)wr + (size_t)gch * 16);
        *(u4v*)(si + i * 544 + ch * 16) = *(const u4v*)((const char*)wi + (size_t)gch * 16);
      }
      __syncthreads();
      const int tbase = 64 * (b & 7) + 16 * ((b >> 3) & 3) + 4 * ((b >> 5) & 3);
      for (int it = tid; it < DF_ * 4; it += 512) {
        const int k = it >> 2, r3 = it & 3;
        u4v o;
#pragma unroll
        for (int r4 = 0; r4 < 4; ++r4) {
          const int i = r3 + 4 * r4;
          const int off = i * 544 + (((b + 128 * i) * (DF_ * 2)) & 15) + 2 * k;
          o[r4] = (unsigned)*(const u16*)(sr + off) | ((unsigned)*(const u16*)(si + off) << 16);
        }
        ST128(o, OFF_WT + (k * 512 + tbase + r3) * 16);
      }
    } else {
      b -= K0_WT;
      u16* t = (u16*)smem;
      const int g = lane >> 4, r = lane & 15;
      const int ng = b & 3, kp = b >> 2;
#pragma unroll
      for (int it = 0; it < 2; ++it) {
        const int idx = it * 512 + tid;
        const int rr = idx >> 4, ch = idx & 15;
        const int ks2 = kp * 2 + (rr >> 5), gg = (rr >> 3) & 3, j = rr & 7;
        const int e = ks2 / 7, i7 = ks2 % 7;
        int T0, T1;
        if (i7 < 3) { T0 = 14 * e + 2 * i7; T1 = T0 + 1; }
        else if (i7 < 6) { T0 = 14 * e + 7 + 2 * (i7 - 3); T1 = T0 + 1; }
        else { T0 = 14 * e + 6; T1 = 14 * e + 13; }
        const int f = (j < 4) ? (16 * T0 + 4 * gg + j) : (16 * T1 + 4 * gg + (j - 4));
        *(u4v*)(t + rr * 136 + ch * 8) = *(const u4v*)(w2 + (size_t)f * D_ + ng * 128 + ch * 8);
      }
      __syncthreads();
      const int nl = 32 * (row >> 1) + 8 * (r >> 2) + 4 * (row & 1) + (r & 3);
#pragma unroll
      for (int ksl = 0; ksl < 2; ++ksl) {
        u4v o;
#pragma unroll
        for (int jj = 0; jj < 4; ++jj)
          o[jj] = (unsigned)t[(ksl * 32 + g * 8 + 2 * jj) * 136 + nl] |
                  ((unsigned)t[(ksl * 32 + g * 8 + 2 * jj + 1) * 136 + nl] << 16);
        const int nt = ng * 8 + row;
        ST128(o, OFF_W2F + ((nt * 56 + kp * 2 + ksl) * 64 + lane) * 16);
      }
    }
    return;
  }
  float* red = (float*)smem;
  const int cgp = b % 48, ks = b / 48;
  const int cg = tid & 7, r = tid >> 3;
  const int rw = ks * 128 + r;
  const u4v wm0 = *(const u4v*)(wmod + (size_t)rw * 3072 + cgp * 64 + cg * 8);
  const u4v wm1 = *(const u4v*)(wmod + (size_t)(rw + 64) * 3072 + cgp * 64 + cg * 8);
  const float cv0 = bf_lo(c[rw]), cv1 = bf_lo(c[rw + 64]);
  const float s0 = bfr(cv0 / (1.0f + __expf(-cv0))), s1 = bfr(cv1 / (1.0f + __expf(-cv1)));
  float acc[8];
#pragma unroll
  for (int i = 0; i < 4; ++i) {
    acc[2 * i] = s0 * bf_lo(wm0[i]) + s1 * bf_lo(wm1[i]);
    acc[2 * i + 1] = s0 * bf_hi(wm0[i]) + s1 * bf_hi(wm1[i]);
  }
#pragma unroll
  for (int i = 0; i < 8; ++i) {
    acc[i] += mdpp<0x128>(acc[i]);
    acc[i] += __shfl_xor(acc[i], 16);
    acc[i] += __shfl_xor(acc[i], 32);
  }
  if (lane < 8) {
#pragma unroll
    for (int i = 0; i < 8; ++i) red[row * 64 + cg * 8 + i] = acc[i];
  }
  __syncthreads();
  if (tid < 64) {
    float sum = 0.f;
#pragma unroll
    for (int i = 0; i < 8; ++i) sum += red[i * 64 + tid];
    ((float*)(ws + OFF_MODP))[ks * 3072 + cgp * 64 + tid] = sum;
  }
}

__device__ __forceinline__ f4 mfma16(u4v a, u4v b, f4 c) {
  return __builtin_amdgcn_mfma_f32_16x16x32_bf16(__builtin_bit_cast(bf16x8, a), __builtin_bit_cast(bf16x8, b), c, 0, 0,
                                                  0);
}
__device__ __forceinline__ f2 silu2(float a, float b) {
  f2 h = f2{a, b};
  f2 t = h * -1.44269504088896340736f;
  f2 d = f2{__builtin_amdgcn_exp2f(t.x), __builtin_amdgcn_exp2f(t.y)} + 1.0f;
  return h * f2{__builtin_amdgcn_rcpf(d.x), __builtin_amdgcn_rcpf(d.y)};
}
__device__ __forceinline__ unsigned silu_pk(float a, float b) {
  f2 r = silu2(a, b);
  return pack_bf(r.x, r.y);
}
typedef __attribute__((address_space(1))) unsigned gu32;
#define DRAIN() asm volatile("s_waitcnt vmcnt(0)" ::: "memory")
typedef __attribute__((address_space(1))) unsigned long long gu64;
constexpr unsigned SPIN_CAP = 1u << 18;
__device__ __forceinline__ void wait_pair(gu64* flags) {
#pragma nounroll
  for (unsigned spin = 0; spin < SPIN_CAP; ++spin) {
    if (__hip_atomic_load(flags, __ATOMIC_RELAXED, __HIP_MEMORY_SCOPE_AGENT) == 0x0000000100000001ull) break;
    __builtin_amdgcn_s_sleep(1);
  }
  __builtin_amdgcn_fence(__ATOMIC_ACQUIRE, "wavefront");
  asm volatile("" ::: "memory");
}

#define PSLEEP 2
__device__ __forceinline__ void wait_cols(gu32* flags) {
#pragma nounroll
  for (unsigned spin = 0; spin < SPIN_CAP; ++spin) {
    unsigned f = 1u;
#pragma unroll
    for (int j = 0; j < 4; ++j)
      f &= __hip_atomic_load(flags + j * 64 * (FSP / 4), __ATOMIC_RELAXED, __HIP_MEMORY_SCOPE_AGENT);
    if (__builtin_amdgcn_ballot_w64(f != 1u) == 0ull) break;
    __builtin_amdgcn_s_sleep(PSLEEP);
  }
}

__device__ __forceinline__ int wt_off(int k, int t) { return (int)OFF_WT + (k * 512 + t) * 16; }

constexpr int L_W1 = 0;
constexpr int L_T = 112 * 1024;
constexpr int L_ZS = 112 * 1024;
constexpr int L_ST = L_ZS + 8 * ZRS * 8;
constexpr int L_TOTAL = 160 * 1024;
__device__ __forceinline__ constexpr int g1_ft(int g, int j) { return g < 8 ? (j & 3) : (g < 12 ? 4 + (j & 1) : 6); }
__device__ __forceinline__ constexpr int g1_ks(int g, int j) {
  return g < 8 ? 2 * g + (j >> 2) : (g < 12 ? 4 * (g - 8) + (j >> 1) : 8 * (g - 12) + j);
}
#define G1WIN 6
#define SCHED() __builtin_amdgcn_sched_barrier(0)
typedef short v4i16 __attribute__((ext_vector_type(4)));
__device__ __forceinline__ u4v w1frag(const char* lw, int ft, int ks) {
  typedef __attribute__((address_space(3))) v4i16* lp;
  const u2v a = __builtin_bit_cast(u2v, __builtin_amdgcn_ds_read_tr16_b64_v4i16((lp)(lw + (2 * ks) * 3584 + ft * 32)));
  const u2v b = __builtin_bit_cast(u2v, __builtin_amdgcn_ds_read_tr16_b64_v4i16((lp)(lw + (2 * ks + 1) * 3584 + ft * 32)));
  return u4v{a[0], a[1], b[0], b[1]};
}
constexpr int L_MODS = L_ZS + 8 * ZRS * 8;
constexpr int L_PX = L_T + 28672;
__global__ __launch_bounds__(512, 2) void kmega(const u16* __restrict__ x, const u16* __restrict__ w1,
                                                const u16* __restrict__ w2, char* ws, u16* __restrict__ out) {
  extern __shared__ __attribute__((aligned(16))) char dsm[];
  const int tid = threadIdx.x, lane = tid & 63;
  const int w = __builtin_amdgcn_readfirstlane(tid >> 6);
  const int bid = blockIdx.x;
  const int p = (bid & 7) * 2 + (bid >> 7), c = (bid >> 3) & 15;
  const int row0 = 128 * p + 8 * c;
  unsigned cw0, cw1, cw2;
  {
    unsigned long long pc;
    asm volatile("s_getpc_b64 %0" : "=s"(pc));
    const char* code = (const char*)(pc & ~255ull) + (tid < 272 ? tid : 0) * 64;
    cw0 = *(const unsigned*)(code);
    cw1 = cw0; cw2 = cw0;
  }
  const auto rs = WS_RSRC(ws);
  gu32* myA = (gu32*)(ws + OFF_CNT) + (p * 8 + (c >> 1)) * 16 + (c & 1);
  gu32* myB = myA + 128 * 16;
  gu64* depA = (gu64*)(ws + OFF_CNT) + (p * 8 + w) * 8;
  gu64* depB = depA + 128 * 8;
  gu32* myE = myB + 8;
  gu64* depE = depB + 4;
  const int mt0 = 8 * p + 4 * (c >> 3), nt0 = 4 * (c & 7);
  const int l56 = lane < 56 ? lane : lane - 56;
  const int r4_ = l56 / 14, ch_ = l56 - 14 * r4_;
  const char* w1g = (const char*)w1 + (size_t)(4 * w + r4_) * (F_ * 2) + c * 224 + ch_ * 16;
  char* w1l = dsm + L_W1 + ((w >> 1) * 32 + (2 * (w & 1) + (r4_ >> 1)) * 4 + (r4_ & 1)) * 224 + ch_ * 16;
  const int Ab = (int)OFF_W2F + ((nt0 * 56 + 7 * w) * 64 + lane) * 16;
  u4v S1[16];
  u4v A2[28];
  auto pf_load = [&](int i) __attribute__((always_inline)) {
    if (i < 16) S1[i] = *(const u4v*)(w1g + (size_t)i * (32 * F_ * 2));
    else if (i < 44) A2[i - 16] = *(const u4v*)(ws + Ab + (((i - 16) & 3) * 56 + ((i - 16) >> 2)) * 1024);
  };
  auto pf_put = [&](int i) __attribute__((always_inline)) {
    *(u4v*)(w1l + ((i & 3) * 128 + (i >> 3) * 16 + ((i >> 2) & 1) * 2) * 224) = S1[i];
  };
  auto pf = [&](int i0, int n) __attribute__((always_inline)) {
    SCHED();
#pragma unroll
    for (int i = 0; i < n; ++i) pf_load(i0 + i);
    SCHED();
  };
  Tw256 tw;
  {
    f2* zs = (f2*)(dsm + L_ZS);
    float* mods = (float*)(dsm + L_MODS);
    const int r0 = bid * 8;
    unsigned xr[4];
    const unsigned* xrow = (const unsigned*)(x + (size_t)(r0 + w) * D_);
#pragma unroll
    for (int j = 0; j < 4; ++j) xr[j] = xrow[lane + 64 * j];
    const float* mp = (const float*)(ws + OFF_MODP);
    f2 sm = *(const f2*)(mp + tid * 2);
#pragma unroll
    for (int k = 1; k < NSPLIT; ++k) sm += *(const f2*)(mp + k * 3072 + tid * 2);
    const int idx = bid * 12 + (tid < 6 ? tid : 0) * 2;
    f2 fin = *(const f2*)(mp + idx);
#pragma unroll
    for (int k = 1; k < NSPLIT; ++k) fin += *(const f2*)(mp + k * 3072 + idx);
    pf(0, 4);
    make_tw256(lane, tw);
    const int rp = tid & 3, kk = tid >> 2;
    f2 wpost = twid((float)kk * (1.0f / 512.0f));
    PIN2(wpost);
    f2 v[4];
#pragma unroll
    for (int j = 0; j < 4; ++j) v[j] = f2{bf_lo(xr[j]), bf_hi(xr[j])};
    float mean, rden;
    ln_stats(v, mean, rden);
    asm volatile("" ::"v"(cw0), "v"(cw1), "v"(cw2));
    sm = bfr2(sm);
    if (tid >= 256) sm = bfr2(sm + 1.0f);
    *(f2*)(mods + tid * 2) = sm;
    if (tid < 6) {
      fin = bfr2(fin);
      ST64(__builtin_bit_cast(u2v, fin), OFF_MODF + idx * 4);
      const int vec = (idx >> 9) - 2, d = idx & 511;
      if (vec >= 0 && vec < 3) {
        if (vec == 2) fin += 1.0f;
        ST32(pack_bf(fin.x, fin.y), OFF_MODC + ((vec * 64 + ((d >> 1) & 63)) * 4 + (d >> 7)) * 4);
      }
    }
    __syncthreads();
    pf(4, 4);
#pragma unroll
    for (int j = 0; j < 4; ++j) {
      const int d = 2 * (lane + 64 * j);
      f2 sa = *(const f2*)(mods + d);
      f2 ca1 = *(const f2*)(mods + 512 + d);
      v[j] = bfr2(__builtin_elementwise_fma(v[j] - mean, ca1 * rden, sa));
    }
    fft256_fwd(v, lane, tw, (f2*)(dsm + L_PX + w * 2048), NoHook{});
    {
      f2* buf = zs + w * ZRS + lane + (lane >> 4);
#pragma unroll
      for (int r4 = 0; r4 < 4; ++r4) buf[68 * r4] = v[r4];
    }
    __syncthreads();
    {
      const int k = kk;
      const int k2 = (k == 0) ? 128 : 256 - k;
      const int pa = zpos(k), pb = zpos(k2);
      f4 oa, ob;
#pragma unroll
      for (int h = 0; h < 2; ++h) {
        const f2* zr = zs + (2 * rp + h) * ZRS;
        f2 za = zr[pa], zb = zr[pb];
        f2 E = f2{za.x + zb.x, za.y - zb.y};
        f2 Dm = f2{za.x - zb.x, za.y + zb.y};
        f2 O = f2{Dm.y, -Dm.x};
        f2 wo = cmul1<false>(O, wpost);
        f2 ra = E + wo;
        f2 t = E - wo;
        f2 rb = f2{t.x, -t.y};
        if (k == 0) {
          ra = f2{2.f * (za.x + za.y), 2.f * (za.x - za.y)};
          rb = f2{2.f * zb.x, -2.f * zb.y};
        }
        oa[2 * h] = ra.x; oa[2 * h + 1] = ra.y;
        ob[2 * h] = rb.x; ob[2 * h + 1] = rb.y;
      }
      ST128(__builtin_bit_cast(u4v, oa), OFF_XT + (k * CP + r0 + 2 * rp) * 8);
      ST128(__builtin_bit_cast(u4v, ob), OFF_XT + (k2 * CP + r0 + 2 * rp) * 8);
    }
    DRAIN();
    __syncthreads();
    if (tid < NREP)
      __hip_atomic_store((gu32*)(ws + OFF_FLR + (tid * 256 + bid) * FSP), 1u, __ATOMIC_RELAXED, __HIP_MEMORY_SCOPE_AGENT);
  }
  {
    f2* T = (f2*)(dsm + L_T);
    f2* T2 = (f2*)(dsm + L_T + 16384);
    f2* T3 = (f2*)(dsm + L_T + 32768);
    const int k = bid;
    f2 tw1[4];
#pragma unroll
    for (int j = 0; j < 4; ++j) {
      tw1[j] = twid((float)((lane + 64 * j) * w) * (1.0f / 2048.0f));
      PIN2(tw1[j]);
    }
    pf(28, 3);
    if (w == 0) wait_cols((gu32*)(ws + OFF_FLR + ((bid % NREP) * 256 + lane) * FSP));
    asm volatile("s_barrier" ::: "memory");
    __builtin_amdgcn_fence(__ATOMIC_ACQUIRE, "wavefront");
    f2 v[4];
#pragma unroll
    for (int j = 0; j < 4; ++j) v[j] = __builtin_bit_cast(f2, LD64(OFF_XT + (k * CP + tid + 512 * j) * 8));
    u4v bv;
    unsigned wn[4], bn[4];
    int mirr[4];
#pragma unroll
    for (int j = 0; j < 4; ++j) PIN2(v[j]);
    pf(8, 4);
    const int hb = w >> 2, a = tid & 255;
    constexpr float R = 0.70710678118654752440f;
    dft4<-1>(v[0], v[1], v[2], v[3]);
    if (hb) {
      v[1] = f2{v[1].x + v[1].y, v[1].y - v[1].x} * R;
      v[2] = f2{v[2].y, -v[2].x};
      v[3] = f2{v[3].y - v[3].x, -v[3].x - v[3].y} * R;
    }
#pragma unroll
    for (int q = 0; q < 4; ++q) T[(hb * 4 + q) * 256 + a] = v[q];
    const u4v wv = LD128(wt_off(k, tid));
    if (k == 0) {
      bv = LD128(wt_off(256, tid));
      const int m0 = (lane >> 4) + 4 * ((lane >> 2) & 3) + 16 * (lane & 3);
#pragma unroll
      for (int r4 = 0; r4 < 4; ++r4) {
        const int un = (S_ - (w + 8 * (m0 + 64 * r4))) & (S_ - 1);
        const int mm = un >> 3;
        mirr[r4] = ((un & 7) * 64 + 16 * (mm & 3) + 4 * ((mm >> 2) & 3) + ((mm >> 4) & 3)) * 4 + (mm >> 6);
      }
      if (w != 0) {
        const u4v a4 = LD128(wt_off(0, 64 * (8 - w) + 63 - lane));
        const u4v b4 = LD128(wt_off(256, 64 * (8 - w) + 63 - lane));
#pragma unroll
        for (int r4 = 0; r4 < 4; ++r4) { wn[r4] = a4[3 - r4]; bn[r4] = b4[3 - r4]; }
      } else {
#pragma unroll
        for (int r4 = 0; r4 < 4; ++r4) {
          wn[r4] = LD32(wt_off(0, mirr[r4] >> 2) + (mirr[r4] & 3) * 4);
          bn[r4] = LD32(wt_off(256, mirr[r4] >> 2) + (mirr[r4] & 3) * 4);
        }
      }
    }
    __syncthreads();
    {
      const int q = w & 3;
      const float sg = (w < 4) ? 1.f : -1.f;
#pragma unroll
      for (int j = 0; j < 4; ++j) {
        f2 e = T[q * 256 + lane + 64 * j], o = T[(4 + q) * 256 + lane + 64 * j];
        v[j] = cmul1<false>(e + o * sg, tw1[j]);
      }
    }
    fft256_fwd(v, lane, tw, T2 + w * 256, [&](int i) __attribute__((always_inline)) {
      SCHED();
      if (i < 2) {
#pragma unroll
        for (int q = 0; q < 4; ++q) pf_put(4 * i + q);
      } else {
#pragma unroll
        for (int q = 12; q < 16; ++q) pf_load(q);
      }
      SCHED();
    });
    if (k != 0) {
#pragma unroll
      for (int i = 0; i < 4; ++i) {
        v[i] = cmul1<false>(v[i], f2{bf_lo(wv[i]), bf_hi(wv[i])});
      }
    } else {
#pragma unroll
      for (int r4 = 0; r4 < 4; ++r4) T3[r4 * 512 + tid] = v[r4];
      __syncthreads();
#pragma unroll
      for (int r4 = 0; r4 < 4; ++r4) {
        f2 cm = T3[(mirr[r4] & 3) * 512 + (mirr[r4] >> 2)];
        f2 h0 = f2{bf_lo(wv[r4]) + bf_lo(wn[r4]), bf_hi(wv[r4]) - bf_hi(wn[r4])};
        f2 h1 = f2{bf_lo(bv[r4]) + bf_lo(bn[r4]), bf_hi(bv[r4]) - bf_hi(bn[r4])};
        f2 wp = (h0 + h1) * 0.25f, wm = (h0 - h1) * 0.25f;
        f2 cc = f2{cm.x, -cm.y};
        v[r4] = cmul(v[r4], wp) + cmul(cc, wm);
      }
    }
    fft256_inv(v, lane, tw, T2 + w * 256, [&](int i) __attribute__((always_inline)) {
      SCHED();
      if (i == 0) {
#pragma unroll
        for (int q = 8; q < 12; ++q) pf_put(q);
      }
      if (i == 1) {
#pragma unroll
        for (int q = 16; q < 20; ++q) pf_load(q);
      }
      SCHED();
    });
#pragma unroll
    for (int j = 0; j < 4; ++j) T2[w * 256 + lane + 64 * j] = cmul1<true>(v[j], tw1[j]);
#pragma unroll
    for (int q = 12; q < 16; ++q) pf_put(q);
    __syncthreads();
    {
      const float sg = hb ? -1.f : 1.f;
#pragma unroll
      for (int q = 0; q < 4; ++q) v[q] = T2[q * 256 + a] + T2[(q + 4) * 256 + a] * sg;
      if (hb) {
        v[1] = f2{v[1].x - v[1].y, v[1].y + v[1].x} * R;
        v[2] = f2{-v[2].y, v[2].x};
        v[3] = f2{-v[3].x - v[3].y, v[3].x - v[3].y} * R;
      }
    }
    dft4<1>(v[0], v[1], v[2], v[3]);
#pragma unroll
    for (int j = 0; j < 4; ++j) ST64(__builtin_bit_cast(u2v, v[j]), OFF_ZT + (k * CP + tid + 512 * j) * 8);
    DRAIN();
    __syncthreads();
    if (tid < NREP)
      __hip_atomic_store((gu32*)(ws + OFF_FLG + (tid * 256 + k) * FSP), 1u, __ATOMIC_RELAXED, __HIP_MEMORY_SCOPE_AGENT);
  }
  {
    const char* lw = dsm + L_W1 + (lane >> 2) * 224 + (lane & 3) * 8 + w * (2 * 7168);
    u4v fr[14];
#pragma unroll
    for (int i = 0; i < 14; ++i) fr[i] = w1frag(lw, i % 7, i / 7);
    asm volatile("" ::: "memory");
#pragma unroll
    for (int i = 0; i < 14; ++i) *(u4v*)(dsm + L_W1 + w * (2 * 7168) + i * 1024 + lane * 16) = fr[i];
  }
  {
    f2* zs = (f2*)(dsm + L_ZS);
    const int rp = tid & 3, k = tid >> 2;
    const int k2 = (k == 0) ? 128 : 256 - k;
    u4v xr;
    const unsigned* xrow = (const unsigned*)(x + (size_t)(row0 + w) * D_);
#pragma unroll
    for (int j = 0; j < 4; ++j) xr[j] = xrow[lane + 64 * j];
    const u4v gav = LD128(OFF_MODC + lane * 16);
    const u4v sbv = LD128(OFF_MODC + 1024 + lane * 16);
    const u4v cbv = LD128(OFF_MODC + 2048 + lane * 16);
    f2 wq = twid((float)k * (1.0f / 512.0f));
    PIN2(wq);
    const int pa = zpos(k), pb = zpos(k2);
    pf(31, 3);
    if (w == 0) wait_cols((gu32*)(ws + OFF_FLG + ((bid % NREP) * 256 + lane) * FSP));
    asm volatile("s_barrier" ::: "memory");
    __builtin_amdgcn_fence(__ATOMIC_ACQUIRE, "wavefront");
    const f4 ya = __builtin_bit_cast(f4, LD128(OFF_ZT + (k * CP + row0 + 2 * rp) * 8));
    const f4 yb = __builtin_bit_cast(f4, LD128(OFF_ZT + (k2 * CP + row0 + 2 * rp) * 8));
#pragma unroll
    for (int h = 0; h < 2; ++h) {
      f2 a = f2{ya[2 * h], ya[2 * h + 1]}, b = f2{yb[2 * h], yb[2 * h + 1]};
      f2* zr = zs + (2 * rp + h) * ZRS;
      f2 Ze = f2{a.x + b.x, a.y - b.y};
      f2 Dm = f2{a.x - b.x, a.y + b.y};
      f2 Zo = cmul1<true>(Dm, wq);
      f2 za = f2{Ze.x - Zo.y, Ze.y + Zo.x};
      f2 zb = f2{Ze.x + Zo.y, -Ze.y + Zo.x};
      if (k == 0) {
        za = f2{a.x + a.y, a.x - a.y};
        zb = f2{2.f * b.x, -2.f * b.y};
      }
      zr[pa] = za;
      zr[pb] = zb;
    }
    pf(20, 4);
    __syncthreads();
    f2 v[4];
    {
      const f2* buf = zs + w * ZRS + lane + (lane >> 4);
#pragma unroll
      for (int r4 = 0; r4 < 4; ++r4) v[r4] = buf[68 * r4];
    }
    fft256_inv(v, lane, tw, (f2*)(dsm + L_PX + w * 2048), NoHook{});
    pf(24, 4);
    u4v x1p;
#pragma unroll
    for (int j = 0; j < 4; ++j) {
      f2 t = __builtin_elementwise_fma(f2{bf_lo(gav[j]), bf_hi(gav[j])} * FSC, v[j], f2{bf_lo(xr[j]), bf_hi(xr[j])});
      x1p[j] = pack_bf(t.x, t.y);
      v[j] = f2{bf_lo(x1p[j]), bf_hi(x1p[j])};
    }
    ST128(x1p, OFF_X1 + ((row0 + w) * 64 + lane) * 16);
    float mean, rden;
    ln_stats(v, mean, rden);
    u4v hp;
#pragma unroll
    for (int j = 0; j < 4; ++j) {
      f2 t = __builtin_elementwise_fma(v[j] - mean, f2{bf_lo(cbv[j]), bf_hi(cbv[j])} * rden,
                                       f2{bf_lo(sbv[j]), bf_hi(sbv[j])});
      hp[j] = pack_bf(t.x, t.y);
    }
    *(u4v*)(dsm + L_ST + (lane * 9 + w) * 16) = hp;
    __syncthreads();
    {
      const int ch = tid >> 3, r = tid & 7;
      u4v o = *(const u4v*)(dsm + L_ST + (ch * 9 + r) * 16);
      const int mt = row0 >> 4, r16 = (row0 & 8) + r;
      ST128(o, OFF_H2F + ((mt * 16 + (ch >> 2)) * 64 + (ch & 3) * 16 + r16) * 16);
    }
  }
  DRAIN();
  __syncthreads();
  if (tid == 0) __hip_atomic_store(myA, 1u, __ATOMIC_RELAXED, __HIP_MEMORY_SCOPE_AGENT);
  {
    const int ch = tid >> 3, r = tid & 7;
    u4v t = LD128(OFF_H2F + (((row0 >> 4) * 16 + (ch >> 2)) * 64 + (ch & 3) * 16 + (row0 & 8) + r) * 16);
    asm volatile("" ::"v"(t));
  }
  wait_pair(depA);
  u4v rb0, rb1;
  {
    u4v B[16];
#pragma unroll
    for (int ks = 0; ks < G1WIN; ++ks) B[ks] = LD128(OFF_H2F + (((8 * p + w) * 16 + ks) * 64 + lane) * 16);
    f4 acc[7];
#pragma unroll
    for (int ft = 0; ft < 7; ++ft) acc[ft] = f4{0.f, 0.f, 0.f, 0.f};
    const char* lw = dsm + L_W1 + lane * 16;
    unsigned off2 = L_W1 + lane * 16 + 8 * 7168;
    asm volatile("" : "+v"(off2));
    const char* lw2 = dsm + off2;
    const int e = c >> 1, odd = c & 1;
    const int hb = (int)OFF_HF + (((8 * p + w) * 56 + 7 * e) * 64 + lane) * 16;
    u4v Af[2][8];
#pragma unroll
    for (int j = 0; j < 8; ++j) Af[0][j] = *(const u4v*)(lw + (g1_ks(0, j) * 7 + g1_ft(0, j)) * 1024);
#pragma unroll
    for (int g = 0; g < 14; ++g) {
#pragma unroll
      for (int j = 0; j < 8; ++j) {
        acc[g1_ft(g, j)] = mfma16(Af[g & 1][j], B[g1_ks(g, j)], acc[g1_ft(g, j)]);
        if (g + 1 < 14)
          Af[(g + 1) & 1][j] = g1_ks(g + 1, j) < 8 ? *(const u4v*)(lw + (g1_ks(g + 1, j) * 7 + g1_ft(g + 1, j)) * 1024)
                                                   : *(const u4v*)(lw2 + ((g1_ks(g + 1, j) - 8) * 7 + g1_ft(g + 1, j)) * 1024);
        SCHED();
      }
#pragma unroll
      for (int q = 0; q < 2; ++q)
        if (2 * g + G1WIN + q < 16)
          B[2 * g + G1WIN + q] = LD128(OFF_H2F + (((8 * p + w) * 16 + 2 * g + G1WIN + q) * 64 + lane) * 16);
      if (g >= 5 && g < 10) { pf_load(24 + 2 * g); pf_load(25 + 2 * g); }
      if (g == 8 || g == 9 || g == 12) {
        const int i = g == 12 ? 2 : g - 8;
        f4 a = acc[2 * i], b = acc[2 * i + 1];
        u4v o = u4v{silu_pk(a[0], a[1]), silu_pk(a[2], a[3]), silu_pk(b[0], b[1]), silu_pk(b[2], b[3])};
        ST128(o, hb + (odd * 3 + i) * 1024);
      }
      if (g == 11) {
        DRAIN();
        asm volatile("s_barrier" ::: "memory");
        if (tid == 0) __hip_atomic_store(myE, 1u, __ATOMIC_RELAXED, __HIP_MEMORY_SCOPE_AGENT);
        rb0 = LD128(hb + odd * 3072);
        rb1 = LD128(hb + odd * 3072 + 1024);
      }
      SCHED();
    }
    wait_pair(depE);
    asm volatile("" ::"v"(rb0), "v"(rb1));
    f4 a = acc[6];
    u2v o = u2v{silu_pk(a[0], a[1]), silu_pk(a[2], a[3])};
    ST64(o, hb + 6 * 1024 + odd * 8);
  }
  {
    const int Bb = (int)OFF_HF + ((mt0 * 56 + 7 * w) * 64 + lane) * 16;
    u4v B[7][4];
    constexpr int KO[7] = {0, 1, 3, 4, 2, 5, 6};
    SCHED();
#pragma unroll
    for (int i = 0; i < 2; ++i)
#pragma unroll
      for (int mt = 0; mt < 4; ++mt) B[i][mt] = LD128(Bb + (mt * 56 + KO[i]) * 1024);
    SCHED();
    asm volatile("s_waitcnt vmcnt(8) lgkmcnt(0)\n\ts_barrier" ::: "memory");
    if (tid == 0) __hip_atomic_store(myB, 1u, __ATOMIC_RELAXED, __HIP_MEMORY_SCOPE_AGENT);
    u4v rb2 = LD128((int)OFF_HF + (((8 * p + w) * 56 + 7 * (c >> 1) + 3 * (c & 1) + 2) * 64 + lane) * 16);
    f4 acc[4][4];
#pragma unroll
    for (int i = 0; i < 4; ++i)
#pragma unroll
      for (int j = 0; j < 4; ++j) acc[i][j] = f4{0.f, 0.f, 0.f, 0.f};
#pragma unroll
    for (int i = 2; i < 4; ++i)
#pragma unroll
      for (int mt = 0; mt < 4; ++mt) B[i][mt] = LD128(Bb + (mt * 56 + KO[i]) * 1024);
    SCHED();
#pragma unroll
    for (int i = 0; i < 7; ++i) {
      if (i == 2) {
        wait_pair(depB);
        asm volatile("" ::"v"(rb2));
#pragma unroll
        for (int q = 4; q < 7; ++q)
#pragma unroll
          for (int mt = 0; mt < 4; ++mt) B[q][mt] = LD128(Bb + (mt * 56 + KO[q]) * 1024);
        SCHED();
      }
#pragma unroll
      for (int nt = 0; nt < 4; ++nt)
#pragma unroll
        for (int mt = 0; mt < 4; ++mt) acc[nt][mt] = mfma16(A2[KO[i] * 4 + nt], B[i][mt], acc[nt][mt]);
      SCHED();
    }
    const int g = lane >> 4, mr = lane & 15;
    const int fm = w >> 1, P = w & 1;
    const int m = (mt0 + fm) * 16 + mr;
    const int n = nt0 * 16 + 32 * P + 8 * g;
    unsigned xv[4];
#pragma unroll
    for (int i = 0; i < 4; ++i)
      xv[i] = __builtin_amdgcn_raw_buffer_load_b32(
          rs, (int)OFF_X1 + (m * 64 + (((n >> 1) + i) & 63)) * 16 + (n >> 7) * 4, 0, AUX_SC1);
    const f4 g0 = __builtin_bit_cast(f4, LD128(OFF_MODF + (2560 + n) * 4));
    const f4 g1 = __builtin_bit_cast(f4, LD128(OFF_MODF + (2560 + n + 4) * 4));
    f4* red = (f4*)dsm;
#pragma unroll
    for (int nt = 0; nt < 4; ++nt)
#pragma unroll
      for (int mt = 0; mt < 4; ++mt) red[(w * 16 + nt * 4 + mt) * 64 + lane] = acc[nt][mt];
    __syncthreads();
    f4 s0 = f4{0.f, 0.f, 0.f, 0.f}, s1 = s0;
#pragma unroll
    for (int ww = 0; ww < 8; ++ww) {
      s0 += red[(ww * 16 + (2 * P) * 4 + fm) * 64 + lane];
      s1 += red[(ww * 16 + (2 * P + 1) * 4 + fm) * 64 + lane];
    }
    u4v o;
    {
      f2 t0 = bfr2(f2{g0[0], g0[1]} * bfr2(f2{s0[0], s0[1]})), t1 = bfr2(f2{g0[2], g0[3]} * bfr2(f2{s0[2], s0[3]}));
      f2 t2 = bfr2(f2{g1[0], g1[1]} * bfr2(f2{s1[0], s1[1]})), t3 = bfr2(f2{g1[2], g1[3]} * bfr2(f2{s1[2], s1[3]}));
      o[0] = pack_bf(bf_lo(xv[0]) + t0.x, bf_hi(xv[0]) + t0.y);
      o[1] = pack_bf(bf_lo(xv[1]) + t1.x, bf_hi(xv[1]) + t1.y);
      o[2] = pack_bf(bf_lo(xv[2]) + t2.x, bf_hi(xv[2]) + t2.y);
      o[3] = pack_bf(bf_lo(xv[3]) + t3.x, bf_hi(xv[3]) + t3.y);
    }
    const auto ro = __builtin_amdgcn_make_buffer_rsrc(out, 0, S_ * D_ * 2, 0x00020000);
    __builtin_amdgcn_raw_buffer_store_b128(o, ro, (m * D_ + n) * 2, 0, AUX_SC1);
  }
}

extern "C" void kernel_launch(void* const* d_in, const int* in_sizes, int n_in, void* d_out, int out_size, void* d_ws,
                              size_t ws_size, hipStream_t stream) {
  const u16* x = (const u16*)d_in[0];
  const u16* wr = (const u16*)d_in[1];
  const u16* wi = (const u16*)d_in[2];
  const u16* w1 = (const u16*)d_in[4];
  const u16* w2 = (const u16*)d_in[5];
  const u16* c = (const u16*)d_in[6];
  const u16* wmod = (const u16*)d_in[7];
  char* ws = (char*)d_ws + ((ws_size - (size_t)WS_SPAN) & ~(size_t)((1 << 21) - 1));
  u16* out = (u16*)d_out;
  (void)hipFuncSetAttribute((const void*)kmega, hipFuncAttributeMaxDynamicSharedMemorySize, L_TOTAL);
  k0_mod<<<K0_GEMV + K0_WT + K0_W2 + K0_Z, 512, 0, stream>>>(c, wmod, x, wr, wi, w1, w2, ws);
  kmega<<<256, 512, L_TOTAL, stream>>>(x, w1, w2, ws, out);
}
```

```cpp
#include <hip/hip_runtime.h>
#include <hip/hip_bf16.h>
#include <stdint.h>

typedef float f2 __attribute__((ext_vector_type(2)));
typedef float f4 __attribute__((ext_vector_type(4)));
typedef unsigned u2v __attribute__((ext_vector_type(2)));
typedef unsigned u4v __attribute__((ext_vector_type(4)));
typedef short bf16x8 __attribute__((ext_vector_type(8)));
typedef unsigned short u16;

constexpr int S_ = 2048, D_ = 512, F_ = 1792, DF_ = 257;
constexpr int NSPLIT = 4;

constexpr size_t OFF_CNT = 0;
#define FSPACE 16
#define FREP 16
constexpr int FSP = FSPACE, NREP = FREP;
constexpr size_t FTAB = (size_t)NREP * 256 * FSP;
constexpr size_t OFF_FLG = 16384;
constexpr size_t OFF_FLR = OFF_FLG + FTAB;
constexpr size_t FLAG_BYTES = OFF_FLR + FTAB;
constexpr size_t OFF_MODP = 900 << 10;
constexpr size_t OFF_MODF = OFF_MODP + (size_t)NSPLIT * 3072 * 4;
constexpr size_t OFF_MODC = OFF_MODF + 3072 * 4;
constexpr size_t OFF_WT = 1 << 20;
constexpr size_t OFF_XT = 4 << 20;
constexpr size_t OFF_ZT = 28 << 20;
constexpr size_t OFF_H2F = 12 << 20;
constexpr size_t OFF_X1 = 14 << 20;
constexpr size_t OFF_W2F = 18 << 20;
constexpr size_t OFF_HF = 20 << 20;
constexpr int WS_SPAN = 36 << 20;
#define CPITCH (2048 + 32)
constexpr int CP = CPITCH;
constexpr float FSC = 1.0f / (2.0f * 2048.0f * 512.0f);

__device__ __forceinline__ float bf_lo(unsigned u) { return __builtin_bit_cast(float, u << 16); }
__device__ __forceinline__ float bf_hi(unsigned u) { return __builtin_bit_cast(float, u & 0xffff0000u); }
__device__ __forceinline__ float bfr(float x) {
  return (float)(__bf16)x;
}
__device__ __forceinline__ unsigned pack_bf(float lo, float hi) {
  typedef __bf16 b2 __attribute__((ext_vector_type(2)));
  return __builtin_bit_cast(unsigned, __builtin_convertvector(f2{lo, hi}, b2));
}
__device__ __forceinline__ f2 cmul(f2 a, f2 b) {
  f2 t = a * f2{b.x, b.x};
  return __builtin_elementwise_fma(f2{a.y, a.x}, f2{-b.y, b.y}, t);
}
__device__ __forceinline__ f2 bfr2(f2 a) {
  unsigned u = pack_bf(a.x, a.y);
  asm("" : "+v"(u));
  return f2{bf_lo(u), bf_hi(u)};
}
#define PIN2(x) asm volatile("" : "+v"(x))
constexpr int AUX_SC1 = 16;
#define WS_RSRC(ws) __builtin_amdgcn_make_buffer_rsrc((ws), 0, WS_SPAN, 0x00020000)
#define ST128(v, off) __builtin_amdgcn_raw_buffer_store_b128((v), rs, (int)(off), 0, AUX_SC1)
#define ST64(v, off) __builtin_amdgcn_raw_buffer_store_b64((v), rs, (int)(off), 0, AUX_SC1)
#define ST32(v, off) __builtin_amdgcn_raw_buffer_store_b32((v), rs, (int)(off), 0, AUX_SC1)
#define LD64(off) __builtin_amdgcn_raw_buffer_load_b64(rs, (int)(off), 0, AUX_SC1)
#define LD32(off) __builtin_amdgcn_raw_buffer_load_b32(rs, (int)(off), 0, AUX_SC1)
#define LD128(off) __builtin_amdgcn_raw_buffer_load_b128(rs, (int)(off), 0, AUX_SC1)
__device__ __forceinline__ f2 twid(float rev) { return f2{__builtin_amdgcn_cosf(rev), -__builtin_amdgcn_sinf(rev)}; }

template <int DIR>
__device__ __forceinline__ void dft4(f2& c0, f2& c1, f2& c2, f2& c3) {
  f2 d0 = c0 + c2, d2 = c0 - c2, d1 = c1 + c3, e = c1 - c3;
  c0 = d0 + d1;
  c2 = d0 - d1;
  const f2 es = f2{e.y, e.x};
  c1 = __builtin_elementwise_fma(es, DIR < 0 ? f2{1.f, -1.f} : f2{-1.f, 1.f}, d2);
  c3 = __builtin_elementwise_fma(es, DIR < 0 ? f2{-1.f, 1.f} : f2{1.f, -1.f}, d2);
}

__device__ __forceinline__ void sw32(float& a, float& b) {
  auto r = __builtin_amdgcn_permlane32_swap(__builtin_bit_cast(unsigned, a), __builtin_bit_cast(unsigned, b), false, false);
  a = __builtin_bit_cast(float, (unsigned)r[0]);
  b = __builtin_bit_cast(float, (unsigned)r[1]);
}
__device__ __forceinline__ void sw16(float& a, float& b) {
  auto r = __builtin_amdgcn_permlane16_swap(__builtin_bit_cast(unsigned, a), __builtin_bit_cast(unsigned, b), false, false);
  a = __builtin_bit_cast(float, (unsigned)r[0]);
  b = __builtin_bit_cast(float, (unsigned)r[1]);
}
template <int CTRL, int BANK>
__device__ __forceinline__ float udpp(float old, float src) {
  return __builtin_bit_cast(float, __builtin_amdgcn_update_dpp(__builtin_bit_cast(int, old), __builtin_bit_cast(int, src),
                                                               CTRL, 0xf, BANK, false));
}
template <int CTRL>
__device__ __forceinline__ float mdpp(float src) {
  return __builtin_bit_cast(float, __builtin_amdgcn_mov_dpp(__builtin_bit_cast(int, src), CTRL, 0xf, 0xf, false));
}
__device__ __forceinline__ void xp_hi1(float& v0, float& v1, float& v2, float& v3) {
  sw32(v0, v2); sw32(v1, v3); sw16(v0, v1); sw16(v2, v3);
}
__device__ __forceinline__ void xp_mid1(float& v0, float& v1, float& v2, float& v3) {
  float a0 = udpp<0x128, 0xC>(v0, v2), a2 = udpp<0x128, 0x3>(v2, v0);
  float a1 = udpp<0x128, 0xC>(v1, v3), a3 = udpp<0x128, 0x3>(v3, v1);
  v0 = udpp<0x124, 0xA>(a0, a1); v1 = udpp<0x12C, 0x5>(a1, a0);
  v2 = udpp<0x124, 0xA>(a2, a3); v3 = udpp<0x12C, 0x5>(a3, a2);
}
#define XP_CPLX(NAME, FN, ...)                                        \
  {                                                                   \
    float x0 = v[0].x, x1 = v[1].x, x2 = v[2].x, x3 = v[3].x;         \
    float y0 = v[0].y, y1 = v[1].y, y2 = v[2].y, y3 = v[3].y;         \
    FN(x0, x1, x2, x3, ##__VA_ARGS__);                                \
    FN(y0, y1, y2, y3, ##__VA_ARGS__);                                \
    v[0] = f2{x0, y0}; v[1] = f2{x1, y1}; v[2] = f2{x2, y2}; v[3] = f2{x3, y3}; \
  }
__device__ __forceinline__ void xp_hi(f2 (&v)[4]) XP_CPLX(hi, xp_hi1)
__device__ __forceinline__ void xp_mid(f2 (&v)[4]) XP_CPLX(mid, xp_mid1)

template <bool CONJ>
__device__ __forceinline__ void cmul3(f2 (&v)[4], const f2 (&t)[3]) {
  f2 a, b, c;
  if (!CONJ)
    asm("v_pk_mul_f32 %3, %0, %6 op_sel_hi:[1,0]\n\t"
        "v_pk_mul_f32 %4, %1, %7 op_sel_hi:[1,0]\n\t"
        "v_pk_mul_f32 %5, %2, %8 op_sel_hi:[1,0]\n\t"
        "v_pk_fma_f32 %0, %0, %6, %3 op_sel:[1,1,0] op_sel_hi:[0,1,1] neg_lo:[0,1,0]\n\t"
        "v_pk_fma_f32 %1, %1, %7, %4 op_sel:[1,1,0] op_sel_hi:[0,1,1] neg_lo:[0,1,0]\n\t"
        "v_pk_fma_f32 %2, %2, %8, %5 op_sel:[1,1,0] op_sel_hi:[0,1,1] neg_lo:[0,1,0]\n\t"
        "s_nop 1"
        : "+v"(v[1]), "+v"(v[2]), "+v"(v[3]), "=&v"(a), "=&v"(b), "=&v"(c)
        : "v"(t[0]), "v"(t[1]), "v"(t[2]));
  else
    asm("v_pk_mul_f32 %3, %0, %6 op_sel_hi:[1,0]\n\t"
        "v_pk_mul_f32 %4, %1, %7 op_sel_hi:[1,0]\n\t"
        "v_pk_mul_f32 %5, %2, %8 op_sel_hi:[1,0]\n\t"
        "v_pk_fma_f32 %0, %0, %6, %3 op_sel:[1,1,0] op_sel_hi:[0,1,1] neg_hi:[0,1,0]\n\t"
        "v_pk_fma_f32 %1, %1, %7, %4 op_sel:[1,1,0] op_sel_hi:[0,1,1] neg_hi:[0,1,0]\n\t"
        "v_pk_fma_f32 %2, %2, %8, %5 op_sel:[1,1,0] op_sel_hi:[0,1,1] neg_hi:[0,1,0]"
        : "+v"(v[1]), "+v"(v[2]), "+v"(v[3]), "=&v"(a), "=&v"(b), "=&v"(c)
        : "v"(t[0]), "v"(t[1]), "v"(t[2]));
}
template <bool CONJ>
__device__ __forceinline__ f2 cmul1(f2 v, f2 t) {
  f2 a;
  if (!CONJ)
    asm("v_pk_mul_f32 %1, %0, %2 op_sel_hi:[1,0]\n\t"
        "v_pk_fma_f32 %0, %0, %2, %1 op_sel:[1,1,0] op_sel_hi:[0,1,1] neg_lo:[0,1,0]"
        : "+v"(v), "=&v"(a) : "v"(t));
  else
    asm("v_pk_mul_f32 %1, %0, %2 op_sel_hi:[1,0]\n\t"
        "v_pk_fma_f32 %0, %0, %2, %1 op_sel:[1,1,0] op_sel_hi:[0,1,1] neg_hi:[0,1,0]"
        : "+v"(v), "=&v"(a) : "v"(t));
  return v;
}
__device__ __forceinline__ void xp_lo_lds(f2 (&v)[4], f2* patch, int lane) {
  f2* q = patch + (lane >> 2) * 16;
  const int c = lane & 3, sw = (lane >> 2) & 3;
#pragma unroll
  for (int j = 0; j < 4; ++j) q[4 * (j ^ sw) + c] = v[j];
  __builtin_amdgcn_fence(__ATOMIC_RELEASE, "wavefront");
  __builtin_amdgcn_wave_barrier();
  __builtin_amdgcn_fence(__ATOMIC_ACQUIRE, "wavefront");
  const f4 a = *(const f4*)(q + 4 * (c ^ sw)), b = *(const f4*)(q + 4 * (c ^ sw) + 2);
  v[0] = f2{a[0], a[1]}; v[1] = f2{a[2], a[3]}; v[2] = f2{b[0], b[1]}; v[3] = f2{b[2], b[3]};
  __builtin_amdgcn_fence(__ATOMIC_RELEASE, "wavefront");
  __builtin_amdgcn_wave_barrier();
}

struct Tw256 {
  f2 a[3], b[3], c[3];
};
__device__ __forceinline__ void make_tw256(int lane, Tw256& t) {
#pragma unroll
  for (int r = 1; r < 4; ++r) {
    t.a[r - 1] = twid((float)(lane * r) * (1.0f / 256.0f));
    t.b[r - 1] = twid((float)((lane & 15) * r) * (1.0f / 64.0f));
    t.c[r - 1] = twid((float)((lane & 3) * r) * (1.0f / 16.0f));
  }
#pragma unroll
  for (int r = 0; r < 3; ++r) {
    PIN2(t.a[r]);
    PIN2(t.b[r]);
    PIN2(t.c[r]);
  }
}
struct NoHook {
  __device__ __forceinline__ void operator()(int) const {}
};
template <class H>
__device__ __forceinline__ void fft256_fwd(f2 (&v)[4], int lane, const Tw256& t, f2* patch, const H& hook) {
  dft4<-1>(v[0], v[1], v[2], v[3]);
  cmul3<false>(v, t.a);
  xp_hi(v);
  hook(0);
  dft4<-1>(v[0], v[1], v[2], v[3]);
  cmul3<false>(v, t.b);
  xp_mid(v);
  hook(1);
  dft4<-1>(v[0], v[1], v[2], v[3]);
  cmul3<false>(v, t.c);
  xp_lo_lds(v, patch, lane);
  hook(2);
  dft4<-1>(v[0], v[1], v[2], v[3]);
}
template <class H>
__device__ __forceinline__ void fft256_inv(f2 (&v)[4], int lane, const Tw256& t, f2* patch, const H& hook) {
  dft4<1>(v[0], v[1], v[2], v[3]);
  xp_lo_lds(v, patch, lane);
  hook(0);
  cmul3<true>(v, t.c);
  dft4<1>(v[0], v[1], v[2], v[3]);
  xp_mid(v);
  hook(1);
  cmul3<true>(v, t.b);
  dft4<1>(v[0], v[1], v[2], v[3]);
  xp_hi(v);
  hook(2);
  cmul3<true>(v, t.a);
  dft4<1>(v[0], v[1], v[2], v[3]);
}

__device__ __forceinline__ float wave_sum(float x) {
  x += mdpp<0xB1>(x);
  x += mdpp<0x4E>(x);
  x += mdpp<0x141>(x);
  x += mdpp<0x140>(x);
  float r0 = __builtin_bit_cast(float, __builtin_amdgcn_readlane(__builtin_bit_cast(int, x), 0));
  float r1 = __builtin_bit_cast(float, __builtin_amdgcn_readlane(__builtin_bit_cast(int, x), 16));
  float r2 = __builtin_bit_cast(float, __builtin_amdgcn_readlane(__builtin_bit_cast(int, x), 32));
  float r3 = __builtin_bit_cast(float, __builtin_amdgcn_readlane(__builtin_bit_cast(int, x), 48));
  return (r0 + r1) + (r2 + r3);
}

__device__ __forceinline__ void ln_stats(const f2 (&v)[4], float& mean, float& rden) {
  const float K = __builtin_bit_cast(float, __builtin_amdgcn_readfirstlane(__builtin_bit_cast(int, v[0].x)));
  f2 s2 = f2{0.f, 0.f}, q2 = f2{0.f, 0.f};
#pragma unroll
  for (int j = 0; j < 4; ++j) {
    const f2 d = v[j] - K;
    s2 += d;
    q2 = __builtin_elementwise_fma(d, d, q2);
  }
  const float sm = wave_sum(s2.x + s2.y) * (1.0f / D_), qm = wave_sum(q2.x + q2.y) * (1.0f / D_);
  const float var = fmaxf(qm - sm * sm, 0.f);
  mean = bfr(K + sm);
  rden = __builtin_amdgcn_rcpf(bfr(__builtin_amdgcn_sqrtf(bfr(bfr(var) + 1e-5f))));
}

__device__ __forceinline__ int zpos(int k) { return 17 * (k & 3) + 4 * ((k >> 2) & 3) + ((k >> 4) & 3) + 68 * (k >> 6); }
constexpr int ZRS = 276;

constexpr int K0_GEMV = 48 * NSPLIT, K0_WT = 128, K0_W2 = 112, K0_Z = (int)(FLAG_BYTES / 8192);
__global__ __launch_bounds__(512) void k0_mod(const u16* __restrict__ c, const u16* __restrict__ wmod,
                                              const u16* __restrict__ x, const u16* __restrict__ wr,
                                              const u16* __restrict__ wi, const u16* __restrict__ w1,
                                              const u16* __restrict__ w2, char* __restrict__ ws) {
  __shared__ __attribute__((aligned(16))) char smem[17408];
  const int tid = threadIdx.x, row = tid >> 6, lane = tid & 63;
  const auto rs = WS_RSRC(ws);
  int b = blockIdx.x;
  if (b >= K0_GEMV + K0_WT + K0_W2) {
    int q = b - (K0_GEMV + K0_WT + K0_W2);
    if (q < K0_Z) {
      ((u4v*)ws)[q * 512 + tid] = u4v{0u, 0u, 0u, 0u};
      return;
    }
    return;
  }
  if (b >= K0_GEMV) {
    b -= K0_GEMV;
    if (b < K0_WT) {
      char* sr = smem;
      char* si = smem + 16 * 544;
      constexpr int LASTCH = (S_ * DF_ * 2) / 16 - 1;
      for (int it = tid; it < 16 * 34; it += 512) {
        const int i = it / 34, ch = it % 34;
        int gch = ((b + 128 * i) * (DF_ * 2)) / 16 + ch;
        gch = gch > LASTCH ? LASTCH : gch;
        *(u4v*)(sr + i * 544 + ch * 16) = *(const u4v*)((const char*)wr + (size_t)gch * 16);
        *(u4v*)(si + i * 544 + ch * 16) = *(const u4v*)((const char*)wi + (size_t)gch * 16);
      }
      __syncthreads();
      const int tbase = 64 * (b & 7) + 16 * ((b >> 3) & 3) + 4 * ((b >> 5) & 3);
      for (int it = tid; it < DF_ * 4; it += 512) {
        const int k = it >> 2, r3 = it & 3;
        u4v o;
#pragma unroll
        for (int r4 = 0; r4 < 4; ++r4) {
          const int i = r3 + 4 * r4;
          const int off = i * 544 + (((b + 128 * i) * (DF_ * 2)) & 15) + 2 * k;
          o[r4] = (unsigned)*(const u16*)(sr + off) | ((unsigned)*(const u16*)(si + off) << 16);
        }
        ST128(o, OFF_WT + (k * 512 + tbase + r3) * 16);
      }
    } else {
      b -= K0_WT;
      u16* t = (u16*)smem;
      const int g = lane >> 4, r = lane & 15;
      const int ng = b & 3, kp = b >> 2;
#pragma unroll
      for (int it = 0; it < 2; ++it) {
        const int idx = it * 512 + tid;
        const int rr = idx >> 4, ch = idx & 15;
        const int ks2 = kp * 2 + (rr >> 5), gg = (rr >> 3) & 3, j = rr & 7;
        const int e = ks2 / 7, i7 = ks2 % 7;
        int T0, T1;
        if (i7 < 3) { T0 = 14 * e + 2 * i7; T1 = T0 + 1; }
        else if (i7 < 6) { T0 = 14 * e + 7 + 2 * (i7 - 3); T1 = T0 + 1; }
        else { T0 = 14 * e + 6; T1 = 14 * e + 13; }
        const int f = (j < 4) ? (16 * T0 + 4 * gg + j) : (16 * T1 + 4 * gg + (j - 4));
        *(u4v*)(t + rr * 136 + ch * 8) = *(const u4v*)(w2 + (size_t)f * D_ + ng * 128 + ch * 8);
      }
      __syncthreads();
      const int nl = 32 * (row >> 1) + 8 * (r >> 2) + 4 * (row & 1) + (r & 3);
#pragma unroll
      for (int ksl = 0; ksl < 2; ++ksl) {
        u4v o;
#pragma unroll
        for (int jj = 0; jj < 4; ++jj)
          o[jj] = (unsigned)t[(ksl * 32 + g * 8 + 2 * jj) * 136 + nl] |
                  ((unsigned)t[(ksl * 32 + g * 8 + 2 * jj + 1) * 136 + nl] << 16);
        const int nt = ng * 8 + row;
        ST128(o, OFF_W2F + ((nt * 56 + kp * 2 + ksl) * 64 + lane) * 16);
      }
    }
    return;
  }
  float* red = (float*)smem;
  const int cgp = b % 48, ks = b / 48;
  const int cg = tid & 7, r = tid >> 3;
  const int rw = ks * 128 + r;
  const u4v wm0 = *(const u4v*)(wmod + (size_t)rw * 3072 + cgp * 64 + cg * 8);
  const u4v wm1 = *(const u4v*)(wmod + (size_t)(rw + 64) * 3072 + cgp * 64 + cg * 8);
  const float cv0 = bf_lo(c[rw]), cv1 = bf_lo(c[rw + 64]);
  const float s0 = bfr(cv0 / (1.0f + __expf(-cv0))), s1 = bfr(cv1 / (1.0f + __expf(-cv1)));
  float acc[8];
#pragma unroll
  for (int i = 0; i < 4; ++i) {
    acc[2 * i] = s0 * bf_lo(wm0[i]) + s1 * bf_lo(wm1[i]);
    acc[2 * i + 1] = s0 * bf_hi(wm0[i]) + s1 * bf_hi(wm1[i]);
  }
#pragma unroll
  for (int i = 0; i < 8; ++i) {
    acc[i] += mdpp<0x128>(acc[i]);
    acc[i] += __shfl_xor(acc[i], 16);
    acc[i] += __shfl_xor(acc[i], 32);
  }
  if (lane < 8) {
#pragma unroll
    for (int i = 0; i < 8; ++i) red[row * 64 + cg * 8 + i] = acc[i];
  }
  __syncthreads();
  if (tid < 64) {
    float sum = 0.f;
#pragma unroll
    for (int i = 0; i < 8; ++i) sum += red[i * 64 + tid];
    ((float*)(ws + OFF_MODP))[ks * 3072 + cgp * 64 + tid] = sum;
  }
}

__device__ __forceinline__ f4 mfma16(u4v a, u4v b, f4 c) {
  return __builtin_amdgcn_mfma_f32_16x16x32_bf16(__builtin_bit_cast(bf16x8, a), __builtin_bit_cast(bf16x8, b), c, 0, 0,
                                                  0);
}
__device__ __forceinline__ f2 silu2(float a, float b) {
  f2 h = f2{a, b};
  f2 t = h * -1.44269504088896340736f;
  f2 d = f2{__builtin_amdgcn_exp2f(t.x), __builtin_amdgcn_exp2f(t.y)} + 1.0f;
  return h * f2{__builtin_amdgcn_rcpf(d.x), __builtin_amdgcn_rcpf(d.y)};
}
__device__ __forceinline__ unsigned silu_pk(float a, float b) {
  f2 r = silu2(a, b);
  return pack_bf(r.x, r.y);
}
typedef __attribute__((address_space(1))) unsigned gu32;
#define DRAIN() asm volatile("s_waitcnt vmcnt(0)" ::: "memory")
typedef __attribute__((address_space(1))) unsigned long long gu64;
constexpr unsigned SPIN_CAP = 1u << 18;
__device__ __forceinline__ void wait_pair(gu64* flags) {
#pragma nounroll
  for (unsigned spin = 0; spin < SPIN_CAP; ++spin) {
    if (__hip_atomic_load(flags, __ATOMIC_RELAXED, __HIP_MEMORY_SCOPE_AGENT) == 0x0000000100000001ull) break;
    __builtin_amdgcn_s_sleep(1);
  }
  __builtin_amdgcn_fence(__ATOMIC_ACQUIRE, "wavefront");
  asm volatile("" ::: "memory");
}

#define PSLEEP 2
__device__ __forceinline__ void wait_cols(gu32* flags) {
#pragma nounroll
  for (unsigned spin = 0; spin < SPIN_CAP; ++spin) {
    unsigned f = 1u;
#pragma unroll
    for (int j = 0; j < 4; ++j)
      f &= __hip_atomic_load(flags + j * 64 * (FSP / 4), __ATOMIC_RELAXED, __HIP_MEMORY_SCOPE_AGENT);
    if (__builtin_amdgcn_ballot_w64(f != 1u) == 0ull) break;
    __builtin_amdgcn_s_sleep(PSLEEP);
  }
}

__device__ __forceinline__ int wt_off(int k, int t) { return (int)OFF_WT + (k * 512 + t) * 16; }

constexpr int L_W1 = 0;
constexpr int L_T = 112 * 1024;
constexpr int L_ZS = 112 * 1024;
constexpr int L_ST = L_ZS + 8 * ZRS * 8;
constexpr int L_TOTAL = 160 * 1024;
__device__ __forceinline__ constexpr int g1_ft(int g, int j) { return g < 8 ? (j & 3) : (g < 12 ? 4 + (j & 1) : 6); }
__device__ __forceinline__ constexpr int g1_ks(int g, int j) {
  return g < 8 ? 2 * g + (j >> 2) : (g < 12 ? 4 * (g - 8) + (j >> 1) : 8 * (g - 12) + j);
}
#define G1WIN 6
#define SCHED() __builtin_amdgcn_sched_barrier(0)
typedef short v4i16 __attribute__((ext_vector_type(4)));
__device__ __forceinline__ u4v w1frag(const char* lw, int ft, int ks) {
  typedef __attribute__((address_space(3))) v4i16* lp;
  const u2v a = __builtin_bit_cast(u2v, __builtin_amdgcn_ds_read_tr16_b64_v4i16((lp)(lw + (2 * ks) * 3584 + ft * 32)));
  const u2v b = __builtin_bit_cast(u2v, __builtin_amdgcn_ds_read_tr16_b64_v4i16((lp)(lw + (2 * ks + 1) * 3584 + ft * 32)));
  return u4v{a[0], a[1], b[0], b[1]};
}
constexpr int L_MODS = L_ZS + 8 * ZRS * 8;
constexpr int L_PX = L_T + 28672;
__global__ __launch_bounds__(512, 2) void kmega(const u16* __restrict__ x, const u16* __restrict__ w1,
                                                const u16* __restrict__ w2, char* ws, u16* __restrict__ out) {
  extern __shared__ __attribute__((aligned(16))) char dsm[];
  const int tid = threadIdx.x, lane = tid & 63;
  const int w = __builtin_amdgcn_readfirstlane(tid >> 6);
  const int bid = blockIdx.x;
  const int p = (bid & 7) * 2 + (bid >> 7), c = (bid >> 3) & 15;
  const int row0 = 128 * p + 8 * c;
  unsigned cw0, cw1, cw2;
  {
    unsigned long long pc;
    asm volatile("s_getpc_b64 %0" : "=s"(pc));
    const char* code = (const char*)(pc & ~255ull) + (tid < 272 ? tid : 0) * 64;
    cw0 = *(const unsigned*)(code);
    cw1 = cw0; cw2 = cw0;
  }
  const auto rs = WS_RSRC(ws);
  gu32* myA = (gu32*)(ws + OFF_CNT) + (p * 8 + (c >> 1)) * 16 + (c & 1);
  gu32* myB = myA + 128 * 16;
  gu64* depA = (gu64*)(ws + OFF_CNT) + (p * 8 + w) * 8;
  gu64* depB = depA + 128 * 8;
  gu32* myE = myB + 8;
  gu64* depE = depB + 4;
  const int mt0 = 8 * p + 4 * (c >> 3), nt0 = 4 * (c & 7);
  const int l56 = lane < 56 ? lane : lane - 56;
  const int r4_ = l56 / 14, ch_ = l56 - 14 * r4_;
  const char* w1g = (const char*)w1 + (size_t)(4 * w + r4_) * (F_ * 2) + c * 224 + ch_ * 16;
  char* w1l = dsm + L_W1 + ((w >> 1) * 32 + (2 * (w & 1) + (r4_ >> 1)) * 4 + (r4_ & 1)) * 224 + ch_ * 16;
  const int Ab = (int)OFF_W2F + ((nt0 * 56 + 7 * w) * 64 + lane) * 16;
  u4v S1[16];
  u4v A2[28];
  auto pf_load = [&](int i) __attribute__((always_inline)) {
    if (i < 16) S1[i] = *(const u4v*)(w1g + (size_t)i * (32 * F_ * 2));
    else if (i < 44) A2[i - 16] = *(const u4v*)(ws + Ab + (((i - 16) & 3) * 56 + ((i - 16) >> 2)) * 1024);
  };
  auto pf_put = [&](int i) __attribute__((always_inline)) {
    *(u4v*)(w1l + ((i & 3) * 128 + (i >> 3) * 16 + ((i >> 2) & 1) * 2) * 224) = S1[i];
  };
  auto pf = [&](int i0, int n) __attribute__((always_inline)) {
    SCHED();
#pragma unroll
    for (int i = 0; i < n; ++i) pf_load(i0 + i);
    SCHED();
  };
  u4v wv, bv;
  unsigned wn[4], bn[4];
  int mirr[4];
  Tw256 tw;
  {
    f2* zs = (f2*)(dsm + L_ZS);
    float* mods = (float*)(dsm + L_MODS);
    const int r0 = bid * 8;
    unsigned xr[4];
    const unsigned* xrow = (const unsigned*)(x + (size_t)(r0 + w) * D_);
#pragma unroll
    for (int j = 0; j < 4; ++j) xr[j] = xrow[lane + 64 * j];
    const float* mp = (const float*)(ws + OFF_MODP);
    f2 sm = *(const f2*)(mp + tid * 2);
#pragma unroll
    for (int k = 1; k < NSPLIT; ++k) sm += *(const f2*)(mp + k * 3072 + tid * 2);
    const int idx = bid * 12 + (tid < 6 ? tid : 0) * 2;
    f2 fin = *(const f2*)(mp + idx);
#pragma unroll
    for (int k = 1; k < NSPLIT; ++k) fin += *(const f2*)(mp + k * 3072 + idx);
    pf(0, 4);
    make_tw256(lane, tw);
    const int rp = tid & 3, kk = tid >> 2;
    f2 wpost = twid((float)kk * (1.0f / 512.0f));
    PIN2(wpost);
    f2 v[4];
#pragma unroll
    for (int j = 0; j < 4; ++j) v[j] = f2{bf_lo(xr[j]), bf_hi(xr[j])};
    float mean, rden;
    ln_stats(v, mean, rden);
    asm volatile("" ::"v"(cw0), "v"(cw1), "v"(cw2));
    sm = bfr2(sm);
    if (tid >= 256) sm = bfr2(sm + 1.0f);
    *(f2*)(mods + tid * 2) = sm;
    if (tid < 6) {
      fin = bfr2(fin);
      ST64(__builtin_bit_cast(u2v, fin), OFF_MODF + idx * 4);
      const int vec = (idx >> 9) - 2, d = idx & 511;
      if (vec >= 0 && vec < 3) {
        if (vec == 2) fin += 1.0f;
        ST32(pack_bf(fin.x, fin.y), OFF_MODC + ((vec * 64 + ((d >> 1) & 63)) * 4 + (d >> 7)) * 4);
      }
    }
    __syncthreads();
    wv = LD128(wt_off(bid, tid));
    if (bid == 0) {
      bv = LD128(wt_off(256, tid));
      const int m0 = (lane >> 4) + 4 * ((lane >> 2) & 3) + 16 * (lane & 3);
#pragma unroll
      for (int r4 = 0; r4 < 4; ++r4) {
        const int un = (S_ - (w + 8 * (m0 + 64 * r4))) & (S_ - 1);
        const int mm = un >> 3;
        mirr[r4] = ((un & 7) * 64 + 16 * (mm & 3) + 4 * ((mm >> 2) & 3) + ((mm >> 4) & 3)) * 4 + (mm >> 6);
      }
      if (w != 0) {
        const u4v a4 = LD128(wt_off(0, 64 * (8 - w) + 63 - lane));
        const u4v b4 = LD128(wt_off(256, 64 * (8 - w) + 63 - lane));
#pragma unroll
        for (int r4 = 0; r4 < 4; ++r4) { wn[r4] = a4[3 - r4]; bn[r4] = b4[3 - r4]; }
      } else {
#pragma unroll
        for (int r4 = 0; r4 < 4; ++r4) {
          wn[r4] = LD32(wt_off(0, mirr[r4] >> 2) + (mirr[r4] & 3) * 4);
          bn[r4] = LD32(wt_off(256, mirr[r4] >> 2) + (mirr[r4] & 3) * 4);
        }
      }
    }
    pf(4, 4);
#pragma unroll
    for (int j = 0; j < 4; ++j) {
      const int d = 2 * (lane + 64 * j);
      f2 sa = *(const f2*)(mods + d);
      f2 ca1 = *(const f2*)(mods + 512 + d);
      v[j] = bfr2(__builtin_elementwise_fma(v[j] - mean, ca1 * rden, sa));
    }
    fft256_fwd(v, lane, tw, (f2*)(dsm + L_PX + w * 2048), NoHook{});
    {
      f2* buf = zs + w * ZRS + lane + (lane >> 4);
#pragma unroll
      for (int r4 = 0; r4 < 4; ++r4) buf[68 * r4] = v[r4];
    }
    __syncthreads();
    {
      const int k = kk;
      const int k2 = (k == 0) ? 128 : 256 - k;
      const int pa = zpos(k), pb = zpos(k2);
      f4 oa, ob;
#pragma unroll
      for (int h = 0; h < 2; ++h) {
        const f2* zr = zs + (2 * rp + h) * ZRS;
        f2 za = zr[pa], zb = zr[pb];
        f2 E = f2{za.x + zb.x, za.y - zb.y};
        f2 Dm = f2{za.x - zb.x, za.y + zb.y};
        f2 O = f2{Dm.y, -Dm.x};
        f2 wo = cmul1<false>(O, wpost);
        f2 ra = E + wo;
        f2 t = E - wo;
        f2 rb = f2{t.x, -t.y};
        if (k == 0) {
          ra = f2{2.f * (za.x + za.y), 2.f * (za.x - za.y)};
          rb = f2{2.f * zb.x, -2.f * zb.y};
        }
        oa[2 * h] = ra.x; oa[2 * h + 1] = ra.y;
        ob[2 * h] = rb.x; ob[2 * h + 1] = rb.y;
      }
      ST128(__builtin_bit_cast(u4v, oa), OFF_XT + (k * CP + r0 + 2 * rp) * 8);
      ST128(__builtin_bit_cast(u4v, ob), OFF_XT + (k2 * CP + r0 + 2 * rp) * 8);
    }
    DRAIN();
    __syncthreads();
    if (tid < NREP)
      __hip_atomic_store((gu32*)(ws + OFF_FLR + (tid * 256 + bid) * FSP), 1u, __ATOMIC_RELAXED, __HIP_MEMORY_SCOPE_AGENT);
  }
  {
    f2* T = (f2*)(dsm + L_T);
    f2* T2 = (f2*)(dsm + L_T + 16384);
    f2* T3 = (f2*)(dsm + L_T + 32768);
    const int k = bid;
    f2 tw1[4];
#pragma unroll
    for (int j = 0; j < 4; ++j) {
      tw1[j] = twid((float)((lane + 64 * j) * w) * (1.0f / 2048.0f));
      PIN2(tw1[j]);
    }
    f2 wp[4], wm[4];
    if (k == 0) {
#pragma unroll
      for (int r4 = 0; r4 < 4; ++r4) {
        f2 h0 = f2{bf_lo(wv[r4]) + bf_lo(wn[r4]), bf_hi(wv[r4]) - bf_hi(wn[r4])};
        f2 h1 = f2{bf_lo(bv[r4]) + bf_lo(bn[r4]), bf_hi(bv[r4]) - bf_hi(bn[r4])};
        wp[r4] = (h0 + h1) * 0.25f;
        wm[r4] = (h0 - h1) * 0.25f;
        PIN2(wp[r4]);
        PIN2(wm[r4]);
      }
    } else {
#pragma unroll
      for (int r4 = 0; r4 < 4; ++r4) {
        wp[r4] = f2{bf_lo(wv[r4]), bf_hi(wv[r4])};
        PIN2(wp[r4]);
      }
    }
    pf(28, 3);
    if (w == 0) wait_cols((gu32*)(ws + OFF_FLR + ((bid % NREP) * 256 + lane) * FSP));
    asm volatile("s_barrier" ::: "memory");
    __builtin_amdgcn_fence(__ATOMIC_ACQUIRE, "wavefront");
    f2 v[4];
#pragma unroll
    for (int j = 0; j < 4; ++j) v[j] = __builtin_bit_cast(f2, LD64(OFF_XT + (k * CP + tid + 512 * j) * 8));
#pragma unroll
    for (int j = 0; j < 4; ++j) PIN2(v[j]);
    pf(8, 4);
    const int hb = w >> 2, a = tid & 255;
    constexpr float R = 0.70710678118654752440f;
    dft4<-1>(v[0], v[1], v[2], v[3]);
    if (hb) {
      v[1] = f2{v[1].x + v[1].y, v[1].y - v[1].x} * R;
      v[2] = f2{v[2].y, -v[2].x};
      v[3] = f2{v[3].y - v[3].x, -v[3].x - v[3].y} * R;
    }
#pragma unroll
    for (int q = 0; q < 4; ++q) T[(hb * 4 + q) * 256 + a] = v[q];
    __syncthreads();
    {
      const int q = w & 3;
      const float sg = (w < 4) ? 1.f : -1.f;
#pragma unroll
      for (int j = 0; j < 4; ++j) {
        f2 e = T[q * 256 + lane + 64 * j], o = T[(4 + q) * 256 + lane + 64 * j];
        v[j] = cmul1<false>(e + o * sg, tw1[j]);
      }
    }
    fft256_fwd(v, lane, tw, T2 + w * 256, [&](int i) __attribute__((always_inline)) {
      SCHED();
      if (i < 2) {
#pragma unroll
        for (int q = 0; q < 4; ++q) pf_put(4 * i + q);
      } else {
#pragma unroll
        for (int q = 12; q < 16; ++q) pf_load(q);
      }
      SCHED();
    });
    if (k != 0) {
#pragma unroll
      for (int i = 0; i < 4; ++i) {
        v[i] = cmul1<false>(v[i], wp[i]);
      }
    } else {
#pragma unroll
      for (int r4 = 0; r4 < 4; ++r4) T3[r4 * 512 + tid] = v[r4];
      __syncthreads();
#pragma unroll
      for (int r4 = 0; r4 < 4; ++r4) {
        f2 cm = T3[(mirr[r4] & 3) * 512 + (mirr[r4] >> 2)];
        v[r4] = cmul1<false>(v[r4], wp[r4]) + cmul1<false>(f2{cm.x, -cm.y}, wm[r4]);
      }
    }
    fft256_inv(v, lane, tw, T2 + w * 256, [&](int i) __attribute__((always_inline)) {
      SCHED();
      if (i == 0) {
#pragma unroll
        for (int q = 8; q < 12; ++q) pf_put(q);
      }
      if (i == 1) {
#pragma unroll
        for (int q = 16; q < 20; ++q) pf_load(q);
      }
      SCHED();
    });
#pragma unroll
    for (int j = 0; j < 4; ++j) T2[w * 256 + lane + 64 * j] = cmul1<true>(v[j], tw1[j]);
#pragma unroll
    for (int q = 12; q < 16; ++q) pf_put(q);
    __syncthreads();
    {
      const float sg = hb ? -1.f : 1.f;
#pragma unroll
      for (int q = 0; q < 4; ++q) v[q] = T2[q * 256 + a] + T2[(q + 4) * 256 + a] * sg;
      if (hb) {
        v[1] = f2{v[1].x - v[1].y, v[1].y + v[1].x} * R;
        v[2] = f2{-v[2].y, v[2].x};
        v[3] = f2{-v[3].x - v[3].y, v[3].x - v[3].y} * R;
      }
    }
    dft4<1>(v[0], v[1], v[2], v[3]);
#pragma unroll
    for (int j = 0; j < 4; ++j) ST64(__builtin_bit_cast(u2v, v[j]), OFF_ZT + (k * CP + tid + 512 * j) * 8);
    DRAIN();
    __syncthreads();
    if (tid < NREP)
      __hip_atomic_store((gu32*)(ws + OFF_FLG + (tid * 256 + k) * FSP), 1u, __ATOMIC_RELAXED, __HIP_MEMORY_SCOPE_AGENT);
  }
  {
    const char* lw = dsm + L_W1 + (lane >> 2) * 224 + (lane & 3) * 8 + w * (2 * 7168);
    u4v fr[14];
#pragma unroll
    for (int i = 0; i < 14; ++i) fr[i] = w1frag(lw, i % 7, i / 7);
    asm volatile("" ::: "memory");
#pragma unroll
    for (int i = 0; i < 14; ++i) *(u4v*)(dsm + L_W1 + w * (2 * 7168) + i * 1024 + lane * 16) = fr[i];
  }
  {
    f2* zs = (f2*)(dsm + L_ZS);
    const int rp = tid & 3, k = tid >> 2;
    const int k2 = (k == 0) ? 128 : 256 - k;
    u4v xr;
    const unsigned* xrow = (const unsigned*)(x + (size_t)(row0 + w) * D_);
#pragma unroll
    for (int j = 0; j < 4; ++j) xr[j] = xrow[lane + 64 * j];
    const u4v gav = LD128(OFF_MODC + lane * 16);
    const u4v sbv = LD128(OFF_MODC + 1024 + lane * 16);
    const u4v cbv = LD128(OFF_MODC + 2048 + lane * 16);
    f2 wq = twid((float)k * (1.0f / 512.0f));
    PIN2(wq);
    const int pa = zpos(k), pb = zpos(k2);
    pf(31, 3);
    if (w == 0) wait_cols((gu32*)(ws + OFF_FLG + ((bid % NREP) * 256 + lane) * FSP));
    asm volatile("s_barrier" ::: "memory");
    __builtin_amdgcn_fence(__ATOMIC_ACQUIRE, "wavefront");
    const f4 ya = __builtin_bit_cast(f4, LD128(OFF_ZT + (k * CP + row0 + 2 * rp) * 8));
    const f4 yb = __builtin_bit_cast(f4, LD128(OFF_ZT + (k2 * CP + row0 + 2 * rp) * 8));
#pragma unroll
    for (int h = 0; h < 2; ++h) {
      f2 a = f2{ya[2 * h], ya[2 * h + 1]}, b = f2{yb[2 * h], yb[2 * h + 1]};
      f2* zr = zs + (2 * rp + h) * ZRS;
      f2 Ze = f2{a.x + b.x, a.y - b.y};
      f2 Dm = f2{a.x - b.x, a.y + b.y};
      f2 Zo = cmul1<true>(Dm, wq);
      f2 za = f2{Ze.x - Zo.y, Ze.y + Zo.x};
      f2 zb = f2{Ze.x + Zo.y, -Ze.y + Zo.x};
      if (k == 0) {
        za = f2{a.x + a.y, a.x - a.y};
        zb = f2{2.f * b.x, -2.f * b.y};
      }
      zr[pa] = za;
      zr[pb] = zb;
    }
    pf(20, 4);
    __syncthreads();
    f2 v[4];
    {
      const f2* buf = zs + w * ZRS + lane + (lane >> 4);
#pragma unroll
      for (int r4 = 0; r4 < 4; ++r4) v[r4] = buf[68 * r4];
    }
    fft256_inv(v, lane, tw, (f2*)(dsm + L_PX + w * 2048), NoHook{});
    pf(24, 4);
    u4v x1p;
#pragma unroll
    for (int j = 0; j < 4; ++j) {
      f2 t = __builtin_elementwise_fma(f2{bf_lo(gav[j]), bf_hi(gav[j])} * FSC, v[j], f2{bf_lo(xr[j]), bf_hi(xr[j])});
      x1p[j] = pack_bf(t.x, t.y);
      v[j] = f2{bf_lo(x1p[j]), bf_hi(x1p[j])};
    }
    ST128(x1p, OFF_X1 + ((row0 + w) * 64 + lane) * 16);
    float mean, rden;
    ln_stats(v, mean, rden);
    u4v hp;
#pragma unroll
    for (int j = 0; j < 4; ++j) {
      f2 t = __builtin_elementwise_fma(v[j] - mean, f2{bf_lo(cbv[j]), bf_hi(cbv[j])} * rden,
                                       f2{bf_lo(sbv[j]), bf_hi(sbv[j])});
      hp[j] = pack_bf(t.x, t.y);
    }
    *(u4v*)(dsm + L_ST + (lane * 9 + w) * 16) = hp;
    __syncthreads();
    {
      const int ch = tid >> 3, r = tid & 7;
      u4v o = *(const u4v*)(dsm + L_ST + (ch * 9 + r) * 16);
      const int mt = row0 >> 4, r16 = (row0 & 8) + r;
      ST128(o, OFF_H2F + ((mt * 16 + (ch >> 2)) * 64 + (ch & 3) * 16 + r16) * 16);
    }
  }
  DRAIN();
  __syncthreads();
  if (tid == 0) __hip_atomic_store(myA, 1u, __ATOMIC_RELAXED, __HIP_MEMORY_SCOPE_AGENT);
  {
    const int ch = tid >> 3, r = tid & 7;
    u4v t = LD128(OFF_H2F + (((row0 >> 4) * 16 + (ch >> 2)) * 64 + (ch & 3) * 16 + (row0 & 8) + r) * 16);
    asm volatile("" ::"v"(t));
  }
  wait_pair(depA);
  u4v rb0, rb1;
  {
    u4v B[16];
#pragma unroll
    for (int ks = 0; ks < G1WIN; ++ks) B[ks] = LD128(OFF_H2F + (((8 * p + w) * 16 + ks) * 64 + lane) * 16);
    f4 acc[7];
#pragma unroll
    for (int ft = 0; ft < 7; ++ft) acc[ft] = f4{0.f, 0.f, 0.f, 0.f};
    const char* lw = dsm + L_W1 + lane * 16;
    unsigned off2 = L_W1 + lane * 16 + 8 * 7168;
    asm volatile("" : "+v"(off2));
    const char* lw2 = dsm + off2;
    const int e = c >> 1, odd = c & 1;
    const int hb = (int)OFF_HF + (((8 * p + w) * 56 + 7 * e) * 64 + lane) * 16;
    u4v Af[2][8];
#pragma unroll
    for (int j = 0; j < 8; ++j) Af[0][j] = *(const u4v*)(lw + (g1_ks(0, j) * 7 + g1_ft(0, j)) * 1024);
#pragma unroll
    for (int g = 0; g < 14; ++g) {
#pragma unroll
      for (int j = 0; j < 8; ++j) {
        acc[g1_ft(g, j)] = mfma16(Af[g & 1][j], B[g1_ks(g, j)], acc[g1_ft(g, j)]);
        if (g + 1 < 14)
          Af[(g + 1) & 1][j] = g1_ks(g + 1, j) < 8 ? *(const u4v*)(lw + (g1_ks(g + 1, j) * 7 + g1_ft(g + 1, j)) * 1024)
                                                   : *(const u4v*)(lw2 + ((g1_ks(g + 1, j) - 8) * 7 + g1_ft(g + 1, j)) * 1024);
        SCHED();
      }
#pragma unroll
      for (int q = 0; q < 2; ++q)
        if (2 * g + G1WIN + q < 16)
          B[2 * g + G1WIN + q] = LD128(OFF_H2F + (((8 * p + w) * 16 + 2 * g + G1WIN + q) * 64 + lane) * 16);
      if (g >= 5 && g < 10) { pf_load(24 + 2 * g); pf_load(25 + 2 * g); }
      if (g == 8 || g == 9 || g == 12) {
        const int i = g == 12 ? 2 : g - 8;
        f4 a = acc[2 * i], b = acc[2 * i + 1];
        u4v o = u4v{silu_pk(a[0], a[1]), silu_pk(a[2], a[3]), silu_pk(b[0], b[1]), silu_pk(b[2], b[3])};
        ST128(o, hb + (odd * 3 + i) * 1024);
      }
      if (g == 11) {
        DRAIN();
        asm volatile("s_barrier" ::: "memory");
        if (tid == 0) __hip_atomic_store(myE, 1u, __ATOMIC_RELAXED, __HIP_MEMORY_SCOPE_AGENT);
        rb0 = LD128(hb + odd * 3072);
        rb1 = LD128(hb + odd * 3072 + 1024);
      }
      SCHED();
    }
    wait_pair(depE);
    asm volatile("" ::"v"(rb0), "v"(rb1));
    f4 a = acc[6];
    u2v o = u2v{silu_pk(a[0], a[1]), silu_pk(a[2], a[3])};
    ST64(o, hb + 6 * 1024 + odd * 8);
  }
  {
    const int Bb = (int)OFF_HF + ((mt0 * 56 + 7 * w) * 64 + lane) * 16;
    u4v B[7][4];
    constexpr int KO[7] = {0, 1, 3, 4, 2, 5, 6};
    SCHED();
#pragma unroll
    for (int i = 0; i < 2; ++i)
#pragma unroll
      for (int mt = 0; mt < 4; ++mt) B[i][mt] = LD128(Bb + (mt * 56 + KO[i]) * 1024);
    SCHED();
    asm volatile("s_waitcnt vmcnt(8) lgkmcnt(0)\n\ts_barrier" ::: "memory");
    if (tid == 0) __hip_atomic_store(myB, 1u, __ATOMIC_RELAXED, __HIP_MEMORY_SCOPE_AGENT);
    u4v rb2 = LD128((int)OFF_HF + (((8 * p + w) * 56 + 7 * (c >> 1) + 3 * (c & 1) + 2) * 64 + lane) * 16);
    f4 acc[4][4];
#pragma unroll
    for (int i = 0; i < 4; ++i)
#pragma unroll
      for (int j = 0; j < 4; ++j) acc[i][j] = f4{0.f, 0.f, 0.f, 0.f};
#pragma unroll
    for (int i = 2; i < 4; ++i)
#pragma unroll
      for (int mt = 0; mt < 4; ++mt) B[i][mt] = LD128(Bb + (mt * 56 + KO[i]) * 1024);
    SCHED();
#pragma unroll
    for (int i = 0; i < 7; ++i) {
      if (i == 2) {
        wait_pair(depB);
        asm volatile("" ::"v"(rb2));
#pragma unroll
        for (int q = 4; q < 7; ++q)
#pragma unroll
          for (int mt = 0; mt < 4; ++mt) B[q][mt] = LD128(Bb + (mt * 56 + KO[q]) * 1024);
        SCHED();
      }
#pragma unroll
      for (int nt = 0; nt < 4; ++nt)
#pragma unroll
        for (int mt = 0; mt < 4; ++mt) acc[nt][mt] = mfma16(A2[KO[i] * 4 + nt], B[i][mt], acc[nt][mt]);
      SCHED();
    }
    const int g = lane >> 4, mr = lane & 15;
    const int fm = w >> 1, P = w & 1;
    const int m = (mt0 + fm) * 16 + mr;
    const int n = nt0 * 16 + 32 * P + 8 * g;
    unsigned xv[4];
#pragma unroll
    for (int i = 0; i < 4; ++i)
      xv[i] = __builtin_amdgcn_raw_buffer_load_b32(
          rs, (int)OFF_X1 + (m * 64 + (((n >> 1) + i) & 63)) * 16 + (n >> 7) * 4, 0, AUX_SC1);
    const f4 g0 = __builtin_bit_cast(f4, LD128(OFF_MODF + (2560 + n) * 4));
    const f4 g1 = __builtin_bit_cast(f4, LD128(OFF_MODF + (2560 + n + 4) * 4));
    f4* red = (f4*)dsm;
#pragma unroll
    for (int nt = 0; nt < 4; ++nt)
#pragma unroll
      for (int mt = 0; mt < 4; ++mt) red[(w * 16 + nt * 4 + mt) * 64 + lane] = acc[nt][mt];
    __syncthreads();
    f4 s0 = f4{0.f, 0.f, 0.f, 0.f}, s1 = s0;
#pragma unroll
    for (int ww = 0; ww < 8; ++ww) {
      s0 += red[(ww * 16 + (2 * P) * 4 + fm) * 64 + lane];
      s1 += red[(ww * 16 + (2 * P + 1) * 4 + fm) * 64 + lane];
    }
    u4v o;
    {
      f2 t0 = bfr2(f2{g0[0], g0[1]} * bfr2(f2{s0[0], s0[1]})), t1 = bfr2(f2{g0[2], g0[3]} * bfr2(f2{s0[2], s0[3]}));
      f2 t2 = bfr2(f2{g1[0], g1[1]} * bfr2(f2{s1[0], s1[1]})), t3 = bfr2(f2{g1[2], g1[3]} * bfr2(f2{s1[2], s1[3]}));
      o[0] = pack_bf(bf_lo(xv[0]) + t0.x, bf_hi(xv[0]) + t0.y);
      o[1] = pack_bf(bf_lo(xv[1]) + t1.x, bf_hi(xv[1]) + t1.y);
      o[2] = pack_bf(bf_lo(xv[2]) + t2.x, bf_hi(xv[2]) + t2.y);
      o[3] = pack_bf(bf_lo(xv[3]) + t3.x, bf_hi(xv[3]) + t3.y);
    }
    const auto ro = __builtin_amdgcn_make_buffer_rsrc(out, 0, S_ * D_ * 2, 0x00020000);
    __builtin_amdgcn_raw_buffer_store_b128(o, ro, (m * D_ + n) * 2, 0, AUX_SC1);
  }
}

extern "C" void kernel_launch(void* const* d_in, const int* in_sizes, int n_in, void* d_out, int out_size, void* d_ws,
                              size_t ws_size, hipStream_t stream) {
  const u16* x = (const u16*)d_in[0];
  const u16* wr = (const u16*)d_in[1];
  const u16* wi = (const u16*)d_in[2];
  const u16* w1 = (const u16*)d_in[4];
  const u16* w2 = (const u16*)d_in[5];
  const u16* c = (const u16*)d_in[6];
  const u16* wmod = (const u16*)d_in[7];
  char* ws = (char*)d_ws + ((ws_size - (size_t)WS_SPAN) & ~(size_t)((1 << 21) - 1));
  u16* out = (u16*)d_out;
  (void)hipFuncSetAttribute((const void*)kmega, hipFuncAttributeMaxDynamicSharedMemorySize, L_TOTAL);
  k0_mod<<<K0_GEMV + K0_WT + K0_W2 + K0_Z, 512, 0, stream>>>(c, wmod, x, wr, wi, w1, w2, ws);
  kmega<<<256, 512, L_TOTAL, stream>>>(x, w1, w2, ws, out);
}
```

```cpp
#include <hip/hip_runtime.h>
#include <hip/hip_bf16.h>
#include <stdint.h>

typedef float f2 __attribute__((ext_vector_type(2)));
typedef float f4 __attribute__((ext_vector_type(4)));
typedef unsigned u2v __attribute__((ext_vector_type(2)));
typedef unsigned u4v __attribute__((ext_vector_type(4)));
typedef short bf16x8 __attribute__((ext_vector_type(8)));
typedef unsigned short u16;

constexpr int S_ = 2048, D_ = 512, F_ = 1792, DF_ = 257;
constexpr int NSPLIT = 4;

constexpr size_t OFF_CNT = 0;
#define FSPACE 16
#define FREP 16
constexpr int FSP = FSPACE, NREP = FREP;
constexpr size_t FTAB = (size_t)NREP * 256 * FSP;
constexpr size_t OFF_FLG = 16384;
constexpr size_t OFF_FLR = OFF_FLG + FTAB;
constexpr size_t FLAG_BYTES = OFF_FLR + FTAB;
constexpr size_t OFF_MODP = 900 << 10;
constexpr size_t OFF_MODF = OFF_MODP + (size_t)NSPLIT * 3072 * 4;
constexpr size_t OFF_MODC = OFF_MODF + 3072 * 4;
constexpr size_t OFF_WT = 1 << 20;
constexpr size_t OFF_XT = 4 << 20;
constexpr size_t OFF_ZT = 28 << 20;
constexpr size_t OFF_H2F = 12 << 20;
constexpr size_t OFF_X1 = 14 << 20;
constexpr size_t OFF_W1F = 16 << 20;
constexpr size_t OFF_W2F = 18 << 20;
constexpr size_t OFF_HF = 20 << 20;
constexpr int WS_SPAN = 36 << 20;
#define CPITCH (2048 + 32)
constexpr int CP = CPITCH;
constexpr float FSC = 1.0f / (2.0f * 2048.0f * 512.0f);

__device__ __forceinline__ float bf_lo(unsigned u) { return __builtin_bit_cast(float, u << 16); }
__device__ __forceinline__ float bf_hi(unsigned u) { return __builtin_bit_cast(float, u & 0xffff0000u); }
__device__ __forceinline__ float bfr(float x) {
  return (float)(__bf16)x;
}
__device__ __forceinline__ unsigned pack_bf(float lo, float hi) {
  typedef __bf16 b2 __attribute__((ext_vector_type(2)));
  return __builtin_bit_cast(unsigned, __builtin_convertvector(f2{lo, hi}, b2));
}
__device__ __forceinline__ f2 cmul(f2 a, f2 b) {
  f2 t = a * f2{b.x, b.x};
  return __builtin_elementwise_fma(f2{a.y, a.x}, f2{-b.y, b.y}, t);
}
__device__ __forceinline__ f2 bfr2(f2 a) {
  unsigned u = pack_bf(a.x, a.y);
  asm("" : "+v"(u));
  return f2{bf_lo(u), bf_hi(u)};
}
#define PIN2(x) asm volatile("" : "+v"(x))
constexpr int AUX_SC1 = 16;
#define WS_RSRC(ws) __builtin_amdgcn_make_buffer_rsrc((ws), 0, WS_SPAN, 0x00020000)
#define ST128(v, off) __builtin_amdgcn_raw_buffer_store_b128((v), rs, (int)(off), 0, AUX_SC1)
#define ST64(v, off) __builtin_amdgcn_raw_buffer_store_b64((v), rs, (int)(off), 0, AUX_SC1)
#define ST32(v, off) __builtin_amdgcn_raw_buffer_store_b32((v), rs, (int)(off), 0, AUX_SC1)
#define LD64(off) __builtin_amdgcn_raw_buffer_load_b64(rs, (int)(off), 0, AUX_SC1)
#define LD32(off) __builtin_amdgcn_raw_buffer_load_b32(rs, (int)(off), 0, AUX_SC1)
#define LD128(off) __builtin_amdgcn_raw_buffer_load_b128(rs, (int)(off), 0, AUX_SC1)
__device__ __forceinline__ f2 twid(float rev) { return f2{__builtin_amdgcn_cosf(rev), -__builtin_amdgcn_sinf(rev)}; }

template <int DIR>
__device__ __forceinline__ void dft4(f2& c0, f2& c1, f2& c2, f2& c3) {
  f2 d0 = c0 + c2, d2 = c0 - c2, d1 = c1 + c3, e = c1 - c3;
  c0 = d0 + d1;
  c2 = d0 - d1;
  const f2 es = f2{e.y, e.x};
  c1 = __builtin_elementwise_fma(es, DIR < 0 ? f2{1.f, -1.f} : f2{-1.f, 1.f}, d2);
  c3 = __builtin_elementwise_fma(es, DIR < 0 ? f2{-1.f, 1.f} : f2{1.f, -1.f}, d2);
}

__device__ __forceinline__ void sw32(float& a, float& b) {
  auto r = __builtin_amdgcn_permlane32_swap(__builtin_bit_cast(unsigned, a), __builtin_bit_cast(unsigned, b), false, false);
  a = __builtin_bit_cast(float, (unsigned)r[0]);
  b = __builtin_bit_cast(float, (unsigned)r[1]);
}
__device__ __forceinline__ void sw16(float& a, float& b) {
  auto r = __builtin_amdgcn_permlane16_swap(__builtin_bit_cast(unsigned, a), __builtin_bit_cast(unsigned, b), false, false);
  a = __builtin_bit_cast(float, (unsigned)r[0]);
  b = __builtin_bit_cast(float, (unsigned)r[1]);
}
template <int CTRL, int BANK>
__device__ __forceinline__ float udpp(float old, float src) {
  return __builtin_bit_cast(float, __builtin_amdgcn_update_dpp(__builtin_bit_cast(int, old), __builtin_bit_cast(int, src),
                                                               CTRL, 0xf, BANK, false));
}
template <int CTRL>
__device__ __forceinline__ float mdpp(float src) {
  return __builtin_bit_cast(float, __builtin_amdgcn_mov_dpp(__builtin_bit_cast(int, src), CTRL, 0xf, 0xf, false));
}
__device__ __forceinline__ void xp_hi1(float& v0, float& v1, float& v2, float& v3) {
  sw32(v0, v2); sw32(v1, v3); sw16(v0, v1); sw16(v2, v3);
}
__device__ __forceinline__ void xp_mid1(float& v0, float& v1, float& v2, float& v3) {
  float a0 = udpp<0x128, 0xC>(v0, v2), a2 = udpp<0x128, 0x3>(v2, v0);
  float a1 = udpp<0x128, 0xC>(v1, v3), a3 = udpp<0x128, 0x3>(v3, v1);
  v0 = udpp<0x124, 0xA>(a0, a1); v1 = udpp<0x12C, 0x5>(a1, a0);
  v2 = udpp<0x124, 0xA>(a2, a3); v3 = udpp<0x12C, 0x5>(a3, a2);
}
#define XP_CPLX(NAME, FN, ...)                                        \
  {                                                                   \
    float x0 = v[0].x, x1 = v[1].x, x2 = v[2].x, x3 = v[3].x;         \
    float y0 = v[0].y, y1 = v[1].y, y2 = v[2].y, y3 = v[3].y;         \
    FN(x0, x1, x2, x3, ##__VA_ARGS__);                                \
    FN(y0, y1, y2, y3, ##__VA_ARGS__);                                \
    v[0] = f2{x0, y0}; v[1] = f2{x1, y1}; v[2] = f2{x2, y2}; v[3] = f2{x3, y3}; \
  }
__device__ __forceinline__ void xp_hi(f2 (&v)[4]) XP_CPLX(hi, xp_hi1)
__device__ __forceinline__ void xp_mid(f2 (&v)[4]) XP_CPLX(mid, xp_mid1)

template <bool CONJ>
__device__ __forceinline__ void cmul3(f2 (&v)[4], const f2 (&t)[3]) {
  f2 a, b, c;
  if (!CONJ)
    asm("v_pk_mul_f32 %3, %0, %6 op_sel_hi:[1,0]\n\t"
        "v_pk_mul_f32 %4, %1, %7 op_sel_hi:[1,0]\n\t"
        "v_pk_mul_f32 %5, %2, %8 op_sel_hi:[1,0]\n\t"
        "v_pk_fma_f32 %0, %0, %6, %3 op_sel:[1,1,0] op_sel_hi:[0,1,1] neg_lo:[0,1,0]\n\t"
        "v_pk_fma_f32 %1, %1, %7, %4 op_sel:[1,1,0] op_sel_hi:[0,1,1] neg_lo:[0,1,0]\n\t"
        "v_pk_fma_f32 %2, %2, %8, %5 op_sel:[1,1,0] op_sel_hi:[0,1,1] neg_lo:[0,1,0]\n\t"
        "s_nop 1"
        : "+v"(v[1]), "+v"(v[2]), "+v"(v[3]), "=&v"(a), "=&v"(b), "=&v"(c)
        : "v"(t[0]), "v"(t[1]), "v"(t[2]));
  else
    asm("v_pk_mul_f32 %3, %0, %6 op_sel_hi:[1,0]\n\t"
        "v_pk_mul_f32 %4, %1, %7 op_sel_hi:[1,0]\n\t"
        "v_pk_mul_f32 %5, %2, %8 op_sel_hi:[1,0]\n\t"
        "v_pk_fma_f32 %0, %0, %6, %3 op_sel:[1,1,0] op_sel_hi:[0,1,1] neg_hi:[0,1,0]\n\t"
        "v_pk_fma_f32 %1, %1, %7, %4 op_sel:[1,1,0] op_sel_hi:[0,1,1] neg_hi:[0,1,0]\n\t"
        "v_pk_fma_f32 %2, %2, %8, %5 op_sel:[1,1,0] op_sel_hi:[0,1,1] neg_hi:[0,1,0]"
        : "+v"(v[1]), "+v"(v[2]), "+v"(v[3]), "=&v"(a), "=&v"(b), "=&v"(c)
        : "v"(t[0]), "v"(t[1]), "v"(t[2]));
}
template <bool CONJ>
__device__ __forceinline__ f2 cmul1(f2 v, f2 t) {
  f2 a;
  if (!CONJ)
    asm("v_pk_mul_f32 %1, %0, %2 op_sel_hi:[1,0]\n\t"
        "v_pk_fma_f32 %0, %0, %2, %1 op_sel:[1,1,0] op_sel_hi:[0,1,1] neg_lo:[0,1,0]"
        : "+v"(v), "=&v"(a) : "v"(t));
  else
    asm("v_pk_mul_f32 %1, %0, %2 op_sel_hi:[1,0]\n\t"
        "v_pk_fma_f32 %0, %0, %2, %1 op_sel:[1,1,0] op_sel_hi:[0,1,1] neg_hi:[0,1,0]"
        : "+v"(v), "=&v"(a) : "v"(t));
  return v;
}
__device__ __forceinline__ void xp_lo_lds(f2 (&v)[4], f2* patch, int lane) {
  f2* q = patch + (lane >> 2) * 16;
  const int c = lane & 3, sw = (lane >> 2) & 3;
#pragma unroll
  for (int j = 0; j < 4; ++j) q[4 * (j ^ sw) + c] = v[j];
  __builtin_amdgcn_fence(__ATOMIC_RELEASE, "wavefront");
  __builtin_amdgcn_wave_barrier();
  __builtin_amdgcn_fence(__ATOMIC_ACQUIRE, "wavefront");
  const f4 a = *(const f4*)(q + 4 * (c ^ sw)), b = *(const f4*)(q + 4 * (c ^ sw) + 2);
  v[0] = f2{a[0], a[1]}; v[1] = f2{a[2], a[3]}; v[2] = f2{b[0], b[1]}; v[3] = f2{b[2], b[3]};
  __builtin_amdgcn_fence(__ATOMIC_RELEASE, "wavefront");
  __builtin_amdgcn_wave_barrier();
}

struct Tw256 {
  f2 a[3], b[3], c[3];
};
__device__ __forceinline__ void make_tw256(int lane, Tw256& t) {
#pragma unroll
  for (int r = 1; r < 4; ++r) {
    t.a[r - 1] = twid((float)(lane * r) * (1.0f / 256.0f));
    t.b[r - 1] = twid((float)((lane & 15) * r) * (1.0f / 64.0f));
    t.c[r - 1] = twid((float)((lane & 3) * r) * (1.0f / 16.0f));
  }
#pragma unroll
  for (int r = 0; r < 3; ++r) {
    PIN2(t.a[r]);
    PIN2(t.b[r]);
    PIN2(t.c[r]);
  }
}
struct NoHook {
  __device__ __forceinline__ void operator()(int) const {}
};
template <class H>
__device__ __forceinline__ void fft256_fwd(f2 (&v)[4], int lane, const Tw256& t, f2* patch, const H& hook) {
  dft4<-1>(v[0], v[1], v[2], v[3]);
  cmul3<false>(v, t.a);
  xp_hi(v);
  hook(0);
  dft4<-1>(v[0], v[1], v[2], v[3]);
  cmul3<false>(v, t.b);
  xp_mid(v);
  hook(1);
  dft4<-1>(v[0], v[1], v[2], v[3]);
  cmul3<false>(v, t.c);
  xp_lo_lds(v, patch, lane);
  hook(2);
  dft4<-1>(v[0], v[1], v[2], v[3]);
}
template <class H>
__device__ __forceinline__ void fft256_inv(f2 (&v)[4], int lane, const Tw256& t, f2* patch, const H& hook) {
  dft4<1>(v[0], v[1], v[2], v[3]);
  xp_lo_lds(v, patch, lane);
  hook(0);
  cmul3<true>(v, t.c);
  dft4<1>(v[0], v[1], v[2], v[3]);
  xp_mid(v);
  hook(1);
  cmul3<true>(v, t.b);
  dft4<1>(v[0], v[1], v[2], v[3]);
  xp_hi(v);
  hook(2);
  cmul3<true>(v, t.a);
  dft4<1>(v[0], v[1], v[2], v[3]);
}

__device__ __forceinline__ float wave_sum(float x) {
  x += mdpp<0xB1>(x);
  x += mdpp<0x4E>(x);
  x += mdpp<0x141>(x);
  x += mdpp<0x140>(x);
  float r0 = __builtin_bit_cast(float, __builtin_amdgcn_readlane(__builtin_bit_cast(int, x), 0));
  float r1 = __builtin_bit_cast(float, __builtin_amdgcn_readlane(__builtin_bit_cast(int, x), 16));
  float r2 = __builtin_bit_cast(float, __builtin_amdgcn_readlane(__builtin_bit_cast(int, x), 32));
  float r3 = __builtin_bit_cast(float, __builtin_amdgcn_readlane(__builtin_bit_cast(int, x), 48));
  return (r0 + r1) + (r2 + r3);
}

__device__ __forceinline__ void ln_stats(const f2 (&v)[4], float& mean, float& rden) {
  const float K = __builtin_bit_cast(float, __builtin_amdgcn_readfirstlane(__builtin_bit_cast(int, v[0].x)));
  f2 s2 = f2{0.f, 0.f}, q2 = f2{0.f, 0.f};
#pragma unroll
  for (int j = 0; j < 4; ++j) {
    const f2 d = v[j] - K;
    s2 += d;
    q2 = __builtin_elementwise_fma(d, d, q2);
  }
  const float sm = wave_sum(s2.x + s2.y) * (1.0f / D_), qm = wave_sum(q2.x + q2.y) * (1.0f / D_);
  const float var = fmaxf(qm - sm * sm, 0.f);
  mean = bfr(K + sm);
  rden = __builtin_amdgcn_rcpf(bfr(__builtin_amdgcn_sqrtf(bfr(bfr(var) + 1e-5f))));
}

__device__ __forceinline__ int zpos(int k) { return 17 * (k & 3) + 4 * ((k >> 2) & 3) + ((k >> 4) & 3) + 68 * (k >> 6); }
constexpr int ZRS = 276;

constexpr int K0_GEMV = 48 * NSPLIT, K0_WT = 128, K0_W2 = 224, K0_Z = (int)(FLAG_BYTES / 8192);
__global__ __launch_bounds__(512) void k0_mod(const u16* __restrict__ c, const u16* __restrict__ wmod,
                                              const u16* __restrict__ x, const u16* __restrict__ wr,
                                              const u16* __restrict__ wi, const u16* __restrict__ w1,
                                              const u16* __restrict__ w2, char* __restrict__ ws) {
  __shared__ __attribute__((aligned(16))) char smem[17408];
  const int tid = threadIdx.x, row = tid >> 6, lane = tid & 63;
  const auto rs = WS_RSRC(ws);
  int b = blockIdx.x;
  if (b >= K0_GEMV + K0_WT + K0_W2) {
    int q = b - (K0_GEMV + K0_WT + K0_W2);
    if (q < K0_Z) {
      ((u4v*)ws)[q * 512 + tid] = u4v{0u, 0u, 0u, 0u};
      return;
    }
    return;
  }
  if (b >= K0_GEMV) {
    b -= K0_GEMV;
    if (b < K0_WT) {
      char* sr = smem;
      char* si = smem + 16 * 544;
      constexpr int LASTCH = (S_ * DF_ * 2) / 16 - 1;
      for (int it = tid; it < 16 * 34; it += 512) {
        const int i = it / 34, ch = it % 34;
        int gch = ((b + 128 * i) * (DF_ * 2)) / 16 + ch;
        gch = gch > LASTCH ? LASTCH : gch;
        *(u4v*)(sr + i * 544 + ch * 16) = *(const u4v*)((const char*)wr + (size_t)gch * 16);
        *(u4v*)(si + i * 544 + ch * 16) = *(const u4v*)((const char*)wi + (size_t)gch * 16);
      }
      __syncthreads();
      const int tbase = 64 * (b & 7) + 16 * ((b >> 3) & 3) + 4 * ((b >> 5) & 3);
      for (int it = tid; it < DF_ * 4; it += 512) {
        const int k = it >> 2, r3 = it & 3;
        u4v o;
#pragma unroll
        for (int r4 = 0; r4 < 4; ++r4) {
          const int i = r3 + 4 * r4;
          const int off = i * 544 + (((b + 128 * i) * (DF_ * 2)) & 15) + 2 * k;
          o[r4] = (unsigned)*(const u16*)(sr + off) | ((unsigned)*(const u16*)(si + off) << 16);
        }
        ST128(o, OFF_WT + (k * 512 + tbase + r3) * 16);
      }
    } else {
      b -= K0_WT;
      u16* t = (u16*)smem;
      const int g = lane >> 4, r = lane & 15;
      if (b >= 112) {
        b -= 112;
        const int fg = b % 14, kp = b / 14;
#pragma unroll
        for (int it = 0; it < 2; ++it) {
          const int idx = it * 512 + tid;
          const int rr = idx >> 4, ch = idx & 15;
          const int ks = kp * 2 + (rr >> 5), gg = (rr >> 3) & 3, jj = rr & 7;
          const int d = 2 * (4 * ks + gg) + 128 * (jj >> 1) + (jj & 1);
          *(u4v*)(t + rr * 136 + ch * 8) = *(const u4v*)(w1 + (size_t)d * F_ + fg * 128 + ch * 8);
        }
        __syncthreads();
        const int ft = fg * 8 + row, cc = ft / 7, fl = ft - 7 * cc;
#pragma unroll
        for (int ksl = 0; ksl < 2; ++ksl) {
          u4v o;
#pragma unroll
          for (int jj = 0; jj < 4; ++jj)
            o[jj] = (unsigned)t[(ksl * 32 + g * 8 + 2 * jj) * 136 + row * 16 + r] |
                    ((unsigned)t[(ksl * 32 + g * 8 + 2 * jj + 1) * 136 + row * 16 + r] << 16);
          ST128(o, OFF_W1F + (((cc * 16 + kp * 2 + ksl) * 7 + fl) * 64 + lane) * 16);
        }
        return;
      }
      const int ng = b & 3, kp = b >> 2;
#pragma unroll
      for (int it = 0; it < 2; ++it) {
        const int idx = it * 512 + tid;
        const int rr = idx >> 4, ch = idx & 15;
        const int ks2 = kp * 2 + (rr >> 5), gg = (rr >> 3) & 3, j = rr & 7;
        const int e = ks2 / 7, i7 = ks2 % 7;
        int T0, T1;
        if (i7 < 3) { T0 = 14 * e + 2 * i7; T1 = T0 + 1; }
        else if (i7 < 6) { T0 = 14 * e + 7 + 2 * (i7 - 3); T1 = T0 + 1; }
        else { T0 = 14 * e + 6; T1 = 14 * e + 13; }
        const int f = (j < 4) ? (16 * T0 + 4 * gg + j) : (16 * T1 + 4 * gg + (j - 4));
        *(u4v*)(t + rr * 136 + ch * 8) = *(const u4v*)(w2 + (size_t)f * D_ + ng * 128 + ch * 8);
      }
      __syncthreads();
      const int nl = 32 * (row >> 1) + 8 * (r >> 2) + 4 * (row & 1) + (r & 3);
#pragma unroll
      for (int ksl = 0; ksl < 2; ++ksl) {
        u4v o;
#pragma unroll
        for (int jj = 0; jj < 4; ++jj)
          o[jj] = (unsigned)t[(ksl * 32 + g * 8 + 2 * jj) * 136 + nl] |
                  ((unsigned)t[(ksl * 32 + g * 8 + 2 * jj + 1) * 136 + nl] << 16);
        const int nt = ng * 8 + row;
        ST128(o, OFF_W2F + ((nt * 56 + kp * 2 + ksl) * 64 + lane) * 16);
      }
    }
    return;
  }
  float* red = (float*)smem;
  const int cgp = b % 48, ks = b / 48;
  const int cg = tid & 7, r = tid >> 3;
  const int rw = ks * 128 + r;
  const u4v wm0 = *(const u4v*)(wmod + (size_t)rw * 3072 + cgp * 64 + cg * 8);
  const u4v wm1 = *(const u4v*)(wmod + (size_t)(rw + 64) * 3072 + cgp * 64 + cg * 8);
  const float cv0 = bf_lo(c[rw]), cv1 = bf_lo(c[rw + 64]);
  const float s0 = bfr(cv0 / (1.0f + __expf(-cv0))), s1 = bfr(cv1 / (1.0f + __expf(-cv1)));
  float acc[8];
#pragma unroll
  for (int i = 0; i < 4; ++i) {
    acc[2 * i] = s0 * bf_lo(wm0[i]) + s1 * bf_lo(wm1[i]);
    acc[2 * i + 1] = s0 * bf_hi(wm0[i]) + s1 * bf_hi(wm1[i]);
  }
#pragma unroll
  for (int i = 0; i < 8; ++i) {
    acc[i] += mdpp<0x128>(acc[i]);
    acc[i] += __shfl_xor(acc[i], 16);
    acc[i] += __shfl_xor(acc[i], 32);
  }
  if (lane < 8) {
#pragma unroll
    for (int i = 0; i < 8; ++i) red[row * 64 + cg * 8 + i] = acc[i];
  }
  __syncthreads();
  if (tid < 64) {
    float sum = 0.f;
#pragma unroll
    for (int i = 0; i < 8; ++i) sum += red[i * 64 + tid];
    ((float*)(ws + OFF_MODP))[ks * 3072 + cgp * 64 + tid] = sum;
  }
}

__device__ __forceinline__ f4 mfma16(u4v a, u4v b, f4 c) {
  return __builtin_amdgcn_mfma_f32_16x16x32_bf16(__builtin_bit_cast(bf16x8, a), __builtin_bit_cast(bf16x8, b), c, 0, 0,
                                                  0);
}
__device__ __forceinline__ f2 silu2(float a, float b) {
  f2 h = f2{a, b};
  f2 t = h * -1.44269504088896340736f;
  f2 d = f2{__builtin_amdgcn_exp2f(t.x), __builtin_amdgcn_exp2f(t.y)} + 1.0f;
  return h * f2{__builtin_amdgcn_rcpf(d.x), __builtin_amdgcn_rcpf(d.y)};
}
__device__ __forceinline__ unsigned silu_pk(float a, float b) {
  f2 r = silu2(a, b);
  return pack_bf(r.x, r.y);
}
typedef __attribute__((address_space(1))) unsigned gu32;
#define DRAIN() asm volatile("s_waitcnt vmcnt(0)" ::: "memory")
typedef __attribute__((address_space(1))) unsigned long long gu64;
constexpr unsigned SPIN_CAP = 1u << 18;
__device__ __forceinline__ void wait_pair(gu64* flags) {
#pragma nounroll
  for (unsigned spin = 0; spin < SPIN_CAP; ++spin) {
    if (__hip_atomic_load(flags, __ATOMIC_RELAXED, __HIP_MEMORY_SCOPE_AGENT) == 0x0000000100000001ull) break;
    __builtin_amdgcn_s_sleep(1);
  }
  __builtin_amdgcn_fence(__ATOMIC_ACQUIRE, "wavefront");
  asm volatile("" ::: "memory");
}

#define PSLEEP 2
__device__ __forceinline__ void wait_cols(gu32* flags) {
#pragma nounroll
  for (unsigned spin = 0; spin < SPIN_CAP; ++spin) {
    unsigned f = 1u;
#pragma unroll
    for (int j = 0; j < 4; ++j)
      f &= __hip_atomic_load(flags + j * 64 * (FSP / 4), __ATOMIC_RELAXED, __HIP_MEMORY_SCOPE_AGENT);
    if (__builtin_amdgcn_ballot_w64(f != 1u) == 0ull) break;
    __builtin_amdgcn_s_sleep(PSLEEP);
  }
}

__device__ __forceinline__ int wt_off(int k, int t) { return (int)OFF_WT + (k * 512 + t) * 16; }

constexpr int L_W1 = 0;
constexpr int L_T = 112 * 1024;
constexpr int L_ZS = 112 * 1024;
constexpr int L_ST = L_ZS + 8 * ZRS * 8;
constexpr int L_TOTAL = 160 * 1024;
__device__ __forceinline__ constexpr int g1_ft(int g, int j) { return g < 8 ? (j & 3) : (g < 12 ? 4 + (j & 1) : 6); }
__device__ __forceinline__ constexpr int g1_ks(int g, int j) {
  return g < 8 ? 2 * g + (j >> 2) : (g < 12 ? 4 * (g - 8) + (j >> 1) : 8 * (g - 12) + j);
}
#define G1WIN 6
#define SCHED() __builtin_amdgcn_sched_barrier(0)
typedef short v4i16 __attribute__((ext_vector_type(4)));
__device__ __forceinline__ u4v w1frag(const char* lw, int ft, int ks) {
  typedef __attribute__((address_space(3))) v4i16* lp;
  const u2v a = __builtin_bit_cast(u2v, __builtin_amdgcn_ds_read_tr16_b64_v4i16((lp)(lw + (2 * ks) * 3584 + ft * 32)));
  const u2v b = __builtin_bit_cast(u2v, __builtin_amdgcn_ds_read_tr16_b64_v4i16((lp)(lw + (2 * ks + 1) * 3584 + ft * 32)));
  return u4v{a[0], a[1], b[0], b[1]};
}
constexpr int L_MODS = L_ZS + 8 * ZRS * 8;
constexpr int L_PX = L_T + 28672;
__global__ __launch_bounds__(512, 2) void kmega(const u16* __restrict__ x, const u16* __restrict__ w1,
                                                const u16* __restrict__ w2, char* ws, u16* __restrict__ out) {
  extern __shared__ __attribute__((aligned(16))) char dsm[];
  const int tid = threadIdx.x, lane = tid & 63;
  const int w = __builtin_amdgcn_readfirstlane(tid >> 6);
  const int bid = blockIdx.x;
  const int p = (bid & 7) * 2 + (bid >> 7), c = (bid >> 3) & 15;
  const int row0 = 128 * p + 8 * c;
  unsigned cw0;
  {
    unsigned long long pc;
    asm volatile("s_getpc_b64 %0" : "=s"(pc));
    cw0 = *(const gu32*)((pc & ~255ull) + (tid < 256 ? tid : 0) * 64);
  }
  const auto rs = WS_RSRC(ws);
  gu32* myA = (gu32*)(ws + OFF_CNT) + (p * 8 + (c >> 1)) * 16 + (c & 1);
  gu32* myB = myA + 128 * 16;
  gu64* depA = (gu64*)(ws + OFF_CNT) + (p * 8 + w) * 8;
  gu64* depB = depA + 128 * 8;
  gu32* myE = myB + 8;
  gu64* depE = depB + 4;
  const int mt0 = 8 * p + 4 * (c >> 3), nt0 = 4 * (c & 7);
  const char* w1g = ws + OFF_W1F + (size_t)c * (112 * 1024) + w * 1024 + lane * 16;
  char* w1l = dsm + L_W1 + w * 1024 + lane * 16;
  const int Ab = (int)OFF_W2F + ((nt0 * 56 + 7 * w) * 64 + lane) * 16;
  u4v S1[16];
  u4v A2[28];
  auto pf_load = [&](int i) __attribute__((always_inline)) {
    if (i < 14) S1[i] = *(const u4v*)(w1g + i * 8192);
    else if (i >= 16 && i < 44) A2[i - 16] = *(const u4v*)(ws + Ab + (((i - 16) & 3) * 56 + ((i - 16) >> 2)) * 1024);
  };
  auto pf_put = [&](int i) __attribute__((always_inline)) {
    if (i < 14) *(u4v*)(w1l + i * 8192) = S1[i];
  };
  auto pf = [&](int i0, int n) __attribute__((always_inline)) {
    SCHED();
#pragma unroll
    for (int i = 0; i < n; ++i) pf_load(i0 + i);
    SCHED();
  };
  u4v wv, bv;
  unsigned wn[4], bn[4];
  int mirr[4];
  Tw256 tw;
  {
    f2* zs = (f2*)(dsm + L_ZS);
    float* mods = (float*)(dsm + L_MODS);
    const int r0 = bid * 8;
    unsigned xr[4];
    const unsigned* xrow = (const unsigned*)(x + (size_t)(r0 + w) * D_);
#pragma unroll
    for (int j = 0; j < 4; ++j) xr[j] = xrow[lane + 64 * j];
    const float* mp = (const float*)(ws + OFF_MODP);
    f2 sm = *(const f2*)(mp + tid * 2);
#pragma unroll
    for (int k = 1; k < NSPLIT; ++k) sm += *(const f2*)(mp + k * 3072 + tid * 2);
    const int idx = bid * 12 + (tid < 6 ? tid : 0) * 2;
    f2 fin = *(const f2*)(mp + idx);
#pragma unroll
    for (int k = 1; k < NSPLIT; ++k) fin += *(const f2*)(mp + k * 3072 + idx);
    pf(0, 4);
    make_tw256(lane, tw);
    const int rp = tid & 3, kk = tid >> 2;
    f2 wpost = twid((float)kk * (1.0f / 512.0f));
    PIN2(wpost);
    f2 v[4];
#pragma unroll
    for (int j = 0; j < 4; ++j) v[j] = f2{bf_lo(xr[j]), bf_hi(xr[j])};
    float mean, rden;
    ln_stats(v, mean, rden);
    asm volatile("" ::"v"(cw0));
    sm = bfr2(sm);
    if (tid >= 256) sm = bfr2(sm + 1.0f);
    *(f2*)(mods + tid * 2) = sm;
    if (tid < 6) {
      fin = bfr2(fin);
      ST64(__builtin_bit_cast(u2v, fin), OFF_MODF + idx * 4);
      const int vec = (idx >> 9) - 2, d = idx & 511;
      if (vec >= 0 && vec < 3) {
        if (vec == 2) fin += 1.0f;
        ST32(pack_bf(fin.x, fin.y), OFF_MODC + ((vec * 64 + ((d >> 1) & 63)) * 4 + (d >> 7)) * 4);
      }
    }
    __syncthreads();
    wv = LD128(wt_off(bid, tid));
    if (bid == 0) {
      bv = LD128(wt_off(256, tid));
      const int m0 = (lane >> 4) + 4 * ((lane >> 2) & 3) + 16 * (lane & 3);
#pragma unroll
      for (int r4 = 0; r4 < 4; ++r4) {
        const int un = (S_ - (w + 8 * (m0 + 64 * r4))) & (S_ - 1);
        const int mm = un >> 3;
        mirr[r4] = ((un & 7) * 64 + 16 * (mm & 3) + 4 * ((mm >> 2) & 3) + ((mm >> 4) & 3)) * 4 + (mm >> 6);
      }
      if (w != 0) {
        const u4v a4 = LD128(wt_off(0, 64 * (8 - w) + 63 - lane));
        const u4v b4 = LD128(wt_off(256, 64 * (8 - w) + 63 - lane));
#pragma unroll
        for (int r4 = 0; r4 < 4; ++r4) { wn[r4] = a4[3 - r4]; bn[r4] = b4[3 - r4]; }
      } else {
#pragma unroll
        for (int r4 = 0; r4 < 4; ++r4) {
          wn[r4] = LD32(wt_off(0, mirr[r4] >> 2) + (mirr[r4] & 3) * 4);
          bn[r4] = LD32(wt_off(256, mirr[r4] >> 2) + (mirr[r4] & 3) * 4);
        }
      }
    }
    pf(4, 4);
#pragma unroll
    for (int j = 0; j < 4; ++j) {
      const int d = 2 * (lane + 64 * j);
      f2 sa = *(const f2*)(mods + d);
      f2 ca1 = *(const f2*)(mods + 512 + d);
      v[j] = bfr2(__builtin_elementwise_fma(v[j] - mean, ca1 * rden, sa));
    }
    fft256_fwd(v, lane, tw, (f2*)(dsm + L_PX + w * 2048), NoHook{});
    {
      f2* buf = zs + w * ZRS + lane + (lane >> 4);
#pragma unroll
      for (int r4 = 0; r4 < 4; ++r4) buf[68 * r4] = v[r4];
    }
    __syncthreads();
    {
      const int k = kk;
      const int k2 = (k == 0) ? 128 : 256 - k;
      const int pa = zpos(k), pb = zpos(k2);
      f4 oa, ob;
#pragma unroll
      for (int h = 0; h < 2; ++h) {
        const f2* zr = zs + (2 * rp + h) * ZRS;
        f2 za = zr[pa], zb = zr[pb];
        f2 E = f2{za.x + zb.x, za.y - zb.y};
        f2 Dm = f2{za.x - zb.x, za.y + zb.y};
        f2 O = f2{Dm.y, -Dm.x};
        f2 wo = cmul1<false>(O, wpost);
        f2 ra = E + wo;
        f2 t = E - wo;
        f2 rb = f2{t.x, -t.y};
        if (k == 0) {
          ra = f2{2.f * (za.x + za.y), 2.f * (za.x - za.y)};
          rb = f2{2.f * zb.x, -2.f * zb.y};
        }
        oa[2 * h] = ra.x; oa[2 * h + 1] = ra.y;
        ob[2 * h] = rb.x; ob[2 * h + 1] = rb.y;
      }
      ST128(__builtin_bit_cast(u4v, oa), OFF_XT + (k * CP + r0 + 2 * rp) * 8);
      ST128(__builtin_bit_cast(u4v, ob), OFF_XT + (k2 * CP + r0 + 2 * rp) * 8);
    }
    DRAIN();
    __syncthreads();
    if (tid < NREP)
      __hip_atomic_store((gu32*)(ws + OFF_FLR + (tid * 256 + bid) * FSP), 1u, __ATOMIC_RELAXED, __HIP_MEMORY_SCOPE_AGENT);
  }
  {
    f2* T = (f2*)(dsm + L_T);
    f2* T2 = (f2*)(dsm + L_T + 16384);
    f2* T3 = (f2*)(dsm + L_T + 32768);
    const int k = bid;
    f2 tw1[4];
#pragma unroll
    for (int j = 0; j < 4; ++j) {
      tw1[j] = twid((float)((lane + 64 * j) * w) * (1.0f / 2048.0f));
      PIN2(tw1[j]);
    }
    f2 wp[4], wm[4];
    if (k == 0) {
#pragma unroll
      for (int r4 = 0; r4 < 4; ++r4) {
        f2 h0 = f2{bf_lo(wv[r4]) + bf_lo(wn[r4]), bf_hi(wv[r4]) - bf_hi(wn[r4])};
        f2 h1 = f2{bf_lo(bv[r4]) + bf_lo(bn[r4]), bf_hi(bv[r4]) - bf_hi(bn[r4])};
        wp[r4] = (h0 + h1) * 0.25f;
        wm[r4] = (h0 - h1) * 0.25f;
        PIN2(wp[r4]);
        PIN2(wm[r4]);
      }
    } else {
#pragma unroll
      for (int r4 = 0; r4 < 4; ++r4) {
        wp[r4] = f2{bf_lo(wv[r4]), bf_hi(wv[r4])};
        PIN2(wp[r4]);
      }
    }
    pf(28, 3);
    if (w == 0) wait_cols((gu32*)(ws + OFF_FLR + ((bid % NREP) * 256 + lane) * FSP));
    asm volatile("s_barrier" ::: "memory");
    __builtin_amdgcn_fence(__ATOMIC_ACQUIRE, "wavefront");
    f2 v[4];
#pragma unroll
    for (int j = 0; j < 4; ++j) v[j] = __builtin_bit_cast(f2, LD64(OFF_XT + (k * CP + tid + 512 * j) * 8));
#pragma unroll
    for (int j = 0; j < 4; ++j) PIN2(v[j]);
    pf(8, 4);
    const int hb = w >> 2, a = tid & 255;
    constexpr float R = 0.70710678118654752440f;
    dft4<-1>(v[0], v[1], v[2], v[3]);
    if (hb) {
      v[1] = f2{v[1].x + v[1].y, v[1].y - v[1].x} * R;
      v[2] = f2{v[2].y, -v[2].x};
      v[3] = f2{v[3].y - v[3].x, -v[3].x - v[3].y} * R;
    }
#pragma unroll
    for (int q = 0; q < 4; ++q) T[(hb * 4 + q) * 256 + a] = v[q];
    __syncthreads();
    {
      const int q = w & 3;
      const float sg = (w < 4) ? 1.f : -1.f;
#pragma unroll
      for (int j = 0; j < 4; ++j) {
        f2 e = T[q * 256 + lane + 64 * j], o = T[(4 + q) * 256 + lane + 64 * j];
        v[j] = cmul1<false>(e + o * sg, tw1[j]);
      }
    }
    fft256_fwd(v, lane, tw, T2 + w * 256, [&](int i) __attribute__((always_inline)) {
      SCHED();
      if (i < 2) {
#pragma unroll
        for (int q = 0; q < 4; ++q) pf_put(4 * i + q);
      } else {
#pragma unroll
        for (int q = 12; q < 16; ++q) pf_load(q);
      }
      SCHED();
    });
    if (k != 0) {
#pragma unroll
      for (int i = 0; i < 4; ++i) {
        v[i] = cmul1<false>(v[i], wp[i]);
      }
    } else {
#pragma unroll
      for (int r4 = 0; r4 < 4; ++r4) T3[r4 * 512 + tid] = v[r4];
      __syncthreads();
#pragma unroll
      for (int r4 = 0; r4 < 4; ++r4) {
        f2 cm = T3[(mirr[r4] & 3) * 512 + (mirr[r4] >> 2)];
        v[r4] = cmul1<false>(v[r4], wp[r4]) + cmul1<false>(f2{cm.x, -cm.y}, wm[r4]);
      }
    }
    fft256_inv(v, lane, tw, T2 + w * 256, [&](int i) __attribute__((always_inline)) {
      SCHED();
      if (i == 0) {
#pragma unroll
        for (int q = 8; q < 12; ++q) pf_put(q);
      }
      if (i == 1) {
#pragma unroll
        for (int q = 16; q < 20; ++q) pf_load(q);
      }
      SCHED();
    });
#pragma unroll
    for (int j = 0; j < 4; ++j) T2[w * 256 + lane + 64 * j] = cmul1<true>(v[j], tw1[j]);
#pragma unroll
    for (int q = 12; q < 16; ++q) pf_put(q);
    __syncthreads();
    {
      const float sg = hb ? -1.f : 1.f;
#pragma unroll
      for (int q = 0; q < 4; ++q) v[q] = T2[q * 256 + a] + T2[(q + 4) * 256 + a] * sg;
      if (hb) {
        v[1] = f2{v[1].x - v[1].y, v[1].y + v[1].x} * R;
        v[2] = f2{-v[2].y, v[2].x};
        v[3] = f2{-v[3].x - v[3].y, v[3].x - v[3].y} * R;
      }
    }
    dft4<1>(v[0], v[1], v[2], v[3]);
#pragma unroll
    for (int j = 0; j < 4; ++j) ST64(__builtin_bit_cast(u2v, v[j]), OFF_ZT + (k * CP + tid + 512 * j) * 8);
    DRAIN();
    __syncthreads();
    if (tid < NREP)
      __hip_atomic_store((gu32*)(ws + OFF_FLG + (tid * 256 + k) * FSP), 1u, __ATOMIC_RELAXED, __HIP_MEMORY_SCOPE_AGENT);
  }
  {
    f2* zs = (f2*)(dsm + L_ZS);
    const int rp = tid & 3, k = tid >> 2;
    const int k2 = (k == 0) ? 128 : 256 - k;
    u4v xr;
    const unsigned* xrow = (const unsigned*)(x + (size_t)(row0 + w) * D_);
#pragma unroll
    for (int j = 0; j < 4; ++j) xr[j] = xrow[lane + 64 * j];
    const u4v gav = LD128(OFF_MODC + lane * 16);
    const u4v sbv = LD128(OFF_MODC + 1024 + lane * 16);
    const u4v cbv = LD128(OFF_MODC + 2048 + lane * 16);
    f2 wq = twid((float)k * (1.0f / 512.0f));
    PIN2(wq);
    const int pa = zpos(k), pb = zpos(k2);
    pf(31, 3);
    if (w == 0) wait_cols((gu32*)(ws + OFF_FLG + ((bid % NREP) * 256 + lane) * FSP));
    asm volatile("s_barrier" ::: "memory");
    __builtin_amdgcn_fence(__ATOMIC_ACQUIRE, "wavefront");
    const f4 ya = __builtin_bit_cast(f4, LD128(OFF_ZT + (k * CP + row0 + 2 * rp) * 8));
    const f4 yb = __builtin_bit_cast(f4, LD128(OFF_ZT + (k2 * CP + row0 + 2 * rp) * 8));
#pragma unroll
    for (int h = 0; h < 2; ++h) {
      f2 a = f2{ya[2 * h], ya[2 * h + 1]}, b = f2{yb[2 * h], yb[2 * h + 1]};
      f2* zr = zs + (2 * rp + h) * ZRS;
      f2 Ze = f2{a.x + b.x, a.y - b.y};
      f2 Dm = f2{a.x - b.x, a.y + b.y};
      f2 Zo = cmul1<true>(Dm, wq);
      f2 za = f2{Ze.x - Zo.y, Ze.y + Zo.x};
      f2 zb = f2{Ze.x + Zo.y, -Ze.y + Zo.x};
      if (k == 0) {
        za = f2{a.x + a.y, a.x - a.y};
        zb = f2{2.f * b.x, -2.f * b.y};
      }
      zr[pa] = za;
      zr[pb] = zb;
    }
    pf(20, 4);
    __syncthreads();
    f2 v[4];
    {
      const f2* buf = zs + w * ZRS + lane + (lane >> 4);
#pragma unroll
      for (int r4 = 0; r4 < 4; ++r4) v[r4] = buf[68 * r4];
    }
    fft256_inv(v, lane, tw, (f2*)(dsm + L_PX + w * 2048), NoHook{});
    pf(24, 4);
    u4v x1p;
#pragma unroll
    for (int j = 0; j < 4; ++j) {
      f2 t = __builtin_elementwise_fma(f2{bf_lo(gav[j]), bf_hi(gav[j])} * FSC, v[j], f2{bf_lo(xr[j]), bf_hi(xr[j])});
      x1p[j] = pack_bf(t.x, t.y);
      v[j] = f2{bf_lo(x1p[j]), bf_hi(x1p[j])};
    }
    ST128(x1p, OFF_X1 + ((row0 + w) * 64 + lane) * 16);
    float mean, rden;
    ln_stats(v, mean, rden);
    u4v hp;
#pragma unroll
    for (int j = 0; j < 4; ++j) {
      f2 t = __builtin_elementwise_fma(v[j] - mean, f2{bf_lo(cbv[j]), bf_hi(cbv[j])} * rden,
                                       f2{bf_lo(sbv[j]), bf_hi(sbv[j])});
      hp[j] = pack_bf(t.x, t.y);
    }
    *(u4v*)(dsm + L_ST + (lane * 9 + w) * 16) = hp;
    __syncthreads();
    {
      const int ch = tid >> 3, r = tid & 7;
      u4v o = *(const u4v*)(dsm + L_ST + (ch * 9 + r) * 16);
      const int mt = row0 >> 4, r16 = (row0 & 8) + r;
      ST128(o, OFF_H2F + ((mt * 16 + (ch >> 2)) * 64 + (ch & 3) * 16 + r16) * 16);
    }
  }
  DRAIN();
  __syncthreads();
  if (tid == 0) __hip_atomic_store(myA, 1u, __ATOMIC_RELAXED, __HIP_MEMORY_SCOPE_AGENT);
  {
    const int ch = tid >> 3, r = tid & 7;
    u4v t = LD128(OFF_H2F + (((row0 >> 4) * 16 + (ch >> 2)) * 64 + (ch & 3) * 16 + (row0 & 8) + r) * 16);
    asm volatile("" ::"v"(t));
  }
  wait_pair(depA);
  u4v rb0, rb1;
  {
    u4v B[16];
#pragma unroll
    for (int ks = 0; ks < G1WIN; ++ks) B[ks] = LD128(OFF_H2F + (((8 * p + w) * 16 + ks) * 64 + lane) * 16);
    f4 acc[7];
#pragma unroll
    for (int ft = 0; ft < 7; ++ft) acc[ft] = f4{0.f, 0.f, 0.f, 0.f};
    const char* lw = dsm + L_W1 + lane * 16;
    unsigned off2 = L_W1 + lane * 16 + 8 * 7168;
    asm volatile("" : "+v"(off2));
    const char* lw2 = dsm + off2;
    const int e = c >> 1, odd = c & 1;
    const int hb = (int)OFF_HF + (((8 * p + w) * 56 + 7 * e) * 64 + lane) * 16;
    u4v Af[2][8];
#pragma unroll
    for (int j = 0; j < 8; ++j) Af[0][j] = *(const u4v*)(lw + (g1_ks(0, j) * 7 + g1_ft(0, j)) * 1024);
#pragma unroll
    for (int g = 0; g < 14; ++g) {
#pragma unroll
      for (int j = 0; j < 8; ++j) {
        acc[g1_ft(g, j)] = mfma16(Af[g & 1][j], B[g1_ks(g, j)], acc[g1_ft(g, j)]);
        if (g + 1 < 14)
          Af[(g + 1) & 1][j] = g1_ks(g + 1, j) < 8 ? *(const u4v*)(lw + (g1_ks(g + 1, j) * 7 + g1_ft(g + 1, j)) * 1024)
                                                   : *(const u4v*)(lw2 + ((g1_ks(g + 1, j) - 8) * 7 + g1_ft(g + 1, j)) * 1024);
        SCHED();
      }
#pragma unroll
      for (int q = 0; q < 2; ++q)
        if (2 * g + G1WIN + q < 16)
          B[2 * g + G1WIN + q] = LD128(OFF_H2F + (((8 * p + w) * 16 + 2 * g + G1WIN + q) * 64 + lane) * 16);
      if (g >= 5 && g < 10) { pf_load(24 + 2 * g); pf_load(25 + 2 * g); }
      if (g == 8 || g == 9 || g == 12) {
        const int i = g == 12 ? 2 : g - 8;
        f4 a = acc[2 * i], b = acc[2 * i + 1];
        u4v o = u4v{silu_pk(a[0], a[1]), silu_pk(a[2], a[3]), silu_pk(b[0], b[1]), silu_pk(b[2], b[3])};
        ST128(o, hb + (odd * 3 + i) * 1024);
      }
      if (g == 11) {
        DRAIN();
        asm volatile("s_barrier" ::: "memory");
        if (tid == 0) __hip_atomic_store(myE, 1u, __ATOMIC_RELAXED, __HIP_MEMORY_SCOPE_AGENT);
        rb0 = LD128(hb + odd * 3072);
        rb1 = LD128(hb + odd * 3072 + 1024);
      }
      SCHED();
    }
    wait_pair(depE);
    asm volatile("" ::"v"(rb0), "v"(rb1));
    f4 a = acc[6];
    u2v o = u2v{silu_pk(a[0], a[1]), silu_pk(a[2], a[3])};
    ST64(o, hb + 6 * 1024 + odd * 8);
  }
  {
    const int Bb = (int)OFF_HF + ((mt0 * 56 + 7 * w) * 64 + lane) * 16;
    u4v B[7][4];
    constexpr int KO[7] = {0, 1, 3, 4, 2, 5, 6};
    SCHED();
#pragma unroll
    for (int i = 0; i < 2; ++i)
#pragma unroll
      for (int mt = 0; mt < 4; ++mt) B[i][mt] = LD128(Bb + (mt * 56 + KO[i]) * 1024);
    SCHED();
    asm volatile("s_waitcnt vmcnt(8) lgkmcnt(0)\n\ts_barrier" ::: "memory");
    if (tid == 0) __hip_atomic_store(myB, 1u, __ATOMIC_RELAXED, __HIP_MEMORY_SCOPE_AGENT);
    u4v rb2 = LD128((int)OFF_HF + (((8 * p + w) * 56 + 7 * (c >> 1) + 3 * (c & 1) + 2) * 64 + lane) * 16);
    f4 acc[4][4];
#pragma unroll
    for (int i = 0; i < 4; ++i)
#pragma unroll
      for (int j = 0; j < 4; ++j) acc[i][j] = f4{0.f, 0.f, 0.f, 0.f};
#pragma unroll
    for (int i = 2; i < 4; ++i)
#pragma unroll
      for (int mt = 0; mt < 4; ++mt) B[i][mt] = LD128(Bb + (mt * 56 + KO[i]) * 1024);
    SCHED();
#pragma unroll
    for (int i = 0; i < 7; ++i) {
      if (i == 2) {
        wait_pair(depB);
        asm volatile("" ::"v"(rb2));
#pragma unroll
        for (int q = 4; q < 7; ++q)
#pragma unroll
          for (int mt = 0; mt < 4; ++mt) B[q][mt] = LD128(Bb + (mt * 56 + KO[q]) * 1024);
        SCHED();
      }
#pragma unroll
      for (int nt = 0; nt < 4; ++nt)
#pragma unroll
        for (int mt = 0; mt < 4; ++mt) acc[nt][mt] = mfma16(A2[KO[i] * 4 + nt], B[i][mt], acc[nt][mt]);
      SCHED();
    }
    const int g = lane >> 4, mr = lane & 15;
    const int fm = w >> 1, P = w & 1;
    const int m = (mt0 + fm) * 16 + mr;
    const int n = nt0 * 16 + 32 * P + 8 * g;
    unsigned xv[4];
#pragma unroll
    for (int i = 0; i < 4; ++i)
      xv[i] = __builtin_amdgcn_raw_buffer_load_b32(
          rs, (int)OFF_X1 + (m * 64 + (((n >> 1) + i) & 63)) * 16 + (n >> 7) * 4, 0, AUX_SC1);
    const f4 g0 = __builtin_bit_cast(f4, LD128(OFF_MODF + (2560 + n) * 4));
    const f4 g1 = __builtin_bit_cast(f4, LD128(OFF_MODF + (2560 + n + 4) * 4));
    f4* red = (f4*)dsm;
#pragma unroll
    for (int nt = 0; nt < 4; ++nt)
#pragma unroll
      for (int mt = 0; mt < 4; ++mt) red[(w * 16 + nt * 4 + mt) * 64 + lane] = acc[nt][mt];
    __syncthreads();
    f4 s0 = f4{0.f, 0.f, 0.f, 0.f}, s1 = s0;
#pragma unroll
    for (int ww = 0; ww < 8; ++ww) {
      s0 += red[(ww * 16 + (2 * P) * 4 + fm) * 64 + lane];
      s1 += red[(ww * 16 + (2 * P + 1) * 4 + fm) * 64 + lane];
    }
    u4v o;
    {
      f2 t0 = bfr2(f2{g0[0], g0[1]} * bfr2(f2{s0[0], s0[1]})), t1 = bfr2(f2{g0[2], g0[3]} * bfr2(f2{s0[2], s0[3]}));
      f2 t2 = bfr2(f2{g1[0], g1[1]} * bfr2(f2{s1[0], s1[1]})), t3 = bfr2(f2{g1[2], g1[3]} * bfr2(f2{s1[2], s1[3]}));
      o[0] = pack_bf(bf_lo(xv[0]) + t0.x, bf_hi(xv[0]) + t0.y);
      o[1] = pack_bf(bf_lo(xv[1]) + t1.x, bf_hi(xv[1]) + t1.y);
      o[2] = pack_bf(bf_lo(xv[2]) + t2.x, bf_hi(xv[2]) + t2.y);
      o[3] = pack_bf(bf_lo(xv[3]) + t3.x, bf_hi(xv[3]) + t3.y);
    }
    const auto ro = __builtin_amdgcn_make_buffer_rsrc(out, 0, S_ * D_ * 2, 0x00020000);
    __builtin_amdgcn_raw_buffer_store_b128(o, ro, (m * D_ + n) * 2, 0, AUX_SC1);
  }
}

extern "C" void kernel_launch(void* const* d_in, const int* in_sizes, int n_in, void* d_out, int out_size, void* d_ws,
                              size_t ws_size, hipStream_t stream) {
  const u16* x = (const u16*)d_in[0];
  const u16* wr = (const u16*)d_in[1];
  const u16* wi = (const u16*)d_in[2];
  const u16* w1 = (const u16*)d_in[4];
  const u16* w2 = (const u16*)d_in[5];
  const u16* c = (const u16*)d_in[6];
  const u16* wmod = (const u16*)d_in[7];
  char* ws = (char*)d_ws + ((ws_size - (size_t)WS_SPAN) & ~(size_t)((1 << 21) - 1));
  u16* out = (u16*)d_out;
  (void)hipFuncSetAttribute((const void*)kmega, hipFuncAttributeMaxDynamicSharedMemorySize, L_TOTAL);
  k0_mod<<<K0_GEMV + K0_WT + K0_W2 + K0_Z, 512, 0, stream>>>(c, wmod, x, wr, wi, w1, w2, ws);
  kmega<<<256, 512, L_TOTAL, stream>>>(x, w1, w2, ws, out);
}
```

```cpp
#include <hip/hip_runtime.h>
#include <hip/hip_bf16.h>
#include <stdint.h>

typedef float f2 __attribute__((ext_vector_type(2)));
typedef float f4 __attribute__((ext_vector_type(4)));
typedef unsigned u2v __attribute__((ext_vector_type(2)));
typedef unsigned u4v __attribute__((ext_vector_type(4)));
typedef short bf16x8 __attribute__((ext_vector_type(8)));
typedef unsigned short u16;

constexpr int S_ = 2048, D_ = 512, F_ = 1792, DF_ = 257;
constexpr int NSPLIT = 4;

constexpr size_t OFF_CNT = 0;
#define FSPACE 16
#define FREP 16
constexpr int FSP = FSPACE, NREP = FREP;
constexpr size_t FTAB = (size_t)NREP * 256 * FSP;
constexpr size_t OFF_FLG = 16384;
constexpr size_t OFF_FLR = OFF_FLG + FTAB;
constexpr size_t FLAG_BYTES = OFF_FLR + FTAB;
constexpr size_t OFF_MODP = 900 << 10;
constexpr size_t OFF_MODF = OFF_MODP + (size_t)NSPLIT * 3072 * 4;
constexpr size_t OFF_MODC = OFF_MODF + 3072 * 4;
constexpr size_t OFF_WT = 1 << 20;
constexpr size_t OFF_XT = 4 << 20;
constexpr size_t OFF_ZT = 28 << 20;
constexpr size_t OFF_H2F = 12 << 20;
constexpr size_t OFF_X1 = 14 << 20;
constexpr size_t OFF_W2F = 18 << 20;
constexpr size_t OFF_HF = 20 << 20;
constexpr int WS_SPAN = 36 << 20;
#define CPITCH (2048 + 32)
constexpr int CP = CPITCH;
constexpr float FSC = 1.0f / (2.0f * 2048.0f * 512.0f);

__device__ __forceinline__ float bf_lo(unsigned u) { return __builtin_bit_cast(float, u << 16); }
__device__ __forceinline__ float bf_hi(unsigned u) { return __builtin_bit_cast(float, u & 0xffff0000u); }
__device__ __forceinline__ float bfr(float x) {
  return (float)(__bf16)x;
}
__device__ __forceinline__ unsigned pack_bf(float lo, float hi) {
  typedef __bf16 b2 __attribute__((ext_vector_type(2)));
  return __builtin_bit_cast(unsigned, __builtin_convertvector(f2{lo, hi}, b2));
}
__device__ __forceinline__ f2 cmul(f2 a, f2 b) {
  f2 t = a * f2{b.x, b.x};
  return __builtin_elementwise_fma(f2{a.y, a.x}, f2{-b.y, b.y}, t);
}
__device__ __forceinline__ f2 bfr2(f2 a) {
  unsigned u = pack_bf(a.x, a.y);
  asm("" : "+v"(u));
  return f2{bf_lo(u), bf_hi(u)};
}
#define PIN2(x) asm volatile("" : "+v"(x))
constexpr int AUX_SC1 = 16;
#define WS_RSRC(ws) __builtin_amdgcn_make_buffer_rsrc((ws), 0, WS_SPAN, 0x00020000)
#define ST128(v, off) __builtin_amdgcn_raw_buffer_store_b128((v), rs, (int)(off), 0, AUX_SC1)
#define ST64(v, off) __builtin_amdgcn_raw_buffer_store_b64((v), rs, (int)(off), 0, AUX_SC1)
#define ST32(v, off) __builtin_amdgcn_raw_buffer_store_b32((v), rs, (int)(off), 0, AUX_SC1)
#define LD64(off) __builtin_amdgcn_raw_buffer_load_b64(rs, (int)(off), 0, AUX_SC1)
#define LD32(off) __builtin_amdgcn_raw_buffer_load_b32(rs, (int)(off), 0, AUX_SC1)
#define LD128(off) __builtin_amdgcn_raw_buffer_load_b128(rs, (int)(off), 0, AUX_SC1)
__device__ __forceinline__ f2 twid(float rev) { return f2{__builtin_amdgcn_cosf(rev), -__builtin_amdgcn_sinf(rev)}; }

template <int DIR>
__device__ __forceinline__ void dft4(f2& c0, f2& c1, f2& c2, f2& c3) {
  f2 d0 = c0 + c2, d2 = c0 - c2, d1 = c1 + c3, e = c1 - c3;
  c0 = d0 + d1;
  c2 = d0 - d1;
  const f2 es = f2{e.y, e.x};
  c1 = __builtin_elementwise_fma(es, DIR < 0 ? f2{1.f, -1.f} : f2{-1.f, 1.f}, d2);
  c3 = __builtin_elementwise_fma(es, DIR < 0 ? f2{-1.f, 1.f} : f2{1.f, -1.f}, d2);
}

__device__ __forceinline__ void sw32(float& a, float& b) {
  auto r = __builtin_amdgcn_permlane32_swap(__builtin_bit_cast(unsigned, a), __builtin_bit_cast(unsigned, b), false, false);
  a = __builtin_bit_cast(float, (unsigned)r[0]);
  b = __builtin_bit_cast(float, (unsigned)r[1]);
}
__device__ __forceinline__ void sw16(float& a, float& b) {
  auto r = __builtin_amdgcn_permlane16_swap(__builtin_bit_cast(unsigned, a), __builtin_bit_cast(unsigned, b), false, false);
  a = __builtin_bit_cast(float, (unsigned)r[0]);
  b = __builtin_bit_cast(float, (unsigned)r[1]);
}
template <int CTRL, int BANK>
__device__ __forceinline__ float udpp(float old, float src) {
  return __builtin_bit_cast(float, __builtin_amdgcn_update_dpp(__builtin_bit_cast(int, old), __builtin_bit_cast(int, src),
                                                               CTRL, 0xf, BANK, false));
}
template <int CTRL>
__device__ __forceinline__ float mdpp(float src) {
  return __builtin_bit_cast(float, __builtin_amdgcn_mov_dpp(__builtin_bit_cast(int, src), CTRL, 0xf, 0xf, false));
}
__device__ __forceinline__ void xp_hi1(float& v0, float& v1, float& v2, float& v3) {
  sw32(v0, v2); sw32(v1, v3); sw16(v0, v1); sw16(v2, v3);
}
__device__ __forceinline__ void xp_mid1(float& v0, float& v1, float& v2, float& v3) {
  float a0 = udpp<0x128, 0xC>(v0, v2), a2 = udpp<0x128, 0x3>(v2, v0);
  float a1 = udpp<0x128, 0xC>(v1, v3), a3 = udpp<0x128, 0x3>(v3, v1);
  v0 = udpp<0x124, 0xA>(a0, a1); v1 = udpp<0x12C, 0x5>(a1, a0);
  v2 = udpp<0x124, 0xA>(a2, a3); v3 = udpp<0x12C, 0x5>(a3, a2);
}
#define XP_CPLX(NAME, FN, ...)                                        \
  {                                                                   \
    float x0 = v[0].x, x1 = v[1].x, x2 = v[2].x, x3 = v[3].x;         \
    float y0 = v[0].y, y1 = v[1].y, y2 = v[2].y, y3 = v[3].y;         \
    FN(x0, x1, x2, x3, ##__VA_ARGS__);                                \
    FN(y0, y1, y2, y3, ##__VA_ARGS__);                                \
    v[0] = f2{x0, y0}; v[1] = f2{x1, y1}; v[2] = f2{x2, y2}; v[3] = f2{x3, y3}; \
  }
__device__ __forceinline__ void xp_hi(f2 (&v)[4]) XP_CPLX(hi, xp_hi1)
__device__ __forceinline__ void xp_mid(f2 (&v)[4]) XP_CPLX(mid, xp_mid1)

template <bool CONJ>
__device__ __forceinline__ void cmul3(f2 (&v)[4], const f2 (&t)[3]) {
  f2 a, b, c;
  if (!CONJ)
    asm("v_pk_mul_f32 %3, %0, %6 op_sel_hi:[1,0]\n\t"
        "v_pk_mul_f32 %4, %1, %7 op_sel_hi:[1,0]\n\t"
        "v_pk_mul_f32 %5, %2, %8 op_sel_hi:[1,0]\n\t"
        "v_pk_fma_f32 %0, %0, %6, %3 op_sel:[1,1,0] op_sel_hi:[0,1,1] neg_lo:[0,1,0]\n\t"
        "v_pk_fma_f32 %1, %1, %7, %4 op_sel:[1,1,0] op_sel_hi:[0,1,1] neg_lo:[0,1,0]\n\t"
        "v_pk_fma_f32 %2, %2, %8, %5 op_sel:[1,1,0] op_sel_hi:[0,1,1] neg_lo:[0,1,0]\n\t"
        "s_nop 1"
        : "+v"(v[1]), "+v"(v[2]), "+v"(v[3]), "=&v"(a), "=&v"(b), "=&v"(c)
        : "v"(t[0]), "v"(t[1]), "v"(t[2]));
  else
    asm("v_pk_mul_f32 %3, %0, %6 op_sel_hi:[1,0]\n\t"
        "v_pk_mul_f32 %4, %1, %7 op_sel_hi:[1,0]\n\t"
        "v_pk_mul_f32 %5, %2, %8 op_sel_hi:[1,0]\n\t"
        "v_pk_fma_f32 %0, %0, %6, %3 op_sel:[1,1,0] op_sel_hi:[0,1,1] neg_hi:[0,1,0]\n\t"
        "v_pk_fma_f32 %1, %1, %7, %4 op_sel:[1,1,0] op_sel_hi:[0,1,1] neg_hi:[0,1,0]\n\t"
        "v_pk_fma_f32 %2, %2, %8, %5 op_sel:[1,1,0] op_sel_hi:[0,1,1] neg_hi:[0,1,0]"
        : "+v"(v[1]), "+v"(v[2]), "+v"(v[3]), "=&v"(a), "=&v"(b), "=&v"(c)
        : "v"(t[0]), "v"(t[1]), "v"(t[2]));
}
template <bool CONJ>
__device__ __forceinline__ f2 cmul1(f2 v, f2 t) {
  f2 a;
  if (!CONJ)
    asm("v_pk_mul_f32 %1, %0, %2 op_sel_hi:[1,0]\n\t"
        "v_pk_fma_f32 %0, %0, %2, %1 op_sel:[1,1,0] op_sel_hi:[0,1,1] neg_lo:[0,1,0]"
        : "+v"(v), "=&v"(a) : "v"(t));
  else
    asm("v_pk_mul_f32 %1, %0, %2 op_sel_hi:[1,0]\n\t"
        "v_pk_fma_f32 %0, %0, %2, %1 op_sel:[1,1,0] op_sel_hi:[0,1,1] neg_hi:[0,1,0]"
        : "+v"(v), "=&v"(a) : "v"(t));
  return v;
}
__device__ __forceinline__ void xp_lo_lds(f2 (&v)[4], f2* patch, int lane) {
  f2* q = patch + (lane >> 2) * 16;
  const int c = lane & 3, sw = (lane >> 2) & 3;
#pragma unroll
  for (int j = 0; j < 4; ++j) q[4 * (j ^ sw) + c] = v[j];
  __builtin_amdgcn_fence(__ATOMIC_RELEASE, "wavefront");
  __builtin_amdgcn_wave_barrier();
  __builtin_amdgcn_fence(__ATOMIC_ACQUIRE, "wavefront");
  const f4 a = *(const f4*)(q + 4 * (c ^ sw)), b = *(const f4*)(q + 4 * (c ^ sw) + 2);
  v[0] = f2{a[0], a[1]}; v[1] = f2{a[2], a[3]}; v[2] = f2{b[0], b[1]}; v[3] = f2{b[2], b[3]};
  __builtin_amdgcn_fence(__ATOMIC_RELEASE, "wavefront");
  __builtin_amdgcn_wave_barrier();
}

struct Tw256 {
  f2 a[3], b[3], c[3];
};
__device__ __forceinline__ void make_tw256(int lane, Tw256& t) {
#pragma unroll
  for (int r = 1; r < 4; ++r) {
    t.a[r - 1] = twid((float)(lane * r) * (1.0f / 256.0f));
    t.b[r - 1] = twid((float)((lane & 15) * r) * (1.0f / 64.0f));
    t.c[r - 1] = twid((float)((lane & 3) * r) * (1.0f / 16.0f));
  }
#pragma unroll
  for (int r = 0; r < 3; ++r) {
    PIN2(t.a[r]);
    PIN2(t.b[r]);
    PIN2(t.c[r]);
  }
}
struct NoHook {
  __device__ __forceinline__ void operator()(int) const {}
};
template <class H>
__device__ __forceinline__ void fft256_fwd(f2 (&v)[4], int lane, const Tw256& t, f2* patch, const H& hook) {
  dft4<-1>(v[0], v[1], v[2], v[3]);
  cmul3<false>(v, t.a);
  xp_hi(v);
  hook(0);
  dft4<-1>(v[0], v[1], v[2], v[3]);
  cmul3<false>(v, t.b);
  xp_mid(v);
  hook(1);
  dft4<-1>(v[0], v[1], v[2], v[3]);
  cmul3<false>(v, t.c);
  xp_lo_lds(v, patch, lane);
  hook(2);
  dft4<-1>(v[0], v[1], v[2], v[3]);
}
template <class H>
__device__ __forceinline__ void fft256_inv(f2 (&v)[4], int lane, const Tw256& t, f2* patch, const H& hook) {
  dft4<1>(v[0], v[1], v[2], v[3]);
  xp_lo_lds(v, patch, lane);
  hook(0);
  cmul3<true>(v, t.c);
  dft4<1>(v[0], v[1], v[2], v[3]);
  xp_mid(v);
  hook(1);
  cmul3<true>(v, t.b);
  dft4<1>(v[0], v[1], v[2], v[3]);
  xp_hi(v);
  hook(2);
  cmul3<true>(v, t.a);
  dft4<1>(v[0], v[1], v[2], v[3]);
}

__device__ __forceinline__ float wave_sum(float x) {
  x += mdpp<0xB1>(x);
  x += mdpp<0x4E>(x);
  x += mdpp<0x141>(x);
  x += mdpp<0x140>(x);
  float r0 = __builtin_bit_cast(float, __builtin_amdgcn_readlane(__builtin_bit_cast(int, x), 0));
  float r1 = __builtin_bit_cast(float, __builtin_amdgcn_readlane(__builtin_bit_cast(int, x), 16));
  float r2 = __builtin_bit_cast(float, __builtin_amdgcn_readlane(__builtin_bit_cast(int, x), 32));
  float r3 = __builtin_bit_cast(float, __builtin_amdgcn_readlane(__builtin_bit_cast(int, x), 48));
  return (r0 + r1) + (r2 + r3);
}

__device__ __forceinline__ void ln_stats(const f2 (&v)[4], float& mean, float& rden) {
  const float K = __builtin_bit_cast(float, __builtin_amdgcn_readfirstlane(__builtin_bit_cast(int, v[0].x)));
  f2 s2 = f2{0.f, 0.f}, q2 = f2{0.f, 0.f};
#pragma unroll
  for (int j = 0; j < 4; ++j) {
    const f2 d = v[j] - K;
    s2 += d;
    q2 = __builtin_elementwise_fma(d, d, q2);
  }
  const float sm = wave_sum(s2.x + s2.y) * (1.0f / D_), qm = wave_sum(q2.x + q2.y) * (1.0f / D_);
  const float var = fmaxf(qm - sm * sm, 0.f);
  mean = bfr(K + sm);
  rden = __builtin_amdgcn_rcpf(bfr(__builtin_amdgcn_sqrtf(bfr(bfr(var) + 1e-5f))));
}

__device__ __forceinline__ int zpos(int k) { return 17 * (k & 3) + 4 * ((k >> 2) & 3) + ((k >> 4) & 3) + 68 * (k >> 6); }
constexpr int ZRS = 276;

constexpr int K0_GEMV = 48 * NSPLIT, K0_WT = 128, K0_W2 = 112, K0_Z = (int)(FLAG_BYTES / 8192);
__global__ __launch_bounds__(512) void k0_mod(const u16* __restrict__ c, const u16* __restrict__ wmod,
                                              const u16* __restrict__ x, const u16* __restrict__ wr,
                                              const u16* __restrict__ wi, const u16* __restrict__ w1,
                                              const u16* __restrict__ w2, char* __restrict__ ws) {
  __shared__ __attribute__((aligned(16))) char smem[17408];
  const int tid = threadIdx.x, row = tid >> 6, lane = tid & 63;
  const auto rs = WS_RSRC(ws);
  int b = blockIdx.x;
  if (b >= K0_GEMV + K0_WT + K0_W2) {
    int q = b - (K0_GEMV + K0_WT + K0_W2);
    if (q < K0_Z) {
      ((u4v*)ws)[q * 512 + tid] = u4v{0u, 0u, 0u, 0u};
      return;
    }
    return;
  }
  if (b >= K0_GEMV) {
    b -= K0_GEMV;
    if (b < K0_WT) {
      char* sr = smem;
      char* si = smem + 16 * 544;
      constexpr int LASTCH = (S_ * DF_ * 2) / 16 - 1;
      for (int it = tid; it < 16 * 34; it += 512) {
        const int i = it / 34, ch = it % 34;
        int gch = ((b + 128 * i) * (DF_ * 2)) / 16 + ch;
        gch = gch > LASTCH ? LASTCH : gch;
        *(u4v*)(sr + i * 544 + ch * 16) = *(const u4v*)((const char*)wr + (size_t)gch * 16);
        *(u4v*)(si + i * 544 + ch * 16) = *(const u4v*)((const char*)wi + (size_t)gch * 16);
      }
      __syncthreads();
      const int tbase = 64 * (b & 7) + 16 * ((b >> 3) & 3) + 4 * ((b >> 5) & 3);
      for (int it = tid; it < DF_ * 4; it += 512) {
        const int k = it >> 2, r3 = it & 3;
        u4v o;
#pragma unroll
        for (int r4 = 0; r4 < 4; ++r4) {
          const int i = r3 + 4 * r4;
          const int off = i * 544 + (((b + 128 * i) * (DF_ * 2)) & 15) + 2 * k;
          o[r4] = (unsigned)*(const u16*)(sr + off) | ((unsigned)*(const u16*)(si + off) << 16);
        }
        ST128(o, OFF_WT + (k * 512 + tbase + r3) * 16);
      }
    } else {
      b -= K0_WT;
      u16* t = (u16*)smem;
      const int g = lane >> 4, r = lane & 15;
      const int ng = b & 3, kp = b >> 2;
#pragma unroll
      for (int it = 0; it < 2; ++it) {
        const int idx = it * 512 + tid;
        const int rr = idx >> 4, ch = idx & 15;
        const int ks2 = kp * 2 + (rr >> 5), gg = (rr >> 3) & 3, j = rr & 7;
        const int e = ks2 / 7, i7 = ks2 % 7;
        int T0, T1;
        if (i7 < 3) { T0 = 14 * e + 2 * i7; T1 = T0 + 1; }
        else if (i7 < 6) { T0 = 14 * e + 7 + 2 * (i7 - 3); T1 = T0 + 1; }
        else { T0 = 14 * e + 6; T1 = 14 * e + 13; }
        const int f = (j < 4) ? (16 * T0 + 4 * gg + j) : (16 * T1 + 4 * gg + (j - 4));
        *(u4v*)(t + rr * 136 + ch * 8) = *(const u4v*)(w2 + (size_t)f * D_ + ng * 128 + ch * 8);
      }
      __syncthreads();
      const int nl = 32 * (row >> 1) + 8 * (r >> 2) + 4 * (row & 1) + (r & 3);
#pragma unroll
      for (int ksl = 0; ksl < 2; ++ksl) {
        u4v o;
#pragma unroll
        for (int jj = 0; jj < 4; ++jj)
          o[jj] = (unsigned)t[(ksl * 32 + g * 8 + 2 * jj) * 136 + nl] |
                  ((unsigned)t[(ksl * 32 + g * 8 + 2 * jj + 1) * 136 + nl] << 16);
        const int nt = ng * 8 + row;
        ST128(o, OFF_W2F + ((nt * 56 + kp * 2 + ksl) * 64 + lane) * 16);
      }
    }
    return;
  }
  float* red = (float*)smem;
  const int cgp = b % 48, ks = b / 48;
  const int cg = tid & 7, r = tid >> 3;
  const int rw = ks * 128 + r;
  const u4v wm0 = *(const u4v*)(wmod + (size_t)rw * 3072 + cgp * 64 + cg * 8);
  const u4v wm1 = *(const u4v*)(wmod + (size_t)(rw + 64) * 3072 + cgp * 64 + cg * 8);
  const float cv0 = bf_lo(c[rw]), cv1 = bf_lo(c[rw + 64]);
  const float s0 = bfr(cv0 / (1.0f + __expf(-cv0))), s1 = bfr(cv1 / (1.0f + __expf(-cv1)));
  float acc[8];
#pragma unroll
  for (int i = 0; i < 4; ++i) {
    acc[2 * i] = s0 * bf_lo(wm0[i]) + s1 * bf_lo(wm1[i]);
    acc[2 * i + 1] = s0 * bf_hi(wm0[i]) + s1 * bf_hi(wm1[i]);
  }
#pragma unroll
  for (int i = 0; i < 8; ++i) {
    acc[i] += mdpp<0x128>(acc[i]);
    acc[i] += __shfl_xor(acc[i], 16);
    acc[i] += __shfl_xor(acc[i], 32);
  }
  if (lane < 8) {
#pragma unroll
    for (int i = 0; i < 8; ++i) red[row * 64 + cg * 8 + i] = acc[i];
  }
  __syncthreads();
  if (tid < 64) {
    float sum = 0.f;
#pragma unroll
    for (int i = 0; i < 8; ++i) sum += red[i * 64 + tid];
    ((float*)(ws + OFF_MODP))[ks * 3072 + cgp * 64 + tid] = sum;
  }
}

__device__ __forceinline__ f4 mfma16(u4v a, u4v b, f4 c) {
  return __builtin_amdgcn_mfma_f32_16x16x32_bf16(__builtin_bit_cast(bf16x8, a), __builtin_bit_cast(bf16x8, b), c, 0, 0,
                                                  0);
}
__device__ __forceinline__ f2 silu2(float a, float b) {
  f2 h = f2{a, b};
  f2 t = h * -1.44269504088896340736f;
  f2 d = f2{__builtin_amdgcn_exp2f(t.x), __builtin_amdgcn_exp2f(t.y)} + 1.0f;
  return h * f2{__builtin_amdgcn_rcpf(d.x), __builtin_amdgcn_rcpf(d.y)};
}
__device__ __forceinline__ unsigned silu_pk(float a, float b) {
  f2 r = silu2(a, b);
  return pack_bf(r.x, r.y);
}
typedef __attribute__((address_space(1))) unsigned gu32;
#define DRAIN() asm volatile("s_waitcnt vmcnt(0)" ::: "memory")
typedef __attribute__((address_space(1))) unsigned long long gu64;
constexpr unsigned SPIN_CAP = 1u << 18;
__device__ __forceinline__ void wait_pair(gu64* flags) {
#pragma nounroll
  for (unsigned spin = 0; spin < SPIN_CAP; ++spin) {
    if (__hip_atomic_load(flags, __ATOMIC_RELAXED, __HIP_MEMORY_SCOPE_AGENT) == 0x0000000100000001ull) break;
    __builtin_amdgcn_s_sleep(1);
  }
  __builtin_amdgcn_fence(__ATOMIC_ACQUIRE, "wavefront");
  asm volatile("" ::: "memory");
}

#define PSLEEP 2
__device__ __forceinline__ void wait_cols(gu32* flags) {
#pragma nounroll
  for (unsigned spin = 0; spin < SPIN_CAP; ++spin) {
    unsigned f = 1u;
#pragma unroll
    for (int j = 0; j < 4; ++j)
      f &= __hip_atomic_load(flags + j * 64 * (FSP / 4), __ATOMIC_RELAXED, __HIP_MEMORY_SCOPE_AGENT);
    if (__builtin_amdgcn_ballot_w64(f != 1u) == 0ull) break;
    __builtin_amdgcn_s_sleep(PSLEEP);
  }
}

__device__ __forceinline__ int wt_off(int k, int t) { return (int)OFF_WT + (k * 512 + t) * 16; }

constexpr int L_W1 = 0;
constexpr int L_T = 112 * 1024;
constexpr int L_ZS = 112 * 1024;
constexpr int L_ST = L_ZS + 8 * ZRS * 8;
constexpr int L_TOTAL = 160 * 1024;
__device__ __forceinline__ constexpr int g1_ft(int g, int j) { return g < 8 ? (j & 3) : (g < 12 ? 4 + (j & 1) : 6); }
__device__ __forceinline__ constexpr int g1_ks(int g, int j) {
  return g < 8 ? 2 * g + (j >> 2) : (g < 12 ? 4 * (g - 8) + (j >> 1) : 8 * (g - 12) + j);
}
#define G1WIN 6
#define SCHED() __builtin_amdgcn_sched_barrier(0)
typedef short v4i16 __attribute__((ext_vector_type(4)));
__device__ __forceinline__ u4v w1frag(const char* lw, int ft, int ks) {
  typedef __attribute__((address_space(3))) v4i16* lp;
  const u2v a = __builtin_bit_cast(u2v, __builtin_amdgcn_ds_read_tr16_b64_v4i16((lp)(lw + (2 * ks) * 3584 + ft * 32)));
  const u2v b = __builtin_bit_cast(u2v, __builtin_amdgcn_ds_read_tr16_b64_v4i16((lp)(lw + (2 * ks + 1) * 3584 + ft * 32)));
  return u4v{a[0], a[1], b[0], b[1]};
}
constexpr int L_MODS = L_ZS + 8 * ZRS * 8;
constexpr int L_PX = L_T + 28672;
__global__ __launch_bounds__(512, 2) void kmega(const u16* __restrict__ x, const u16* __restrict__ w1,
                                                const u16* __restrict__ w2, char* ws, u16* __restrict__ out) {
  extern __shared__ __attribute__((aligned(16))) char dsm[];
  const int tid = threadIdx.x, lane = tid & 63;
  const int w = __builtin_amdgcn_readfirstlane(tid >> 6);
  const int bid = blockIdx.x;
  const int p = (bid & 7) * 2 + (bid >> 7), c = (bid >> 3) & 15;
  const int row0 = 128 * p + 8 * c;
  unsigned cw0;
  {
    unsigned long long pc;
    asm volatile("s_getpc_b64 %0" : "=s"(pc));
    cw0 = *(const gu32*)((pc & ~255ull) + (tid < 272 ? tid : 0) * 64);
  }
  const auto rs = WS_RSRC(ws);
  gu32* myA = (gu32*)(ws + OFF_CNT) + (p * 8 + (c >> 1)) * 16 + (c & 1);
  gu32* myB = myA + 128 * 16;
  gu64* depA = (gu64*)(ws + OFF_CNT) + (p * 8 + w) * 8;
  gu64* depB = depA + 128 * 8;
  gu32* myE = myB + 8;
  gu64* depE = depB + 4;
  const int mt0 = 8 * p + 4 * (c >> 3), nt0 = 4 * (c & 7);
  const int l56 = lane < 56 ? lane : lane - 56;
  const int r4_ = l56 / 14, ch_ = l56 - 14 * r4_;
  const char* w1g = (const char*)w1 + (size_t)(4 * w + r4_) * (F_ * 2) + c * 224 + ch_ * 16;
  char* w1l = dsm + L_W1 + ((w >> 1) * 32 + (2 * (w & 1) + (r4_ >> 1)) * 4 + (r4_ & 1)) * 224 + ch_ * 16;
  const int Ab = (int)OFF_W2F + ((nt0 * 56 + 7 * w) * 64 + lane) * 16;
  u4v S1[16];
  u4v A2[28];
  auto pf_load = [&](int i) __attribute__((always_inline)) {
    if (i < 16) S1[i] = *(const u4v*)(w1g + (size_t)i * (32 * F_ * 2));
    else if (i < 44) A2[i - 16] = *(const u4v*)(ws + Ab + (((i - 16) & 3) * 56 + ((i - 16) >> 2)) * 1024);
  };
  auto pf_put = [&](int i) __attribute__((always_inline)) {
    *(u4v*)(w1l + ((i & 3) * 128 + (i >> 3) * 16 + ((i >> 2) & 1) * 2) * 224) = S1[i];
  };
  auto pf = [&](int i0, int n) __attribute__((always_inline)) {
    SCHED();
#pragma unroll
    for (int i = 0; i < n; ++i) pf_load(i0 + i);
    SCHED();
  };
  u4v wv, bv;
  unsigned wn[4], bn[4];
  int mirr[4];
  Tw256 tw;
  {
    f2* zs = (f2*)(dsm + L_ZS);
    float* mods = (float*)(dsm + L_MODS);
    const int r0 = bid * 8;
    unsigned xr[4];
    const unsigned* xrow = (const unsigned*)(x + (size_t)(r0 + w) * D_);
#pragma unroll
    for (int j = 0; j < 4; ++j) xr[j] = xrow[lane + 64 * j];
    const float* mp = (const float*)(ws + OFF_MODP);
    f2 sm = *(const f2*)(mp + tid * 2);
#pragma unroll
    for (int k = 1; k < NSPLIT; ++k) sm += *(const f2*)(mp + k * 3072 + tid * 2);
    const int idx = bid * 12 + (tid < 6 ? tid : 0) * 2;
    f2 fin = *(const f2*)(mp + idx);
#pragma unroll
    for (int k = 1; k < NSPLIT; ++k) fin += *(const f2*)(mp + k * 3072 + idx);
    pf(0, 4);
    make_tw256(lane, tw);
    const int rp = tid & 3, kk = tid >> 2;
    f2 wpost = twid((float)kk * (1.0f / 512.0f));
    PIN2(wpost);
    f2 v[4];
#pragma unroll
    for (int j = 0; j < 4; ++j) v[j] = f2{bf_lo(xr[j]), bf_hi(xr[j])};
    float mean, rden;
    ln_stats(v, mean, rden);
    asm volatile("" ::"v"(cw0));
    sm = bfr2(sm);
    if (tid >= 256) sm = bfr2(sm + 1.0f);
    *(f2*)(mods + tid * 2) = sm;
    if (tid < 6) {
      fin = bfr2(fin);
      ST64(__builtin_bit_cast(u2v, fin), OFF_MODF + idx * 4);
      const int vec = (idx >> 9) - 2, d = idx & 511;
      if (vec >= 0 && vec < 3) {
        if (vec == 2) fin += 1.0f;
        ST32(pack_bf(fin.x, fin.y), OFF_MODC + ((vec * 64 + ((d >> 1) & 63)) * 4 + (d >> 7)) * 4);
      }
    }
    __syncthreads();
    wv = LD128(wt_off(bid, tid));
    if (bid == 0) {
      bv = LD128(wt_off(256, tid));
      const int m0 = (lane >> 4) + 4 * ((lane >> 2) & 3) + 16 * (lane & 3);
#pragma unroll
      for (int r4 = 0; r4 < 4; ++r4) {
        const int un = (S_ - (w + 8 * (m0 + 64 * r4))) & (S_ - 1);
        const int mm = un >> 3;
        mirr[r4] = ((un & 7) * 64 + 16 * (mm & 3) + 4 * ((mm >> 2) & 3) + ((mm >> 4) & 3)) * 4 + (mm >> 6);
      }
      if (w != 0) {
        const u4v a4 = LD128(wt_off(0, 64 * (8 - w) + 63 - lane));
        const u4v b4 = LD128(wt_off(256, 64 * (8 - w) + 63 - lane));
#pragma unroll
        for (int r4 = 0; r4 < 4; ++r4) { wn[r4] = a4[3 - r4]; bn[r4] = b4[3 - r4]; }
      } else {
#pragma unroll
        for (int r4 = 0; r4 < 4; ++r4) {
          wn[r4] = LD32(wt_off(0, mirr[r4] >> 2) + (mirr[r4] & 3) * 4);
          bn[r4] = LD32(wt_off(256, mirr[r4] >> 2) + (mirr[r4] & 3) * 4);
        }
      }
    }
    pf(4, 4);
#pragma unroll
    for (int j = 0; j < 4; ++j) {
      const int d = 2 * (lane + 64 * j);
      f2 sa = *(const f2*)(mods + d);
      f2 ca1 = *(const f2*)(mods + 512 + d);
      v[j] = bfr2(__builtin_elementwise_fma(v[j] - mean, ca1 * rden, sa));
    }
    fft256_fwd(v, lane, tw, (f2*)(dsm + L_PX + w * 2048), NoHook{});
    {
      f2* buf = zs + w * ZRS + lane + (lane >> 4);
#pragma unroll
      for (int r4 = 0; r4 < 4; ++r4) buf[68 * r4] = v[r4];
    }
    __syncthreads();
    {
      const int k = kk;
      const int k2 = (k == 0) ? 128 : 256 - k;
      const int pa = zpos(k), pb = zpos(k2);
      f4 oa, ob;
#pragma unroll
      for (int h = 0; h < 2; ++h) {
        const f2* zr = zs + (2 * rp + h) * ZRS;
        f2 za = zr[pa], zb = zr[pb];
        f2 E = f2{za.x + zb.x, za.y - zb.y};
        f2 Dm = f2{za.x - zb.x, za.y + zb.y};
        f2 O = f2{Dm.y, -Dm.x};
        f2 wo = cmul1<false>(O, wpost);
        f2 ra = E + wo;
        f2 t = E - wo;
        f2 rb = f2{t.x, -t.y};
        if (k == 0) {
          ra = f2{2.f * (za.x + za.y), 2.f * (za.x - za.y)};
          rb = f2{2.f * zb.x, -2.f * zb.y};
        }
        oa[2 * h] = ra.x; oa[2 * h + 1] = ra.y;
        ob[2 * h] = rb.x; ob[2 * h + 1] = rb.y;
      }
      ST128(__builtin_bit_cast(u4v, oa), OFF_XT + (k * CP + r0 + 2 * rp) * 8);
      ST128(__builtin_bit_cast(u4v, ob), OFF_XT + (k2 * CP + r0 + 2 * rp) * 8);
    }
    DRAIN();
    __syncthreads();
    if (tid < NREP)
      __hip_atomic_store((gu32*)(ws + OFF_FLR + (tid * 256 + bid) * FSP), 1u, __ATOMIC_RELAXED, __HIP_MEMORY_SCOPE_AGENT);
  }
  {
    f2* T = (f2*)(dsm + L_T);
    f2* T2 = (f2*)(dsm + L_T + 16384);
    f2* T3 = (f2*)(dsm + L_T + 32768);
    const int k = bid;
    f2 tw1[4];
#pragma unroll
    for (int j = 0; j < 4; ++j) {
      tw1[j] = twid((float)((lane + 64 * j) * w) * (1.0f / 2048.0f));
      PIN2(tw1[j]);
    }
    f2 wp[4], wm[4];
    if (k == 0) {
#pragma unroll
      for (int r4 = 0; r4 < 4; ++r4) {
        f2 h0 = f2{bf_lo(wv[r4]) + bf_lo(wn[r4]), bf_hi(wv[r4]) - bf_hi(wn[r4])};
        f2 h1 = f2{bf_lo(bv[r4]) + bf_lo(bn[r4]), bf_hi(bv[r4]) - bf_hi(bn[r4])};
        wp[r4] = (h0 + h1) * 0.25f;
        wm[r4] = (h0 - h1) * 0.25f;
        PIN2(wp[r4]);
        PIN2(wm[r4]);
      }
    } else {
#pragma unroll
      for (int r4 = 0; r4 < 4; ++r4) {
        wp[r4] = f2{bf_lo(wv[r4]), bf_hi(wv[r4])};
        PIN2(wp[r4]);
      }
    }
    pf(28, 3);
    if (w == 0) wait_cols((gu32*)(ws + OFF_FLR + ((bid % NREP) * 256 + lane) * FSP));
    asm volatile("s_barrier" ::: "memory");
    __builtin_amdgcn_fence(__ATOMIC_ACQUIRE, "wavefront");
    f2 v[4];
#pragma unroll
    for (int j = 0; j < 4; ++j) v[j] = __builtin_bit_cast(f2, LD64(OFF_XT + (k * CP + tid + 512 * j) * 8));
#pragma unroll
    for (int j = 0; j < 4; ++j) PIN2(v[j]);
    pf(8, 4);
    const int hb = w >> 2, a = tid & 255;
    constexpr float R = 0.70710678118654752440f;
    dft4<-1>(v[0], v[1], v[2], v[3]);
    if (hb) {
      v[1] = f2{v[1].x + v[1].y, v[1].y - v[1].x} * R;
      v[2] = f2{v[2].y, -v[2].x};
      v[3] = f2{v[3].y - v[3].x, -v[3].x - v[3].y} * R;
    }
#pragma unroll
    for (int q = 0; q < 4; ++q) T[(hb * 4 + q) * 256 + a] = v[q];
    __syncthreads();
    {
      const int q = w & 3;
      const float sg = (w < 4) ? 1.f : -1.f;
#pragma unroll
      for (int j = 0; j < 4; ++j) {
        f2 e = T[q * 256 + lane + 64 * j], o = T[(4 + q) * 256 + lane + 64 * j];
        v[j] = cmul1<false>(e + o * sg, tw1[j]);
      }
    }
    fft256_fwd(v, lane, tw, T2 + w * 256, [&](int i) __attribute__((always_inline)) {
      SCHED();
      if (i < 2) {
#pragma unroll
        for (int q = 0; q < 4; ++q) pf_put(4 * i + q);
      } else {
#pragma unroll
        for (int q = 12; q < 16; ++q) pf_load(q);
      }
      SCHED();
    });
    if (k != 0) {
#pragma unroll
      for (int i = 0; i < 4; ++i) {
        v[i] = cmul1<false>(v[i], wp[i]);
      }
    } else {
#pragma unroll
      for (int r4 = 0; r4 < 4; ++r4) T3[r4 * 512 + tid] = v[r4];
      __syncthreads();
#pragma unroll
      for (int r4 = 0; r4 < 4; ++r4) {
        f2 cm = T3[(mirr[r4] & 3) * 512 + (mirr[r4] >> 2)];
        v[r4] = cmul1<false>(v[r4], wp[r4]) + cmul1<false>(f2{cm.x, -cm.y}, wm[r4]);
      }
    }
    fft256_inv(v, lane, tw, T2 + w * 256, [&](int i) __attribute__((always_inline)) {
      SCHED();
      if (i == 0) {
#pragma unroll
        for (int q = 8; q < 12; ++q) pf_put(q);
      }
      if (i == 1) {
#pragma unroll
        for (int q = 16; q < 20; ++q) pf_load(q);
      }
      SCHED();
    });
#pragma unroll
    for (int j = 0; j < 4; ++j) T2[w * 256 + lane + 64 * j] = cmul1<true>(v[j], tw1[j]);
#pragma unroll
    for (int q = 12; q < 16; ++q) pf_put(q);
    __syncthreads();
    {
      const float sg = hb ? -1.f : 1.f;
#pragma unroll
      for (int q = 0; q < 4; ++q) v[q] = T2[q * 256 + a] + T2[(q + 4) * 256 + a] * sg;
      if (hb) {
        v[1] = f2{v[1].x - v[1].y, v[1].y + v[1].x} * R;
        v[2] = f2{-v[2].y, v[2].x};
        v[3] = f2{-v[3].x - v[3].y, v[3].x - v[3].y} * R;
      }
    }
    dft4<1>(v[0], v[1], v[2], v[3]);
#pragma unroll
    for (int j = 0; j < 4; ++j) ST64(__builtin_bit_cast(u2v, v[j]), OFF_ZT + (k * CP + tid + 512 * j) * 8);
    DRAIN();
    __syncthreads();
    if (tid < NREP)
      __hip_atomic_store((gu32*)(ws + OFF_FLG + (tid * 256 + k) * FSP), 1u, __ATOMIC_RELAXED, __HIP_MEMORY_SCOPE_AGENT);
  }
  {
    const char* lw = dsm + L_W1 + (lane >> 2) * 224 + (lane & 3) * 8 + w * (2 * 7168);
    u4v fr[14];
#pragma unroll
    for (int i = 0; i < 14; ++i) fr[i] = w1frag(lw, i % 7, i / 7);
    asm volatile("" ::: "memory");
#pragma unroll
    for (int i = 0; i < 14; ++i) *(u4v*)(dsm + L_W1 + w * (2 * 7168) + i * 1024 + lane * 16) = fr[i];
  }
  {
    f2* zs = (f2*)(dsm + L_ZS);
    const int rp = tid & 3, k = tid >> 2;
    const int k2 = (k == 0) ? 128 : 256 - k;
    u4v xr;
    const unsigned* xrow = (const unsigned*)(x + (size_t)(row0 + w) * D_);
#pragma unroll
    for (int j = 0; j < 4; ++j) xr[j] = xrow[lane + 64 * j];
    const u4v gav = LD128(OFF_MODC + lane * 16);
    const u4v sbv = LD128(OFF_MODC + 1024 + lane * 16);
    const u4v cbv = LD128(OFF_MODC + 2048 + lane * 16);
    f2 wq = twid((float)k * (1.0f / 512.0f));
    PIN2(wq);
    const int pa = zpos(k), pb = zpos(k2);
    pf(31, 3);
    if (w == 0) wait_cols((gu32*)(ws + OFF_FLG + ((bid % NREP) * 256 + lane) * FSP));
    asm volatile("s_barrier" ::: "memory");
    __builtin_amdgcn_fence(__ATOMIC_ACQUIRE, "wavefront");
    const f4 ya = __builtin_bit_cast(f4, LD128(OFF_ZT + (k * CP + row0 + 2 * rp) * 8));
    const f4 yb = __builtin_bit_cast(f4, LD128(OFF_ZT + (k2 * CP + row0 + 2 * rp) * 8));
#pragma unroll
    for (int h = 0; h < 2; ++h) {
      f2 a = f2{ya[2 * h], ya[2 * h + 1]}, b = f2{yb[2 * h], yb[2 * h + 1]};
      f2* zr = zs + (2 * rp + h) * ZRS;
      f2 Ze = f2{a.x + b.x, a.y - b.y};
      f2 Dm = f2{a.x - b.x, a.y + b.y};
      f2 Zo = cmul1<true>(Dm, wq);
      f2 za = f2{Ze.x - Zo.y, Ze.y + Zo.x};
      f2 zb = f2{Ze.x + Zo.y, -Ze.y + Zo.x};
      if (k == 0) {
        za = f2{a.x + a.y, a.x - a.y};
        zb = f2{2.f * b.x, -2.f * b.y};
      }
      zr[pa] = za;
      zr[pb] = zb;
    }
    pf(20, 4);
    __syncthreads();
    f2 v[4];
    {
      const f2* buf = zs + w * ZRS + lane + (lane >> 4);
#pragma unroll
      for (int r4 = 0; r4 < 4; ++r4) v[r4] = buf[68 * r4];
    }
    fft256_inv(v, lane, tw, (f2*)(dsm + L_PX + w * 2048), NoHook{});
    pf(24, 4);
    u4v x1p;
#pragma unroll
    for (int j = 0; j < 4; ++j) {
      f2 t = __builtin_elementwise_fma(f2{bf_lo(gav[j]), bf_hi(gav[j])} * FSC, v[j], f2{bf_lo(xr[j]), bf_hi(xr[j])});
      x1p[j] = pack_bf(t.x, t.y);
      v[j] = f2{bf_lo(x1p[j]), bf_hi(x1p[j])};
    }
    ST128(x1p, OFF_X1 + ((row0 + w) * 64 + lane) * 16);
    float mean, rden;
    ln_stats(v, mean, rden);
    u4v hp;
#pragma unroll
    for (int j = 0; j < 4; ++j) {
      f2 t = __builtin_elementwise_fma(v[j] - mean, f2{bf_lo(cbv[j]), bf_hi(cbv[j])} * rden,
                                       f2{bf_lo(sbv[j]), bf_hi(sbv[j])});
      hp[j] = pack_bf(t.x, t.y);
    }
    *(u4v*)(dsm + L_ST + (lane * 9 + w) * 16) = hp;
    __syncthreads();
    {
      const int ch = tid >> 3, r = tid & 7;
      u4v o = *(const u4v*)(dsm + L_ST + (ch * 9 + r) * 16);
      const int mt = row0 >> 4, r16 = (row0 & 8) + r;
      ST128(o, OFF_H2F + ((mt * 16 + (ch >> 2)) * 64 + (ch & 3) * 16 + r16) * 16);
    }
  }
  DRAIN();
  __syncthreads();
  if (tid == 0) __hip_atomic_store(myA, 1u, __ATOMIC_RELAXED, __HIP_MEMORY_SCOPE_AGENT);
  {
    const int ch = tid >> 3, r = tid & 7;
    u4v t = LD128(OFF_H2F + (((row0 >> 4) * 16 + (ch >> 2)) * 64 + (ch & 3) * 16 + (row0 & 8) + r) * 16);
    asm volatile("" ::"v"(t));
  }
  wait_pair(depA);
  u4v rb0, rb1;
  {
    u4v B[16];
#pragma unroll
    for (int ks = 0; ks < G1WIN; ++ks) B[ks] = LD128(OFF_H2F + (((8 * p + w) * 16 + ks) * 64 + lane) * 16);
    f4 acc[7];
#pragma unroll
    for (int ft = 0; ft < 7; ++ft) acc[ft] = f4{0.f, 0.f, 0.f, 0.f};
    const char* lw = dsm + L_W1 + lane * 16;
    unsigned off2 = L_W1 + lane * 16 + 8 * 7168;
    asm volatile("" : "+v"(off2));
    const char* lw2 = dsm + off2;
    const int e = c >> 1, odd = c & 1;
    const int hb = (int)OFF_HF + (((8 * p + w) * 56 + 7 * e) * 64 + lane) * 16;
    u4v Af[2][8];
#pragma unroll
    for (int j = 0; j < 8; ++j) Af[0][j] = *(const u4v*)(lw + (g1_ks(0, j) * 7 + g1_ft(0, j)) * 1024);
#pragma unroll
    for (int g = 0; g < 14; ++g) {
#pragma unroll
      for (int j = 0; j < 8; ++j) {
        acc[g1_ft(g, j)] = mfma16(Af[g & 1][j], B[g1_ks(g, j)], acc[g1_ft(g, j)]);
        if (g + 1 < 14)
          Af[(g + 1) & 1][j] = g1_ks(g + 1, j) < 8 ? *(const u4v*)(lw + (g1_ks(g + 1, j) * 7 + g1_ft(g + 1, j)) * 1024)
                                                   : *(const u4v*)(lw2 + ((g1_ks(g + 1, j) - 8) * 7 + g1_ft(g + 1, j)) * 1024);
        SCHED();
      }
#pragma unroll
      for (int q = 0; q < 2; ++q)
        if (2 * g + G1WIN + q < 16)
          B[2 * g + G1WIN + q] = LD128(OFF_H2F + (((8 * p + w) * 16 + 2 * g + G1WIN + q) * 64 + lane) * 16);
      if (g >= 5 && g < 10) { pf_load(24 + 2 * g); pf_load(25 + 2 * g); }
      if (g == 8 || g == 9 || g == 12) {
        const int i = g == 12 ? 2 : g - 8;
        f4 a = acc[2 * i], b = acc[2 * i + 1];
        u4v o = u4v{silu_pk(a[0], a[1]), silu_pk(a[2], a[3]), silu_pk(b[0], b[1]), silu_pk(b[2], b[3])};
        ST128(o, hb + (odd * 3 + i) * 1024);
      }
      if (g == 11) {
        DRAIN();
        asm volatile("s_barrier" ::: "memory");
        if (tid == 0) __hip_atomic_store(myE, 1u, __ATOMIC_RELAXED, __HIP_MEMORY_SCOPE_AGENT);
        rb0 = LD128(hb + odd * 3072);
        rb1 = LD128(hb + odd * 3072 + 1024);
      }
      SCHED();
    }
    wait_pair(depE);
    asm volatile("" ::"v"(rb0), "v"(rb1));
    f4 a = acc[6];
    u2v o = u2v{silu_pk(a[0], a[1]), silu_pk(a[2], a[3])};
    ST64(o, hb + 6 * 1024 + odd * 8);
  }
  {
    const int Bb = (int)OFF_HF + ((mt0 * 56 + 7 * w) * 64 + lane) * 16;
    u4v B[7][4];
    constexpr int KO[7] = {0, 1, 3, 4, 2, 5, 6};
    SCHED();
#pragma unroll
    for (int i = 0; i < 2; ++i)
#pragma unroll
      for (int mt = 0; mt < 4; ++mt) B[i][mt] = LD128(Bb + (mt * 56 + KO[i]) * 1024);
    SCHED();
    asm volatile("s_waitcnt vmcnt(8) lgkmcnt(0)\n\ts_barrier" ::: "memory");
    if (tid == 0) __hip_atomic_store(myB, 1u, __ATOMIC_RELAXED, __HIP_MEMORY_SCOPE_AGENT);
    u4v rb2 = LD128((int)OFF_HF + (((8 * p + w) * 56 + 7 * (c >> 1) + 3 * (c & 1) + 2) * 64 + lane) * 16);
    f4 acc[4][4];
#pragma unroll
    for (int i = 0; i < 4; ++i)
#pragma unroll
      for (int j = 0; j < 4; ++j) acc[i][j] = f4{0.f, 0.f, 0.f, 0.f};
#pragma unroll
    for (int i = 2; i < 4; ++i)
#pragma unroll
      for (int mt = 0; mt < 4; ++mt) B[i][mt] = LD128(Bb + (mt * 56 + KO[i]) * 1024);
    SCHED();
#pragma unroll
    for (int i = 0; i < 7; ++i) {
      if (i == 2) {
        wait_pair(depB);
        asm volatile("" ::"v"(rb2));
#pragma unroll
        for (int q = 4; q < 7; ++q)
#pragma unroll
          for (int mt = 0; mt < 4; ++mt) B[q][mt] = LD128(Bb + (mt * 56 + KO[q]) * 1024);
        SCHED();
      }
#pragma unroll
      for (int nt = 0; nt < 4; ++nt)
#pragma unroll
        for (int mt = 0; mt < 4; ++mt) acc[nt][mt] = mfma16(A2[KO[i] * 4 + nt], B[i][mt], acc[nt][mt]);
      SCHED();
    }
    const int g = lane >> 4, mr = lane & 15;
    const int fm = w >> 1, P = w & 1;
    const int m = (mt0 + fm) * 16 + mr;
    const int n = nt0 * 16 + 32 * P + 8 * g;
    unsigned xv[4];
#pragma unroll
    for (int i = 0; i < 4; ++i)
      xv[i] = __builtin_amdgcn_raw_buffer_load_b32(
          rs, (int)OFF_X1 + (m * 64 + (((n >> 1) + i) & 63)) * 16 + (n >> 7) * 4, 0, AUX_SC1);
    const f4 g0 = __builtin_bit_cast(f4, LD128(OFF_MODF + (2560 + n) * 4));
    const f4 g1 = __builtin_bit_cast(f4, LD128(OFF_MODF + (2560 + n + 4) * 4));
    f4* red = (f4*)dsm;
#pragma unroll
    for (int nt = 0; nt < 4; ++nt)
#pragma unroll
      for (int mt = 0; mt < 4; ++mt) red[(w * 16 + nt * 4 + mt) * 64 + lane] = acc[nt][mt];
    __syncthreads();
    f4 s0 = f4{0.f, 0.f, 0.f, 0.f}, s1 = s0;
#pragma unroll
    for (int ww = 0; ww < 8; ++ww) {
      s0 += red[(ww * 16 + (2 * P) * 4 + fm) * 64 + lane];
      s1 += red[(ww * 16 + (2 * P + 1) * 4 + fm) * 64 + lane];
    }
    u4v o;
    {
      f2 t0 = bfr2(f2{g0[0], g0[1]} * bfr2(f2{s0[0], s0[1]})), t1 = bfr2(f2{g0[2], g0[3]} * bfr2(f2{s0[2], s0[3]}));
      f2 t2 = bfr2(f2{g1[0], g1[1]} * bfr2(f2{s1[0], s1[1]})), t3 = bfr2(f2{g1[2], g1[3]} * bfr2(f2{s1[2], s1[3]}));
      o[0] = pack_bf(bf_lo(xv[0]) + t0.x, bf_hi(xv[0]) + t0.y);
      o[1] = pack_bf(bf_lo(xv[1]) + t1.x, bf_hi(xv[1]) + t1.y);
      o[2] = pack_bf(bf_lo(xv[2]) + t2.x, bf_hi(xv[2]) + t2.y);
      o[3] = pack_bf(bf_lo(xv[3]) + t3.x, bf_hi(xv[3]) + t3.y);
    }
    const auto ro = __builtin_amdgcn_make_buffer_rsrc(out, 0, S_ * D_ * 2, 0x00020000);
    __builtin_amdgcn_raw_buffer_store_b128(o, ro, (m * D_ + n) * 2, 0, AUX_SC1);
  }
}

extern "C" void kernel_launch(void* const* d_in, const int* in_sizes, int n_in, void* d_out, int out_size, void* d_ws,
                              size_t ws_size, hipStream_t stream) {
  const u16* x = (const u16*)d_in[0];
  const u16* wr = (const u16*)d_in[1];
  const u16* wi = (const u16*)d_in[2];
  const u16* w1 = (const u16*)d_in[4];
  const u16* w2 = (const u16*)d_in[5];
  const u16* c = (const u16*)d_in[6];
  const u16* wmod = (const u16*)d_in[7];
  char* ws = (char*)d_ws + ((ws_size - (size_t)WS_SPAN) & ~(size_t)((1 << 21) - 1));
  u16* out = (u16*)d_out;
  (void)hipFuncSetAttribute((const void*)kmega, hipFuncAttributeMaxDynamicSharedMemorySize, L_TOTAL);
  k0_mod<<<K0_GEMV + K0_WT + K0_W2 + K0_Z, 512, 0, stream>>>(c, wmod, x, wr, wi, w1, w2, ws);
  kmega<<<256, 512, L_TOTAL, stream>>>(x, w1, w2, ws, out);
}
```
